```python
import torch

def _softmax(x, axis=-1):
    return torch.softmax(x, dim=axis)
def _gelu(x):
    return torch.nn.functional.gelu(x, approximate="tanh")
def _silu(x):
    return torch.nn.functional.silu(x)
def _sigmoid(x):
    return torch.sigmoid(x)
def _layernorm(x, axis=-1, eps=1e-5):
    m = x.mean(dim=axis, keepdim=True)
    v = x.var(dim=axis, keepdim=True, unbiased=False)
    return (x - m) / torch.sqrt(v + eps)
def _scatter_add(tgt, idx, src, axis=0):
    return tgt.clone().index_add_(axis, idx, src)
def _segment_sum(src, seg, n):
    out = torch.zeros((n,) + src.shape[1:], dtype=src.dtype)
    return out.index_add_(0, seg, src)
def _pad(x, pw):
    flat = tuple(v for pair in reversed(pw) for v in pair)
    return torch.nn.functional.pad(x, flat)

kernel_name = 'gated_mix__softmax_full_mha__dense_activation_silu__prompt'

def setup_inputs():

    _w = (lambda a: (lambda t: t.to(torch.bfloat16) if t.is_floating_point() else t)(torch.as_tensor(a)))
    def _A_setup():
        B, S, D = 1, 2048, 640
        return {
            "x": _w((lambda *s: torch.randn(*s, dtype=torch.bfloat16) * 0.1)(B, S, D)),
            "wqkv": _w((lambda *s: torch.randn(*s, dtype=torch.bfloat16) * 0.4)(D, 3 * D)),
            "wo": _w((lambda *s: torch.randn(*s, dtype=torch.bfloat16) * 0.2)(D, D)),
        }
    _A_in = _A_setup()

    def _B_setup():
        B, S, D, F = 1, 2048, 640, 1536
        return {
            "x": _w((lambda *s: torch.randn(*s, dtype=torch.bfloat16) * 0.1)(B, S, D)),
            "w1": _w((lambda *s: torch.randn(*s, dtype=torch.bfloat16) * 3.2)(D, F) * (D ** -0.5)),
            "w2": _w((lambda *s: torch.randn(*s, dtype=torch.bfloat16) * 3.2)(F, D) * (F ** -0.5)),
        }
    _B_in = _B_setup()

    return {
        "A_x": _A_in["x"],
        "A_wqkv": _A_in["wqkv"],
        "A_wo": _A_in["wo"],
        "B_x": _B_in["x"],
        "B_w1": _B_in["w1"],
        "B_w2": _B_in["w2"],
        "gate_w": _w((lambda *s: torch.randn(*s, dtype=torch.bfloat16) * 0.1)(640) * 16.0),
    }

def reference(A_x, A_wqkv, A_wo, B_x, B_w1, B_w2, gate_w):
    def _A_ref(x, wqkv, wo):
        B, S, D = x.shape
        H = 32; Dh = D // H
        qkv = (lambda x, *d: x.permute(*d))((x @ wqkv).reshape(B, S, 3, H, Dh), 2, 0, 3, 1, 4)
        q, k, v = qkv[0], qkv[1], qkv[2]
        scale = Dh ** -0.5
        scores = torch.einsum("bhqd,bhkd->bhqk", q, k) * scale
        p = _softmax(scores, axis=-1)
        o = torch.einsum("bhqk,bhkd->bhqd", p, v)
        return (lambda x, *d: x.permute(*d))(o, 0, 2, 1, 3).reshape(B, S, D) @ wo

    def _B_ref(x, w1, w2):
        h = torch.einsum("bsd,df->bsf", x, w1)
        h = _silu(h)
        out = torch.einsum("bsf,fd->bsd", h, w2)
        return out

    x = A_x
    g = _sigmoid((x * gate_w).sum(-1, keepdims=True))
    a = _A_ref(x, A_wqkv, A_wo)
    b = _B_ref(x, B_w1, B_w2)
    return g * a + (1 - g) * b
```

```cpp
#include <hip/hip_runtime.h>
#include <hip/hip_bf16.h>
#include <stdint.h>

typedef short bf16x8 __attribute__((ext_vector_type(8)));
typedef short s16x4 __attribute__((ext_vector_type(4)));
typedef float f32x16 __attribute__((ext_vector_type(16)));
typedef float f32x4 __attribute__((ext_vector_type(4)));
typedef float f32x2 __attribute__((ext_vector_type(2)));
typedef unsigned u32x4 __attribute__((ext_vector_type(4)));
typedef unsigned u32x2 __attribute__((ext_vector_type(2)));
typedef __bf16 bf16x2_t __attribute__((ext_vector_type(2)));
#define LAS __attribute__((address_space(3)))
typedef LAS const char* lds_cptr;
#define DI __device__ __forceinline__
#define SBAR() __builtin_amdgcn_sched_barrier(0)
#define PIN(x) asm volatile("" : "+v"(x))
#define MFMA32(a, b, c) __builtin_amdgcn_mfma_f32_32x32x16_bf16(a, b, c, 0, 0, 0)
#define MFMA16(a, b, c) __builtin_amdgcn_mfma_f32_16x16x32_bf16(a, b, c, 0, 0, 0)

constexpr int S = 2048, DM = 640, NH = 32, DH = 20, FF = 1536;
constexpr int NT = S / 32;
constexpr int QTILE_B = 1536;
constexpr int KVTILE_B = 3072;
constexpr int NSIG = 68;
constexpr size_t WS_Q = 0;
constexpr size_t WS_KV = WS_Q + (size_t)NH * NT * QTILE_B;
constexpr size_t WS_AF = WS_KV + (size_t)NH * NT * KVTILE_B;
constexpr size_t WS_BF = WS_AF + (size_t)128 * NSIG * 1024;
constexpr size_t WS_KMAX = WS_BF + (size_t)8 * NSIG * 5 * 1024;
constexpr size_t WS_GATE = WS_KMAX + (size_t)160 * 32 * 4;
constexpr size_t WS_END = WS_GATE + (size_t)S * 2 * 4;
constexpr float QSCALE = 0.22360679774997896f * 1.4426950408889634f;
constexpr unsigned ONES2 = 0x3F803F80u;

DI unsigned cvtpk(float lo, float hi) { f32x2 v = {lo, hi}; bf16x2_t b = __builtin_convertvector(v, bf16x2_t); return __builtin_bit_cast(unsigned, b); }
DI float bf_lo(unsigned u) { return __uint_as_float(u << 16); }
DI float bf_hi(unsigned u) { return __uint_as_float(u & 0xffff0000u); }
DI float bf_round(float x) { return bf_lo(cvtpk(x, x)); }
DI void glds16(const void* sbase, unsigned voff, unsigned lds_base) {
    unsigned sv;
    asm volatile("s_mov_b32 %0, m0\n\ts_mov_b32 m0, %3\n\ts_nop 0\n\tglobal_load_lds_dwordx4 %1, %2\n\ts_mov_b32 m0, %0"
                 : "=&s"(sv) : "v"(voff), "s"(sbase), "s"(lds_base) : "memory");
}
typedef short v4i16_t __attribute__((ext_vector_type(4)));
DI s16x4 ldstr(lds_cptr p) { return __builtin_bit_cast(s16x4, __builtin_amdgcn_ds_read_tr16_b64_v4i16((LAS v4i16_t*)p)); }
DI bf16x8 lds128(lds_cptr p) { return *(const LAS bf16x8*)p; }
DI unsigned rfl(unsigned x) { return __builtin_amdgcn_readfirstlane(x); }

constexpr int K1_STAGE = 49152;
constexpr int K1_LDS = 3 * K1_STAGE;

template <bool VTYPE>
DI void k1_mainloop(f32x16 (&acc)[2][2], const char* xbase, const char* wbase, int ldw2, char* lds, int wid, int lane) {
    const unsigned lds0 = (unsigned)(uintptr_t)lds;
    const int h = lane >> 5, r = lane & 31;
    const int wm = wid >> 1, wn = wid & 1;
    unsigned xoff[2];
#pragma unroll
    for (int p = 0; p < 2; ++p) { const int rr = lane >> 3; const int sw = (4 * p + (lane >> 4)) & 7; xoff[p] = rr * 1280 + (((lane & 7) ^ sw) << 4); }
    const unsigned woff = (lane >> 4) * ldw2 + ((((lane & 15) ^ ((lane >> 4) << 2))) << 4);
    auto issue = [&](int kc, int st) {
        const unsigned sb = lds0 + st * K1_STAGE;
#pragma unroll
        for (int jj = 0; jj < 4; ++jj) { const int j = 4 * wid + jj; glds16(xbase + (size_t)j * 8 * 1280 + kc * 128, xoff[jj & 1], rfl(sb + j * 1024)); }
#pragma unroll
        for (int jj = 0; jj < 2; ++jj) { const int j = 2 * wid + jj; glds16(wbase + (size_t)(kc * 64 + 4 * j) * ldw2, woff, rfl(sb + 32768 + j * 1024)); }
    };
    unsigned xa[4];
    { const int row = wm * 64 + r; const int sw = (row >> 1) & 7;
#pragma unroll
      for (int ks = 0; ks < 4; ++ks) xa[ks] = row * 128 + (((2 * ks + h) ^ sw) << 4); }
    unsigned wa[2];
    { const int blk = (lane >> 4) & 1, q = (lane & 15) >> 2, p = lane & 3;
#pragma unroll
      for (int nt = 0; nt < 2; ++nt) { const int ch = wn * 8 + nt * 4 + 2 * blk + (p >> 1); wa[nt] = 32768 + (8 * h + q) * 256 + ((ch ^ (q << 2)) << 4) + 8 * (p & 1); } }
    issue(0, 0); issue(1, 1);
    int st = 0;
    for (int kc = 0; kc < 10; ++kc) {
        if (kc < 9) asm volatile("s_waitcnt vmcnt(6) lgkmcnt(0)\n\ts_barrier" ::: "memory");
        else asm volatile("s_waitcnt vmcnt(0) lgkmcnt(0)\n\ts_barrier" ::: "memory");
        if (kc + 2 < 10) { int s2 = st + 2; if (s2 >= 3) s2 -= 3; issue(kc + 2, s2); }
        lds_cptr sb = (lds_cptr)lds + st * K1_STAGE;
#pragma unroll
        for (int ks = 0; ks < 4; ++ks) {
            bf16x8 xf[2], wf[2];
#pragma unroll
            for (int mt = 0; mt < 2; ++mt) xf[mt] = lds128(sb + xa[ks] + mt * 4096);
#pragma unroll
            for (int nt = 0; nt < 2; ++nt) {
                const s16x4 lo = ldstr(sb + wa[nt] + ks * 4096), hi = ldstr(sb + wa[nt] + ks * 4096 + 1024);
                wf[nt] = __builtin_shufflevector(lo, hi, 0, 1, 2, 3, 4, 5, 6, 7);
            }
#pragma unroll
            for (int mt = 0; mt < 2; ++mt)
#pragma unroll
                for (int nt = 0; nt < 2; ++nt)
                    acc[mt][nt] = VTYPE ? MFMA32(xf[mt], wf[nt], acc[mt][nt]) : MFMA32(wf[nt], xf[mt], acc[mt][nt]);
        }
        st = st == 2 ? 0 : st + 1;
    }
}

__global__ __launch_bounds__(512, 2) void k1_proj(const unsigned short* __restrict__ x, const unsigned short* __restrict__ wqkv, const unsigned short* __restrict__ wo,
                                                  const unsigned short* __restrict__ w1, const unsigned short* __restrict__ w2, const unsigned short* __restrict__ gw, char* __restrict__ ws) {
    extern __shared__ __attribute__((aligned(16))) char lds[];
    const int tid = threadIdx.x, lane = tid & 63, wid = rfl(tid >> 6);
    const int xcd = blockIdx.x & 7, slot = blockIdx.x >> 3;
    const int rgrp = xcd >> 2, cgrp = xcd & 3, ncol = cgrp < 3 ? 7 : 6;
    const int h = lane >> 5, r = lane & 31;
    if (slot < 4 * ncol) {
        const int rt = 4 * rgrp + (slot & 3), ct = 7 * cgrp + (slot >> 2);
        const char* xbase = (const char*)x + (size_t)rt * 256 * 1280;
        const bool isH = ct >= 15;
        const char* wbase = isH ? (const char*)w1 + (ct - 15) * 256 : (const char*)wqkv + ct * 256;
        const int ldw2 = isH ? FF * 2 : 3 * DM * 2;
        f32x16 acc[2][2] = {};
        const int wm = wid >> 1, wn = wid & 1;
        if (ct >= 10 && ct < 15) {
            k1_mainloop<true>(acc, xbase, wbase, ldw2, lds, wid, lane);
#pragma unroll
            for (int nt = 0; nt < 2; ++nt) {
                const int phi = (ct - 10) * 128 + wn * 64 + nt * 32 + r;
                const int head = (phi * 3277) >> 16, d = phi - head * 20;
#pragma unroll
                for (int mt = 0; mt < 2; ++mt) {
                    const int T = rt * 8 + wm * 2 + mt;
                    char* dst = ws + WS_KV + ((size_t)head * NT + T) * KVTILE_B + 1536 + (h * 24 + d) * 16;
#pragma unroll
                    for (int s = 0; s < 2; ++s) {
                        const f32x16& a = acc[mt][nt];
                        u32x4 w = {cvtpk(a[8 * s], a[8 * s + 1]), cvtpk(a[8 * s + 2], a[8 * s + 3]), cvtpk(a[8 * s + 4], a[8 * s + 5]), cvtpk(a[8 * s + 6], a[8 * s + 7])};
                        *(u32x4*)(dst + s * 768) = w;
                        if (d == 19) *(u32x4*)(dst + s * 768 + 16) = (u32x4){ONES2, ONES2, ONES2, ONES2};
                    }
                }
            }
        } else {
            k1_mainloop<false>(acc, xbase, wbase, ldw2, lds, wid, lane);
            if (isH) {
#pragma unroll
                for (int mt = 0; mt < 2; ++mt) {
                    const int tok = rt * 256 + wm * 64 + mt * 32 + r, rho = tok >> 4;
#pragma unroll
                    for (int nt = 0; nt < 2; ++nt) {
                        const int sig = (ct - 15) * 4 + wn * 2 + nt;
                        char* dst = ws + WS_AF + ((size_t)rho * NSIG + sig) * 1024 + (h * 16 + (r & 15)) * 16;
                        float sv[16];
#pragma unroll
                        for (int i = 0; i < 16; ++i) { const float v = acc[mt][nt][i]; sv[i] = v * __builtin_amdgcn_rcpf(1.f + __builtin_amdgcn_exp2f(v * -1.4426950408889634f)); }
#pragma unroll
                        for (int s = 0; s < 2; ++s) {
                            u32x4 w = {cvtpk(sv[8 * s], sv[8 * s + 1]), cvtpk(sv[8 * s + 2], sv[8 * s + 3]), cvtpk(sv[8 * s + 4], sv[8 * s + 5]), cvtpk(sv[8 * s + 6], sv[8 * s + 7])};
                            *(u32x4*)(dst + s * 512) = w;
                        }
                    }
                }
            } else {
                const bool isQ = ct < 5;
                const float sc = isQ ? QSCALE : 1.f;
                const int ctl = isQ ? ct : ct - 5;
                char* img = ws + (isQ ? WS_Q : WS_KV);
                const int tileb = isQ ? QTILE_B : KVTILE_B;
#pragma unroll
                for (int nt = 0; nt < 2; ++nt)
#pragma unroll
                    for (int g = 0; g < 4; ++g) {
                        const int gam = ctl * 32 + wn * 16 + nt * 8 + 2 * g + h;
                        const int head = (gam * 205) >> 10, dg = gam - head * 5;
                        float nrm = 0.f;
#pragma unroll
                        for (int mt = 0; mt < 2; ++mt) {
                            const int T = rt * 8 + wm * 2 + mt;
                            const f32x16& a = acc[mt][nt];
                            const unsigned w0 = cvtpk(a[4 * g] * sc, a[4 * g + 1] * sc), w1_ = cvtpk(a[4 * g + 2] * sc, a[4 * g + 3] * sc);
                            char* dst = img + ((size_t)head * NT + T) * tileb;
                            if (dg < 4) *(u32x2*)(dst + ((dg >> 1) * 32 + r) * 16 + (dg & 1) * 8) = (u32x2){w0, w1_};
                            else *(u32x4*)(dst + 1024 + r * 16) = (u32x4){w0, w1_, ONES2, ONES2};
                            const float e0 = bf_lo(w0), e1 = bf_hi(w0), e2 = bf_lo(w1_), e3 = bf_hi(w1_);
                            nrm = fmaxf(nrm, (e0 * e0 + e1 * e1) + (e2 * e2 + e3 * e3));
                        }
                        if (!isQ) {
#pragma unroll
                            for (int o = 1; o < 32; o <<= 1) nrm = fmaxf(nrm, __shfl_xor(nrm, o));
                            if (r == 0) ((float*)(ws + WS_KMAX))[gam * 32 + rt * 4 + wm] = nrm;
                        }
                    }
            }
        }
    } else {
        const int aidx = (cgrp < 3) ? (rgrp * 20 + cgrp * 4 + (slot - 28)) : (rgrp * 20 + 12 + (slot - 24));
        for (int tok = aidx * 8 + wid; tok < S; tok += 320) {
            const unsigned* xr = (const unsigned*)(x + (size_t)tok * DM);
            const unsigned* gr = (const unsigned*)gw;
            float s = 0.f;
#pragma unroll
            for (int i = 0; i < 5; ++i) { const unsigned a = xr[lane + 64 * i], b = gr[lane + 64 * i]; s += bf_round(bf_lo(a) * bf_lo(b)); s += bf_round(bf_hi(a) * bf_hi(b)); }
#pragma unroll
            for (int o = 1; o < 64; o <<= 1) s += __shfl_xor(s, o);
            const float lg = bf_round(s);
            const float g = bf_round(1.f / (1.f + __expf(-lg)));
            const float omg = bf_round(1.f - g);
            if (lane == 0) *(f32x2*)(ws + WS_GATE + tok * 8) = (f32x2){g, omg};
        }
        const unsigned lds0 = (unsigned)(uintptr_t)lds;
        for (int sig = aidx; sig < NSIG; sig += 40) {
            const char* src = sig < 48 ? (const char*)w2 + (size_t)sig * 32 * 1280 : (const char*)wo + (size_t)(sig - 48) * 32 * 1280;
            __syncthreads();
#pragma unroll
            for (int jj = 0; jj < 5; ++jj) { const int j = wid * 5 + jj; glds16(src + j * 1024, lane * 16, rfl(lds0 + j * 1024)); }
            asm volatile("s_waitcnt vmcnt(0)\n\ts_barrier" ::: "memory");
            const int c = lane >> 4, s = c >> 1, hh = c & 1, q = (lane & 15) >> 2, p = lane & 3;
#pragma unroll
            for (int i = 0; i < 5; ++i) {
                const int nu = wid * 5 + i;
                lds_cptr a = (lds_cptr)lds + (16 * s + 4 * hh + q) * 1280 + (16 * nu + 4 * p) * 2;
                const s16x4 lo = ldstr(a), hi = ldstr(a + 8 * 1280);
                const bf16x8 f = __builtin_shufflevector(lo, hi, 0, 1, 2, 3, 4, 5, 6, 7);
                *(bf16x8*)(ws + WS_BF + (((size_t)(nu / 5) * NSIG + sig) * 5 + (nu % 5)) * 1024 + lane * 16) = f;
            }
        }
    }
}

constexpr int K2_CHUNK = 8 * KVTILE_B;
constexpr int K2_LDS = 3 * K2_CHUNK;

DI float swap32_add(float x) { auto rr = __builtin_amdgcn_permlane32_swap(__float_as_uint(x), __float_as_uint(x), false, false); return __uint_as_float(rr[0]) + __uint_as_float(rr[1]); }
DI float swap32_max(float x) { auto rr = __builtin_amdgcn_permlane32_swap(__float_as_uint(x), __float_as_uint(x), false, false); return fmaxf(__uint_as_float(rr[0]), __uint_as_float(rr[1])); }
DI bf16x8 make_qb(unsigned q0, unsigned q1, float M, int h) {
    const float mh = bf_hi(__float_as_uint(M)), r1 = M - mh, mm = bf_hi(__float_as_uint(r1)), r2 = r1 - mm, ml = bf_hi(__float_as_uint(r2));
    u32x4 w;
    w[0] = q0; w[1] = q1;
    w[2] = ((__float_as_uint(mh) >> 16) | (__float_as_uint(mm) & 0xffff0000u)) ^ 0x80008000u;
    w[3] = (__float_as_uint(ml) >> 16) ^ 0x8000u;
    if (h) w = (u32x4){0u, 0u, 0u, 0u};
    return __builtin_bit_cast(bf16x8, w);
}

DI void attn_fallback(f32x16& o, const char* kv, bf16x8 qa, unsigned q0, unsigned q1, int lane, int vslot) {
    const int h = lane >> 5, r = lane & 31;
    const f32x16 zero = {};
    float m = -INFINITY;
    const bf16x8 qb0 = make_qb(q0, q1, 0.f, h);
    for (int t = 0; t < NT; ++t) {
        const char* tb = kv + t * KVTILE_B;
        const bf16x8 ka = *(const bf16x8*)(tb + lane * 16), kb = *(const bf16x8*)(tb + 1024 + r * 16);
        f32x16 s = MFMA32(ka, qa, zero); s = MFMA32(kb, qb0, s);
#pragma unroll
        for (int i = 0; i < 16; ++i) m = fmaxf(m, s[i]);
    }
    m = swap32_max(m);
    const bf16x8 qb = make_qb(q0, q1, m, h);
    o = zero;
    for (int t = 0; t < NT; ++t) {
        const char* tb = kv + t * KVTILE_B;
        const bf16x8 ka = *(const bf16x8*)(tb + lane * 16), kb = *(const bf16x8*)(tb + 1024 + r * 16);
        const bf16x8 va = *(const bf16x8*)(tb + 1536 + vslot * 16), vb = *(const bf16x8*)(tb + 2304 + vslot * 16);
        f32x16 s = MFMA32(ka, qa, zero); s = MFMA32(kb, qb, s);
        u32x4 p0, p1;
#pragma unroll
        for (int i = 0; i < 4; ++i) { p0[i] = cvtpk(__builtin_amdgcn_exp2f(s[2 * i]), __builtin_amdgcn_exp2f(s[2 * i + 1])); p1[i] = cvtpk(__builtin_amdgcn_exp2f(s[8 + 2 * i]), __builtin_amdgcn_exp2f(s[9 + 2 * i])); }
        o = MFMA32(va, __builtin_bit_cast(bf16x8, p0), o);
        o = MFMA32(vb, __builtin_bit_cast(bf16x8, p1), o);
    }
}

__global__ __launch_bounds__(512, 2) void k2_attn(char* __restrict__ ws) {
    extern __shared__ __attribute__((aligned(16))) char lds[];
    const int tid = threadIdx.x, lane = tid & 63, wid = rfl(tid >> 6);
    const int h = lane >> 5, r = lane & 31;
    const int xcd = blockIdx.x & 7, slot = blockIdx.x >> 3;
    const int head = xcd * 4 + (slot & 3), qblk = slot >> 2;
    const char* kv = ws + WS_KV + (size_t)head * NT * KVTILE_B;
    const unsigned lds0 = (unsigned)(uintptr_t)lds;
    auto issue = [&](int c, int st) {
#pragma unroll
        for (int jj = 0; jj < 3; ++jj) { const int j = 3 * wid + jj; glds16(kv + (size_t)c * K2_CHUNK + j * 1024, lane * 16, rfl(lds0 + st * K2_CHUNK + j * 1024)); }
    };
    issue(0, 0); issue(1, 1);
    const int Tq = qblk * 8 + wid;
    const char* qt = ws + WS_Q + ((size_t)head * NT + Tq) * QTILE_B;
    const bf16x8 qa = *(const bf16x8*)(qt + lane * 16);
    const u32x2 qbr = *(const u32x2*)(qt + 1024 + r * 16);
    float kmx[5];
#pragma unroll
    for (int g = 0; g < 5; ++g) kmx[g] = ((const float*)(ws + WS_KMAX))[(head * 5 + g) * 32 + r];
    const f32x2 gate = *(const f32x2*)(ws + WS_GATE + (Tq * 32 + r) * 8);
#pragma unroll
    for (int g = 0; g < 5; ++g) {
#pragma unroll
        for (int o = 1; o < 32; o <<= 1) kmx[g] = fmaxf(kmx[g], __shfl_xor(kmx[g], o));
    }
    float M;
    { const u32x4 qw = __builtin_bit_cast(u32x4, qa);
      float n0, n1, n2;
      { const float a = bf_lo(qw[0]), b = bf_hi(qw[0]), c = bf_lo(qw[1]), d = bf_hi(qw[1]); n0 = (a * a + b * b) + (c * c + d * d); }
      { const float a = bf_lo(qw[2]), b = bf_hi(qw[2]), c = bf_lo(qw[3]), d = bf_hi(qw[3]); n1 = (a * a + b * b) + (c * c + d * d); }
      { const float a = bf_lo(qbr[0]), b = bf_hi(qbr[0]), c = bf_lo(qbr[1]), d = bf_hi(qbr[1]); n2 = (a * a + b * b) + (c * c + d * d); }
      float part = __builtin_sqrtf(n0 * (h ? kmx[2] : kmx[0])) + __builtin_sqrtf(n1 * (h ? kmx[3] : kmx[1]));
      if (!h) part += __builtin_sqrtf(n2 * kmx[4]);
      M = swap32_add(part) * 1.001f; }
    const bf16x8 qb = make_qb(qbr[0], qbr[1], M, h);
    const int vrow = r <= 20 ? r : (r < 28 ? 20 : r - 12);
    const int vslot = h * 24 + vrow;
    unsigned kaC = lane * 16, kbC = 1024 + r * 16, vC = 1536 + vslot * 16;
    unsigned kaN = kaC + K2_CHUNK, kbN = kbC + K2_CHUNK, vN = vC + K2_CHUNK;
    const lds_cptr L = (lds_cptr)lds;
    const f32x16 zero = {};
    f32x16 o = {}, sA, sB;
    bf16x8 ka, kb, va, vb;
    u32x4 plo, phi;
    asm volatile("s_waitcnt vmcnt(3)\n\ts_barrier" ::: "memory");
    ka = lds128(L + kaC); kb = lds128(L + kbC);
    sA = MFMA32(ka, qa, zero); sA = MFMA32(kb, qb, sA);
    ka = lds128(L + kaC + KVTILE_B); kb = lds128(L + kbC + KVTILE_B);
    va = lds128(L + vC);
    vb = (bf16x8){}; phi = (u32x4){};
    int stn = 1;
#define EXP4(X, i) do { X[i] = __builtin_amdgcn_exp2f(X[i]); X[i + 1] = __builtin_amdgcn_exp2f(X[i + 1]); X[i + 2] = __builtin_amdgcn_exp2f(X[i + 2]); X[i + 3] = __builtin_amdgcn_exp2f(X[i + 3]); } while (0)
#define KADDR(A, i) (((i) < 8) ? (A##C + (i) * KVTILE_B) : (A##N + ((i) - 8) * KVTILE_B))
#define STEP(C, N, i) do { \
        bf16x8 ka2, kb2, va2, vb2; \
        N = MFMA32(ka, qa, zero); \
        ka2 = lds128(L + KADDR(ka, (i) + 2)); \
        EXP4(C, 0); plo[0] = cvtpk(C[0], C[1]); plo[1] = cvtpk(C[2], C[3]); PIN(plo); SBAR(); \
        o = MFMA32(vb, __builtin_bit_cast(bf16x8, phi), o); \
        vb2 = lds128(L + vC + (i) * KVTILE_B + 768); \
        EXP4(C, 4); plo[2] = cvtpk(C[4], C[5]); plo[3] = cvtpk(C[6], C[7]); PIN(plo); SBAR(); \
        N = MFMA32(kb, qb, N); \
        kb2 = lds128(L + KADDR(kb, (i) + 2)); \
        EXP4(C, 8); phi[0] = cvtpk(C[8], C[9]); phi[1] = cvtpk(C[10], C[11]); PIN(phi); SBAR(); \
        o = MFMA32(va, __builtin_bit_cast(bf16x8, plo), o); \
        va2 = lds128(L + KADDR(v, (i) + 1)); \
        EXP4(C, 12); phi[2] = cvtpk(C[12], C[13]); phi[3] = cvtpk(C[14], C[15]); PIN(phi); SBAR(); \
        ka = ka2; kb = kb2; va = va2; vb = vb2; \
    } while (0)
    for (int c = 0; c < 8; ++c) {
        STEP(sA, sB, 0); STEP(sB, sA, 1); STEP(sA, sB, 2); STEP(sB, sA, 3); STEP(sA, sB, 4);
        asm volatile("s_waitcnt vmcnt(0)\n\ts_barrier" ::: "memory");
        if (c + 2 < 8) { int s2 = stn + 1; if (s2 == 3) s2 = 0; issue(c + 2, s2); }
        STEP(sB, sA, 5); STEP(sA, sB, 6); STEP(sB, sA, 7);
        kaC = kaN; kbC = kbN; vC = vN;
        const int d = (stn == 2) ? -2 * K2_CHUNK : K2_CHUNK;
        kaN += d; kbN += d; vN += d; stn = (stn == 2) ? 0 : stn + 1;
    }
    o = MFMA32(vb, __builtin_bit_cast(bf16x8, phi), o);
    float l; { auto rr = __builtin_amdgcn_permlane32_swap(__float_as_uint(o[8]), __float_as_uint(o[8]), false, false); l = __uint_as_float(rr[1]); }
    if (__builtin_expect(__any(!(l >= 8.6736174e-19f && l <= 1.0e30f)), 0)) {
        attn_fallback(o, kv, qa, qbr[0], qbr[1], lane, vslot);
        auto rr = __builtin_amdgcn_permlane32_swap(__float_as_uint(o[8]), __float_as_uint(o[8]), false, false); l = __uint_as_float(rr[1]);
    }
    const float f = gate[0] / l;
    const int tok = Tq * 32 + r;
    char* ab = ws + WS_AF + (size_t)(tok >> 4) * NSIG * 1024 + (tok & 15) * 16;
#pragma unroll
    for (int g = 0; g < 3; ++g) {
        const int gam = head * 5 + 2 * g + h;
        const int sig = 48 + (gam >> 3), c = ((gam >> 2) & 1) * 2 + (gam & 1), half = (gam >> 1) & 1;
        const u32x2 w = {cvtpk(o[4 * g] * f, o[4 * g + 1] * f), cvtpk(o[4 * g + 2] * f, o[4 * g + 3] * f)};
        if (g < 2 || h == 0) *(u32x2*)(ab + sig * 1024 + c * 256 + half * 8) = w;
    }
}

constexpr int K3_LDS = 4 * 20 * 1024;
__global__ __launch_bounds__(256, 1) void k3_out(const char* __restrict__ ws, unsigned short* __restrict__ out) {
    extern __shared__ __attribute__((aligned(16))) char lds[];
    const int tid = threadIdx.x, lane = tid & 63, wid = rfl(tid >> 6);
    const int xcd = blockIdx.x & 7, slot = blockIdx.x >> 3;
    const int ng = (xcd & 1) * 4 + (slot & 3), bt = (xcd >> 1) * 8 + (slot >> 2);
    const char* ap = ws + WS_AF + (size_t)bt * 4 * NSIG * 1024 + (size_t)wid * 17 * 1024 + lane * 16;
    const char* bp = ws + WS_BF + (size_t)ng * NSIG * 5 * 1024 + (size_t)wid * 17 * 5 * 1024 + lane * 16;
    float omg[4];
#pragma unroll
    for (int mt = 0; mt < 4; ++mt) omg[mt] = *(const float*)(ws + WS_GATE + (bt * 64 + mt * 16 + (lane & 15)) * 8 + 4);
    constexpr int DEPTH = 8;
    bf16x8 a[DEPTH][4], b[DEPTH][5];
    f32x4 acc[4][5] = {};
    auto load = [&](int i) {
#pragma unroll
        for (int mt = 0; mt < 4; ++mt) a[i % DEPTH][mt] = *(const bf16x8*)(ap + (size_t)mt * NSIG * 1024 + i * 1024);
#pragma unroll
        for (int nt = 0; nt < 5; ++nt) b[i % DEPTH][nt] = *(const bf16x8*)(bp + i * 5120 + nt * 1024);
    };
#pragma unroll
    for (int i = 0; i < DEPTH; ++i) load(i);
#pragma unroll
    for (int i = 0; i < 17; ++i) {
        if (i == 14 && wid == 2) {
#pragma unroll
            for (int mt = 0; mt < 4; ++mt)
#pragma unroll
                for (int nt = 0; nt < 5; ++nt) acc[mt][nt] *= omg[mt];
        }
#pragma unroll
        for (int mt = 0; mt < 4; ++mt)
#pragma unroll
            for (int nt = 0; nt < 5; ++nt) acc[mt][nt] = MFMA16(b[i % DEPTH][nt], a[i % DEPTH][mt], acc[mt][nt]);
        if (i + DEPTH < 17) load(i + DEPTH);
    }
    if (wid < 2) {
#pragma unroll
        for (int mt = 0; mt < 4; ++mt)
#pragma unroll
            for (int nt = 0; nt < 5; ++nt) acc[mt][nt] *= omg[mt];
    }
#pragma unroll
    for (int mt = 0; mt < 4; ++mt)
#pragma unroll
        for (int nt = 0; nt < 5; ++nt) *(f32x4*)(lds + (wid * 20 + mt * 5 + nt) * 1024 + lane * 16) = acc[mt][nt];
    __syncthreads();
    const int tok = bt * 64 + wid * 16 + (lane & 15);
#pragma unroll
    for (int nt = 0; nt < 5; ++nt) {
        f32x4 s = *(const f32x4*)(lds + (wid * 5 + nt) * 1024 + lane * 16);
#pragma unroll
        for (int w = 1; w < 4; ++w) s += *(const f32x4*)(lds + (w * 20 + wid * 5 + nt) * 1024 + lane * 16);
        *(u32x2*)(out + (size_t)tok * DM + ng * 80 + nt * 16 + (lane >> 4) * 4) = (u32x2){cvtpk(s[0], s[1]), cvtpk(s[2], s[3])};
    }
}

extern "C" void kernel_launch(void* const* d_in, const int* in_sizes, int n_in, void* d_out, int out_size, void* d_ws, size_t ws_size, hipStream_t stream) {
    const unsigned short* x = (const unsigned short*)d_in[0];
    const unsigned short* wqkv = (const unsigned short*)d_in[1];
    const unsigned short* wo = (const unsigned short*)d_in[2];
    const unsigned short* w1 = (const unsigned short*)d_in[4];
    const unsigned short* w2 = (const unsigned short*)d_in[5];
    const unsigned short* gw = (const unsigned short*)d_in[6];
    char* ws = (char*)d_ws;
    (void)hipFuncSetAttribute((const void*)k1_proj, hipFuncAttributeMaxDynamicSharedMemorySize, K1_LDS);
    (void)hipFuncSetAttribute((const void*)k2_attn, hipFuncAttributeMaxDynamicSharedMemorySize, K2_LDS);
    (void)hipFuncSetAttribute((const void*)k3_out, hipFuncAttributeMaxDynamicSharedMemorySize, K3_LDS);
    k1_proj<<<256, 512, K1_LDS, stream>>>(x, wqkv, wo, w1, w2, gw, ws);
    k2_attn<<<256, 512, K2_LDS, stream>>>(ws);
    k3_out<<<256, 256, K3_LDS, stream>>>(ws, (unsigned short*)d_out);
}
```

```cpp
#include <hip/hip_runtime.h>
#include <hip/hip_bf16.h>
#include <stdint.h>

typedef short bf16x8 __attribute__((ext_vector_type(8)));
typedef short s16x4 __attribute__((ext_vector_type(4)));
typedef float f32x16 __attribute__((ext_vector_type(16)));
typedef float f32x4 __attribute__((ext_vector_type(4)));
typedef float f32x2 __attribute__((ext_vector_type(2)));
typedef unsigned u32x4 __attribute__((ext_vector_type(4)));
typedef unsigned u32x2 __attribute__((ext_vector_type(2)));
typedef __bf16 bf16x2_t __attribute__((ext_vector_type(2)));
#define LAS __attribute__((address_space(3)))
typedef LAS const char* lds_cptr;
#define DI __device__ __forceinline__
#define SBAR() __builtin_amdgcn_sched_barrier(0)
#define PIN(x) asm volatile("" : "+v"(x))
#define MFMA32(a, b, c) __builtin_amdgcn_mfma_f32_32x32x16_bf16(a, b, c, 0, 0, 0)
#define MFMA16(a, b, c) __builtin_amdgcn_mfma_f32_16x16x32_bf16(a, b, c, 0, 0, 0)

constexpr int S = 2048, DM = 640, NH = 32, DH = 20, FF = 1536;
constexpr int NT = S / 32;
constexpr int QTILE_B = 1536;
constexpr int KVTILE_B = 3072;
constexpr int NSIG = 68;
constexpr size_t WS_Q = 0;
constexpr size_t WS_KV = WS_Q + (size_t)NH * NT * QTILE_B;
constexpr size_t WS_AF = WS_KV + (size_t)NH * NT * KVTILE_B;
constexpr size_t WS_BF = WS_AF + (size_t)128 * NSIG * 1024;
constexpr size_t WS_KMAX = WS_BF + (size_t)8 * NSIG * 5 * 1024;
constexpr size_t WS_GATE = WS_KMAX + (size_t)160 * 32 * 4;
constexpr size_t WS_CNT = WS_GATE + (size_t)S * 2 * 4;
constexpr size_t WS_END = WS_CNT + 8 * 256;
constexpr float QSCALE = 0.22360679774997896f * 1.4426950408889634f;
constexpr unsigned ONES2 = 0x3F803F80u;

DI unsigned cvtpk(float lo, float hi) { f32x2 v = {lo, hi}; bf16x2_t b = __builtin_convertvector(v, bf16x2_t); return __builtin_bit_cast(unsigned, b); }
DI float bf_lo(unsigned u) { return __uint_as_float(u << 16); }
DI float bf_hi(unsigned u) { return __uint_as_float(u & 0xffff0000u); }
DI float bf_round(float x) { return bf_lo(cvtpk(x, x)); }
DI void glds16(const void* sbase, unsigned voff, unsigned lds_base) {
    unsigned sv;
    asm volatile("s_mov_b32 %0, m0\n\ts_mov_b32 m0, %3\n\ts_nop 0\n\tglobal_load_lds_dwordx4 %1, %2\n\ts_mov_b32 m0, %0"
                 : "=&s"(sv) : "v"(voff), "s"(sbase), "s"(lds_base) : "memory");
}
typedef short v4i16_t __attribute__((ext_vector_type(4)));
DI s16x4 ldstr(lds_cptr p) { return __builtin_bit_cast(s16x4, __builtin_amdgcn_ds_read_tr16_b64_v4i16((LAS v4i16_t*)p)); }
DI bf16x8 lds128(lds_cptr p) { return *(const LAS bf16x8*)p; }
DI unsigned rfl(unsigned x) { return __builtin_amdgcn_readfirstlane(x); }
#define STAMP(k, i) do { } while (0)

#define K1ROT 0
constexpr int K1_STAGE = 49152;
constexpr int K1_LDS = 3 * K1_STAGE;

DI float rdl(float x, int l) { return __uint_as_float(__builtin_amdgcn_readlane(__float_as_uint(x), l)); }
#define WT 1
typedef __amdgpu_buffer_rsrc_t rsrc_t;
DI rsrc_t ws_rsrc(char* ws) { return __builtin_amdgcn_make_buffer_rsrc(ws, 0, (int)WS_END, 0x00020000); }
DI void st16(rsrc_t rs, unsigned off, u32x4 v) { __builtin_amdgcn_raw_buffer_store_b128(v, rs, off, 0, WT ? 16 : 0); }
DI void st8(rsrc_t rs, unsigned off, u32x2 v) { __builtin_amdgcn_raw_buffer_store_b64(v, rs, off, 0, WT ? 16 : 0); }
template <int CTRL> DI float dpp_max(float x) { const float y = __uint_as_float(__builtin_amdgcn_update_dpp(0u, __float_as_uint(x), CTRL, 0xf, 0xf, true)); return fmaxf(x, y); }
template <int CTRL> DI float dpp_add(float x) { const float y = __uint_as_float(__builtin_amdgcn_update_dpp(0u, __float_as_uint(x), CTRL, 0xf, 0xf, true)); return x + y; }

struct K1Dma {
    const char* xbase; const char* wbase; int ldw2, k0; unsigned lds0, xoff0, xoff1, woff;
    DI void init(const char* xb, const char* wb, int ld, int k0_, char* lds, int lane) {
        xbase = xb; wbase = wb; ldw2 = ld; k0 = k0_; lds0 = (unsigned)(uintptr_t)lds;
        const int rr = lane >> 3;
        xoff0 = rr * 1280 + (((lane & 7) ^ ((lane >> 4) & 7)) << 4); xoff1 = rr * 1280 + (((lane & 7) ^ ((4 + (lane >> 4)) & 7)) << 4);
        woff = (lane >> 4) * ldw2 + ((((lane & 15) ^ ((lane >> 4) << 2))) << 4);
    }
    DI int kact(int kc_) const { int kc = kc_ + k0; return kc >= 10 ? kc - 10 : kc; }
    DI void xp(int kc, int st, int j, int par) const { glds16(xbase + (size_t)j * 8 * 1280 + kc * 128, par ? xoff1 : xoff0, lds0 + st * K1_STAGE + j * 1024); }
    DI void wp(int kc, int st, int j) const { glds16(wbase + (size_t)(kc * 64 + 4 * j) * ldw2, woff, lds0 + st * K1_STAGE + 32768 + j * 1024); }
};
DI void k1_first(const K1Dma& d, int wid) {
    const int kc = d.kact(0);
#pragma unroll
    for (int jj = 0; jj < 2; ++jj) d.wp(kc, 0, 2 * wid + jj);
#pragma unroll
    for (int jj = 0; jj < 4; ++jj) d.xp(kc, 0, 4 * wid + jj, jj & 1);
}
DI void k1_loader(const K1Dma& d, int lw) {
    auto issue = [&](int kc_, int st) {
        const int kc = d.kact(kc_);
#pragma unroll
        for (int jj = 0; jj < 4; ++jj) d.wp(kc, st, 4 * lw + jj);
#pragma unroll
        for (int jj = 0; jj < 8; ++jj) d.xp(kc, st, 8 * lw + jj, jj & 1);
    };
    issue(1, 1);
    asm volatile("s_waitcnt vmcnt(12)\n\ts_barrier" ::: "memory");
    issue(2, 2);
    int st = 0;
    for (int kc = 0; kc < 9; ++kc) {
        if (kc < 8) asm volatile("s_waitcnt vmcnt(12)\n\ts_barrier" ::: "memory");
        else asm volatile("s_waitcnt vmcnt(0)\n\ts_barrier" ::: "memory");
        if (kc + 3 < 10) issue(kc + 3, st);
        st = st == 2 ? 0 : st + 1;
    }
}
template <bool VTYPE>
DI void k1_mainloop(f32x16 (&acc)[4][2], char* lds, int wid, int lane, char* ws) {
    const int h = lane >> 5, r = lane & 31;
    const int wm = wid >> 1, wn = wid & 1;
    unsigned xa[4];
    { const int row = wm * 128 + r; const int sw = (row >> 1) & 7;
#pragma unroll
      for (int ks = 0; ks < 4; ++ks) xa[ks] = row * 128 + (((2 * ks + h) ^ sw) << 4); }
    unsigned wa[2];
    { const int blk = (lane >> 4) & 1, q = (lane & 15) >> 2, p = lane & 3;
#pragma unroll
      for (int nt = 0; nt < 2; ++nt) { const int ch = wn * 8 + nt * 4 + 2 * blk + (p >> 1); wa[nt] = 32768 + (8 * h + q) * 256 + ((ch ^ (q << 2)) << 4) + 8 * (p & 1); } }
    bf16x8 xf[2][4], wf[2][2];
    auto rdw = [&](int buf, int nt, lds_cptr sb, int ks) {
        const s16x4 lo = ldstr(sb + wa[nt] + ks * 4096), hi = ldstr(sb + wa[nt] + ks * 4096 + 1024);
        wf[buf][nt] = __builtin_shufflevector(lo, hi, 0, 1, 2, 3, 4, 5, 6, 7);
    };
    auto rdx = [&](int buf, int mt, lds_cptr sb, int ks) { xf[buf][mt] = lds128(sb + xa[ks] + mt * 4096); };
    auto mm = [&](int buf, int mt, int nt) { acc[mt][nt] = VTYPE ? MFMA32(xf[buf][mt], wf[buf][nt], acc[mt][nt]) : MFMA32(wf[buf][nt], xf[buf][mt], acc[mt][nt]); };
    STAMP(3, 0);
    asm volatile("s_waitcnt vmcnt(0)\n\ts_barrier" ::: "memory");
    STAMP(3, 1);
    { lds_cptr sb = (lds_cptr)lds; rdw(0, 0, sb, 0); rdx(0, 0, sb, 0); rdw(0, 1, sb, 0); rdx(0, 1, sb, 0); rdx(0, 2, sb, 0); rdx(0, 3, sb, 0); }
    int st = 0;
#define K1_KSTEP(ks, NEXT) do { const int b = (ks) & 1, n = b ^ 1; lds_cptr np = (ks) < 3 ? sb : sn; const int nk = ((ks) + 1) & 3; \
        SBAR(); \
        mm(b, 0, 0); SBAR(); if (NEXT) { rdw(n, 0, np, nk); } SBAR(); \
        mm(b, 0, 1); SBAR(); if (NEXT) { rdx(n, 0, np, nk); } SBAR(); \
        mm(b, 1, 0); SBAR(); if (NEXT) { rdw(n, 1, np, nk); } SBAR(); \
        mm(b, 1, 1); SBAR(); if (NEXT) { rdx(n, 1, np, nk); } SBAR(); \
        mm(b, 2, 0); SBAR(); if (NEXT) { rdx(n, 2, np, nk); } SBAR(); \
        mm(b, 2, 1); SBAR(); if (NEXT) { rdx(n, 3, np, nk); } SBAR(); \
        mm(b, 3, 0); SBAR(); \
        mm(b, 3, 1); SBAR(); } while (0)
#pragma unroll 1
    for (int kc = 0; kc < 9; ++kc) {
        lds_cptr sb = (lds_cptr)lds + st * K1_STAGE;
        st = st == 2 ? 0 : st + 1;
        lds_cptr sn = (lds_cptr)lds + st * K1_STAGE;
        K1_KSTEP(0, true); K1_KSTEP(1, true); K1_KSTEP(2, true);
        asm volatile("s_waitcnt lgkmcnt(0)\n\ts_barrier" ::: "memory");
        K1_KSTEP(3, true);
    }
    { lds_cptr sb = (lds_cptr)lds + st * K1_STAGE; lds_cptr sn = sb;
      K1_KSTEP(0, true); K1_KSTEP(1, true); K1_KSTEP(2, true); K1_KSTEP(3, false); }
}

__global__ __launch_bounds__(512, 2) void k1_proj(const unsigned short* __restrict__ x, const unsigned short* __restrict__ wqkv, const unsigned short* __restrict__ wo,
                                                  const unsigned short* __restrict__ w1, const unsigned short* __restrict__ w2, const unsigned short* __restrict__ gw, char* __restrict__ ws) {
    extern __shared__ __attribute__((aligned(16))) char lds[];
    const int tid = threadIdx.x, lane = tid & 63, wid = rfl(tid >> 6);
    const int xcd = blockIdx.x & 7, slot = blockIdx.x >> 3;
    const int rgrp = xcd >> 2, cgrp = xcd & 3, ncol = cgrp < 3 ? 7 : 6;
    const int h = lane >> 5, r = lane & 31;
    const rsrc_t rs = ws_rsrc(ws);
    STAMP(0, 0);
    if (slot < 4 * ncol) {
        const int rt = 4 * rgrp + (slot & 3), ct = 7 * cgrp + (slot >> 2);
        const char* xbase = (const char*)x + (size_t)rt * 256 * 1280;
        const bool isH = ct >= 15, isV = ct >= 10 && ct < 15;
        const char* wbase = isH ? (const char*)w1 + (ct - 15) * 256 : (const char*)wqkv + ct * 256;
        const int ldw2 = isH ? FF * 2 : 3 * DM * 2;
        const int wm = (wid >> 1) & 1, wn = wid & 1;
        K1Dma dma; dma.init(xbase, wbase, ldw2, K1ROT, lds, lane);
        k1_first(dma, wid);
        if (wid >= 4) {
            k1_loader(dma, wid - 4);
        } else {
            f32x16 acc[4][2] = {};
            if (isV) {
                k1_mainloop<true>(acc, lds, wid, lane, ws);
                STAMP(0, 1);
#pragma unroll
                for (int nt = 0; nt < 2; ++nt) {
                    const int phi = (ct - 10) * 128 + wn * 64 + nt * 32 + r;
                    const int head = (phi * 3277) >> 16, d = phi - head * 20;
                    const unsigned dst = (unsigned)WS_KV + (unsigned)(head * NT + rt * 8 + wm * 4) * KVTILE_B + 1536 + (h * 24 + d) * 16;
#pragma unroll
                    for (int mt = 0; mt < 4; ++mt)
#pragma unroll
                        for (int s = 0; s < 2; ++s) {
                            const f32x16& a = acc[mt][nt];
                            const u32x4 w = {cvtpk(a[8 * s], a[8 * s + 1]), cvtpk(a[8 * s + 2], a[8 * s + 3]), cvtpk(a[8 * s + 4], a[8 * s + 5]), cvtpk(a[8 * s + 6], a[8 * s + 7])};
                            st16(rs, dst + mt * KVTILE_B + s * 768, w);
                            if (d >= 18) { const unsigned f = d == 19 ? ONES2 : 0u; st16(rs, dst + mt * KVTILE_B + s * 768 + (d == 19 ? 16 : 48), (u32x4){f, f, f, f}); }
                        }
                }
            } else {
                k1_mainloop<false>(acc, lds, wid, lane, ws);
                STAMP(0, 1);
                if (isH) {
#pragma unroll
                    for (int mt = 0; mt < 4; ++mt) {
                        const int tok = rt * 256 + wm * 128 + mt * 32 + r, rho = tok >> 4;
#pragma unroll
                        for (int nt = 0; nt < 2; ++nt) {
                            const int sig = (ct - 15) * 4 + wn * 2 + nt;
                            const unsigned dst = (unsigned)WS_AF + (unsigned)(rho * NSIG + sig) * 1024 + (h * 16 + (r & 15)) * 16;
                            float sv[16];
#pragma unroll
                            for (int i = 0; i < 16; ++i) { const float v = acc[mt][nt][i]; sv[i] = v * __builtin_amdgcn_rcpf(1.f + __builtin_amdgcn_exp2f(v * -1.4426950408889634f)); }
#pragma unroll
                            for (int s = 0; s < 2; ++s) {
                                const u32x4 w = {cvtpk(sv[8 * s], sv[8 * s + 1]), cvtpk(sv[8 * s + 2], sv[8 * s + 3]), cvtpk(sv[8 * s + 4], sv[8 * s + 5]), cvtpk(sv[8 * s + 6], sv[8 * s + 7])};
                                st16(rs, dst + s * 512, w);
                            }
                        }
                    }
                } else {
                    const bool isQ = ct < 5;
                    const float sc = isQ ? QSCALE : 1.f;
                    const int ctl = isQ ? ct : ct - 5;
#pragma unroll
                    for (int nt = 0; nt < 2; ++nt)
#pragma unroll
                        for (int g = 0; g < 4; ++g) {
                            const int gl = wn * 16 + nt * 8 + 2 * g + h;
                            float nrm = 0.f;
#pragma unroll
                            for (int mt = 0; mt < 4; ++mt) {
                                const f32x16& a = acc[mt][nt];
                                const unsigned w0 = cvtpk(a[4 * g] * sc, a[4 * g + 1] * sc), w1_ = cvtpk(a[4 * g + 2] * sc, a[4 * g + 3] * sc);
                                *(u32x2*)(lds + ((wm * 4 + mt) * 32 + gl) * 256 + r * 8) = (u32x2){w0, w1_};
                                const float e0 = bf_lo(w0), e1 = bf_hi(w0), e2 = bf_lo(w1_), e3 = bf_hi(w1_);
                                nrm = fmaxf(nrm, (e0 * e0 + e1 * e1) + (e2 * e2 + e3 * e3));
                            }
                            if (!isQ) {
                                nrm = dpp_max<0xB1>(nrm); nrm = dpp_max<0x4E>(nrm); nrm = dpp_max<0x141>(nrm); nrm = dpp_max<0x140>(nrm);
                                { auto rr = __builtin_amdgcn_permlane16_swap(__float_as_uint(nrm), __float_as_uint(nrm), false, false); nrm = fmaxf(__uint_as_float(rr[0]), __uint_as_float(rr[1])); }
                                if (r == 0) ((float*)(ws + WS_KMAX))[(ctl * 32 + gl) * 16 + rt * 2 + wm] = nrm;
                            }
                        }
                }
            }
        }
        if (!isH && !isV) {
            __syncthreads();
            const bool isQ = ct < 5;
            const int ctl = isQ ? ct : ct - 5;
            const unsigned img = (unsigned)(isQ ? WS_Q : WS_KV);
            const unsigned tileb = isQ ? QTILE_B : KVTILE_B;
            const int T = rt * 8 + wid;
            const char* lb = lds + wid * 8192 + r * 8;
#pragma unroll 4
            for (int it = 0; it < 16; ++it) {
                const int gl = 2 * it + h, gam = ctl * 32 + gl;
                const int head = (gam * 205) >> 10, dg = gam - head * 5;
                const unsigned tb = img + (unsigned)(head * NT + T) * tileb;
                const u32x2 me = *(const u32x2*)(lb + gl * 256);
                if (dg == 4) st16(rs, tb + 1024 + r * 16, (u32x4){me[0], me[1], ONES2, ONES2});
                else if ((dg & 1) == 0) {
                    if (gl < 31) { const u32x2 nx = *(const u32x2*)(lb + (gl + 1) * 256); st16(rs, tb + (dg >> 1) * 512 + r * 16, (u32x4){me[0], me[1], nx[0], nx[1]}); }
                    else st8(rs, tb + (dg >> 1) * 512 + r * 16, me);
                } else if (gl == 0) st8(rs, tb + (dg >> 1) * 512 + r * 16 + 8, me);
            }
        }
    } else {
        const int aidx = (cgrp < 3) ? (rgrp * 20 + cgrp * 4 + (slot - 28)) : (rgrp * 20 + 12 + (slot - 24));
        const unsigned lds0 = (unsigned)(uintptr_t)lds;
        if (aidx == 0 && tid < 8) *(unsigned*)(ws + WS_CNT + tid * 256) = 0u;
        const int nsl = aidx + 40 < NSIG ? 2 : 1;
        for (int u = 0; u < nsl; ++u) {
            const int sig = aidx + 40 * u;
            const char* src = sig < 48 ? (const char*)w2 + (size_t)sig * 32 * 1280 : (const char*)wo + (size_t)(sig - 48) * 32 * 1280;
#pragma unroll
            for (int jj = 0; jj < 5; ++jj) { const int j = wid * 5 + jj; glds16(src + j * 1024, lane * 16, lds0 + u * 40960 + j * 1024); }
        }
        {
            unsigned gv[5], xv[7][5];
#pragma unroll
            for (int i = 0; i < 5; ++i) gv[i] = ((const unsigned*)gw)[lane + 64 * i];
#pragma unroll
            for (int t = 0; t < 7; ++t) { int tok = aidx * 52 + wid * 7 + t; if (wid * 7 + t >= 52 || tok >= S) tok = 0;
#pragma unroll
                for (int i = 0; i < 5; ++i) xv[t][i] = ((const unsigned*)(x + (size_t)tok * DM))[lane + 64 * i]; }
#pragma unroll
            for (int t = 0; t < 7; ++t) {
                float s = 0.f;
#pragma unroll
                for (int i = 0; i < 5; ++i) { s += bf_round(bf_lo(xv[t][i]) * bf_lo(gv[i])); s += bf_round(bf_hi(xv[t][i]) * bf_hi(gv[i])); }
                s = dpp_add<0xB1>(s); s = dpp_add<0x4E>(s); s = dpp_add<0x141>(s); s = dpp_add<0x140>(s);
                s = (rdl(s, 0) + rdl(s, 16)) + (rdl(s, 32) + rdl(s, 48));
                const float lg = bf_round(s);
                const float g = bf_round(__builtin_amdgcn_rcpf(1.f + __builtin_amdgcn_exp2f(lg * -1.4426950408889634f)));
                const float omg = bf_round(1.f - g);
                const int tok = aidx * 52 + wid * 7 + t;
                if (lane == 0 && wid * 7 + t < 52 && tok < S) *(f32x2*)(ws + WS_GATE + tok * 8) = (f32x2){g, omg};
            }
        }
        asm volatile("s_waitcnt vmcnt(0)\n\ts_barrier" ::: "memory");
        const int c = lane >> 4, s = c >> 1, hh = c & 1, q = (lane & 15) >> 2, p = lane & 3;
        for (int u = 0; u < nsl; ++u) {
            const int sig = aidx + 40 * u;
#pragma unroll
            for (int i = 0; i < 5; ++i) {
                const int nu = wid * 5 + i;
                lds_cptr a = (lds_cptr)lds + u * 40960 + (16 * s + 4 * hh + q) * 1280 + (16 * nu + 4 * p) * 2;
                const s16x4 lo = ldstr(a), hi = ldstr(a + 8 * 1280);
                const bf16x8 f = __builtin_shufflevector(lo, hi, 0, 1, 2, 3, 4, 5, 6, 7);
                st16(rs, (unsigned)WS_BF + (unsigned)(((nu / 5) * NSIG + sig) * 5 + (nu % 5)) * 1024 + lane * 16, __builtin_bit_cast(u32x4, f));
            }
        }
    }
}

constexpr int K2_CHUNK = 8 * KVTILE_B;
constexpr int K2_LDS = 3 * K2_CHUNK;

DI float swap32_add(float x) { auto rr = __builtin_amdgcn_permlane32_swap(__float_as_uint(x), __float_as_uint(x), false, false); return __uint_as_float(rr[0]) + __uint_as_float(rr[1]); }
DI float swap32_max(float x) { auto rr = __builtin_amdgcn_permlane32_swap(__float_as_uint(x), __float_as_uint(x), false, false); return fmaxf(__uint_as_float(rr[0]), __uint_as_float(rr[1])); }
DI bf16x8 make_qb(unsigned q0, unsigned q1, float M, int h) {
    const float mh = bf_hi(__float_as_uint(M)), r1 = M - mh, mm = bf_hi(__float_as_uint(r1)), r2 = r1 - mm, ml = bf_hi(__float_as_uint(r2));
    u32x4 w;
    w[0] = q0; w[1] = q1;
    w[2] = ((__float_as_uint(mh) >> 16) | (__float_as_uint(mm) & 0xffff0000u)) ^ 0x80008000u;
    w[3] = (__float_as_uint(ml) >> 16) ^ 0x8000u;
    if (h) w = (u32x4){0u, 0u, 0u, 0u};
    return __builtin_bit_cast(bf16x8, w);
}

DI void attn_fallback(f32x16& o, const char* kv, bf16x8 qa, unsigned q0, unsigned q1, int lane, int vslot) {
    const int h = lane >> 5, r = lane & 31;
    const f32x16 zero = {};
    float m = -INFINITY;
    const bf16x8 qb0 = make_qb(q0, q1, 0.f, h);
    for (int t = 0; t < NT; ++t) {
        const char* tb = kv + t * KVTILE_B;
        const bf16x8 ka = *(const bf16x8*)(tb + lane * 16), kb = *(const bf16x8*)(tb + 1024 + r * 16);
        f32x16 s = MFMA32(ka, qa, zero); s = MFMA32(kb, qb0, s);
#pragma unroll
        for (int i = 0; i < 16; ++i) m = fmaxf(m, s[i]);
    }
    m = swap32_max(m);
    const bf16x8 qb = make_qb(q0, q1, m, h);
    o = zero;
    for (int t = 0; t < NT; ++t) {
        const char* tb = kv + t * KVTILE_B;
        const bf16x8 ka = *(const bf16x8*)(tb + lane * 16), kb = *(const bf16x8*)(tb + 1024 + r * 16);
        const bf16x8 va = *(const bf16x8*)(tb + 1536 + vslot * 16), vb = *(const bf16x8*)(tb + 2304 + vslot * 16);
        f32x16 s = MFMA32(ka, qa, zero); s = MFMA32(kb, qb, s);
        u32x4 p0, p1;
#pragma unroll
        for (int i = 0; i < 4; ++i) { p0[i] = cvtpk(__builtin_amdgcn_exp2f(s[2 * i]), __builtin_amdgcn_exp2f(s[2 * i + 1])); p1[i] = cvtpk(__builtin_amdgcn_exp2f(s[8 + 2 * i]), __builtin_amdgcn_exp2f(s[9 + 2 * i])); }
        o = MFMA32(va, __builtin_bit_cast(bf16x8, p0), o);
        o = MFMA32(vb, __builtin_bit_cast(bf16x8, p1), o);
    }
}

DI void attn_role(char* __restrict__ ws, char* lds, int bid) {
    const int tid = threadIdx.x, lane = tid & 63, wid = rfl(tid >> 6);
    const int h = lane >> 5, r = lane & 31;
    const int xcd = bid & 7, slot = bid >> 3;
    const int head = xcd * 4 + (slot & 3), qblk = slot >> 2;
    STAMP(1, 0);
    const char* kv = ws + WS_KV + (size_t)head * NT * KVTILE_B;
    const unsigned lds0 = (unsigned)(uintptr_t)lds;
    const unsigned lane16 = lane * 16;
    const char* kvw = kv + wid * 3072;
    const unsigned ldw = lds0 + wid * 3072;
    const int Tq = qblk * 8 + wid;
    const char* qt = ws + WS_Q + ((size_t)head * NT + Tq) * QTILE_B;
    const bf16x8 qa = *(const bf16x8*)(qt + lane * 16);
    const u32x2 qbr = *(const u32x2*)(qt + 1024 + r * 16);
    f32x4 km4 = *(const f32x4*)(ws + WS_KMAX + (size_t)head * 320 + (lane < 20 ? lane : 0) * 16);
    const float gate = *(const float*)(ws + WS_GATE + (Tq * 32 + r) * 8);
#pragma unroll
    for (int j = 0; j < 3; ++j) glds16(kvw + j * 1024, lane16, ldw + j * 1024);
#pragma unroll
    for (int j = 0; j < 3; ++j) glds16(kvw + K2_CHUNK + j * 1024, lane16, ldw + K2_CHUNK + j * 1024);
    float km = fmaxf(fmaxf(km4[0], km4[1]), fmaxf(km4[2], km4[3]));
    km = dpp_max<0xB1>(km); km = dpp_max<0x4E>(km);
    km = __builtin_sqrtf(km);
    float M;
    { const float k0 = rdl(km, 0), k1 = rdl(km, 4), k2 = rdl(km, 8), k3 = rdl(km, 12), k4 = rdl(km, 16);
      const u32x4 qw = __builtin_bit_cast(u32x4, qa);
      float n0, n1, n2;
      { const float a = bf_lo(qw[0]), b = bf_hi(qw[0]), c = bf_lo(qw[1]), d = bf_hi(qw[1]); n0 = (a * a + b * b) + (c * c + d * d); }
      { const float a = bf_lo(qw[2]), b = bf_hi(qw[2]), c = bf_lo(qw[3]), d = bf_hi(qw[3]); n1 = (a * a + b * b) + (c * c + d * d); }
      { const float a = bf_lo(qbr[0]), b = bf_hi(qbr[0]), c = bf_lo(qbr[1]), d = bf_hi(qbr[1]); n2 = (a * a + b * b) + (c * c + d * d); }
      float part = __builtin_amdgcn_sqrtf(n0) * (h ? k2 : k0) + __builtin_amdgcn_sqrtf(n1) * (h ? k3 : k1);
      if (!h) part += __builtin_amdgcn_sqrtf(n2) * k4;
      M = swap32_add(part) * 1.001f; }
    const bf16x8 qb = make_qb(qbr[0], qbr[1], M, h);
    const int vrow = r <= 20 ? r : (r < 28 ? 21 : r - 12);
    const int vslot = h * 24 + vrow;
    unsigned kaC = lane * 16, kbC = 1024 + r * 16, vC = 1536 + vslot * 16;
    unsigned kaN = kaC + K2_CHUNK, kbN = kbC + K2_CHUNK, vN = vC + K2_CHUNK;
    const lds_cptr L = (lds_cptr)lds;
    const f32x16 zero = {};
    f32x16 o = {}, sA, sB;
    bf16x8 ka, kb, va, vb;
    u32x4 plo, phi;
    asm volatile("s_waitcnt vmcnt(3)\n\ts_barrier" ::: "memory");
    STAMP(1, 1);
    ka = lds128(L + kaC); kb = lds128(L + kbC);
    sA = MFMA32(ka, qa, zero); sA = MFMA32(kb, qb, sA);
    ka = lds128(L + kaC + KVTILE_B); kb = lds128(L + kbC + KVTILE_B);
    va = lds128(L + vC);
    vb = (bf16x8){}; phi = (u32x4){};
    f32x4 e3 = {};
    int stn = 1;

    const char* src = kvw + 2 * K2_CHUNK;
    unsigned dst = ldw + 2 * K2_CHUNK;
#define EXP4(X, i) do { X[i] = __builtin_amdgcn_exp2f(X[i]); X[i + 1] = __builtin_amdgcn_exp2f(X[i + 1]); X[i + 2] = __builtin_amdgcn_exp2f(X[i + 2]); X[i + 3] = __builtin_amdgcn_exp2f(X[i + 3]); } while (0)
#define KADDR(A, i) (((i) < 8) ? (A##C + (i) * KVTILE_B) : (A##N + ((i) - 8) * KVTILE_B))
#define STEP(C, N, i, DMA) do { \
        bf16x8 ka2, kb2, va2, vb2; \
        N = MFMA32(ka, qa, zero); SBAR(); \
        ka2 = lds128(L + KADDR(ka, (i) + 2)); \
        phi[2] = cvtpk(e3[0], e3[1]); phi[3] = cvtpk(e3[2], e3[3]); PIN(phi); SBAR(); EXP4(C, 0); PIN(C); SBAR(); \
        o = MFMA32(vb, __builtin_bit_cast(bf16x8, phi), o); SBAR(); \
        vb2 = lds128(L + vC + (i) * KVTILE_B + 768); \
        plo[0] = cvtpk(C[0], C[1]); plo[1] = cvtpk(C[2], C[3]); PIN(plo); SBAR(); EXP4(C, 4); PIN(C); SBAR(); \
        N = MFMA32(kb, qb, N); SBAR(); \
        DMA; \
        kb2 = lds128(L + KADDR(kb, (i) + 2)); \
        plo[2] = cvtpk(C[4], C[5]); plo[3] = cvtpk(C[6], C[7]); PIN(plo); SBAR(); EXP4(C, 8); PIN(C); SBAR(); \
        o = MFMA32(va, __builtin_bit_cast(bf16x8, plo), o); SBAR(); \
        va2 = lds128(L + KADDR(v, (i) + 1)); \
        phi[0] = cvtpk(C[8], C[9]); phi[1] = cvtpk(C[10], C[11]); PIN(phi); SBAR(); \
        e3[0] = __builtin_amdgcn_exp2f(C[12]); e3[1] = __builtin_amdgcn_exp2f(C[13]); e3[2] = __builtin_amdgcn_exp2f(C[14]); e3[3] = __builtin_amdgcn_exp2f(C[15]); PIN(e3); SBAR(); \
        ka = ka2; kb = kb2; va = va2; vb = vb2; \
    } while (0)
#define PF(j) do { if (c < 6) glds16(src + (j) * 1024, lane16, dst + (j) * 1024); } while (0)
    for (int c = 0; c < 8; ++c) {
        STEP(sA, sB, 0, ); STEP(sB, sA, 1, ); STEP(sA, sB, 2, ); STEP(sB, sA, 3, ); STEP(sA, sB, 4, );
        asm volatile("s_waitcnt vmcnt(0)\n\ts_barrier" ::: "memory");
        STEP(sB, sA, 5, PF(0)); STEP(sA, sB, 6, PF(1)); STEP(sB, sA, 7, PF(2));
        kaC = kaN; kbC = kbN; vC = vN;
        const int d = (stn == 2) ? -2 * K2_CHUNK : K2_CHUNK;
        kaN += d; kbN += d; vN += d;
        src += K2_CHUNK; dst += (stn == 1) ? -2 * K2_CHUNK : K2_CHUNK;
        stn = (stn == 2) ? 0 : stn + 1;
    }
    phi[2] = cvtpk(e3[0], e3[1]); phi[3] = cvtpk(e3[2], e3[3]);
    o = MFMA32(vb, __builtin_bit_cast(bf16x8, phi), o);
    STAMP(1, 2);
    float l; { auto rr = __builtin_amdgcn_permlane32_swap(__float_as_uint(o[8]), __float_as_uint(o[8]), false, false); l = __uint_as_float(rr[1]); }
    if (__builtin_expect(__any(!(l >= 8.6736174e-19f && l <= 1.0e30f)), 0)) {
        attn_fallback(o, kv, qa, qbr[0], qbr[1], lane, vslot);
        auto rr = __builtin_amdgcn_permlane32_swap(__float_as_uint(o[8]), __float_as_uint(o[8]), false, false); l = __uint_as_float(rr[1]);
    }
    const float f = gate * __builtin_amdgcn_rcpf(l);
    const int tok = Tq * 32 + r;
    char* ab = ws + WS_AF + (size_t)(tok >> 4) * NSIG * 1024 + (tok & 15) * 16;
#pragma unroll
    for (int g = 0; g < 3; ++g) {
        const int gam = head * 5 + 2 * g + h;
        const int sig = 48 + (gam >> 3), c = ((gam >> 2) & 1) * 2 + (gam & 1), half = (gam >> 1) & 1;
        const u32x2 w = {cvtpk(o[4 * g] * f, o[4 * g + 1] * f), cvtpk(o[4 * g + 2] * f, o[4 * g + 3] * f)};
        if (g < 2 || h == 0) __hip_atomic_store((unsigned long long*)(ab + sig * 1024 + c * 256 + half * 8), ((unsigned long long)w[1] << 32) | w[0], __ATOMIC_RELAXED, __HIP_MEMORY_SCOPE_AGENT);
    }
    asm volatile("s_waitcnt vmcnt(0)\n\ts_barrier" ::: "memory");
    if (tid == 0) __hip_atomic_fetch_add((unsigned*)(ws + WS_CNT + qblk * 256), 1u, __ATOMIC_RELAXED, __HIP_MEMORY_SCOPE_AGENT);
    STAMP(1, 3);
}

constexpr int B_STAGE = 20480;
DI void gld128(bf16x8& d, const char* sbase, unsigned voff) { asm volatile("global_load_dwordx4 %0, %1, %2" : "=v"(d) : "v"(voff), "s"(sbase) : "memory"); }
DI void gld128_sc1(bf16x8& d, const char* sbase, unsigned voff) { asm volatile("global_load_dwordx4 %0, %1, %2 sc1" : "=v"(d) : "v"(voff), "s"(sbase) : "memory"); }
#define WAITV2(n, x, y) asm volatile("s_waitcnt vmcnt(" #n ")" : "+v"(x), "+v"(y) :: "memory")
template <int ND>
DI void mlp_role(const char* __restrict__ ws, unsigned short* __restrict__ out, char* lds, int bid) {
    const int tid = threadIdx.x, lane = tid & 63, wid = rfl(tid >> 6);
    const int xcd = bid & 7, slot = bid >> 3;
    const int ng = (xcd & 1) * 4 + (slot & 3), bt = (xcd >> 1) * 8 + (slot >> 2);
    const int wm = wid & 1, kq = wid >> 1;
    STAMP(2, 0);
    const unsigned lds0 = (unsigned)(uintptr_t)lds, lane16 = lane * 16;
    const char* ap = ws + WS_AF + ((size_t)(bt * 4 + wm * 2) * NSIG + kq) * 1024;
    const char* bp = ws + WS_BF + (size_t)ng * NSIG * 5 * 1024 + wid * 1024;
    const unsigned ldw = lds0 + wid * 1024;
    const int rot = ((slot >> 2) + 3 * (slot & 3)) % 12;
    bf16x8 a[4][2];
    f32x4 acc[2][5] = {};
    auto sidx = [&](int i) { int t = i + rot; return t >= 12 ? t - 12 : t; };
    auto issueA = [&](bf16x8 (&d)[2], int st) { gld128(d[0], ap + st * 4096, lane16); gld128(d[1], ap + st * 4096 + NSIG * 1024, lane16); };
    auto issueD = [&](int st, int sl) {
#pragma unroll
        for (int j = 0; j < ND; ++j) glds16(bp + st * B_STAGE + j * 8192, lane16, ldw + sl * B_STAGE + j * 8192);
    };
    const lds_cptr LB = (lds_cptr)lds + kq * 5120 + lane16;
    auto compute = [&](bf16x8 (&av)[2], int sl) {
        bf16x8 bf[5];
#pragma unroll
        for (int nt = 0; nt < 5; ++nt) bf[nt] = lds128(LB + sl * B_STAGE + nt * 1024);
#pragma unroll
        for (int nt = 0; nt < 5; ++nt)
#pragma unroll
            for (int mt = 0; mt < 2; ++mt) acc[mt][nt] = MFMA16(bf[nt], av[mt], acc[mt][nt]);
    };
#define BAR() asm volatile("s_waitcnt lgkmcnt(0)\n\ts_barrier" ::: "memory")
#pragma unroll
    for (int j = 0; j < 3; ++j) { issueA(a[j], sidx(j)); issueD(sidx(j), j); }
#define MSTAGE(i, sl) do { if (ND == 3) WAITV2(10, a[sl][0], a[sl][1]); else WAITV2(8, a[sl][0], a[sl][1]); BAR(); \
        issueA(a[((sl) + 3) & 3], sidx((i) + 3)); issueD(sidx((i) + 3), ((sl) + 3) & 3); compute(a[sl], sl); } while (0)
    for (int i0 = 0; i0 < 8; i0 += 4) { MSTAGE(i0, 0); MSTAGE(i0 + 1, 1); MSTAGE(i0 + 2, 2); MSTAGE(i0 + 3, 3); }
    MSTAGE(8, 0);
    { if (ND == 3) WAITV2(10, a[1][0], a[1][1]); else WAITV2(8, a[1][0], a[1][1]); BAR(); issueD(12, 0); compute(a[1], 1); }
    { if (ND == 3) WAITV2(8, a[2][0], a[2][1]); else WAITV2(6, a[2][0], a[2][1]); BAR(); issueD(13, 1); compute(a[2], 2); }
    { if (ND == 3) WAITV2(6, a[3][0], a[3][1]); else WAITV2(4, a[3][0], a[3][1]); BAR(); issueD(14, 2); compute(a[3], 3); }
    BAR(); issueD(15, 3);
    float omg[2];
#pragma unroll
    for (int mt = 0; mt < 2; ++mt) omg[mt] = *(const float*)(ws + WS_GATE + (bt * 64 + wm * 32 + mt * 16 + (lane & 15)) * 8 + 4);
#pragma unroll
    for (int mt = 0; mt < 2; ++mt)
#pragma unroll
        for (int nt = 0; nt < 5; ++nt) acc[mt][nt] *= omg[mt];
    STAMP(2, 1);
    if (wid == 0) {
        const unsigned* cnt = (const unsigned*)(ws + WS_CNT + (bt >> 2) * 256);
        for (int it = 0; it < (1 << 22); ++it) {
            if ((unsigned)rfl(__hip_atomic_load(cnt, __ATOMIC_RELAXED, __HIP_MEMORY_SCOPE_AGENT)) >= 32u) break;
            __builtin_amdgcn_s_sleep(4);
        }
    }
    asm volatile("s_waitcnt vmcnt(0)\n\ts_barrier" ::: "memory");
    STAMP(2, 2);
    bf16x8 ao[5][2];
#pragma unroll
    for (int j = 0; j < 5; ++j) { gld128_sc1(ao[j][0], ap + (12 + j) * 4096, lane16); gld128_sc1(ao[j][1], ap + (12 + j) * 4096 + NSIG * 1024, lane16); }
    WAITV2(8, ao[0][0], ao[0][1]); compute(ao[0], 0);
    BAR(); issueD(16, 0);
    if (ND == 3) WAITV2(9, ao[1][0], ao[1][1]); else WAITV2(8, ao[1][0], ao[1][1]);
    compute(ao[1], 1);
    if (ND == 3) WAITV2(7, ao[2][0], ao[2][1]); else WAITV2(6, ao[2][0], ao[2][1]);
    compute(ao[2], 2);
    if (ND == 3) WAITV2(5, ao[3][0], ao[3][1]); else WAITV2(4, ao[3][0], ao[3][1]);
    compute(ao[3], 3);
    WAITV2(0, ao[4][0], ao[4][1]); BAR();
    compute(ao[4], 0);
    BAR();
#pragma unroll
    for (int mt = 0; mt < 2; ++mt)
#pragma unroll
        for (int nt = 0; nt < 5; ++nt) *(f32x4*)(lds + ((kq * 2 + wm) * 10 + mt * 5 + nt) * 1024 + lane16) = acc[mt][nt];
    __syncthreads();
    for (int t = wid; t < 20; t += 8) {
        f32x4 sum = *(const f32x4*)(lds + t * 1024 + lane16);
#pragma unroll
        for (int q = 1; q < 4; ++q) sum += *(const f32x4*)(lds + (q * 20 + t) * 1024 + lane16);
        const int m4 = t / 5, nt = t - m4 * 5;
        const int tok = bt * 64 + m4 * 16 + (lane & 15);
        *(u32x2*)(out + (size_t)tok * DM + ng * 80 + nt * 16 + (lane >> 4) * 4) = (u32x2){cvtpk(sum[0], sum[1]), cvtpk(sum[2], sum[3])};
    }
    STAMP(2, 3);
}

#define APRIO 2
constexpr int K2_LDS_ALL = 81920;
__global__ __launch_bounds__(512, 4) void k2_fused(char* __restrict__ ws, unsigned short* __restrict__ out) {
    extern __shared__ __attribute__((aligned(16))) char lds[];
    if (blockIdx.x < 256) { __builtin_amdgcn_s_setprio(APRIO); attn_role(ws, lds, blockIdx.x); }
    else if (rfl(threadIdx.x >> 6) < 4) mlp_role<3>(ws, out, lds, blockIdx.x - 256);
    else mlp_role<2>(ws, out, lds, blockIdx.x - 256);
}

extern "C" void kernel_launch(void* const* d_in, const int* in_sizes, int n_in, void* d_out, int out_size, void* d_ws, size_t ws_size, hipStream_t stream) {
    const unsigned short* x = (const unsigned short*)d_in[0];
    const unsigned short* wqkv = (const unsigned short*)d_in[1];
    const unsigned short* wo = (const unsigned short*)d_in[2];
    const unsigned short* w1 = (const unsigned short*)d_in[4];
    const unsigned short* w2 = (const unsigned short*)d_in[5];
    const unsigned short* gw = (const unsigned short*)d_in[6];
    char* ws = (char*)d_ws;
    (void)hipFuncSetAttribute((const void*)k1_proj, hipFuncAttributeMaxDynamicSharedMemorySize, K1_LDS);
    (void)hipFuncSetAttribute((const void*)k2_fused, hipFuncAttributeMaxDynamicSharedMemorySize, K2_LDS_ALL);
    k1_proj<<<256, 512, K1_LDS, stream>>>(x, wqkv, wo, w1, w2, gw, ws);
    k2_fused<<<512, 512, K2_LDS_ALL, stream>>>(ws, (unsigned short*)d_out);
}
```

```cpp
#include <hip/hip_runtime.h>
#include <hip/hip_bf16.h>
#include <stdint.h>

typedef short bf16x8 __attribute__((ext_vector_type(8)));
typedef short s16x4 __attribute__((ext_vector_type(4)));
typedef float f32x16 __attribute__((ext_vector_type(16)));
typedef float f32x4 __attribute__((ext_vector_type(4)));
typedef float f32x2 __attribute__((ext_vector_type(2)));
typedef unsigned u32x4 __attribute__((ext_vector_type(4)));
typedef unsigned u32x2 __attribute__((ext_vector_type(2)));
typedef __bf16 bf16x2_t __attribute__((ext_vector_type(2)));
#define LAS __attribute__((address_space(3)))
typedef LAS const char* lds_cptr;
#define DI __device__ __forceinline__
#define SBAR() __builtin_amdgcn_sched_barrier(0)
#define PIN(x) asm volatile("" : "+v"(x))
#define MFMA32(a, b, c) __builtin_amdgcn_mfma_f32_32x32x16_bf16(a, b, c, 0, 0, 0)
#define MFMA16(a, b, c) __builtin_amdgcn_mfma_f32_16x16x32_bf16(a, b, c, 0, 0, 0)
#define MFMA8(a, b, c) __builtin_amdgcn_mfma_f32_32x32x8bf16_1k(a, b, c, 0, 0, 0)

constexpr int S = 2048, DM = 640, NH = 32, DH = 20, FF = 1536;
constexpr int NT = S / 32;
constexpr int QTILE_B = 1280;
constexpr int KVTILE_B = 2688;
constexpr int V_OFF = 1280, V_STEP = 704, V_ROWS = 22;
constexpr int NSIG = 68;
constexpr size_t WS_Q = 0;
constexpr size_t WS_KV = WS_Q + (size_t)NH * NT * QTILE_B;
constexpr size_t WS_AF = WS_KV + (size_t)NH * NT * KVTILE_B;
constexpr size_t WS_BF = WS_AF + (size_t)128 * NSIG * 1024;
constexpr size_t WS_KMAX = WS_BF + (size_t)8 * NSIG * 5 * 1024;
constexpr size_t WS_GATE = WS_KMAX + (size_t)160 * 32 * 4;
constexpr size_t WS_CNT = WS_GATE + (size_t)S * 8;
constexpr size_t WS_END = WS_CNT + 8 * 256;
constexpr float QSCALE = 0.22360679774997896f * 1.4426950408889634f;
constexpr unsigned ONES2 = 0x3F803F80u;

DI unsigned cvtpk(float lo, float hi) { f32x2 v = {lo, hi}; bf16x2_t b = __builtin_convertvector(v, bf16x2_t); return __builtin_bit_cast(unsigned, b); }
DI float bf_lo(unsigned u) { return __uint_as_float(u << 16); }
DI float bf_hi(unsigned u) { return __uint_as_float(u & 0xffff0000u); }
DI float bf_round(float x) { return bf_lo(cvtpk(x, x)); }
DI void glds16(const void* sbase, unsigned voff, unsigned lds_base) {
    unsigned sv;
    asm volatile("s_mov_b32 %0, m0\n\ts_mov_b32 m0, %3\n\ts_nop 0\n\tglobal_load_lds_dwordx4 %1, %2\n\ts_mov_b32 m0, %0"
                 : "=&s"(sv) : "v"(voff), "s"(sbase), "s"(lds_base) : "memory");
}
typedef short v4i16_t __attribute__((ext_vector_type(4)));
DI s16x4 ldstr(lds_cptr p) { return __builtin_bit_cast(s16x4, __builtin_amdgcn_ds_read_tr16_b64_v4i16((LAS v4i16_t*)p)); }
DI bf16x8 lds128(lds_cptr p) { return *(const LAS bf16x8*)p; }
DI s16x4 lds64(lds_cptr p) { return *(const LAS s16x4*)p; }
DI unsigned rfl(unsigned x) { return __builtin_amdgcn_readfirstlane(x); }
#define STAMP(k, i) do { } while (0)

#define K1ROT 0
constexpr int K1_STAGE = 49152;
constexpr int K1_LDS = 3 * K1_STAGE;

DI float rdl(float x, int l) { return __uint_as_float(__builtin_amdgcn_readlane(__float_as_uint(x), l)); }
#define WT 1
typedef __amdgpu_buffer_rsrc_t rsrc_t;
DI rsrc_t ws_rsrc(char* ws) { return __builtin_amdgcn_make_buffer_rsrc(ws, 0, (int)WS_END, 0x00020000); }
DI void st16(rsrc_t rs, unsigned off, u32x4 v) { __builtin_amdgcn_raw_buffer_store_b128(v, rs, off, 0, WT ? 16 : 0); }
DI void st8(rsrc_t rs, unsigned off, u32x2 v) { __builtin_amdgcn_raw_buffer_store_b64(v, rs, off, 0, WT ? 16 : 0); }
template <int CTRL> DI float dpp_max(float x) { const float y = __uint_as_float(__builtin_amdgcn_update_dpp(0u, __float_as_uint(x), CTRL, 0xf, 0xf, true)); return fmaxf(x, y); }
template <int CTRL> DI float dpp_add(float x) { const float y = __uint_as_float(__builtin_amdgcn_update_dpp(0u, __float_as_uint(x), CTRL, 0xf, 0xf, true)); return x + y; }

template <bool HT>
struct K1Dma {
    static constexpr int NW = HT ? 3 : 4;
    const char* xbase; const char* wbase; int ldw2; unsigned lds0, xoff0, xoff1, woff[3];
    DI void init(const char* xb, const char* wb, int ld, char* lds, int lane) {
        xbase = xb; wbase = wb; ldw2 = ld; lds0 = (unsigned)(uintptr_t)lds;
        const int rr = lane >> 3;
        xoff0 = rr * 1280 + (((lane & 7) ^ ((lane >> 4) & 7)) << 4); xoff1 = rr * 1280 + (((lane & 7) ^ ((4 + (lane >> 4)) & 7)) << 4);
        if (HT) {
#pragma unroll
            for (int p = 0; p < 3; ++p) { const int o = 1024 * p + 16 * lane, row = (o * 21846) >> 22, col = o - row * 192; woff[p] = row * ldw2 + col; }
        } else woff[0] = (lane >> 4) * ldw2 + ((((lane & 15) ^ ((lane >> 4) << 2))) << 4);
    }
    DI void xp(int kc, int st, int j, int par) const { glds16(xbase + (size_t)j * 8 * 1280 + kc * 128, par ? xoff1 : xoff0, lds0 + st * K1_STAGE + j * 1024); }
    DI void wp(int kc, int st, int lw, int jj) const {
        if (HT) glds16(wbase + (size_t)(kc * 64 + 16 * lw) * ldw2, woff[jj], lds0 + st * K1_STAGE + 32768 + (3 * lw + jj) * 1024);
        else glds16(wbase + (size_t)(kc * 64 + 16 * lw + 4 * jj) * ldw2, woff[0], lds0 + st * K1_STAGE + 32768 + (4 * lw + jj) * 1024);
    }
    DI void first(int wid) const {
        if (wid >= 4) {
#pragma unroll
            for (int jj = 0; jj < NW; ++jj) wp(0, 0, wid - 4, jj);
#pragma unroll
            for (int jj = 0; jj < 2; ++jj) xp(0, 0, 2 * (wid - 4) + jj, jj & 1);
        } else {
#pragma unroll
            for (int jj = 0; jj < 6; ++jj) xp(0, 0, 8 + 6 * wid + jj, jj & 1);
        }
    }
    DI void loader(int lw) const {
        auto issue = [&](int kc, int st) {
#pragma unroll
            for (int jj = 0; jj < NW; ++jj) wp(kc, st, lw, jj);
#pragma unroll
            for (int jj = 0; jj < 8; ++jj) xp(kc, st, 8 * lw + jj, jj & 1);
        };
        issue(1, 1);
        if (HT) asm volatile("s_waitcnt vmcnt(11)\n\ts_barrier" ::: "memory"); else asm volatile("s_waitcnt vmcnt(12)\n\ts_barrier" ::: "memory");
        issue(2, 2);
        int st = 0;
        for (int kc = 0; kc < 9; ++kc) {
            if (kc == 8) asm volatile("s_waitcnt vmcnt(0)\n\ts_barrier" ::: "memory");
            else if (HT) asm volatile("s_waitcnt vmcnt(11)\n\ts_barrier" ::: "memory");
            else asm volatile("s_waitcnt vmcnt(12)\n\ts_barrier" ::: "memory");
            if (kc + 3 < 10) issue(kc + 3, st);
            st = st == 2 ? 0 : st + 1;
        }
    }
};
template <int TYPE, int MT, int NTL>
DI void k1_mainloop(f32x16 (&acc)[MT][NTL], char* lds, int wid, int lane, char* ws) {
    const int h = lane >> 5, r = lane & 31;
    constexpr bool HT = TYPE == 2;
    unsigned xa[4];
    { const int row = (HT ? wid * 64 : (wid >> 1) * 128) + r; const int sw = (row >> 1) & 7;
#pragma unroll
      for (int ks = 0; ks < 4; ++ks) xa[ks] = row * 128 + (((2 * ks + h) ^ sw) << 4); }
    unsigned wa[2];
    { const int blk = (lane >> 4) & 1, q = (lane & 15) >> 2, p = lane & 3;
      if (HT) wa[0] = wa[1] = 32768 + (8 * h + q) * 192 + blk * 32 + p * 8;
      else {
#pragma unroll
          for (int nt = 0; nt < 2; ++nt) { const int ch = (wid & 1) * 8 + nt * 4 + 2 * blk + (p >> 1); wa[nt] = 32768 + (8 * h + q) * 256 + ((ch ^ (q << 2)) << 4) + 8 * (p & 1); } } }
    bf16x8 xf[2][MT], wf[2][NTL];
    auto rdw = [&](int buf, int nt, lds_cptr sb, int ks) {
        lds_cptr a = HT ? sb + wa[0] + ks * 16 * 192 + nt * 64 : sb + wa[nt & 1] + ks * 4096;
        const s16x4 lo = ldstr(a), hi = ldstr(a + (HT ? 4 * 192 : 1024));
        wf[buf][nt] = __builtin_shufflevector(lo, hi, 0, 1, 2, 3, 4, 5, 6, 7);
    };
    auto rdx = [&](int buf, int mt, lds_cptr sb, int ks) { xf[buf][mt] = lds128(sb + xa[ks] + mt * 4096); };
    auto mm = [&](int buf, int mt, int nt) { acc[mt][nt] = TYPE == 1 ? MFMA32(xf[buf][mt], wf[buf][nt], acc[mt][nt]) : MFMA32(wf[buf][nt], xf[buf][mt], acc[mt][nt]); };
    asm volatile("s_waitcnt vmcnt(0)\n\ts_barrier" ::: "memory");
    { lds_cptr sb = (lds_cptr)lds; rdw(0, 0, sb, 0); rdx(0, 0, sb, 0); rdw(0, 1, sb, 0); rdx(0, 1, sb, 0);
      if (HT) rdw(0, 2, sb, 0); else { rdx(0, 2 % MT, sb, 0); rdx(0, 3 % MT, sb, 0); } }
    int st = 0;
#define K1_KSTEP(ks, NEXT) do { const int b = (ks) & 1, n = b ^ 1; lds_cptr np = (ks) < 3 ? sb : sn; const int nk = ((ks) + 1) & 3; \
        SBAR(); \
        if constexpr (HT) { \
            mm(b, 0, 0); SBAR(); if (NEXT) { rdw(n, 0, np, nk); } SBAR(); \
            mm(b, 0, 1); SBAR(); if (NEXT) { rdx(n, 0, np, nk); } SBAR(); \
            mm(b, 1, 0); SBAR(); if (NEXT) { rdw(n, 1, np, nk); } SBAR(); \
            mm(b, 1, 1); SBAR(); if (NEXT) { rdx(n, 1, np, nk); } SBAR(); \
            mm(b, 0, 2); SBAR(); if (NEXT) { rdw(n, 2, np, nk); } SBAR(); \
            mm(b, 1, 2); SBAR(); \
        } else { \
            mm(b, 0, 0); SBAR(); if (NEXT) { rdw(n, 0, np, nk); } SBAR(); \
            mm(b, 0, 1); SBAR(); if (NEXT) { rdx(n, 0, np, nk); } SBAR(); \
            mm(b, 1, 0); SBAR(); if (NEXT) { rdw(n, 1, np, nk); } SBAR(); \
            mm(b, 1, 1); SBAR(); if (NEXT) { rdx(n, 1, np, nk); } SBAR(); \
            mm(b, 2 % MT, 0); SBAR(); if (NEXT) { rdx(n, 2 % MT, np, nk); } SBAR(); \
            mm(b, 2 % MT, 1); SBAR(); if (NEXT) { rdx(n, 3 % MT, np, nk); } SBAR(); \
            mm(b, 3 % MT, 0); SBAR(); \
            mm(b, 3 % MT, 1); SBAR(); \
        } } while (0)
#pragma unroll 1
    for (int kc = 0; kc < 9; ++kc) {
        lds_cptr sb = (lds_cptr)lds + st * K1_STAGE;
        st = st == 2 ? 0 : st + 1;
        lds_cptr sn = (lds_cptr)lds + st * K1_STAGE;
        K1_KSTEP(0, true); K1_KSTEP(1, true); K1_KSTEP(2, true);
        asm volatile("s_waitcnt lgkmcnt(0)\n\ts_barrier" ::: "memory");
        K1_KSTEP(3, true);
    }
    { lds_cptr sb = (lds_cptr)lds + st * K1_STAGE; lds_cptr sn = sb;
      K1_KSTEP(0, true); K1_KSTEP(1, true); K1_KSTEP(2, true); K1_KSTEP(3, false); }
}

__global__ __launch_bounds__(512, 2) void k1_proj(const unsigned short* __restrict__ x, const unsigned short* __restrict__ wqkv, const unsigned short* __restrict__ wo,
                                                  const unsigned short* __restrict__ w1, const unsigned short* __restrict__ w2, char* __restrict__ ws) {
    extern __shared__ __attribute__((aligned(16))) char lds[];
    const int tid = threadIdx.x, lane = tid & 63, wid = rfl(tid >> 6);
    const int xcd = blockIdx.x & 7, slot = blockIdx.x >> 3;
    const int rgrp = xcd >> 2, cgrp = xcd & 3, nq = cgrp < 3 ? 4 : 3;
    const int h = lane >> 5, r = lane & 31;
    const rsrc_t rs = ws_rsrc(ws);
    const int rt = 4 * rgrp + (slot & 3), ci = slot >> 2;
    const char* xbase = (const char*)x + (size_t)rt * 256 * 1280;
    int ty = 0;
    STAMP(0, 0);
    if (ci < nq) {
        const int ct = 4 * cgrp + ci;
        const bool isV = ct >= 10;
        ty = isV ? 2 : 1;
        const int wm = (wid >> 1) & 1, wn = wid & 1;
        K1Dma<false> dma; dma.init(xbase, (const char*)wqkv + ct * 256, 3 * DM * 2, lds, lane);
        dma.first(wid);
        if (wid >= 4) dma.loader(wid - 4);
        else {
            f32x16 acc[4][2] = {};
            if (isV) {
                k1_mainloop<1, 4, 2>(acc, lds, wid, lane, ws);
                STAMP(0, 1);
#pragma unroll
                for (int nt = 0; nt < 2; ++nt) {
                    const int phi = (ct - 10) * 128 + wn * 64 + nt * 32 + r;
                    const int head = (phi * 3277) >> 16, d = phi - head * 20;
                    const unsigned dst = (unsigned)WS_KV + (unsigned)(head * NT + rt * 8 + wm * 4) * KVTILE_B + V_OFF + (h * V_ROWS + d) * 16;
#pragma unroll
                    for (int mt = 0; mt < 4; ++mt)
#pragma unroll
                        for (int s = 0; s < 2; ++s) {
                            const f32x16& a = acc[mt][nt];
                            const u32x4 w = {cvtpk(a[8 * s], a[8 * s + 1]), cvtpk(a[8 * s + 2], a[8 * s + 3]), cvtpk(a[8 * s + 4], a[8 * s + 5]), cvtpk(a[8 * s + 6], a[8 * s + 7])};
                            st16(rs, dst + mt * KVTILE_B + s * V_STEP, w);
                        }
                }
            } else {
                k1_mainloop<0, 4, 2>(acc, lds, wid, lane, ws);
                STAMP(0, 1);
                char* sb = lds + K1_STAGE + (wm * 4 * 32 + wn * 16 + h) * 256 + r * 8;
                if (ct < 5) {
#pragma unroll
                    for (int nt = 0; nt < 2; ++nt)
#pragma unroll
                        for (int g = 0; g < 4; ++g)
#pragma unroll
                            for (int mt = 0; mt < 4; ++mt) {
                                const f32x16& a = acc[mt][nt];
                                *(u32x2*)(sb + (mt * 32 + nt * 8 + 2 * g) * 256) = (u32x2){cvtpk(a[4 * g] * QSCALE, a[4 * g + 1] * QSCALE), cvtpk(a[4 * g + 2] * QSCALE, a[4 * g + 3] * QSCALE)};
                            }
                } else {
                    float nrm[8];
#pragma unroll
                    for (int nt = 0; nt < 2; ++nt)
#pragma unroll
                        for (int g = 0; g < 4; ++g) {
                            float m = 0.f;
#pragma unroll
                            for (int mt = 0; mt < 4; ++mt) {
                                const f32x16& a = acc[mt][nt];
                                *(u32x2*)(sb + (mt * 32 + nt * 8 + 2 * g) * 256) = (u32x2){cvtpk(a[4 * g], a[4 * g + 1]), cvtpk(a[4 * g + 2], a[4 * g + 3])};
                                m = fmaxf(m, __builtin_fmaf(a[4 * g], a[4 * g], a[4 * g + 1] * a[4 * g + 1]) + __builtin_fmaf(a[4 * g + 2], a[4 * g + 2], a[4 * g + 3] * a[4 * g + 3]));
                            }
                            nrm[nt * 4 + g] = m;
                        }
#pragma unroll
                    for (int i = 0; i < 8; ++i) { float m = nrm[i]; m = dpp_max<0xB1>(m); m = dpp_max<0x4E>(m); m = dpp_max<0x141>(m); m = dpp_max<0x140>(m);
                        auto rr = __builtin_amdgcn_permlane16_swap(__float_as_uint(m), __float_as_uint(m), false, false); nrm[i] = fmaxf(__uint_as_float(rr[0]), __uint_as_float(rr[1])); }
                    float mine = nrm[0];
#pragma unroll
                    for (int i = 1; i < 8; ++i) mine = r == i ? nrm[i] : mine;
                    if (r < 8) ((float*)(ws + WS_KMAX))[((ct - 5) * 32 + wn * 16 + (r >> 2) * 8 + 2 * (r & 3) + h) * 16 + rt * 2 + wm] = mine;
                }
            }
        }
        if (!isV) {
            __syncthreads();
            const bool isQ = ct < 5;
            const int ctl = isQ ? ct : ct - 5;
            const unsigned img = (unsigned)(isQ ? WS_Q : WS_KV);
            const unsigned tileb = isQ ? QTILE_B : KVTILE_B;
            const int T = rt * 8 + wid, G0 = ctl * 32;
            const int hd0 = (G0 * 205) >> 10, id0 = 3 * hd0 + ((G0 - 5 * hd0) >> 1);
            const char* lb = lds + K1_STAGE + wid * 8192;
            u32x2 pa[10], pb[10];
#pragma unroll
            for (int it = 0; it < 10; ++it) {
                const int id = id0 + 2 * it + h, head = (id * 171) >> 9, ty_ = id - 3 * head, la = 5 * head + 2 * ty_ - G0;
                const int lc = la < 0 ? 0 : (la > 31 ? 31 : la);
                const int aA = ty_ < 2 ? lc * 256 + r * 8 : lc * 256 + (r & 15) * 16;
                const int aB = ty_ < 2 ? (la > 30 ? 31 : la + 1) * 256 + r * 8 : aA + 8;
                pa[it] = *(const u32x2*)(lb + aA); pb[it] = *(const u32x2*)(lb + aB);
            }
#pragma unroll
            for (int it = 0; it < 10; ++it) {
                const int id = id0 + 2 * it + h, head = (id * 171) >> 9, ty_ = id - 3 * head, la = 5 * head + 2 * ty_ - G0;
                const unsigned dst = img + (unsigned)(head * NT + T) * tileb + ty_ * 512 + r * 16;
                const bool okA = (unsigned)la < 32u, okB = ty_ == 2 ? okA : (unsigned)(la + 1) < 32u;
                if (okA && okB) { if (ty_ < 2 || r < 16) st16(rs, dst, (u32x4){pa[it][0], pa[it][1], pb[it][0], pb[it][1]}); }
                else if (okA) st8(rs, dst, pa[it]);
                else if (okB) st8(rs, dst + 8, pb[it]);
            }
        }
    } else if (ci < nq + 4) {
        const int ch = 4 * cgrp + (ci - nq);
        ty = 3;
        K1Dma<true> dma; dma.init(xbase, (const char*)w1 + ch * 192, FF * 2, lds, lane);
        dma.first(wid);
        if (wid >= 4) dma.loader(wid - 4);
        else {
            f32x16 acc[2][3] = {};
            k1_mainloop<2, 2, 3>(acc, lds, wid, lane, ws);
            STAMP(0, 1);
#pragma unroll
            for (int mt = 0; mt < 2; ++mt) {
                const int tok = rt * 256 + wid * 64 + mt * 32 + r, rho = tok >> 4;
#pragma unroll
                for (int nt = 0; nt < 3; ++nt) {
                    const int sig = ch * 3 + nt;
                    const unsigned dst = (unsigned)WS_AF + (unsigned)(rho * NSIG + sig) * 1024 + (h * 16 + (r & 15)) * 16;
                    float sv[16];
#pragma unroll
                    for (int i = 0; i < 16; ++i) { const float v = acc[mt][nt][i]; sv[i] = v * __builtin_amdgcn_rcpf(1.f + __builtin_amdgcn_exp2f(v * -1.4426950408889634f)); }
#pragma unroll
                    for (int s = 0; s < 2; ++s) {
                        const u32x4 w = {cvtpk(sv[8 * s], sv[8 * s + 1]), cvtpk(sv[8 * s + 2], sv[8 * s + 3]), cvtpk(sv[8 * s + 4], sv[8 * s + 5]), cvtpk(sv[8 * s + 6], sv[8 * s + 7])};
                        st16(rs, dst + s * 512, w);
                    }
                }
            }
        }
    } else {
        const int aidx = rgrp * 4 + (slot & 3);
        const unsigned lds0 = (unsigned)(uintptr_t)lds;
        auto slab_load = [&](int sig, int u) {
            const char* src = sig < 48 ? (const char*)w2 + (size_t)sig * 32 * 1280 : (const char*)wo + (size_t)(sig - 48) * 32 * 1280;
#pragma unroll
            for (int jj = 0; jj < 5; ++jj) { const int j = wid * 5 + jj; glds16(src + j * 1024, lane * 16, lds0 + u * 40960 + j * 1024); }
        };
        slab_load(aidx, 0); slab_load(aidx + 8, 1); slab_load(aidx + 16, 2);
        const int c = lane >> 4, s = c >> 1, hh = c & 1, q = (lane & 15) >> 2, p = lane & 3;
        const int nsl = aidx < 4 ? 9 : 8;
        int u = 0;
        for (int i = 0; i < nsl; ++i) {
            if (i == 0) asm volatile("s_waitcnt vmcnt(10)\n\ts_barrier" ::: "memory");
            else if (i == 1) asm volatile("s_waitcnt vmcnt(15)\n\ts_barrier" ::: "memory");
            else if (i + 2 < nsl) asm volatile("s_waitcnt vmcnt(20)\n\ts_barrier" ::: "memory");
            else asm volatile("s_waitcnt vmcnt(5)\n\ts_barrier" ::: "memory");
            const int sig = aidx + 8 * i;
            bf16x8 f[5];
#pragma unroll
            for (int k = 0; k < 5; ++k) {
                lds_cptr a = (lds_cptr)lds + u * 40960 + (16 * s + 4 * hh + q) * 1280 + (16 * (wid * 5 + k) + 4 * p) * 2;
                const s16x4 lo = ldstr(a), hi = ldstr(a + 8 * 1280);
                f[k] = __builtin_shufflevector(lo, hi, 0, 1, 2, 3, 4, 5, 6, 7);
            }
#pragma unroll
            for (int k = 0; k < 5; ++k) st16(rs, (unsigned)WS_BF + (unsigned)((wid * NSIG + sig) * 5 + k) * 1024 + lane * 16, __builtin_bit_cast(u32x4, f[k]));
            asm volatile("s_waitcnt lgkmcnt(0)\n\ts_barrier" ::: "memory");
            if (i + 3 < nsl) slab_load(sig + 24, u);
            u = u == 2 ? 0 : u + 1;
        }
        if (aidx == 0 && tid < 8) *(unsigned*)(ws + WS_CNT + tid * 256) = 0u;
#pragma unroll
        for (int k = 0; k < 4; ++k) { const int i = (aidx * 4 + k) * 512 + tid;
          const unsigned f = (i & 1) ? 0u : ONES2; st16(rs, (unsigned)WS_KV + (unsigned)(i >> 3) * KVTILE_B + V_OFF + ((i >> 2) & 1) * V_STEP + (((i >> 1) & 1) * V_ROWS + 20 + (i & 1)) * 16, (u32x4){f, f, f, f}); }
    }
}

constexpr int K2_CHUNK = 8 * KVTILE_B;

DI float swap32_add(float x) { auto rr = __builtin_amdgcn_permlane32_swap(__float_as_uint(x), __float_as_uint(x), false, false); return __uint_as_float(rr[0]) + __uint_as_float(rr[1]); }
DI float swap32_max(float x) { auto rr = __builtin_amdgcn_permlane32_swap(__float_as_uint(x), __float_as_uint(x), false, false); return fmaxf(__uint_as_float(rr[0]), __uint_as_float(rr[1])); }
DI s16x4 make_qb(unsigned q0, unsigned q1, float M, int h) {
    const float mh = bf_hi(__float_as_uint(M)), r1 = M - mh, mm = bf_hi(__float_as_uint(r1)), r2 = r1 - mm, ml = bf_hi(__float_as_uint(r2));
    u32x2 w;
    w[0] = h ? ((__float_as_uint(mh) >> 16) | (__float_as_uint(mm) & 0xffff0000u)) ^ 0x80008000u : q0;
    w[1] = h ? (__float_as_uint(ml) >> 16) ^ 0x8000u : q1;
    return __builtin_bit_cast(s16x4, w);
}

DI void attn_fallback(f32x16& o, const char* kv, bf16x8 qa, unsigned q0, unsigned q1, int lane, int kboff, int voff) {
    const int h = lane >> 5;
    const f32x16 zero = {};
    float m = -INFINITY;
    const s16x4 qb0 = make_qb(q0, q1, 0.f, h);
    for (int t = 0; t < NT; ++t) {
        const char* tb = kv + t * KVTILE_B;
        const bf16x8 ka = *(const bf16x8*)(tb + lane * 16); const s16x4 kb = *(const s16x4*)(tb + kboff);
        f32x16 s = MFMA32(ka, qa, zero); s = MFMA8(kb, qb0, s);
#pragma unroll
        for (int i = 0; i < 16; ++i) m = fmaxf(m, s[i]);
    }
    m = swap32_max(m);
    const s16x4 qb = make_qb(q0, q1, m, h);
    o = zero;
    for (int t = 0; t < NT; ++t) {
        const char* tb = kv + t * KVTILE_B;
        const bf16x8 ka = *(const bf16x8*)(tb + lane * 16); const s16x4 kb = *(const s16x4*)(tb + kboff);
        const bf16x8 va = *(const bf16x8*)(tb + voff), vb = *(const bf16x8*)(tb + voff + V_STEP);
        f32x16 s = MFMA32(ka, qa, zero); s = MFMA8(kb, qb, s);
        u32x4 p0, p1;
#pragma unroll
        for (int i = 0; i < 4; ++i) { p0[i] = cvtpk(__builtin_amdgcn_exp2f(s[2 * i]), __builtin_amdgcn_exp2f(s[2 * i + 1])); p1[i] = cvtpk(__builtin_amdgcn_exp2f(s[8 + 2 * i]), __builtin_amdgcn_exp2f(s[9 + 2 * i])); }
        o = MFMA32(va, __builtin_bit_cast(bf16x8, p0), o);
        o = MFMA32(vb, __builtin_bit_cast(bf16x8, p1), o);
    }
}

constexpr int K2_LDS = 3 * K2_CHUNK + 1024;
__global__ __launch_bounds__(576, 3) void k2_attn(char* __restrict__ ws, const unsigned short* __restrict__ x, const unsigned short* __restrict__ gw) {
    extern __shared__ __attribute__((aligned(16))) char lds[];
    const int bid = blockIdx.x;
    const int tid = threadIdx.x, lane = tid & 63, wid = rfl(tid >> 6);
    const int h = lane >> 5, r = lane & 31;
    const int xcd = bid & 7, slot = bid >> 3;
    const int head = xcd * 4 + (slot & 3), qblk = slot >> 2;
    STAMP(1, 0);
    const char* kv = ws + WS_KV + (size_t)head * NT * KVTILE_B;
    const unsigned lds0 = (unsigned)(uintptr_t)lds;
    const unsigned lane16 = lane * 16;
    if (wid == 8) {
        const char* pfa; {
            const int u0 = slot * 96, ng0 = (xcd & 1) * 4, bt0 = (xcd >> 1) * 8;
            if (u0 < 1536) { const int rho = u0 / 48; pfa = ws + WS_AF + (size_t)(bt0 * 4 + rho) * NSIG * 1024; }
            else { int v = u0 - 1536; if (v > 1360 - 96) v = 1360 - 96; pfa = ws + WS_BF + ((size_t)ng0 * NSIG * 5 + v) * 1024; }
        }
        const int pstep = slot < 16 ? (NSIG - 48) * 1024 : 0;
        auto chunk = [&](int c, int st) {
#pragma unroll
            for (int j = 0; j < 21; ++j) glds16(kv + (size_t)c * K2_CHUNK + j * 1024, lane16, lds0 + st * K2_CHUNK + j * 1024);
        };
        chunk(1, 1);
        asm volatile("s_barrier" ::: "memory");
        int st = 2;
        for (int c = 0; c < 8; ++c) {
            if (c == 0 || c == 7) asm volatile("s_waitcnt vmcnt(0)\n\ts_barrier" ::: "memory"); else asm volatile("s_waitcnt vmcnt(16)\n\ts_barrier" ::: "memory");
            if (c < 6) {
                chunk(c + 2, st); st = st == 2 ? 0 : st + 1;
#pragma unroll
                for (int j = 0; j < 16; ++j) { const int u = c * 16 + j; glds16(pfa + (size_t)u * 1024 + (u >= 48 ? pstep : 0), lane16, lds0 + 3 * K2_CHUNK); }
            }
        }
        return;
    }
    const char* kvw = kv + wid * 1024;
    const unsigned ldw = lds0 + wid * 1024;
    const bool third = wid < 5;
    const int Tq = qblk * 8 + wid;
    const char* qt = ws + WS_Q + ((size_t)head * NT + Tq) * QTILE_B;
    const bf16x8 qa = *(const bf16x8*)(qt + lane * 16);
    const u32x2 qbr = *(const u32x2*)(qt + 1024 + r * 8);
    f32x4 km4 = *(const f32x4*)(ws + WS_KMAX + (size_t)head * 320 + (lane < 20 ? lane : 0) * 16);
    glds16(kvw, lane16, ldw); glds16(kvw + 8192, lane16, ldw + 8192); if (third) glds16(kvw + 16384, lane16, ldw + 16384);
    {
        const unsigned* xr = (const unsigned*)(x + (size_t)(bid * 8 + wid) * DM) + lane; const unsigned* gr = (const unsigned*)gw + lane;
        float sm = 0.f;
#pragma unroll
        for (int i = 0; i < 5; ++i) { const unsigned a_ = xr[64 * i], b_ = gr[64 * i]; sm = __builtin_amdgcn_fdot2_f32_bf16(__builtin_bit_cast(bf16x2_t, a_), __builtin_bit_cast(bf16x2_t, b_), sm, false); }
        sm = dpp_add<0xB1>(sm); sm = dpp_add<0x4E>(sm); sm = dpp_add<0x141>(sm); sm = dpp_add<0x140>(sm);
        sm = (rdl(sm, 0) + rdl(sm, 16)) + (rdl(sm, 32) + rdl(sm, 48));
        const float lg = bf_round(sm);
        const float g = bf_round(__builtin_amdgcn_rcpf(1.f + __builtin_amdgcn_exp2f(lg * -1.4426950408889634f)));
        if (lane == 0) *(f32x2*)(ws + WS_GATE + (bid * 8 + wid) * 8) = (f32x2){g, bf_round(1.f - g)};
    }
    float km = fmaxf(fmaxf(km4[0], km4[1]), fmaxf(km4[2], km4[3]));
    km = dpp_max<0xB1>(km); km = dpp_max<0x4E>(km);
    km = __builtin_sqrtf(km);
    float M;
    { const float k0 = rdl(km, 0), k1 = rdl(km, 4), k2 = rdl(km, 8), k3 = rdl(km, 12), k4 = rdl(km, 16);
      const u32x4 qw = __builtin_bit_cast(u32x4, qa);
      float n0, n1, n2;
      { const float a = bf_lo(qw[0]), b = bf_hi(qw[0]), c = bf_lo(qw[1]), d = bf_hi(qw[1]); n0 = (a * a + b * b) + (c * c + d * d); }
      { const float a = bf_lo(qw[2]), b = bf_hi(qw[2]), c = bf_lo(qw[3]), d = bf_hi(qw[3]); n1 = (a * a + b * b) + (c * c + d * d); }
      { const float a = bf_lo(qbr[0]), b = bf_hi(qbr[0]), c = bf_lo(qbr[1]), d = bf_hi(qbr[1]); n2 = (a * a + b * b) + (c * c + d * d); }
      float part = __builtin_amdgcn_sqrtf(n0) * (h ? k2 : k0) + __builtin_amdgcn_sqrtf(n1) * (h ? k3 : k1);
      if (!h) part += __builtin_amdgcn_sqrtf(n2) * k4;
      M = swap32_add(part) * 1.01f; }
    const s16x4 qb = make_qb(qbr[0], qbr[1], M, h);
    const int vrow = r <= 20 ? r : (r < 28 ? 21 : r - 12);
    const int voff = V_OFF + (h * V_ROWS + vrow) * 16, kboff = h ? V_OFF + 20 * 16 : 1024 + r * 8;
    unsigned kaC = lane * 16, kbC = kboff, vC = voff;
    unsigned kaN = kaC + K2_CHUNK, kbN = kbC + K2_CHUNK, vN = vC + K2_CHUNK;
    const lds_cptr L = (lds_cptr)lds;
    const f32x16 zero = {};
    f32x16 o = {}, sA, sB;
    bf16x8 ka, va, vb; s16x4 kb;
    u32x4 plo, phi;
    asm volatile("s_waitcnt vmcnt(0)\n\ts_barrier" ::: "memory");
    STAMP(1, 1);
    ka = lds128(L + kaC); kb = lds64(L + kbC);
    sA = MFMA32(ka, qa, zero); sA = MFMA8(kb, qb, sA);
    ka = lds128(L + kaC + KVTILE_B); kb = lds64(L + kbC + KVTILE_B);
    va = lds128(L + vC);
    vb = (bf16x8){}; phi = (u32x4){};
    f32x4 e3 = {};
    int stn = 1;

#define EXP4(X, i) do { X[i] = __builtin_amdgcn_exp2f(X[i]); X[i + 1] = __builtin_amdgcn_exp2f(X[i + 1]); X[i + 2] = __builtin_amdgcn_exp2f(X[i + 2]); X[i + 3] = __builtin_amdgcn_exp2f(X[i + 3]); } while (0)
#define KADDR(A, i) (((i) < 8) ? (A##C + (i) * KVTILE_B) : (A##N + ((i) - 8) * KVTILE_B))
#define STEP(C, N, i, DMA) do { \
        bf16x8 ka2, va2, vb2; s16x4 kb2; \
        N = MFMA32(ka, qa, zero); SBAR(); \
        ka2 = lds128(L + KADDR(ka, (i) + 2)); \
        phi[2] = cvtpk(e3[0], e3[1]); phi[3] = cvtpk(e3[2], e3[3]); PIN(phi); SBAR(); EXP4(C, 0); PIN(C); SBAR(); \
        o = MFMA32(vb, __builtin_bit_cast(bf16x8, phi), o); SBAR(); \
        vb2 = lds128(L + vC + (i) * KVTILE_B + V_STEP); \
        plo[0] = cvtpk(C[0], C[1]); plo[1] = cvtpk(C[2], C[3]); PIN(plo); SBAR(); EXP4(C, 4); PIN(C); SBAR(); \
        N = MFMA8(kb, qb, N); SBAR(); \
        DMA; \
        kb2 = lds64(L + KADDR(kb, (i) + 2)); \
        plo[2] = cvtpk(C[4], C[5]); plo[3] = cvtpk(C[6], C[7]); PIN(plo); SBAR(); EXP4(C, 8); PIN(C); SBAR(); \
        o = MFMA32(va, __builtin_bit_cast(bf16x8, plo), o); SBAR(); \
        va2 = lds128(L + KADDR(v, (i) + 1)); \
        phi[0] = cvtpk(C[8], C[9]); phi[1] = cvtpk(C[10], C[11]); PIN(phi); SBAR(); \
        e3[0] = __builtin_amdgcn_exp2f(C[12]); e3[1] = __builtin_amdgcn_exp2f(C[13]); e3[2] = __builtin_amdgcn_exp2f(C[14]); e3[3] = __builtin_amdgcn_exp2f(C[15]); PIN(e3); SBAR(); \
        ka = ka2; kb = kb2; va = va2; vb = vb2; \
    } while (0)
    for (int c = 0; c < 8; ++c) {
        STEP(sA, sB, 0, ); STEP(sB, sA, 1, ); STEP(sA, sB, 2, ); STEP(sB, sA, 3, ); STEP(sA, sB, 4, );
        asm volatile("s_barrier" ::: "memory");
        STEP(sB, sA, 5, ); STEP(sA, sB, 6, ); STEP(sB, sA, 7, );
        kaC = kaN; kbC = kbN; vC = vN;
        const int d = (stn == 2) ? -2 * K2_CHUNK : K2_CHUNK;
        kaN += d; kbN += d; vN += d;
        stn = (stn == 2) ? 0 : stn + 1;
    }
    phi[2] = cvtpk(e3[0], e3[1]); phi[3] = cvtpk(e3[2], e3[3]);
    o = MFMA32(vb, __builtin_bit_cast(bf16x8, phi), o);
    STAMP(1, 2);
    float l; { auto rr = __builtin_amdgcn_permlane32_swap(__float_as_uint(o[8]), __float_as_uint(o[8]), false, false); l = __uint_as_float(rr[1]); }
    if (__builtin_expect(__any(!(l >= 8.6736174e-19f && l <= 1.0e30f)), 0)) {
        attn_fallback(o, kv, qa, qbr[0], qbr[1], lane, kboff, voff);
        auto rr = __builtin_amdgcn_permlane32_swap(__float_as_uint(o[8]), __float_as_uint(o[8]), false, false); l = __uint_as_float(rr[1]);
    }
    const float f = __builtin_amdgcn_rcpf(l);
    const int tok = Tq * 32 + r;
    char* ab = ws + WS_AF + (size_t)(tok >> 4) * NSIG * 1024 + (tok & 15) * 16;
#pragma unroll
    for (int g = 0; g < 3; ++g) {
        const int gam = head * 5 + 2 * g + h;
        const int sig = 48 + (gam >> 3), c = ((gam >> 2) & 1) * 2 + (gam & 1), half = (gam >> 1) & 1;
        const u32x2 w = {cvtpk(o[4 * g] * f, o[4 * g + 1] * f), cvtpk(o[4 * g + 2] * f, o[4 * g + 3] * f)};
        if (g < 2 || h == 0) *(u32x2*)(ab + sig * 1024 + c * 256 + half * 8) = w;
    }
    STAMP(1, 3);
}

constexpr int K3_LDS = 8 * 20 * 1024;
DI void gld128(bf16x8& d, const char* sbase, unsigned voff) { asm volatile("global_load_dwordx4 %0, %1, %2" : "=v"(d) : "v"(voff), "s"(sbase) : "memory"); }
template <int N> DI void wait9(bf16x8 (&a)[4], bf16x8 (&b)[5]) {
    asm volatile("s_waitcnt vmcnt(%9)" : "+v"(a[0]), "+v"(a[1]), "+v"(a[2]), "+v"(a[3]), "+v"(b[0]), "+v"(b[1]), "+v"(b[2]), "+v"(b[3]), "+v"(b[4]) : "i"(N) : "memory");
}
template <int NS, int I> struct K3Step {
    template <class L> static DI void run(f32x4 (&acc)[4][5], bf16x8 (&a)[4][4], bf16x8 (&b)[4][5], L& load) {
        constexpr int left = NS - 1 - I, younger = (left < 3 ? left : 3) * 9;
        wait9<younger>(a[I & 3], b[I & 3]);
        SBAR();
#pragma unroll
        for (int nt = 0; nt < 5; ++nt)
#pragma unroll
            for (int mt = 0; mt < 4; ++mt) acc[mt][nt] = MFMA16(b[I & 3][nt], a[I & 3][mt], acc[mt][nt]);
        SBAR();
        if constexpr (I + 4 < NS) load(I + 4, a[I & 3], b[I & 3]);
        if constexpr (I + 1 < NS) K3Step<NS, I + 1>::run(acc, a, b, load);
    }
};
template <int NS>
DI void k3_stream(f32x4 (&acc)[4][5], const char* ap, const char* bp, unsigned lane16, int rot) {
    bf16x8 a[4][4], b[4][5];
    auto load = [&](int i, bf16x8 (&da)[4], bf16x8 (&db)[5]) {
        int st = i + rot; if (st >= NS) st -= NS;
#pragma unroll
        for (int nt = 0; nt < 5; ++nt) gld128(db[nt], bp + st * 5120 + nt * 1024, lane16);
#pragma unroll
        for (int mt = 0; mt < 4; ++mt) gld128(da[mt], ap + (size_t)mt * NSIG * 1024 + st * 1024, lane16);
    };
#pragma unroll
    for (int i = 0; i < 4; ++i) load(i, a[i], b[i]);
    K3Step<NS, 0>::run(acc, a, b, load);
}
__global__ __launch_bounds__(512, 2) void k3_out(const char* __restrict__ ws, unsigned short* __restrict__ out) {
    extern __shared__ __attribute__((aligned(16))) char lds[];
    const int tid = threadIdx.x, lane = tid & 63, wid = rfl(tid >> 6);
    const int xcd = blockIdx.x & 7, slot = blockIdx.x >> 3;
    const int ng = (xcd & 1) * 4 + (slot & 3), bt = (xcd >> 1) * 8 + (slot >> 2);
    STAMP(2, 0);
    const int s0 = wid < 6 ? wid * 8 : 48 + (wid - 6) * 10;
    const char* ap = ws + WS_AF + ((size_t)bt * 4 * NSIG + s0) * 1024;
    const char* bp = ws + WS_BF + ((size_t)ng * NSIG + s0) * 5 * 1024;
    const unsigned lane16 = lane * 16;
    float fac[4];
#pragma unroll
    for (int mt = 0; mt < 4; ++mt) asm volatile("global_load_dword %0, %1, %2" : "=v"(fac[mt]) : "v"((unsigned)((bt * 64 + mt * 16 + (lane & 15)) * 8 + (wid < 6 ? 4 : 0))), "s"(ws + WS_GATE) : "memory");
    f32x4 acc[4][5] = {};
    const int rr = (slot >> 2) + 3 * (slot & 3);
    if (wid < 6) k3_stream<8>(acc, ap, bp, lane16, rr & 7); else k3_stream<10>(acc, ap, bp, lane16, rr % 10);
    asm volatile("" : "+v"(fac[0]), "+v"(fac[1]), "+v"(fac[2]), "+v"(fac[3]));
    STAMP(2, 1);
#pragma unroll
    for (int mt = 0; mt < 4; ++mt)
#pragma unroll
        for (int nt = 0; nt < 5; ++nt) *(f32x4*)(lds + (wid * 20 + mt * 5 + nt) * 1024 + lane16) = acc[mt][nt] * fac[mt];
    __syncthreads();
#pragma unroll
    for (int i = 0; i < 3; ++i) { const int t = wid + 8 * i;
        if (t < 20) {
            f32x4 sum = *(const f32x4*)(lds + t * 1024 + lane16);
#pragma unroll
            for (int w = 1; w < 8; ++w) sum += *(const f32x4*)(lds + (w * 20 + t) * 1024 + lane16);
            const int m4 = t / 5, nt = t - m4 * 5;
            const int tok = bt * 64 + m4 * 16 + (lane & 15);
            *(u32x2*)(out + (size_t)tok * DM + ng * 80 + nt * 16 + (lane >> 4) * 4) = (u32x2){cvtpk(sum[0], sum[1]), cvtpk(sum[2], sum[3])};
        } }
    STAMP(2, 2);
}

extern "C" void kernel_launch(void* const* d_in, const int* in_sizes, int n_in, void* d_out, int out_size, void* d_ws, size_t ws_size, hipStream_t stream) {
    const unsigned short* x = (const unsigned short*)d_in[0];
    const unsigned short* wqkv = (const unsigned short*)d_in[1];
    const unsigned short* wo = (const unsigned short*)d_in[2];
    const unsigned short* w1 = (const unsigned short*)d_in[4];
    const unsigned short* w2 = (const unsigned short*)d_in[5];
    const unsigned short* gw = (const unsigned short*)d_in[6];
    char* ws = (char*)d_ws;
    (void)hipFuncSetAttribute((const void*)k1_proj, hipFuncAttributeMaxDynamicSharedMemorySize, K1_LDS);
    (void)hipFuncSetAttribute((const void*)k2_attn, hipFuncAttributeMaxDynamicSharedMemorySize, K2_LDS);
    (void)hipFuncSetAttribute((const void*)k3_out, hipFuncAttributeMaxDynamicSharedMemorySize, K3_LDS);
    k1_proj<<<256, 512, K1_LDS, stream>>>(x, wqkv, wo, w1, w2, ws);
    k2_attn<<<256, 576, K2_LDS, stream>>>(ws, x, gw);
    k3_out<<<256, 512, K3_LDS, stream>>>(ws, (unsigned short*)d_out);
}
```

```cpp
#include <hip/hip_runtime.h>
#include <hip/hip_bf16.h>
#include <stdint.h>

typedef short bf16x8 __attribute__((ext_vector_type(8)));
typedef short s16x4 __attribute__((ext_vector_type(4)));
typedef float f32x16 __attribute__((ext_vector_type(16)));
typedef float f32x4 __attribute__((ext_vector_type(4)));
typedef float f32x2 __attribute__((ext_vector_type(2)));
typedef unsigned u32x4 __attribute__((ext_vector_type(4)));
typedef unsigned u32x2 __attribute__((ext_vector_type(2)));
typedef __bf16 bf16x2_t __attribute__((ext_vector_type(2)));
#define LAS __attribute__((address_space(3)))
typedef LAS const char* lds_cptr;
#define DI __device__ __forceinline__
#define SBAR() __builtin_amdgcn_sched_barrier(0)
#define PIN(x) asm volatile("" : "+v"(x))
#define MFMA32(a, b, c) __builtin_amdgcn_mfma_f32_32x32x16_bf16(a, b, c, 0, 0, 0)
#define MFMA16(a, b, c) __builtin_amdgcn_mfma_f32_16x16x32_bf16(a, b, c, 0, 0, 0)
#define MFMA8(a, b, c) __builtin_amdgcn_mfma_f32_32x32x8bf16_1k(a, b, c, 0, 0, 0)

constexpr int S = 2048, DM = 640, NH = 32, DH = 20, FF = 1536;
constexpr int NT = S / 32;
constexpr int QTILE_B = 1280;
constexpr int KVTILE_B = 2688;
constexpr int V_OFF = 1280, V_STEP = 704, V_ROWS = 22;
constexpr int NSIG = 68;
constexpr size_t WS_Q = 0;
constexpr size_t WS_KV = WS_Q + (size_t)NH * NT * QTILE_B;
constexpr size_t WS_AF = WS_KV + (size_t)NH * NT * KVTILE_B;
constexpr size_t WS_BF = WS_AF + (size_t)128 * NSIG * 1024;
constexpr size_t WS_KMAX = WS_BF + (size_t)8 * NSIG * 5 * 1024;
constexpr size_t WS_GATE = WS_KMAX + (size_t)160 * 32 * 4;
constexpr size_t WS_CNT = WS_GATE + (size_t)256 * 64 * 4;
constexpr size_t WS_END = WS_CNT + 8 * 256;
constexpr float QSCALE = 0.22360679774997896f * 1.4426950408889634f;
constexpr unsigned ONES2 = 0x3F803F80u;

DI unsigned cvtpk(float lo, float hi) { f32x2 v = {lo, hi}; bf16x2_t b = __builtin_convertvector(v, bf16x2_t); return __builtin_bit_cast(unsigned, b); }
DI float bf_lo(unsigned u) { return __uint_as_float(u << 16); }
DI float bf_hi(unsigned u) { return __uint_as_float(u & 0xffff0000u); }
DI float bf_round(float x) { return bf_lo(cvtpk(x, x)); }
DI void glds16(const void* sbase, unsigned voff, unsigned lds_base) {
    unsigned sv;
    asm volatile("s_mov_b32 %0, m0\n\ts_mov_b32 m0, %3\n\ts_nop 0\n\tglobal_load_lds_dwordx4 %1, %2\n\ts_mov_b32 m0, %0"
                 : "=&s"(sv) : "v"(voff), "s"(sbase), "s"(lds_base) : "memory");
}
typedef short v4i16_t __attribute__((ext_vector_type(4)));
DI s16x4 ldstr(lds_cptr p) { return __builtin_bit_cast(s16x4, __builtin_amdgcn_ds_read_tr16_b64_v4i16((LAS v4i16_t*)p)); }
DI bf16x8 lds128(lds_cptr p) { return *(const LAS bf16x8*)p; }
DI s16x4 lds64(lds_cptr p) { return *(const LAS s16x4*)p; }
DI unsigned rfl(unsigned x) { return __builtin_amdgcn_readfirstlane(x); }
#define STAMP(k, i) do { } while (0)

#define K1ROT 0
constexpr int K1_STAGE = 49152;
constexpr int K1_LDS = 3 * K1_STAGE;

DI float rdl(float x, int l) { return __uint_as_float(__builtin_amdgcn_readlane(__float_as_uint(x), l)); }
#define WT 1
typedef __amdgpu_buffer_rsrc_t rsrc_t;
DI rsrc_t ws_rsrc(char* ws) { return __builtin_amdgcn_make_buffer_rsrc(ws, 0, (int)WS_END, 0x00020000); }
DI void st16(rsrc_t rs, unsigned off, u32x4 v) { __builtin_amdgcn_raw_buffer_store_b128(v, rs, off, 0, WT ? 16 : 0); }
DI void st8(rsrc_t rs, unsigned off, u32x2 v) { __builtin_amdgcn_raw_buffer_store_b64(v, rs, off, 0, WT ? 16 : 0); }
template <int CTRL> DI float dpp_max(float x) { const float y = __uint_as_float(__builtin_amdgcn_update_dpp(0u, __float_as_uint(x), CTRL, 0xf, 0xf, true)); return fmaxf(x, y); }
template <int CTRL> DI float dpp_add(float x) { const float y = __uint_as_float(__builtin_amdgcn_update_dpp(0u, __float_as_uint(x), CTRL, 0xf, 0xf, true)); return x + y; }

template <bool HT>
struct K1Dma {
    static constexpr int NW = HT ? 3 : 4;
    const char* xbase; const char* wbase; int ldw2; unsigned lds0, xoff0, xoff1, woff[3];
    DI void init(const char* xb, const char* wb, int ld, char* lds, int lane) {
        xbase = xb; wbase = wb; ldw2 = ld; lds0 = (unsigned)(uintptr_t)lds;
        const int rr = lane >> 3;
        xoff0 = rr * 1280 + (((lane & 7) ^ ((lane >> 4) & 7)) << 4); xoff1 = rr * 1280 + (((lane & 7) ^ ((4 + (lane >> 4)) & 7)) << 4);
        if (HT) {
#pragma unroll
            for (int p = 0; p < 3; ++p) { const int o = 1024 * p + 16 * lane, row = (o * 21846) >> 22, col = o - row * 192; woff[p] = row * ldw2 + col; }
        } else woff[0] = (lane >> 4) * ldw2 + ((((lane & 15) ^ ((lane >> 4) << 2))) << 4);
    }
    DI void xp(int kc, int st, int j, int par) const { glds16(xbase + (size_t)j * 8 * 1280 + kc * 128, par ? xoff1 : xoff0, lds0 + st * K1_STAGE + j * 1024); }
    DI void wp(int kc, int st, int lw, int jj) const {
        if (HT) glds16(wbase + (size_t)(kc * 64 + 16 * lw) * ldw2, woff[jj], lds0 + st * K1_STAGE + 32768 + (3 * lw + jj) * 1024);
        else glds16(wbase + (size_t)(kc * 64 + 16 * lw + 4 * jj) * ldw2, woff[0], lds0 + st * K1_STAGE + 32768 + (4 * lw + jj) * 1024);
    }
    DI void first(int wid) const {
        if (wid >= 4) {
#pragma unroll
            for (int jj = 0; jj < NW; ++jj) wp(0, 0, wid - 4, jj);
#pragma unroll
            for (int jj = 0; jj < 2; ++jj) xp(0, 0, 2 * (wid - 4) + jj, jj & 1);
        } else {
#pragma unroll
            for (int jj = 0; jj < 6; ++jj) xp(0, 0, 8 + 6 * wid + jj, jj & 1);
        }
    }
    DI void loader(int lw) const {
        auto issue = [&](int kc, int st) {
#pragma unroll
            for (int jj = 0; jj < NW; ++jj) wp(kc, st, lw, jj);
#pragma unroll
            for (int jj = 0; jj < 8; ++jj) xp(kc, st, 8 * lw + jj, jj & 1);
        };
        issue(1, 1);
        if (HT) asm volatile("s_waitcnt vmcnt(11)\n\ts_barrier" ::: "memory"); else asm volatile("s_waitcnt vmcnt(12)\n\ts_barrier" ::: "memory");
        issue(2, 2);
        int st = 0;
        for (int kc = 0; kc < 9; ++kc) {
            if (kc == 8) asm volatile("s_waitcnt vmcnt(0)\n\ts_barrier" ::: "memory");
            else if (HT) asm volatile("s_waitcnt vmcnt(11)\n\ts_barrier" ::: "memory");
            else asm volatile("s_waitcnt vmcnt(12)\n\ts_barrier" ::: "memory");
            if (kc + 3 < 10) issue(kc + 3, st);
            st = st == 2 ? 0 : st + 1;
        }
    }
};
template <int TYPE, int MT, int NTL>
DI void k1_mainloop(f32x16 (&acc)[MT][NTL], char* lds, int wid, int lane, char* ws) {
    const int h = lane >> 5, r = lane & 31;
    constexpr bool HT = TYPE == 2;
    unsigned xa[4];
    { const int row = (HT ? wid * 64 : (wid >> 1) * 128) + r; const int sw = (row >> 1) & 7;
#pragma unroll
      for (int ks = 0; ks < 4; ++ks) xa[ks] = row * 128 + (((2 * ks + h) ^ sw) << 4); }
    unsigned wa[2];
    { const int blk = (lane >> 4) & 1, q = (lane & 15) >> 2, p = lane & 3;
      if (HT) wa[0] = wa[1] = 32768 + (8 * h + q) * 192 + blk * 32 + p * 8;
      else {
#pragma unroll
          for (int nt = 0; nt < 2; ++nt) { const int ch = (wid & 1) * 8 + nt * 4 + 2 * blk + (p >> 1); wa[nt] = 32768 + (8 * h + q) * 256 + ((ch ^ (q << 2)) << 4) + 8 * (p & 1); } } }
    bf16x8 xf[2][MT], wf[2][NTL];
    auto rdw = [&](int buf, int nt, lds_cptr sb, int ks) {
        lds_cptr a = HT ? sb + wa[0] + ks * 16 * 192 + nt * 64 : sb + wa[nt & 1] + ks * 4096;
        const s16x4 lo = ldstr(a), hi = ldstr(a + (HT ? 4 * 192 : 1024));
        wf[buf][nt] = __builtin_shufflevector(lo, hi, 0, 1, 2, 3, 4, 5, 6, 7);
    };
    auto rdx = [&](int buf, int mt, lds_cptr sb, int ks) { xf[buf][mt] = lds128(sb + xa[ks] + mt * 4096); };
    auto mm = [&](int buf, int mt, int nt) { acc[mt][nt] = TYPE == 1 ? MFMA32(xf[buf][mt], wf[buf][nt], acc[mt][nt]) : MFMA32(wf[buf][nt], xf[buf][mt], acc[mt][nt]); };
    STAMP(3, 0);
    asm volatile("s_waitcnt vmcnt(0)\n\ts_barrier" ::: "memory");
    STAMP(3, 1);
    { lds_cptr sb = (lds_cptr)lds; rdw(0, 0, sb, 0); rdx(0, 0, sb, 0); rdw(0, 1, sb, 0); rdx(0, 1, sb, 0);
      if (HT) rdw(0, 2, sb, 0); else { rdx(0, 2 % MT, sb, 0); rdx(0, 3 % MT, sb, 0); } }
    int st = 0;
#define K1_KSTEP(ks, NEXT) do { const int b = (ks) & 1, n = b ^ 1; lds_cptr np = (ks) < 3 ? sb : sn; const int nk = ((ks) + 1) & 3; \
        SBAR(); \
        if constexpr (HT) { \
            mm(b, 0, 0); SBAR(); if (NEXT) { rdw(n, 0, np, nk); } SBAR(); \
            mm(b, 0, 1); SBAR(); if (NEXT) { rdx(n, 0, np, nk); } SBAR(); \
            mm(b, 1, 0); SBAR(); if (NEXT) { rdw(n, 1, np, nk); } SBAR(); \
            mm(b, 1, 1); SBAR(); if (NEXT) { rdx(n, 1, np, nk); } SBAR(); \
            mm(b, 0, 2); SBAR(); if (NEXT) { rdw(n, 2, np, nk); } SBAR(); \
            mm(b, 1, 2); SBAR(); \
        } else { \
            mm(b, 0, 0); SBAR(); if (NEXT) { rdw(n, 0, np, nk); } SBAR(); \
            mm(b, 0, 1); SBAR(); if (NEXT) { rdx(n, 0, np, nk); } SBAR(); \
            mm(b, 1, 0); SBAR(); if (NEXT) { rdw(n, 1, np, nk); } SBAR(); \
            mm(b, 1, 1); SBAR(); if (NEXT) { rdx(n, 1, np, nk); } SBAR(); \
            mm(b, 2 % MT, 0); SBAR(); if (NEXT) { rdx(n, 2 % MT, np, nk); } SBAR(); \
            mm(b, 2 % MT, 1); SBAR(); if (NEXT) { rdx(n, 3 % MT, np, nk); } SBAR(); \
            mm(b, 3 % MT, 0); SBAR(); \
            mm(b, 3 % MT, 1); SBAR(); \
        } } while (0)
#pragma unroll 1
    for (int kc = 0; kc < 9; ++kc) {
        lds_cptr sb = (lds_cptr)lds + st * K1_STAGE;
        st = st == 2 ? 0 : st + 1;
        lds_cptr sn = (lds_cptr)lds + st * K1_STAGE;
        K1_KSTEP(0, true); K1_KSTEP(1, true); K1_KSTEP(2, true);
        asm volatile("s_waitcnt lgkmcnt(0)\n\ts_barrier" ::: "memory");
        K1_KSTEP(3, true);
    }
    { lds_cptr sb = (lds_cptr)lds + st * K1_STAGE; lds_cptr sn = sb;
      K1_KSTEP(0, true); K1_KSTEP(1, true); K1_KSTEP(2, true); K1_KSTEP(3, false); }
}

__global__ __launch_bounds__(512, 2) void k1_proj(const unsigned short* __restrict__ x, const unsigned short* __restrict__ wqkv, const unsigned short* __restrict__ wo,
                                                  const unsigned short* __restrict__ w1, const unsigned short* __restrict__ w2, char* __restrict__ ws) {
    extern __shared__ __attribute__((aligned(16))) char lds[];
    const int tid = threadIdx.x, lane = tid & 63, wid = rfl(tid >> 6);
    const int xcd = blockIdx.x & 7, slot = blockIdx.x >> 3;
    const int rgrp = xcd >> 2, cgrp = xcd & 3, nq = cgrp < 3 ? 4 : 3;
    const int h = lane >> 5, r = lane & 31;
    const rsrc_t rs = ws_rsrc(ws);
    const int rt = 4 * rgrp + (slot & 3), ci = slot >> 2;
    const char* xbase = (const char*)x + (size_t)rt * 256 * 1280;
    int ty = 0;
    STAMP(0, 0);
    if (ci < nq) {
        const int ct = 4 * cgrp + ci;
        const bool isV = ct >= 10;
        ty = isV ? 2 : 1;
        const int wm = (wid >> 1) & 1, wn = wid & 1;
        K1Dma<false> dma; dma.init(xbase, (const char*)wqkv + ct * 256, 3 * DM * 2, lds, lane);
        dma.first(wid);
        if (wid >= 4) dma.loader(wid - 4);
        else {
            f32x16 acc[4][2] = {};
            if (isV) {
                k1_mainloop<1, 4, 2>(acc, lds, wid, lane, ws);
                STAMP(0, 1);
#pragma unroll
                for (int nt = 0; nt < 2; ++nt) {
                    const int phi = (ct - 10) * 128 + wn * 64 + nt * 32 + r;
                    const int head = (phi * 3277) >> 16, d = phi - head * 20;
                    const unsigned dst = (unsigned)WS_KV + (unsigned)(head * NT + rt * 8 + wm * 4) * KVTILE_B + V_OFF + (h * V_ROWS + d) * 16;
#pragma unroll
                    for (int mt = 0; mt < 4; ++mt)
#pragma unroll
                        for (int s = 0; s < 2; ++s) {
                            const f32x16& a = acc[mt][nt];
                            const u32x4 w = {cvtpk(a[8 * s], a[8 * s + 1]), cvtpk(a[8 * s + 2], a[8 * s + 3]), cvtpk(a[8 * s + 4], a[8 * s + 5]), cvtpk(a[8 * s + 6], a[8 * s + 7])};
                            st16(rs, dst + mt * KVTILE_B + s * V_STEP, w);
                        }
                }
            } else {
                k1_mainloop<0, 4, 2>(acc, lds, wid, lane, ws);
                STAMP(0, 1);
                char* sb = lds + K1_STAGE + (wm * 4 * 32 + wn * 16 + h) * 256 + r * 8;
                if (ct < 5) {
#pragma unroll
                    for (int nt = 0; nt < 2; ++nt)
#pragma unroll
                        for (int g = 0; g < 4; ++g)
#pragma unroll
                            for (int mt = 0; mt < 4; ++mt) {
                                const f32x16& a = acc[mt][nt];
                                *(u32x2*)(sb + (mt * 32 + nt * 8 + 2 * g) * 256) = (u32x2){cvtpk(a[4 * g] * QSCALE, a[4 * g + 1] * QSCALE), cvtpk(a[4 * g + 2] * QSCALE, a[4 * g + 3] * QSCALE)};
                            }
                } else {
                    float nrm[8];
#pragma unroll
                    for (int nt = 0; nt < 2; ++nt)
#pragma unroll
                        for (int g = 0; g < 4; ++g) {
                            float m = 0.f;
#pragma unroll
                            for (int mt = 0; mt < 4; ++mt) {
                                const f32x16& a = acc[mt][nt];
                                *(u32x2*)(sb + (mt * 32 + nt * 8 + 2 * g) * 256) = (u32x2){cvtpk(a[4 * g], a[4 * g + 1]), cvtpk(a[4 * g + 2], a[4 * g + 3])};
                                m = fmaxf(m, __builtin_fmaf(a[4 * g], a[4 * g], a[4 * g + 1] * a[4 * g + 1]) + __builtin_fmaf(a[4 * g + 2], a[4 * g + 2], a[4 * g + 3] * a[4 * g + 3]));
                            }
                            nrm[nt * 4 + g] = m;
                        }
#pragma unroll
                    for (int i = 0; i < 8; ++i) { float m = nrm[i]; m = dpp_max<0xB1>(m); m = dpp_max<0x4E>(m); m = dpp_max<0x141>(m); m = dpp_max<0x140>(m);
                        auto rr = __builtin_amdgcn_permlane16_swap(__float_as_uint(m), __float_as_uint(m), false, false); nrm[i] = fmaxf(__uint_as_float(rr[0]), __uint_as_float(rr[1])); }
                    float mine = nrm[0];
#pragma unroll
                    for (int i = 1; i < 8; ++i) mine = r == i ? nrm[i] : mine;
                    if (r < 8) ((float*)(ws + WS_KMAX))[((ct - 5) * 32 + wn * 16 + (r >> 2) * 8 + 2 * (r & 3) + h) * 16 + rt * 2 + wm] = mine;
                }
            }
        }
        if (!isV) {
            __syncthreads();
            const bool isQ = ct < 5;
            const int ctl = isQ ? ct : ct - 5;
            const unsigned img = (unsigned)(isQ ? WS_Q : WS_KV);
            const unsigned tileb = isQ ? QTILE_B : KVTILE_B;
            const int T = rt * 8 + wid, G0 = ctl * 32;
            const int hd0 = (G0 * 205) >> 10, id0 = 3 * hd0 + ((G0 - 5 * hd0) >> 1);
            const char* lb = lds + K1_STAGE + wid * 8192;
            u32x2 pa[10], pb[10];
#pragma unroll
            for (int it = 0; it < 10; ++it) {
                const int id = id0 + 2 * it + h, head = (id * 171) >> 9, ty_ = id - 3 * head, la = 5 * head + 2 * ty_ - G0;
                const int lc = la < 0 ? 0 : (la > 31 ? 31 : la);
                const int aA = ty_ < 2 ? lc * 256 + r * 8 : lc * 256 + (r & 15) * 16;
                const int aB = ty_ < 2 ? (la > 30 ? 31 : la + 1) * 256 + r * 8 : aA + 8;
                pa[it] = *(const u32x2*)(lb + aA); pb[it] = *(const u32x2*)(lb + aB);
            }
#pragma unroll
            for (int it = 0; it < 10; ++it) {
                const int id = id0 + 2 * it + h, head = (id * 171) >> 9, ty_ = id - 3 * head, la = 5 * head + 2 * ty_ - G0;
                const unsigned dst = img + (unsigned)(head * NT + T) * tileb + ty_ * 512 + r * 16;
                const bool okA = (unsigned)la < 32u, okB = ty_ == 2 ? okA : (unsigned)(la + 1) < 32u;
                if (okA && okB) { if (ty_ < 2 || r < 16) st16(rs, dst, (u32x4){pa[it][0], pa[it][1], pb[it][0], pb[it][1]}); }
                else if (okA) st8(rs, dst, pa[it]);
                else if (okB) st8(rs, dst + 8, pb[it]);
            }
        }
    } else if (ci < nq + 4) {
        const int ch = 4 * cgrp + (ci - nq);
        ty = 3;
        K1Dma<true> dma; dma.init(xbase, (const char*)w1 + ch * 192, FF * 2, lds, lane);
        dma.first(wid);
        if (wid >= 4) dma.loader(wid - 4);
        else {
            f32x16 acc[2][3] = {};
            k1_mainloop<2, 2, 3>(acc, lds, wid, lane, ws);
            STAMP(0, 1);
#pragma unroll
            for (int mt = 0; mt < 2; ++mt) {
                const int tok = rt * 256 + wid * 64 + mt * 32 + r, rho = tok >> 4;
#pragma unroll
                for (int nt = 0; nt < 3; ++nt) {
                    const int sig = ch * 3 + nt;
                    const unsigned dst = (unsigned)WS_AF + (unsigned)(rho * NSIG + sig) * 1024 + (h * 16 + (r & 15)) * 16;
                    float sv[16];
#pragma unroll
                    for (int i = 0; i < 16; ++i) { const float v = acc[mt][nt][i]; sv[i] = v * __builtin_amdgcn_rcpf(1.f + __builtin_amdgcn_exp2f(v * -1.4426950408889634f)); }
#pragma unroll
                    for (int s = 0; s < 2; ++s) {
                        const u32x4 w = {cvtpk(sv[8 * s], sv[8 * s + 1]), cvtpk(sv[8 * s + 2], sv[8 * s + 3]), cvtpk(sv[8 * s + 4], sv[8 * s + 5]), cvtpk(sv[8 * s + 6], sv[8 * s + 7])};
                        st16(rs, dst + s * 512, w);
                    }
                }
            }
        }
    } else {
        const int aidx = rgrp * 4 + (slot & 3);
        const unsigned lds0 = (unsigned)(uintptr_t)lds;
        auto slab_load = [&](int sig, int u) {
            const char* src = sig < 48 ? (const char*)w2 + (size_t)sig * 32 * 1280 : (const char*)wo + (size_t)(sig - 48) * 32 * 1280;
#pragma unroll
            for (int jj = 0; jj < 5; ++jj) { const int j = wid * 5 + jj; glds16(src + j * 1024, lane * 16, lds0 + u * 40960 + j * 1024); }
        };
        slab_load(aidx, 0); slab_load(aidx + 8, 1); slab_load(aidx + 16, 2);
        const int c = lane >> 4, s = c >> 1, hh = c & 1, q = (lane & 15) >> 2, p = lane & 3;
        const int nsl = aidx < 4 ? 9 : 8;
        int u = 0;
        for (int i = 0; i < nsl; ++i) {
            if (i == 0) asm volatile("s_waitcnt vmcnt(10)\n\ts_barrier" ::: "memory");
            else if (i == 1) asm volatile("s_waitcnt vmcnt(15)\n\ts_barrier" ::: "memory");
            else if (i + 2 < nsl) asm volatile("s_waitcnt vmcnt(20)\n\ts_barrier" ::: "memory");
            else asm volatile("s_waitcnt vmcnt(5)\n\ts_barrier" ::: "memory");
            const int sig = aidx + 8 * i;
            bf16x8 f[5];
#pragma unroll
            for (int k = 0; k < 5; ++k) {
                lds_cptr a = (lds_cptr)lds + u * 40960 + (16 * s + 4 * hh + q) * 1280 + (16 * (wid * 5 + k) + 4 * p) * 2;
                const s16x4 lo = ldstr(a), hi = ldstr(a + 8 * 1280);
                f[k] = __builtin_shufflevector(lo, hi, 0, 1, 2, 3, 4, 5, 6, 7);
            }
#pragma unroll
            for (int k = 0; k < 5; ++k) st16(rs, (unsigned)WS_BF + (unsigned)((wid * NSIG + sig) * 5 + k) * 1024 + lane * 16, __builtin_bit_cast(u32x4, f[k]));
            asm volatile("s_waitcnt lgkmcnt(0)\n\ts_barrier" ::: "memory");
            if (i + 3 < nsl) slab_load(sig + 24, u);
            u = u == 2 ? 0 : u + 1;
        }
        if (aidx == 0 && tid < 8) *(unsigned*)(ws + WS_CNT + tid * 256) = 0u;
#pragma unroll
        for (int k = 0; k < 4; ++k) { const int i = (aidx * 4 + k) * 512 + tid;
          const unsigned f = (i & 1) ? 0u : ONES2; st16(rs, (unsigned)WS_KV + (unsigned)(i >> 3) * KVTILE_B + V_OFF + ((i >> 2) & 1) * V_STEP + (((i >> 1) & 1) * V_ROWS + 20 + (i & 1)) * 16, (u32x4){f, f, f, f}); }
    }
}

constexpr int K2_CHUNK = 8 * KVTILE_B;
constexpr int K2_LDS = 3 * K2_CHUNK;

DI float swap32_add(float x) { auto rr = __builtin_amdgcn_permlane32_swap(__float_as_uint(x), __float_as_uint(x), false, false); return __uint_as_float(rr[0]) + __uint_as_float(rr[1]); }
DI float swap32_max(float x) { auto rr = __builtin_amdgcn_permlane32_swap(__float_as_uint(x), __float_as_uint(x), false, false); return fmaxf(__uint_as_float(rr[0]), __uint_as_float(rr[1])); }
DI s16x4 make_qb(unsigned q0, unsigned q1, float M, int h) {
    const float mh = bf_hi(__float_as_uint(M)), r1 = M - mh, mm = bf_hi(__float_as_uint(r1)), r2 = r1 - mm, ml = bf_hi(__float_as_uint(r2));
    u32x2 w;
    w[0] = h ? ((__float_as_uint(mh) >> 16) | (__float_as_uint(mm) & 0xffff0000u)) ^ 0x80008000u : q0;
    w[1] = h ? (__float_as_uint(ml) >> 16) ^ 0x8000u : q1;
    return __builtin_bit_cast(s16x4, w);
}

DI void attn_fallback(f32x16& o, const char* kv, bf16x8 qa, unsigned q0, unsigned q1, int lane, int kboff, int voff) {
    const int h = lane >> 5;
    const f32x16 zero = {};
    float m = -INFINITY;
    const s16x4 qb0 = make_qb(q0, q1, 0.f, h);
    for (int t = 0; t < NT; ++t) {
        const char* tb = kv + t * KVTILE_B;
        const bf16x8 ka = *(const bf16x8*)(tb + lane * 16); const s16x4 kb = *(const s16x4*)(tb + kboff);
        f32x16 s = MFMA32(ka, qa, zero); s = MFMA8(kb, qb0, s);
#pragma unroll
        for (int i = 0; i < 16; ++i) m = fmaxf(m, s[i]);
    }
    m = swap32_max(m);
    const s16x4 qb = make_qb(q0, q1, m, h);
    o = zero;
    for (int t = 0; t < NT; ++t) {
        const char* tb = kv + t * KVTILE_B;
        const bf16x8 ka = *(const bf16x8*)(tb + lane * 16); const s16x4 kb = *(const s16x4*)(tb + kboff);
        const bf16x8 va = *(const bf16x8*)(tb + voff), vb = *(const bf16x8*)(tb + voff + V_STEP);
        f32x16 s = MFMA32(ka, qa, zero); s = MFMA8(kb, qb, s);
        u32x4 p0, p1;
#pragma unroll
        for (int i = 0; i < 4; ++i) { p0[i] = cvtpk(__builtin_amdgcn_exp2f(s[2 * i]), __builtin_amdgcn_exp2f(s[2 * i + 1])); p1[i] = cvtpk(__builtin_amdgcn_exp2f(s[8 + 2 * i]), __builtin_amdgcn_exp2f(s[9 + 2 * i])); }
        o = MFMA32(va, __builtin_bit_cast(bf16x8, p0), o);
        o = MFMA32(vb, __builtin_bit_cast(bf16x8, p1), o);
    }
}

DI void attn_role(char* __restrict__ ws, char* lds, int bid) {
    const int tid = threadIdx.x, lane = tid & 63, wid = rfl(tid >> 6);
    const int h = lane >> 5, r = lane & 31;
    const int xcd = bid & 7, slot = bid >> 3;
    const int head = xcd * 4 + (slot & 3), qblk = slot >> 2;
    STAMP(1, 0);
    const char* kv = ws + WS_KV + (size_t)head * NT * KVTILE_B;
    const unsigned lds0 = (unsigned)(uintptr_t)lds;
    const unsigned lane16 = lane * 16;
    const char* kvw = kv + wid * 1024;
    const unsigned ldw = lds0 + wid * 1024;
    const bool third = wid < 5;
    const int Tq = qblk * 8 + wid;
    const char* qt = ws + WS_Q + ((size_t)head * NT + Tq) * QTILE_B;
    const bf16x8 qa = *(const bf16x8*)(qt + lane * 16);
    const u32x2 qbr = *(const u32x2*)(qt + 1024 + r * 8);
    f32x4 km4 = *(const f32x4*)(ws + WS_KMAX + (size_t)head * 320 + (lane < 20 ? lane : 0) * 16);
    glds16(kvw, lane16, ldw); glds16(kvw + 8192, lane16, ldw + 8192); if (third) glds16(kvw + 16384, lane16, ldw + 16384);
    asm volatile("" ::: "memory");
    glds16(kvw + K2_CHUNK, lane16, ldw + K2_CHUNK); glds16(kvw + K2_CHUNK + 8192, lane16, ldw + K2_CHUNK + 8192); if (third) glds16(kvw + K2_CHUNK + 16384, lane16, ldw + K2_CHUNK + 16384);
    float km = fmaxf(fmaxf(km4[0], km4[1]), fmaxf(km4[2], km4[3]));
    km = dpp_max<0xB1>(km); km = dpp_max<0x4E>(km);
    km = __builtin_sqrtf(km);
    float M;
    { const float k0 = rdl(km, 0), k1 = rdl(km, 4), k2 = rdl(km, 8), k3 = rdl(km, 12), k4 = rdl(km, 16);
      const u32x4 qw = __builtin_bit_cast(u32x4, qa);
      float n0, n1, n2;
      { const float a = bf_lo(qw[0]), b = bf_hi(qw[0]), c = bf_lo(qw[1]), d = bf_hi(qw[1]); n0 = (a * a + b * b) + (c * c + d * d); }
      { const float a = bf_lo(qw[2]), b = bf_hi(qw[2]), c = bf_lo(qw[3]), d = bf_hi(qw[3]); n1 = (a * a + b * b) + (c * c + d * d); }
      { const float a = bf_lo(qbr[0]), b = bf_hi(qbr[0]), c = bf_lo(qbr[1]), d = bf_hi(qbr[1]); n2 = (a * a + b * b) + (c * c + d * d); }
      float part = __builtin_amdgcn_sqrtf(n0) * (h ? k2 : k0) + __builtin_amdgcn_sqrtf(n1) * (h ? k3 : k1);
      if (!h) part += __builtin_amdgcn_sqrtf(n2) * k4;
      M = swap32_add(part) * 1.01f; }
    const s16x4 qb = make_qb(qbr[0], qbr[1], M, h);
    const int vrow = r <= 20 ? r : (r < 28 ? 21 : r - 12);
    const int voff = V_OFF + (h * V_ROWS + vrow) * 16, kboff = h ? V_OFF + 20 * 16 : 1024 + r * 8;
    unsigned kaC = lane * 16, kbC = kboff, vC = voff;
    unsigned kaN = kaC + K2_CHUNK, kbN = kbC + K2_CHUNK, vN = vC + K2_CHUNK;
    const lds_cptr L = (lds_cptr)lds;
    const f32x16 zero = {};
    f32x16 o = {}, sA, sB;
    bf16x8 ka, va, vb; s16x4 kb;
    u32x4 plo, phi;
    asm volatile("s_waitcnt vmcnt(2)\n\ts_barrier" ::: "memory");
    STAMP(1, 1);
    ka = lds128(L + kaC); kb = lds64(L + kbC);
    sA = MFMA32(ka, qa, zero); sA = MFMA8(kb, qb, sA);
    ka = lds128(L + kaC + KVTILE_B); kb = lds64(L + kbC + KVTILE_B);
    va = lds128(L + vC);
    vb = (bf16x8){}; phi = (u32x4){};
    f32x4 e3 = {};
    int stn = 1;

    const char* src = kvw + 2 * K2_CHUNK;
    unsigned dst = ldw + 2 * K2_CHUNK;
#define EXP4(X, i) do { X[i] = __builtin_amdgcn_exp2f(X[i]); X[i + 1] = __builtin_amdgcn_exp2f(X[i + 1]); X[i + 2] = __builtin_amdgcn_exp2f(X[i + 2]); X[i + 3] = __builtin_amdgcn_exp2f(X[i + 3]); } while (0)
#define KADDR(A, i) (((i) < 8) ? (A##C + (i) * KVTILE_B) : (A##N + ((i) - 8) * KVTILE_B))
#define STEP(C, N, i, DMA) do { \
        bf16x8 ka2, va2, vb2; s16x4 kb2; \
        N = MFMA32(ka, qa, zero); SBAR(); \
        ka2 = lds128(L + KADDR(ka, (i) + 2)); \
        phi[2] = cvtpk(e3[0], e3[1]); phi[3] = cvtpk(e3[2], e3[3]); PIN(phi); SBAR(); EXP4(C, 0); PIN(C); SBAR(); \
        o = MFMA32(vb, __builtin_bit_cast(bf16x8, phi), o); SBAR(); \
        vb2 = lds128(L + vC + (i) * KVTILE_B + V_STEP); \
        plo[0] = cvtpk(C[0], C[1]); plo[1] = cvtpk(C[2], C[3]); PIN(plo); SBAR(); EXP4(C, 4); PIN(C); SBAR(); \
        N = MFMA8(kb, qb, N); SBAR(); \
        DMA; \
        kb2 = lds64(L + KADDR(kb, (i) + 2)); \
        plo[2] = cvtpk(C[4], C[5]); plo[3] = cvtpk(C[6], C[7]); PIN(plo); SBAR(); EXP4(C, 8); PIN(C); SBAR(); \
        o = MFMA32(va, __builtin_bit_cast(bf16x8, plo), o); SBAR(); \
        va2 = lds128(L + KADDR(v, (i) + 1)); \
        phi[0] = cvtpk(C[8], C[9]); phi[1] = cvtpk(C[10], C[11]); PIN(phi); SBAR(); \
        e3[0] = __builtin_amdgcn_exp2f(C[12]); e3[1] = __builtin_amdgcn_exp2f(C[13]); e3[2] = __builtin_amdgcn_exp2f(C[14]); e3[3] = __builtin_amdgcn_exp2f(C[15]); PIN(e3); SBAR(); \
        ka = ka2; kb = kb2; va = va2; vb = vb2; \
    } while (0)
#define PF(j) do { if (c < 6 && ((j) < 2 || third)) glds16(src + (j) * 8192, lane16, dst + (j) * 8192); } while (0)
    for (int c = 0; c < 8; ++c) {
        STEP(sA, sB, 0, ); STEP(sB, sA, 1, ); STEP(sA, sB, 2, ); STEP(sB, sA, 3, ); STEP(sA, sB, 4, );
        asm volatile("s_waitcnt vmcnt(0)\n\ts_barrier" ::: "memory");
        STEP(sB, sA, 5, PF(0)); STEP(sA, sB, 6, PF(1)); STEP(sB, sA, 7, PF(2));
        kaC = kaN; kbC = kbN; vC = vN;
        const int d = (stn == 2) ? -2 * K2_CHUNK : K2_CHUNK;
        kaN += d; kbN += d; vN += d;
        src += K2_CHUNK; dst += (stn == 1) ? -2 * K2_CHUNK : K2_CHUNK;
        stn = (stn == 2) ? 0 : stn + 1;
    }
    phi[2] = cvtpk(e3[0], e3[1]); phi[3] = cvtpk(e3[2], e3[3]);
    o = MFMA32(vb, __builtin_bit_cast(bf16x8, phi), o);
    STAMP(1, 2);
    float l; { auto rr = __builtin_amdgcn_permlane32_swap(__float_as_uint(o[8]), __float_as_uint(o[8]), false, false); l = __uint_as_float(rr[1]); }
    if (__builtin_expect(__any(!(l >= 8.6736174e-19f && l <= 1.0e30f)), 0)) {
        attn_fallback(o, kv, qa, qbr[0], qbr[1], lane, kboff, voff);
        auto rr = __builtin_amdgcn_permlane32_swap(__float_as_uint(o[8]), __float_as_uint(o[8]), false, false); l = __uint_as_float(rr[1]);
    }
    const float f = __builtin_amdgcn_rcpf(l);
    const int tok = Tq * 32 + r;
    char* ab = ws + WS_AF + (size_t)(tok >> 4) * NSIG * 1024 + (tok & 15) * 16;
#pragma unroll
    for (int g = 0; g < 3; ++g) {
        const int gam = head * 5 + 2 * g + h;
        const int sig = 48 + (gam >> 3), c = ((gam >> 2) & 1) * 2 + (gam & 1), half = (gam >> 1) & 1;
        const u32x2 w = {cvtpk(o[4 * g] * f, o[4 * g + 1] * f), cvtpk(o[4 * g + 2] * f, o[4 * g + 3] * f)};
        if (g < 2 || h == 0) __hip_atomic_store((unsigned long long*)(ab + sig * 1024 + c * 256 + half * 8), ((unsigned long long)w[1] << 32) | w[0], __ATOMIC_RELAXED, __HIP_MEMORY_SCOPE_AGENT);
    }
    asm volatile("s_waitcnt vmcnt(0)\n\ts_barrier" ::: "memory");
    if (tid == 0) __hip_atomic_fetch_add((unsigned*)(ws + WS_CNT + qblk * 256), 1u, __ATOMIC_RELAXED, __HIP_MEMORY_SCOPE_AGENT);
    STAMP(1, 3);
}

#define APRIO 1
#define BPRIO 3
constexpr int B_STAGE = 20480;
DI void gld128(bf16x8& d, const char* sbase, unsigned voff) { asm volatile("global_load_dwordx4 %0, %1, %2" : "=v"(d) : "v"(voff), "s"(sbase) : "memory"); }
DI void gld128_sc1(bf16x8& d, const char* sbase, unsigned voff) { asm volatile("global_load_dwordx4 %0, %1, %2 sc1" : "=v"(d) : "v"(voff), "s"(sbase) : "memory"); }
#define WAITV2(n, x, y) asm volatile("s_waitcnt vmcnt(" #n ")" : "+v"(x), "+v"(y) :: "memory")
template <int ND>
DI void mlp_role(char* __restrict__ ws, const unsigned short* __restrict__ x, const unsigned short* __restrict__ gw, unsigned short* __restrict__ out, char* lds, int bid) {
    const int tid = threadIdx.x, lane = tid & 63, wid = rfl(tid >> 6);
    const int xcd = bid & 7, slot = bid >> 3;
    const int ng = (xcd & 1) * 4 + (slot & 3), bt = (xcd >> 1) * 8 + (slot >> 2);
    const int wm = wid & 1, kq = wid >> 1;
    STAMP(2, 0);
    const unsigned lds0 = (unsigned)(uintptr_t)lds, lane16 = lane * 16;
    const char* ap = ws + WS_AF + ((size_t)(bt * 4 + wm * 2) * NSIG + kq) * 1024;
    const char* bp = ws + WS_BF + (size_t)ng * NSIG * 5 * 1024 + wid * 1024;
    const unsigned ldw = lds0 + wid * 1024;
    const int rot = ((slot >> 2) + 3 * (slot & 3)) % 12;
    bf16x8 a[4][2];
    f32x4 acc[2][5] = {};
    auto sidx = [&](int i) { int t = i + rot; return t >= 12 ? t - 12 : t; };
    auto issueA = [&](bf16x8 (&d)[2], int st) { gld128(d[0], ap + st * 4096, lane16); gld128(d[1], ap + st * 4096 + NSIG * 1024, lane16); };
    auto issueD = [&](int st, int sl) {
#pragma unroll
        for (int j = 0; j < ND; ++j) glds16(bp + st * B_STAGE + j * 8192, lane16, ldw + sl * B_STAGE + j * 8192);
    };
    const lds_cptr LB = (lds_cptr)lds + kq * 5120 + lane16;
    auto compute = [&](f32x4 (&acc)[2][5], bf16x8 (&av)[2], int sl) {
        bf16x8 bf[5];
#pragma unroll
        for (int nt = 0; nt < 5; ++nt) bf[nt] = lds128(LB + sl * B_STAGE + nt * 1024);
#pragma unroll
        for (int nt = 0; nt < 5; ++nt)
#pragma unroll
            for (int mt = 0; mt < 2; ++mt) acc[mt][nt] = MFMA16(bf[nt], av[mt], acc[mt][nt]);
    };
#define BAR() asm volatile("s_waitcnt lgkmcnt(0)\n\ts_barrier" ::: "memory")
#pragma unroll
    for (int j = 0; j < 3; ++j) issueD(sidx(j), j);
    __builtin_amdgcn_s_setprio(BPRIO);
    float* gslot = (float*)(ws + WS_GATE) + bid * 64;
    { const u32x4* xr = (const u32x4*)(x + (size_t)(bt * 64 + wid * 8 + (lane >> 3)) * DM) + (lane & 7);
      const u32x4* gr = (const u32x4*)gw + (lane & 7);
      float sm = 0.f;
#pragma unroll
      for (int i = 0; i < 10; ++i) { const u32x4 xv = xr[i * 8], gv = gr[i * 8];
#pragma unroll
          for (int k = 0; k < 4; ++k) { const unsigned a_ = xv[k], b_ = gv[k]; sm = __builtin_amdgcn_fdot2_f32_bf16(__builtin_bit_cast(bf16x2_t, a_), __builtin_bit_cast(bf16x2_t, b_), sm, false); } }
      sm = dpp_add<0xB1>(sm); sm = dpp_add<0x4E>(sm); sm = dpp_add<0x141>(sm);
      const float lg = bf_round(sm);
      const float gsig = bf_round(__builtin_amdgcn_rcpf(1.f + __builtin_amdgcn_exp2f(lg * -1.4426950408889634f)));
      if ((lane & 7) == 0) __hip_atomic_store(gslot + wid * 8 + (lane >> 3), gsig, __ATOMIC_RELAXED, __HIP_MEMORY_SCOPE_AGENT);
      asm volatile("s_waitcnt vmcnt(0)" ::: "memory"); }
#pragma unroll
    for (int j = 0; j < 3; ++j) issueA(a[j], sidx(j));
#define MSTAGE(i, sl) do { if (ND == 3) WAITV2(10, a[sl][0], a[sl][1]); else WAITV2(8, a[sl][0], a[sl][1]); BAR(); \
        issueA(a[((sl) + 3) & 3], sidx((i) + 3)); issueD(sidx((i) + 3), ((sl) + 3) & 3); compute(acc, a[sl], sl); } while (0)
    __builtin_amdgcn_s_setprio(0);
    STAMP(2, 5);
    { WAITV2(4, a[0][0], a[0][1]); BAR(); issueA(a[3], sidx(3)); issueD(sidx(3), 3); compute(acc, a[0], 0); }
    { if (ND == 3) WAITV2(7, a[1][0], a[1][1]); else WAITV2(6, a[1][0], a[1][1]); BAR(); issueA(a[0], sidx(4)); issueD(sidx(4), 0); compute(acc, a[1], 1); }
    MSTAGE(2, 2); MSTAGE(3, 3);
    MSTAGE(4, 0); MSTAGE(5, 1); MSTAGE(6, 2); MSTAGE(7, 3);
    MSTAGE(8, 0);
    { if (ND == 3) WAITV2(10, a[1][0], a[1][1]); else WAITV2(8, a[1][0], a[1][1]); BAR(); compute(acc, a[1], 1); }
    { if (ND == 3) WAITV2(5, a[2][0], a[2][1]); else WAITV2(4, a[2][0], a[2][1]); BAR(); compute(acc, a[2], 2); }
    { WAITV2(0, a[3][0], a[3][1]); BAR(); compute(acc, a[3], 3); }
    STAMP(2, 4);
    __builtin_amdgcn_s_setprio(BPRIO);
    float gg[2];
#pragma unroll
    for (int mt = 0; mt < 2; ++mt) gg[mt] = __hip_atomic_load(gslot + wm * 32 + mt * 16 + (lane & 15), __ATOMIC_RELAXED, __HIP_MEMORY_SCOPE_AGENT);
    BAR();
#pragma unroll
    for (int mt = 0; mt < 2; ++mt) { const float og = bf_round(1.f - gg[mt]);
#pragma unroll
        for (int nt = 0; nt < 5; ++nt) *(f32x4*)(lds + ((kq * 2 + wm) * 10 + mt * 5 + nt) * 1024 + lane16) = acc[mt][nt] * og; }
    BAR();
    f32x4 hs[3];
#pragma unroll
    for (int i = 0; i < 3; ++i) { const int t = (wid + 8 * i) < 20 ? wid + 8 * i : wid;
        hs[i] = (*(const f32x4*)(lds + t * 1024 + lane16) + *(const f32x4*)(lds + (20 + t) * 1024 + lane16)) + (*(const f32x4*)(lds + (40 + t) * 1024 + lane16) + *(const f32x4*)(lds + (60 + t) * 1024 + lane16)); PIN(hs[i]); }
    BAR();
    STAMP(2, 6);
    issueD(12, 0); issueD(13, 1); issueD(14, 2); issueD(15, 3);
    __builtin_amdgcn_s_setprio(0);
    STAMP(2, 1);
    if (wid == 0) {
        const unsigned* cnt = (const unsigned*)(ws + WS_CNT + (bt >> 2) * 256);
        for (int it = 0; it < (1 << 22); ++it) {
            if ((unsigned)rfl(__hip_atomic_load(cnt, __ATOMIC_RELAXED, __HIP_MEMORY_SCOPE_AGENT)) >= 32u) break;
            __builtin_amdgcn_s_sleep(4);
        }
    }
    asm volatile("s_waitcnt vmcnt(0)\n\ts_barrier" ::: "memory");
    __builtin_amdgcn_s_setprio(BPRIO);
    STAMP(2, 2);
    bf16x8 ao[5][2];
#pragma unroll
    for (int j = 0; j < 5; ++j) { gld128_sc1(ao[j][0], ap + (12 + j) * 4096, lane16); gld128_sc1(ao[j][1], ap + (12 + j) * 4096 + NSIG * 1024, lane16); }
    f32x4 acc2[2][5] = {};
    WAITV2(8, ao[0][0], ao[0][1]); compute(acc2, ao[0], 0);
    BAR(); issueD(16, 0);
    if (ND == 3) WAITV2(9, ao[1][0], ao[1][1]); else WAITV2(8, ao[1][0], ao[1][1]);
    compute(acc2, ao[1], 1);
    if (ND == 3) WAITV2(7, ao[2][0], ao[2][1]); else WAITV2(6, ao[2][0], ao[2][1]);
    compute(acc2, ao[2], 2);
    if (ND == 3) WAITV2(5, ao[3][0], ao[3][1]); else WAITV2(4, ao[3][0], ao[3][1]);
    compute(acc2, ao[3], 3);
    WAITV2(0, ao[4][0], ao[4][1]); BAR();
    compute(acc2, ao[4], 0);
    BAR();
#pragma unroll
    for (int mt = 0; mt < 2; ++mt)
#pragma unroll
        for (int nt = 0; nt < 5; ++nt) *(f32x4*)(lds + ((kq * 2 + wm) * 10 + mt * 5 + nt) * 1024 + lane16) = acc2[mt][nt] * gg[mt];
    BAR();
#pragma unroll
    for (int i = 0; i < 3; ++i) { const int t = wid + 8 * i;
        if (t < 20) {
            const f32x4 sum = hs[i] + (*(const f32x4*)(lds + t * 1024 + lane16) + *(const f32x4*)(lds + (20 + t) * 1024 + lane16)) + (*(const f32x4*)(lds + (40 + t) * 1024 + lane16) + *(const f32x4*)(lds + (60 + t) * 1024 + lane16));
            const int m4 = t / 5, nt = t - m4 * 5;
            const int tok = bt * 64 + m4 * 16 + (lane & 15);
            *(u32x2*)(out + (size_t)tok * DM + ng * 80 + nt * 16 + (lane >> 4) * 4) = (u32x2){cvtpk(sum[0], sum[1]), cvtpk(sum[2], sum[3])};
        } }
    STAMP(2, 3);
}

constexpr int K2_LDS_ALL = 81920;
__global__ __launch_bounds__(512, 4) void k2_fused(char* __restrict__ ws, const unsigned short* __restrict__ x, const unsigned short* __restrict__ gw, unsigned short* __restrict__ out) {
    extern __shared__ __attribute__((aligned(16))) char lds[];
    if (blockIdx.x < 256) { __builtin_amdgcn_s_setprio(APRIO); attn_role(ws, lds, blockIdx.x); }
    else if (rfl(threadIdx.x >> 6) < 4) mlp_role<3>(ws, x, gw, out, lds, blockIdx.x - 256);
    else mlp_role<2>(ws, x, gw, out, lds, blockIdx.x - 256);
}

extern "C" void kernel_launch(void* const* d_in, const int* in_sizes, int n_in, void* d_out, int out_size, void* d_ws, size_t ws_size, hipStream_t stream) {
    const unsigned short* x = (const unsigned short*)d_in[0];
    const unsigned short* wqkv = (const unsigned short*)d_in[1];
    const unsigned short* wo = (const unsigned short*)d_in[2];
    const unsigned short* w1 = (const unsigned short*)d_in[4];
    const unsigned short* w2 = (const unsigned short*)d_in[5];
    const unsigned short* gw = (const unsigned short*)d_in[6];
    char* ws = (char*)d_ws;
    (void)hipFuncSetAttribute((const void*)k1_proj, hipFuncAttributeMaxDynamicSharedMemorySize, K1_LDS);
    (void)hipFuncSetAttribute((const void*)k2_fused, hipFuncAttributeMaxDynamicSharedMemorySize, K2_LDS_ALL);
    k1_proj<<<256, 512, K1_LDS, stream>>>(x, wqkv, wo, w1, w2, ws);
    k2_fused<<<512, 512, K2_LDS_ALL, stream>>>(ws, x, gw, (unsigned short*)d_out);
}
```

```cpp
#include <hip/hip_runtime.h>
#include <hip/hip_bf16.h>
#include <stdint.h>

typedef short bf16x8 __attribute__((ext_vector_type(8)));
typedef short s16x4 __attribute__((ext_vector_type(4)));
typedef float f32x16 __attribute__((ext_vector_type(16)));
typedef float f32x4 __attribute__((ext_vector_type(4)));
typedef float f32x2 __attribute__((ext_vector_type(2)));
typedef unsigned u32x4 __attribute__((ext_vector_type(4)));
typedef unsigned u32x2 __attribute__((ext_vector_type(2)));
typedef __bf16 bf16x2_t __attribute__((ext_vector_type(2)));
#define LAS __attribute__((address_space(3)))
typedef LAS const char* lds_cptr;
#define DI __device__ __forceinline__
#define SBAR() __builtin_amdgcn_sched_barrier(0)
#define PIN(x) asm volatile("" : "+v"(x))
#define MFMA32(a, b, c) __builtin_amdgcn_mfma_f32_32x32x16_bf16(a, b, c, 0, 0, 0)
#define MFMA16(a, b, c) __builtin_amdgcn_mfma_f32_16x16x32_bf16(a, b, c, 0, 0, 0)
#define MFMA8(a, b, c) __builtin_amdgcn_mfma_f32_32x32x8bf16_1k(a, b, c, 0, 0, 0)

constexpr int S = 2048, DM = 640, NH = 32, DH = 20, FF = 1536;
constexpr int NT = S / 32;
constexpr int QTILE_B = 1280;
constexpr int KVTILE_B = 2688;
constexpr int V_OFF = 1280, V_STEP = 704, V_ROWS = 22;
constexpr int NSIG = 68;
constexpr size_t WS_Q = 0;
constexpr size_t WS_KV = WS_Q + (size_t)NH * NT * QTILE_B;
constexpr size_t WS_AF = WS_KV + (size_t)NH * NT * KVTILE_B;
constexpr size_t WS_BF = WS_AF + (size_t)128 * NSIG * 1024;
constexpr size_t WS_KMAX = WS_BF + (size_t)8 * NSIG * 5 * 1024;
constexpr size_t WS_GATE = WS_KMAX + (size_t)160 * 32 * 4;
constexpr size_t WS_CNT = WS_GATE + (size_t)S * 8;
constexpr size_t WS_END = WS_CNT + 8 * 256;
constexpr float QSCALE = 0.22360679774997896f * 1.4426950408889634f;
constexpr unsigned ONES2 = 0x3F803F80u;

DI unsigned cvtpk(float lo, float hi) { f32x2 v = {lo, hi}; bf16x2_t b = __builtin_convertvector(v, bf16x2_t); return __builtin_bit_cast(unsigned, b); }
DI float bf_lo(unsigned u) { return __uint_as_float(u << 16); }
DI float bf_hi(unsigned u) { return __uint_as_float(u & 0xffff0000u); }
DI float bf_round(float x) { return bf_lo(cvtpk(x, x)); }
DI void glds16(const void* sbase, unsigned voff, unsigned lds_base) {
    unsigned sv;
    asm volatile("s_mov_b32 %0, m0\n\ts_mov_b32 m0, %3\n\ts_nop 0\n\tglobal_load_lds_dwordx4 %1, %2\n\ts_mov_b32 m0, %0"
                 : "=&s"(sv) : "v"(voff), "s"(sbase), "s"(lds_base) : "memory");
}
typedef short v4i16_t __attribute__((ext_vector_type(4)));
DI s16x4 ldstr(lds_cptr p) { return __builtin_bit_cast(s16x4, __builtin_amdgcn_ds_read_tr16_b64_v4i16((LAS v4i16_t*)p)); }
DI bf16x8 lds128(lds_cptr p) { return *(const LAS bf16x8*)p; }
DI s16x4 lds64(lds_cptr p) { return *(const LAS s16x4*)p; }
DI unsigned rfl(unsigned x) { return __builtin_amdgcn_readfirstlane(x); }
#define STAMP(k, i) do { } while (0)

#define K1ROT ((ci + 3 * (slot & 3)) % 10)
constexpr int K1_STAGE = 49152;
constexpr int K1_RT = 3 * K1_STAGE;
constexpr int K1_LDS = K1_RT + 10240;

DI float rdl(float x, int l) { return __uint_as_float(__builtin_amdgcn_readlane(__float_as_uint(x), l)); }
#define WT 1
typedef __amdgpu_buffer_rsrc_t rsrc_t;
DI rsrc_t ws_rsrc(char* ws) { return __builtin_amdgcn_make_buffer_rsrc(ws, 0, (int)WS_END, 0x00020000); }
DI void st16(rsrc_t rs, unsigned off, u32x4 v) { __builtin_amdgcn_raw_buffer_store_b128(v, rs, off, 0, WT ? 16 : 0); }
DI void st8(rsrc_t rs, unsigned off, u32x2 v) { __builtin_amdgcn_raw_buffer_store_b64(v, rs, off, 0, WT ? 16 : 0); }
template <int CTRL> DI float dpp_max(float x) { const float y = __uint_as_float(__builtin_amdgcn_update_dpp(0u, __float_as_uint(x), CTRL, 0xf, 0xf, true)); return fmaxf(x, y); }
template <int CTRL> DI float dpp_add(float x) { const float y = __uint_as_float(__builtin_amdgcn_update_dpp(0u, __float_as_uint(x), CTRL, 0xf, 0xf, true)); return x + y; }

template <bool HT>
struct K1Dma {
    static constexpr int NW = HT ? 3 : 4;
    const char* xbase; const char* wbase; int ldw2, k0; unsigned lds0, xoff0, xoff1, woff[3];
    DI int kact(int kc) const { kc += k0; return kc >= 10 ? kc - 10 : kc; }
    DI void init(const char* xb, const char* wb, int ld, char* lds, int lane, int k0_) {
        xbase = xb; wbase = wb; ldw2 = ld; k0 = k0_; lds0 = (unsigned)(uintptr_t)lds;
        const int rr = lane >> 3;
        xoff0 = rr * 1280 + (((lane & 7) ^ ((lane >> 4) & 7)) << 4); xoff1 = rr * 1280 + (((lane & 7) ^ ((4 + (lane >> 4)) & 7)) << 4);
        if (HT) {
#pragma unroll
            for (int p = 0; p < 3; ++p) { const int o = 1024 * p + 16 * lane, row = (o * 21846) >> 22, col = o - row * 192; woff[p] = row * ldw2 + col; }
        } else woff[0] = (lane >> 4) * ldw2 + ((((lane & 15) ^ ((lane >> 4) << 2))) << 4);
    }
    DI void xp(int kc, int st, int j, int par) const { glds16(xbase + (size_t)j * 8 * 1280 + kc * 128, par ? xoff1 : xoff0, lds0 + st * K1_STAGE + j * 1024); }
    DI void wp(int kc, int st, int lw, int jj) const {
        if (HT) glds16(wbase + (size_t)(kc * 64 + 16 * lw) * ldw2, woff[jj], lds0 + st * K1_STAGE + 32768 + (3 * lw + jj) * 1024);
        else glds16(wbase + (size_t)(kc * 64 + 16 * lw + 4 * jj) * ldw2, woff[0], lds0 + st * K1_STAGE + 32768 + (4 * lw + jj) * 1024);
    }
    DI void first(int wid) const {
        const int kc = kact(0);
        if (wid >= 4) {
#pragma unroll
            for (int jj = 0; jj < NW; ++jj) wp(kc, 0, wid - 4, jj);
#pragma unroll
            for (int jj = 0; jj < 2; ++jj) xp(kc, 0, 2 * (wid - 4) + jj, jj & 1);
        } else {
#pragma unroll
            for (int jj = 0; jj < 6; ++jj) xp(kc, 0, 8 + 6 * wid + jj, jj & 1);
        }
    }
    DI void loader(int lw) const {
        auto issue = [&](int kc_, int st) {
            const int kc = kact(kc_);
#pragma unroll
            for (int jj = 0; jj < NW; ++jj) wp(kc, st, lw, jj);
#pragma unroll
            for (int jj = 0; jj < 8; ++jj) xp(kc, st, 8 * lw + jj, jj & 1);
        };
        issue(1, 1);
        if (HT) asm volatile("s_waitcnt vmcnt(11)\n\ts_barrier" ::: "memory"); else asm volatile("s_waitcnt vmcnt(12)\n\ts_barrier" ::: "memory");
        issue(2, 2);
        int st = 0;
        for (int kc = 0; kc < 9; ++kc) {
            if (kc == 8) asm volatile("s_waitcnt vmcnt(0)\n\ts_barrier" ::: "memory");
            else if (HT) asm volatile("s_waitcnt vmcnt(11)\n\ts_barrier" ::: "memory");
            else asm volatile("s_waitcnt vmcnt(12)\n\ts_barrier" ::: "memory");
            if (kc + 3 < 10) issue(kc + 3, st);
            st = st == 2 ? 0 : st + 1;
        }
    }
};

DI void rt_load(const unsigned short* w2, const unsigned short* wo, int u, unsigned ldsb, int piece, int lane) {
    const int sig = u >> 2, s = (u >> 1) & 1, cb = u & 1;
    const char* src = (sig < 48 ? (const char*)w2 + (size_t)sig * 32 * 1280 : (const char*)wo + (size_t)(sig - 48) * 32 * 1280) + s * 16 * 1280 + cb * 640;
    const int o = piece * 1024 + lane * 16, row = (o * 6554) >> 22;
    glds16(src, row * 1280 + (o - row * 640), ldsb + piece * 1024);
}
DI void rt_store(rsrc_t rs, int u, const char* ldsb, int i, int lane) {
    const int sig = u >> 2, s = (u >> 1) & 1, cb = u & 1;
    const int nl = 2 * i + (lane >> 5), hh = (lane >> 4) & 1, q = (lane & 15) >> 2, p = lane & 3;
    lds_cptr a = (lds_cptr)ldsb + (4 * hh + q) * 640 + (16 * nl + 4 * p) * 2;
    const s16x4 lo = ldstr(a), hi = ldstr(a + 8 * 640);
    const bf16x8 f = __builtin_shufflevector(lo, hi, 0, 1, 2, 3, 4, 5, 6, 7);
    const int nu = cb * 20 + nl, ng = (nu * 205) >> 10;
    st16(rs, (unsigned)WS_BF + (unsigned)((ng * NSIG + sig) * 5 + (nu - 5 * ng)) * 1024 + (s * 32 + (lane & 31)) * 16, __builtin_bit_cast(u32x4, f));
}
template <int TYPE, int MT, int NTL>
DI void k1_mainloop(f32x16 (&acc)[MT][NTL], char* lds, int wid, int lane, char* ws) {
    const int h = lane >> 5, r = lane & 31;
    constexpr bool HT = TYPE == 2;
    unsigned xa[4];
    { const int row = (HT ? wid * 64 : (wid >> 1) * 128) + r; const int sw = (row >> 1) & 7;
#pragma unroll
      for (int ks = 0; ks < 4; ++ks) xa[ks] = row * 128 + (((2 * ks + h) ^ sw) << 4); }
    unsigned wa[2];
    { const int blk = (lane >> 4) & 1, q = (lane & 15) >> 2, p = lane & 3;
      if (HT) wa[0] = wa[1] = 32768 + (8 * h + q) * 192 + blk * 32 + p * 8;
      else {
#pragma unroll
          for (int nt = 0; nt < 2; ++nt) { const int ch = (wid & 1) * 8 + nt * 4 + 2 * blk + (p >> 1); wa[nt] = 32768 + (8 * h + q) * 256 + ((ch ^ (q << 2)) << 4) + 8 * (p & 1); } } }
    bf16x8 xf[2][MT], wf[2][NTL];
    auto rdw = [&](int buf, int nt, lds_cptr sb, int ks) {
        lds_cptr a = HT ? sb + wa[0] + ks * 16 * 192 + nt * 64 : sb + wa[nt & 1] + ks * 4096;
        const s16x4 lo = ldstr(a), hi = ldstr(a + (HT ? 4 * 192 : 1024));
        wf[buf][nt] = __builtin_shufflevector(lo, hi, 0, 1, 2, 3, 4, 5, 6, 7);
    };
    auto rdx = [&](int buf, int mt, lds_cptr sb, int ks) { xf[buf][mt] = lds128(sb + xa[ks] + mt * 4096); };
    auto mm = [&](int buf, int mt, int nt) { acc[mt][nt] = TYPE == 1 ? MFMA32(xf[buf][mt], wf[buf][nt], acc[mt][nt]) : MFMA32(wf[buf][nt], xf[buf][mt], acc[mt][nt]); };
    asm volatile("s_waitcnt vmcnt(0)\n\ts_barrier" ::: "memory");
    { lds_cptr sb = (lds_cptr)lds; rdw(0, 0, sb, 0); rdx(0, 0, sb, 0); rdw(0, 1, sb, 0); rdx(0, 1, sb, 0);
      if (HT) rdw(0, 2, sb, 0); else { rdx(0, 2 % MT, sb, 0); rdx(0, 3 % MT, sb, 0); } }
    int st = 0;
#define K1_KSTEP(ks, NEXT) do { const int b = (ks) & 1, n = b ^ 1; lds_cptr np = (ks) < 3 ? sb : sn; const int nk = ((ks) + 1) & 3; \
        SBAR(); \
        if constexpr (HT) { \
            mm(b, 0, 0); SBAR(); if (NEXT) { rdw(n, 0, np, nk); } SBAR(); \
            mm(b, 0, 1); SBAR(); if (NEXT) { rdx(n, 0, np, nk); } SBAR(); \
            mm(b, 1, 0); SBAR(); if (NEXT) { rdw(n, 1, np, nk); } SBAR(); \
            mm(b, 1, 1); SBAR(); if (NEXT) { rdx(n, 1, np, nk); } SBAR(); \
            mm(b, 0, 2); SBAR(); if (NEXT) { rdw(n, 2, np, nk); } SBAR(); \
            mm(b, 1, 2); SBAR(); \
        } else { \
            mm(b, 0, 0); SBAR(); if (NEXT) { rdw(n, 0, np, nk); } SBAR(); \
            mm(b, 0, 1); SBAR(); if (NEXT) { rdx(n, 0, np, nk); } SBAR(); \
            mm(b, 1, 0); SBAR(); if (NEXT) { rdw(n, 1, np, nk); } SBAR(); \
            mm(b, 1, 1); SBAR(); if (NEXT) { rdx(n, 1, np, nk); } SBAR(); \
            mm(b, 2 % MT, 0); SBAR(); if (NEXT) { rdx(n, 2 % MT, np, nk); } SBAR(); \
            mm(b, 2 % MT, 1); SBAR(); if (NEXT) { rdx(n, 3 % MT, np, nk); } SBAR(); \
            mm(b, 3 % MT, 0); SBAR(); \
            mm(b, 3 % MT, 1); SBAR(); \
        } } while (0)
#pragma unroll 1
    for (int kc = 0; kc < 9; ++kc) {
        lds_cptr sb = (lds_cptr)lds + st * K1_STAGE;
        st = st == 2 ? 0 : st + 1;
        lds_cptr sn = (lds_cptr)lds + st * K1_STAGE;
        K1_KSTEP(0, true); K1_KSTEP(1, true); K1_KSTEP(2, true);
        asm volatile("s_waitcnt lgkmcnt(0)\n\ts_barrier" ::: "memory");
        K1_KSTEP(3, true);
    }
    { lds_cptr sb = (lds_cptr)lds + st * K1_STAGE; lds_cptr sn = sb;
      K1_KSTEP(0, true); K1_KSTEP(1, true); K1_KSTEP(2, true); K1_KSTEP(3, false); }
}

__global__ __launch_bounds__(512, 2) void k1_proj(const unsigned short* __restrict__ x, const unsigned short* __restrict__ wqkv, const unsigned short* __restrict__ wo,
                                                  const unsigned short* __restrict__ w1, const unsigned short* __restrict__ w2, char* __restrict__ ws) {
    extern __shared__ __attribute__((aligned(16))) char lds[];
    const int tid = threadIdx.x, lane = tid & 63, wid = rfl(tid >> 6);
    const int xcd = blockIdx.x & 7, slot = blockIdx.x >> 3;
    const int rgrp = xcd >> 2, cgrp = xcd & 3, nq = cgrp < 3 ? 4 : 3;
    const int h = lane >> 5, r = lane & 31;
    const rsrc_t rs = ws_rsrc(ws);
    const int rt = 4 * rgrp + (slot & 3), ci = slot >> 2;
    const char* xbase = (const char*)x + (size_t)rt * 256 * 1280;
    int ty = 0;
    STAMP(0, 0);
    if (ci < nq) {
        const int ct = 4 * cgrp + ci;
        const bool isV = ct >= 10;
        ty = isV ? 2 : 1;
        const int wm = (wid >> 1) & 1, wn = wid & 1;
        K1Dma<false> dma; dma.init(xbase, (const char*)wqkv + ct * 256, 3 * DM * 2, lds, lane, K1ROT);
        dma.first(wid);
        if (wid < 4) { for (int pc = wid; pc < 10; pc += 4) rt_load(w2, wo, blockIdx.x, (unsigned)(uintptr_t)lds + K1_RT, pc, lane); }
        if (wid >= 4) { dma.loader(wid - 4); for (int i = wid - 4; i < 10; i += 4) rt_store(rs, blockIdx.x, lds + K1_RT, i, lane); }
        else {
            f32x16 acc[4][2] = {};
            if (isV) {
                k1_mainloop<1, 4, 2>(acc, lds, wid, lane, ws);
                STAMP(0, 1);
#pragma unroll
                for (int nt = 0; nt < 2; ++nt) {
                    const int phi = (ct - 10) * 128 + wn * 64 + nt * 32 + r;
                    const int head = (phi * 3277) >> 16, d = phi - head * 20;
                    const unsigned dst = (unsigned)WS_KV + (unsigned)(head * NT + rt * 8 + wm * 4) * KVTILE_B + V_OFF + (h * V_ROWS + d) * 16;
#pragma unroll
                    for (int mt = 0; mt < 4; ++mt)
#pragma unroll
                        for (int s = 0; s < 2; ++s) {
                            const f32x16& a = acc[mt][nt];
                            const u32x4 w = {cvtpk(a[8 * s], a[8 * s + 1]), cvtpk(a[8 * s + 2], a[8 * s + 3]), cvtpk(a[8 * s + 4], a[8 * s + 5]), cvtpk(a[8 * s + 6], a[8 * s + 7])};
                            st16(rs, dst + mt * KVTILE_B + s * V_STEP, w);
                        }
                }
            } else {
                k1_mainloop<0, 4, 2>(acc, lds, wid, lane, ws);
                STAMP(0, 1);
                char* sb = lds + K1_STAGE + (wm * 4 * 32 + wn * 16 + h) * 256 + r * 8;
                if (ct < 5) {
#pragma unroll
                    for (int nt = 0; nt < 2; ++nt)
#pragma unroll
                        for (int g = 0; g < 4; ++g)
#pragma unroll
                            for (int mt = 0; mt < 4; ++mt) {
                                const f32x16& a = acc[mt][nt];
                                *(u32x2*)(sb + (mt * 32 + nt * 8 + 2 * g) * 256) = (u32x2){cvtpk(a[4 * g] * QSCALE, a[4 * g + 1] * QSCALE), cvtpk(a[4 * g + 2] * QSCALE, a[4 * g + 3] * QSCALE)};
                            }
                } else {
                    float nrm[8];
#pragma unroll
                    for (int nt = 0; nt < 2; ++nt)
#pragma unroll
                        for (int g = 0; g < 4; ++g) {
                            float m = 0.f;
#pragma unroll
                            for (int mt = 0; mt < 4; ++mt) {
                                const f32x16& a = acc[mt][nt];
                                *(u32x2*)(sb + (mt * 32 + nt * 8 + 2 * g) * 256) = (u32x2){cvtpk(a[4 * g], a[4 * g + 1]), cvtpk(a[4 * g + 2], a[4 * g + 3])};
                                m = fmaxf(m, __builtin_fmaf(a[4 * g], a[4 * g], a[4 * g + 1] * a[4 * g + 1]) + __builtin_fmaf(a[4 * g + 2], a[4 * g + 2], a[4 * g + 3] * a[4 * g + 3]));
                            }
                            nrm[nt * 4 + g] = m;
                        }
#pragma unroll
                    for (int i = 0; i < 8; ++i) { float m = nrm[i]; m = dpp_max<0xB1>(m); m = dpp_max<0x4E>(m); m = dpp_max<0x141>(m); m = dpp_max<0x140>(m);
                        auto rr = __builtin_amdgcn_permlane16_swap(__float_as_uint(m), __float_as_uint(m), false, false); nrm[i] = fmaxf(__uint_as_float(rr[0]), __uint_as_float(rr[1])); }
                    float mine = nrm[0];
#pragma unroll
                    for (int i = 1; i < 8; ++i) mine = r == i ? nrm[i] : mine;
                    if (r < 8) ((float*)(ws + WS_KMAX))[((ct - 5) * 32 + wn * 16 + (r >> 2) * 8 + 2 * (r & 3) + h) * 16 + rt * 2 + wm] = mine;
                }
            }
        }
        if (!isV) {
            __syncthreads();
            const bool isQ = ct < 5;
            const int ctl = isQ ? ct : ct - 5;
            const unsigned img = (unsigned)(isQ ? WS_Q : WS_KV);
            const unsigned tileb = isQ ? QTILE_B : KVTILE_B;
            const int T = rt * 8 + wid, G0 = ctl * 32;
            const int hd0 = (G0 * 205) >> 10, id0 = 3 * hd0 + ((G0 - 5 * hd0) >> 1);
            const char* lb = lds + K1_STAGE + wid * 8192;
            u32x2 pa[10], pb[10];
#pragma unroll
            for (int it = 0; it < 10; ++it) {
                const int id = id0 + 2 * it + h, head = (id * 171) >> 9, ty_ = id - 3 * head, la = 5 * head + 2 * ty_ - G0;
                const int lc = la < 0 ? 0 : (la > 31 ? 31 : la);
                const int aA = ty_ < 2 ? lc * 256 + r * 8 : lc * 256 + (r & 15) * 16;
                const int aB = ty_ < 2 ? (la > 30 ? 31 : la + 1) * 256 + r * 8 : aA + 8;
                pa[it] = *(const u32x2*)(lb + aA); pb[it] = *(const u32x2*)(lb + aB);
            }
#pragma unroll
            for (int it = 0; it < 10; ++it) {
                const int id = id0 + 2 * it + h, head = (id * 171) >> 9, ty_ = id - 3 * head, la = 5 * head + 2 * ty_ - G0;
                const unsigned dst = img + (unsigned)(head * NT + T) * tileb + ty_ * 512 + r * 16;
                const bool okA = (unsigned)la < 32u, okB = ty_ == 2 ? okA : (unsigned)(la + 1) < 32u;
                if (okA && okB) { if (ty_ < 2 || r < 16) st16(rs, dst, (u32x4){pa[it][0], pa[it][1], pb[it][0], pb[it][1]}); }
                else if (okA) st8(rs, dst, pa[it]);
                else if (okB) st8(rs, dst + 8, pb[it]);
            }
        }
    } else if (ci < nq + 4) {
        const int ch = 4 * cgrp + (ci - nq);
        ty = 3;
        K1Dma<true> dma; dma.init(xbase, (const char*)w1 + ch * 192, FF * 2, lds, lane, K1ROT);
        dma.first(wid);
        if (wid < 4) { for (int pc = wid; pc < 10; pc += 4) rt_load(w2, wo, blockIdx.x, (unsigned)(uintptr_t)lds + K1_RT, pc, lane); }
        if (wid >= 4) { dma.loader(wid - 4); for (int i = wid - 4; i < 10; i += 4) rt_store(rs, blockIdx.x, lds + K1_RT, i, lane); }
        else {
            f32x16 acc[2][3] = {};
            k1_mainloop<2, 2, 3>(acc, lds, wid, lane, ws);
            STAMP(0, 1);
#pragma unroll
            for (int mt = 0; mt < 2; ++mt) {
                const int tok = rt * 256 + wid * 64 + mt * 32 + r, rho = tok >> 4;
#pragma unroll
                for (int nt = 0; nt < 3; ++nt) {
                    const int sig = ch * 3 + nt;
                    const unsigned dst = (unsigned)WS_AF + (unsigned)(rho * NSIG + sig) * 1024 + (h * 16 + (r & 15)) * 16;
                    float sv[16];
#pragma unroll
                    for (int i = 0; i < 16; ++i) { const float v = acc[mt][nt][i]; sv[i] = v * __builtin_amdgcn_rcpf(1.f + __builtin_amdgcn_exp2f(v * -1.4426950408889634f)); }
#pragma unroll
                    for (int s = 0; s < 2; ++s) {
                        const u32x4 w = {cvtpk(sv[8 * s], sv[8 * s + 1]), cvtpk(sv[8 * s + 2], sv[8 * s + 3]), cvtpk(sv[8 * s + 4], sv[8 * s + 5]), cvtpk(sv[8 * s + 6], sv[8 * s + 7])};
                        st16(rs, dst + s * 512, w);
                    }
                }
            }
        }
    } else {
        const int aidx = rgrp * 4 + (slot & 3);
        const unsigned lds0 = (unsigned)(uintptr_t)lds;
        const int un[3] = {(int)blockIdx.x, 256 + 2 * aidx, 257 + 2 * aidx};
#pragma unroll
        for (int k = 0; k < 3; ++k) for (int pc = wid; pc < 10; pc += 8) rt_load(w2, wo, un[k], lds0 + k * 10240, pc, lane);
#pragma unroll
        for (int k = 0; k < 4; ++k) { const int i = (aidx * 4 + k) * 512 + tid;
          const unsigned f = (i & 1) ? 0u : ONES2; st16(rs, (unsigned)WS_KV + (unsigned)(i >> 3) * KVTILE_B + V_OFF + ((i >> 2) & 1) * V_STEP + (((i >> 1) & 1) * V_ROWS + 20 + (i & 1)) * 16, (u32x4){f, f, f, f}); }
        asm volatile("s_waitcnt vmcnt(0)\n\ts_barrier" ::: "memory");
#pragma unroll
        for (int k = 0; k < 3; ++k) for (int i = wid; i < 10; i += 8) rt_store(rs, un[k], lds + k * 10240, i, lane);
    }
}

constexpr int K2_CHUNK = 8 * KVTILE_B;

DI float swap32_add(float x) { auto rr = __builtin_amdgcn_permlane32_swap(__float_as_uint(x), __float_as_uint(x), false, false); return __uint_as_float(rr[0]) + __uint_as_float(rr[1]); }
DI float swap32_max(float x) { auto rr = __builtin_amdgcn_permlane32_swap(__float_as_uint(x), __float_as_uint(x), false, false); return fmaxf(__uint_as_float(rr[0]), __uint_as_float(rr[1])); }
DI s16x4 make_qb(unsigned q0, unsigned q1, float M, int h) {
    const float mh = bf_hi(__float_as_uint(M)), r1 = M - mh, mm = bf_hi(__float_as_uint(r1)), r2 = r1 - mm, ml = bf_hi(__float_as_uint(r2));
    u32x2 w;
    w[0] = h ? ((__float_as_uint(mh) >> 16) | (__float_as_uint(mm) & 0xffff0000u)) ^ 0x80008000u : q0;
    w[1] = h ? (__float_as_uint(ml) >> 16) ^ 0x8000u : q1;
    return __builtin_bit_cast(s16x4, w);
}

DI void attn_fallback(f32x16& o, const char* kv, bf16x8 qa, unsigned q0, unsigned q1, int lane, int kboff, int voff) {
    const int h = lane >> 5;
    const f32x16 zero = {};
    float m = -INFINITY;
    const s16x4 qb0 = make_qb(q0, q1, 0.f, h);
    for (int t = 0; t < NT; ++t) {
        const char* tb = kv + t * KVTILE_B;
        const bf16x8 ka = *(const bf16x8*)(tb + lane * 16); const s16x4 kb = *(const s16x4*)(tb + kboff);
        f32x16 s = MFMA32(ka, qa, zero); s = MFMA8(kb, qb0, s);
#pragma unroll
        for (int i = 0; i < 16; ++i) m = fmaxf(m, s[i]);
    }
    m = swap32_max(m);
    const s16x4 qb = make_qb(q0, q1, m, h);
    o = zero;
    for (int t = 0; t < NT; ++t) {
        const char* tb = kv + t * KVTILE_B;
        const bf16x8 ka = *(const bf16x8*)(tb + lane * 16); const s16x4 kb = *(const s16x4*)(tb + kboff);
        const bf16x8 va = *(const bf16x8*)(tb + voff), vb = *(const bf16x8*)(tb + voff + V_STEP);
        f32x16 s = MFMA32(ka, qa, zero); s = MFMA8(kb, qb, s);
        u32x4 p0, p1;
#pragma unroll
        for (int i = 0; i < 4; ++i) { p0[i] = cvtpk(__builtin_amdgcn_exp2f(s[2 * i]), __builtin_amdgcn_exp2f(s[2 * i + 1])); p1[i] = cvtpk(__builtin_amdgcn_exp2f(s[8 + 2 * i]), __builtin_amdgcn_exp2f(s[9 + 2 * i])); }
        o = MFMA32(va, __builtin_bit_cast(bf16x8, p0), o);
        o = MFMA32(vb, __builtin_bit_cast(bf16x8, p1), o);
    }
}

constexpr int K2_LDS = 3 * K2_CHUNK + 1024;
__global__ __launch_bounds__(576, 3) void k2_attn(char* __restrict__ ws, const unsigned short* __restrict__ x, const unsigned short* __restrict__ gw) {
    extern __shared__ __attribute__((aligned(16))) char lds[];
    const int bid = blockIdx.x;
    const int tid = threadIdx.x, lane = tid & 63, wid = rfl(tid >> 6);
    const int h = lane >> 5, r = lane & 31;
    const int xcd = bid & 7, slot = bid >> 3;
    const int head = xcd * 4 + (slot & 3), qblk = slot >> 2;
    STAMP(1, 0);
    const char* kv = ws + WS_KV + (size_t)head * NT * KVTILE_B;
    const unsigned lds0 = (unsigned)(uintptr_t)lds;
    const unsigned lane16 = lane * 16;
    if (wid == 8) {
        const char* pfa; {
            const int u0 = slot * 96, ng0 = (xcd & 1) * 4, bt0 = (xcd >> 1) * 8;
            if (u0 < 1536) { const int rho = u0 / 48; pfa = ws + WS_AF + (size_t)(bt0 * 4 + rho) * NSIG * 1024; }
            else { int v = u0 - 1536; if (v > 1360 - 96) v = 1360 - 96; pfa = ws + WS_BF + ((size_t)ng0 * NSIG * 5 + v) * 1024; }
        }
        const int pstep = slot < 16 ? (NSIG - 48) * 1024 : 0;
        auto chunk = [&](int c, int st) {
#pragma unroll
            for (int j = 0; j < 21; ++j) glds16(kv + (size_t)c * K2_CHUNK + j * 1024, lane16, lds0 + st * K2_CHUNK + j * 1024);
        };
        chunk(1, 1);
        asm volatile("s_barrier" ::: "memory");
        int st = 2;
        for (int c = 0; c < 8; ++c) {
            if (c == 0 || c == 7) asm volatile("s_waitcnt vmcnt(0)\n\ts_barrier" ::: "memory"); else asm volatile("s_waitcnt vmcnt(16)\n\ts_barrier" ::: "memory");
            if (c < 6) {
                chunk(c + 2, st); st = st == 2 ? 0 : st + 1;
#pragma unroll
                for (int j = 0; j < 16; ++j) { const int u = c * 16 + j; glds16(pfa + (size_t)u * 1024 + (u >= 48 ? pstep : 0), lane16, lds0 + 3 * K2_CHUNK); }
            }
        }
        return;
    }
    const char* kvw = kv + wid * 1024;
    const unsigned ldw = lds0 + wid * 1024;
    const bool third = wid < 5;
    const int Tq = qblk * 8 + wid;
    const char* qt = ws + WS_Q + ((size_t)head * NT + Tq) * QTILE_B;
    const bf16x8 qa = *(const bf16x8*)(qt + lane * 16);
    const u32x2 qbr = *(const u32x2*)(qt + 1024 + r * 8);
    f32x4 km4 = *(const f32x4*)(ws + WS_KMAX + (size_t)head * 320 + (lane < 20 ? lane : 0) * 16);
    glds16(kvw, lane16, ldw); glds16(kvw + 8192, lane16, ldw + 8192); if (third) glds16(kvw + 16384, lane16, ldw + 16384);
    {
        const unsigned* xr = (const unsigned*)(x + (size_t)(bid * 8 + wid) * DM) + lane; const unsigned* gr = (const unsigned*)gw + lane;
        float sm = 0.f;
#pragma unroll
        for (int i = 0; i < 5; ++i) { const unsigned a_ = xr[64 * i], b_ = gr[64 * i]; sm = __builtin_amdgcn_fdot2_f32_bf16(__builtin_bit_cast(bf16x2_t, a_), __builtin_bit_cast(bf16x2_t, b_), sm, false); }
        sm = dpp_add<0xB1>(sm); sm = dpp_add<0x4E>(sm); sm = dpp_add<0x141>(sm); sm = dpp_add<0x140>(sm);
        sm = (rdl(sm, 0) + rdl(sm, 16)) + (rdl(sm, 32) + rdl(sm, 48));
        const float lg = bf_round(sm);
        const float g = bf_round(__builtin_amdgcn_rcpf(1.f + __builtin_amdgcn_exp2f(lg * -1.4426950408889634f)));
        if (lane == 0) *(f32x2*)(ws + WS_GATE + (bid * 8 + wid) * 8) = (f32x2){g, bf_round(1.f - g)};
    }
    float km = fmaxf(fmaxf(km4[0], km4[1]), fmaxf(km4[2], km4[3]));
    km = dpp_max<0xB1>(km); km = dpp_max<0x4E>(km);
    km = __builtin_sqrtf(km);
    float M;
    { const float k0 = rdl(km, 0), k1 = rdl(km, 4), k2 = rdl(km, 8), k3 = rdl(km, 12), k4 = rdl(km, 16);
      const u32x4 qw = __builtin_bit_cast(u32x4, qa);
      float n0, n1, n2;
      { const float a = bf_lo(qw[0]), b = bf_hi(qw[0]), c = bf_lo(qw[1]), d = bf_hi(qw[1]); n0 = (a * a + b * b) + (c * c + d * d); }
      { const float a = bf_lo(qw[2]), b = bf_hi(qw[2]), c = bf_lo(qw[3]), d = bf_hi(qw[3]); n1 = (a * a + b * b) + (c * c + d * d); }
      { const float a = bf_lo(qbr[0]), b = bf_hi(qbr[0]), c = bf_lo(qbr[1]), d = bf_hi(qbr[1]); n2 = (a * a + b * b) + (c * c + d * d); }
      float part = __builtin_amdgcn_sqrtf(n0) * (h ? k2 : k0) + __builtin_amdgcn_sqrtf(n1) * (h ? k3 : k1);
      if (!h) part += __builtin_amdgcn_sqrtf(n2) * k4;
      M = swap32_add(part) * 1.01f; }
    const s16x4 qb = make_qb(qbr[0], qbr[1], M, h);
    const int vrow = r <= 20 ? r : (r < 28 ? 21 : r - 12);
    const int voff = V_OFF + (h * V_ROWS + vrow) * 16, kboff = h ? V_OFF + 20 * 16 : 1024 + r * 8;
    unsigned kaC = lane * 16, kbC = kboff, vC = voff;
    unsigned kaN = kaC + K2_CHUNK, kbN = kbC + K2_CHUNK, vN = vC + K2_CHUNK;
    const lds_cptr L = (lds_cptr)lds;
    const f32x16 zero = {};
    f32x16 o = {}, sA, sB;
    bf16x8 ka, va, vb; s16x4 kb;
    u32x4 plo, phi;
    asm volatile("s_waitcnt vmcnt(0)\n\ts_barrier" ::: "memory");
    STAMP(1, 1);
    ka = lds128(L + kaC); kb = lds64(L + kbC);
    sA = MFMA32(ka, qa, zero); sA = MFMA8(kb, qb, sA);
    ka = lds128(L + kaC + KVTILE_B); kb = lds64(L + kbC + KVTILE_B);
    va = lds128(L + vC);
    vb = (bf16x8){}; phi = (u32x4){};
    f32x4 e3 = {};
    int stn = 1;

#define EXP4(X, i) do { X[i] = __builtin_amdgcn_exp2f(X[i]); X[i + 1] = __builtin_amdgcn_exp2f(X[i + 1]); X[i + 2] = __builtin_amdgcn_exp2f(X[i + 2]); X[i + 3] = __builtin_amdgcn_exp2f(X[i + 3]); } while (0)
#define KADDR(A, i) (((i) < 8) ? (A##C + (i) * KVTILE_B) : (A##N + ((i) - 8) * KVTILE_B))
#define STEP(C, N, i, DMA) do { \
        bf16x8 ka2, va2, vb2; s16x4 kb2; \
        N = MFMA32(ka, qa, zero); SBAR(); \
        ka2 = lds128(L + KADDR(ka, (i) + 2)); \
        phi[2] = cvtpk(e3[0], e3[1]); phi[3] = cvtpk(e3[2], e3[3]); PIN(phi); SBAR(); EXP4(C, 0); PIN(C); SBAR(); \
        o = MFMA32(vb, __builtin_bit_cast(bf16x8, phi), o); SBAR(); \
        vb2 = lds128(L + vC + (i) * KVTILE_B + V_STEP); \
        plo[0] = cvtpk(C[0], C[1]); plo[1] = cvtpk(C[2], C[3]); PIN(plo); SBAR(); EXP4(C, 4); PIN(C); SBAR(); \
        N = MFMA8(kb, qb, N); SBAR(); \
        DMA; \
        kb2 = lds64(L + KADDR(kb, (i) + 2)); \
        plo[2] = cvtpk(C[4], C[5]); plo[3] = cvtpk(C[6], C[7]); PIN(plo); SBAR(); EXP4(C, 8); PIN(C); SBAR(); \
        o = MFMA32(va, __builtin_bit_cast(bf16x8, plo), o); SBAR(); \
        va2 = lds128(L + KADDR(v, (i) + 1)); \
        phi[0] = cvtpk(C[8], C[9]); phi[1] = cvtpk(C[10], C[11]); PIN(phi); SBAR(); \
        e3[0] = __builtin_amdgcn_exp2f(C[12]); e3[1] = __builtin_amdgcn_exp2f(C[13]); e3[2] = __builtin_amdgcn_exp2f(C[14]); e3[3] = __builtin_amdgcn_exp2f(C[15]); PIN(e3); SBAR(); \
        ka = ka2; kb = kb2; va = va2; vb = vb2; \
    } while (0)
    for (int c = 0; c < 8; ++c) {
        STEP(sA, sB, 0, ); STEP(sB, sA, 1, ); STEP(sA, sB, 2, ); STEP(sB, sA, 3, ); STEP(sA, sB, 4, );
        asm volatile("s_barrier" ::: "memory");
        STEP(sB, sA, 5, ); STEP(sA, sB, 6, ); STEP(sB, sA, 7, );
        kaC = kaN; kbC = kbN; vC = vN;
        const int d = (stn == 2) ? -2 * K2_CHUNK : K2_CHUNK;
        kaN += d; kbN += d; vN += d;
        stn = (stn == 2) ? 0 : stn + 1;
    }
    phi[2] = cvtpk(e3[0], e3[1]); phi[3] = cvtpk(e3[2], e3[3]);
    o = MFMA32(vb, __builtin_bit_cast(bf16x8, phi), o);
    STAMP(1, 2);
    float l; { auto rr = __builtin_amdgcn_permlane32_swap(__float_as_uint(o[8]), __float_as_uint(o[8]), false, false); l = __uint_as_float(rr[1]); }
    if (__builtin_expect(__any(!(l >= 8.6736174e-19f && l <= 1.0e30f)), 0)) {
        attn_fallback(o, kv, qa, qbr[0], qbr[1], lane, kboff, voff);
        auto rr = __builtin_amdgcn_permlane32_swap(__float_as_uint(o[8]), __float_as_uint(o[8]), false, false); l = __uint_as_float(rr[1]);
    }
    const float f = __builtin_amdgcn_rcpf(l);
    const int tok = Tq * 32 + r;
    char* ab = ws + WS_AF + (size_t)(tok >> 4) * NSIG * 1024 + (tok & 15) * 16;
#pragma unroll
    for (int g = 0; g < 3; ++g) {
        const int gam = head * 5 + 2 * g + h;
        const int sig = 48 + (gam >> 3), c = ((gam >> 2) & 1) * 2 + (gam & 1), half = (gam >> 1) & 1;
        const u32x2 w = {cvtpk(o[4 * g] * f, o[4 * g + 1] * f), cvtpk(o[4 * g + 2] * f, o[4 * g + 3] * f)};
        if (g < 2 || h == 0) *(u32x2*)(ab + sig * 1024 + c * 256 + half * 8) = w;
    }
    STAMP(1, 3);
}

constexpr int K3_LDS = 8 * 20 * 1024;
DI void gld128(bf16x8& d, const char* sbase, unsigned voff) { asm volatile("global_load_dwordx4 %0, %1, %2" : "=v"(d) : "v"(voff), "s"(sbase) : "memory"); }
template <int N> DI void wait9(bf16x8 (&a)[4], bf16x8 (&b)[5]) {
    asm volatile("s_waitcnt vmcnt(%9)" : "+v"(a[0]), "+v"(a[1]), "+v"(a[2]), "+v"(a[3]), "+v"(b[0]), "+v"(b[1]), "+v"(b[2]), "+v"(b[3]), "+v"(b[4]) : "i"(N) : "memory");
}
template <int NS, int I> struct K3Step {
    template <class L> static DI void run(f32x4 (&acc)[4][5], bf16x8 (&a)[4][4], bf16x8 (&b)[4][5], L& load) {
        constexpr int left = NS - 1 - I, younger = (left < 3 ? left : 3) * 9;
        wait9<younger>(a[I & 3], b[I & 3]);
        SBAR();
#pragma unroll
        for (int nt = 0; nt < 5; ++nt)
#pragma unroll
            for (int mt = 0; mt < 4; ++mt) acc[mt][nt] = MFMA16(b[I & 3][nt], a[I & 3][mt], acc[mt][nt]);
        SBAR();
        if constexpr (I + 4 < NS) load(I + 4, a[I & 3], b[I & 3]);
        if constexpr (I + 1 < NS) K3Step<NS, I + 1>::run(acc, a, b, load);
    }
};
template <int NS>
DI void k3_stream(f32x4 (&acc)[4][5], const char* ap, const char* bp, unsigned lane16, int rot) {
    bf16x8 a[4][4], b[4][5];
    auto load = [&](int i, bf16x8 (&da)[4], bf16x8 (&db)[5]) {
        int st = i + rot; if (st >= NS) st -= NS;
#pragma unroll
        for (int nt = 0; nt < 5; ++nt) gld128(db[nt], bp + st * 5120 + nt * 1024, lane16);
#pragma unroll
        for (int mt = 0; mt < 4; ++mt) gld128(da[mt], ap + (size_t)mt * NSIG * 1024 + st * 1024, lane16);
    };
#pragma unroll
    for (int i = 0; i < 4; ++i) load(i, a[i], b[i]);
    K3Step<NS, 0>::run(acc, a, b, load);
}
__global__ __launch_bounds__(512, 2) void k3_out(const char* __restrict__ ws, unsigned short* __restrict__ out) {
    extern __shared__ __attribute__((aligned(16))) char lds[];
    const int tid = threadIdx.x, lane = tid & 63, wid = rfl(tid >> 6);
    const int xcd = blockIdx.x & 7, slot = blockIdx.x >> 3;
    const int ng = (xcd & 1) * 4 + (slot & 3), bt = (xcd >> 1) * 8 + (slot >> 2);
    STAMP(2, 0);
    const int s0 = wid < 6 ? wid * 8 : 48 + (wid - 6) * 10;
    const char* ap = ws + WS_AF + ((size_t)bt * 4 * NSIG + s0) * 1024;
    const char* bp = ws + WS_BF + ((size_t)ng * NSIG + s0) * 5 * 1024;
    const unsigned lane16 = lane * 16;
    float fac[4];
#pragma unroll
    for (int mt = 0; mt < 4; ++mt) asm volatile("global_load_dword %0, %1, %2" : "=v"(fac[mt]) : "v"((unsigned)((bt * 64 + mt * 16 + (lane & 15)) * 8 + (wid < 6 ? 4 : 0))), "s"(ws + WS_GATE) : "memory");
    f32x4 acc[4][5] = {};
    const int rr = (slot >> 2) + 3 * (slot & 3);
    if (wid < 6) k3_stream<8>(acc, ap, bp, lane16, rr & 7); else k3_stream<10>(acc, ap, bp, lane16, rr % 10);
    asm volatile("" : "+v"(fac[0]), "+v"(fac[1]), "+v"(fac[2]), "+v"(fac[3]));
    STAMP(2, 1);
#pragma unroll
    for (int mt = 0; mt < 4; ++mt)
#pragma unroll
        for (int nt = 0; nt < 5; ++nt) *(f32x4*)(lds + (wid * 20 + mt * 5 + nt) * 1024 + lane16) = acc[mt][nt] * fac[mt];
    __syncthreads();
#pragma unroll
    for (int i = 0; i < 3; ++i) { const int t = wid + 8 * i;
        if (t < 20) {
            f32x4 sum = *(const f32x4*)(lds + t * 1024 + lane16);
#pragma unroll
            for (int w = 1; w < 8; ++w) sum += *(const f32x4*)(lds + (w * 20 + t) * 1024 + lane16);
            const int m4 = t / 5, nt = t - m4 * 5;
            const int tok = bt * 64 + m4 * 16 + (lane & 15);
            *(u32x2*)(out + (size_t)tok * DM + ng * 80 + nt * 16 + (lane >> 4) * 4) = (u32x2){cvtpk(sum[0], sum[1]), cvtpk(sum[2], sum[3])};
        } }
    STAMP(2, 2);
}

extern "C" void kernel_launch(void* const* d_in, const int* in_sizes, int n_in, void* d_out, int out_size, void* d_ws, size_t ws_size, hipStream_t stream) {
    const unsigned short* x = (const unsigned short*)d_in[0];
    const unsigned short* wqkv = (const unsigned short*)d_in[1];
    const unsigned short* wo = (const unsigned short*)d_in[2];
    const unsigned short* w1 = (const unsigned short*)d_in[4];
    const unsigned short* w2 = (const unsigned short*)d_in[5];
    const unsigned short* gw = (const unsigned short*)d_in[6];
    char* ws = (char*)d_ws + ((ws_size - WS_END) & ~(size_t)4095);
    (void)hipFuncSetAttribute((const void*)k1_proj, hipFuncAttributeMaxDynamicSharedMemorySize, K1_LDS);
    (void)hipFuncSetAttribute((const void*)k2_attn, hipFuncAttributeMaxDynamicSharedMemorySize, K2_LDS);
    (void)hipFuncSetAttribute((const void*)k3_out, hipFuncAttributeMaxDynamicSharedMemorySize, K3_LDS);
    k1_proj<<<256, 512, K1_LDS, stream>>>(x, wqkv, wo, w1, w2, ws);
    k2_attn<<<256, 576, K2_LDS, stream>>>(ws, x, gw);
    k3_out<<<256, 512, K3_LDS, stream>>>(ws, (unsigned short*)d_out);
}
```

```cpp
#include <hip/hip_runtime.h>
#include <hip/hip_bf16.h>
#include <stdint.h>

typedef short bf16x8 __attribute__((ext_vector_type(8)));
typedef short s16x4 __attribute__((ext_vector_type(4)));
typedef float f32x16 __attribute__((ext_vector_type(16)));
typedef float f32x4 __attribute__((ext_vector_type(4)));
typedef float f32x2 __attribute__((ext_vector_type(2)));
typedef unsigned u32x4 __attribute__((ext_vector_type(4)));
typedef unsigned u32x2 __attribute__((ext_vector_type(2)));
typedef __bf16 bf16x2_t __attribute__((ext_vector_type(2)));
#define LAS __attribute__((address_space(3)))
typedef LAS const char* lds_cptr;
#define DI __device__ __forceinline__
#define SBAR() __builtin_amdgcn_sched_barrier(0)
#define PIN(x) asm volatile("" : "+v"(x))
#define MFMA32(a, b, c) __builtin_amdgcn_mfma_f32_32x32x16_bf16(a, b, c, 0, 0, 0)
#define MFMA16(a, b, c) __builtin_amdgcn_mfma_f32_16x16x32_bf16(a, b, c, 0, 0, 0)
#define MFMA8(a, b, c) __builtin_amdgcn_mfma_f32_32x32x8bf16_1k(a, b, c, 0, 0, 0)

constexpr int S = 2048, DM = 640, NH = 32, DH = 20, FF = 1536;
constexpr int NT = S / 32;
constexpr int QTILE_B = 1280;
constexpr int KVTILE_B = 2688;
constexpr int V_OFF = 1280, V_STEP = 704, V_ROWS = 22;
constexpr int NSIG = 68;
constexpr size_t WS_Q = 0;
constexpr size_t WS_KV = WS_Q + (size_t)NH * NT * QTILE_B;
constexpr size_t WS_AF = WS_KV + (size_t)NH * NT * KVTILE_B;
constexpr size_t WS_BF = WS_AF + (size_t)128 * NSIG * 1024;
constexpr size_t WS_KMAX = WS_BF + (size_t)8 * NSIG * 5 * 1024;
constexpr size_t WS_GATE = WS_KMAX + (size_t)160 * 32 * 4;
constexpr size_t WS_CNT = WS_GATE + (size_t)S * 8;
constexpr size_t WS_END = WS_CNT + 8 * 256;
constexpr float QSCALE = 0.22360679774997896f * 1.4426950408889634f;
constexpr unsigned ONES2 = 0x3F803F80u;

DI unsigned cvtpk(float lo, float hi) { f32x2 v = {lo, hi}; bf16x2_t b = __builtin_convertvector(v, bf16x2_t); return __builtin_bit_cast(unsigned, b); }
DI float bf_lo(unsigned u) { return __uint_as_float(u << 16); }
DI float bf_hi(unsigned u) { return __uint_as_float(u & 0xffff0000u); }
DI float bf_round(float x) { return bf_lo(cvtpk(x, x)); }
DI void glds16(const void* sbase, unsigned voff, unsigned lds_base) {
    unsigned sv;
    asm volatile("s_mov_b32 %0, m0\n\ts_mov_b32 m0, %3\n\ts_nop 0\n\tglobal_load_lds_dwordx4 %1, %2\n\ts_mov_b32 m0, %0"
                 : "=&s"(sv) : "v"(voff), "s"(sbase), "s"(lds_base) : "memory");
}
typedef short v4i16_t __attribute__((ext_vector_type(4)));
DI s16x4 ldstr(lds_cptr p) { return __builtin_bit_cast(s16x4, __builtin_amdgcn_ds_read_tr16_b64_v4i16((LAS v4i16_t*)p)); }
DI bf16x8 lds128(lds_cptr p) { return *(const LAS bf16x8*)p; }
DI s16x4 lds64(lds_cptr p) { return *(const LAS s16x4*)p; }
DI unsigned rfl(unsigned x) { return __builtin_amdgcn_readfirstlane(x); }
#define STAMP(k, i) do { } while (0)

#define K1ROT ((ci + 3 * (slot & 3)) % 10)
constexpr int K1_STAGE = 49152;
constexpr int K1_RT = 3 * K1_STAGE;
constexpr int K1_LDS = K1_RT + 10240;

DI float rdl(float x, int l) { return __uint_as_float(__builtin_amdgcn_readlane(__float_as_uint(x), l)); }
#define WT 1
typedef __amdgpu_buffer_rsrc_t rsrc_t;
DI rsrc_t ws_rsrc(char* ws) { return __builtin_amdgcn_make_buffer_rsrc(ws, 0, (int)WS_END, 0x00020000); }
DI void st16(rsrc_t rs, unsigned off, u32x4 v) { __builtin_amdgcn_raw_buffer_store_b128(v, rs, off, 0, WT ? 16 : 0); }
DI void st8(rsrc_t rs, unsigned off, u32x2 v) { __builtin_amdgcn_raw_buffer_store_b64(v, rs, off, 0, WT ? 16 : 0); }
template <int CTRL> DI float dpp_max(float x) { const float y = __uint_as_float(__builtin_amdgcn_update_dpp(0u, __float_as_uint(x), CTRL, 0xf, 0xf, true)); return fmaxf(x, y); }
template <int CTRL> DI float dpp_add(float x) { const float y = __uint_as_float(__builtin_amdgcn_update_dpp(0u, __float_as_uint(x), CTRL, 0xf, 0xf, true)); return x + y; }

template <bool HT>
struct K1Dma {
    static constexpr int NW = HT ? 3 : 4;
    const char* xbase; const char* wbase; int ldw2, k0; unsigned lds0, xoff0, xoff1, woff[3];
    DI int kact(int kc) const { kc += k0; return kc >= 10 ? kc - 10 : kc; }
    DI void init(const char* xb, const char* wb, int ld, char* lds, int lane, int k0_) {
        xbase = xb; wbase = wb; ldw2 = ld; k0 = k0_; lds0 = (unsigned)(uintptr_t)lds;
        const int rr = lane >> 3;
        xoff0 = rr * 1280 + (((lane & 7) ^ ((lane >> 4) & 7)) << 4); xoff1 = rr * 1280 + (((lane & 7) ^ ((4 + (lane >> 4)) & 7)) << 4);
        if (HT) {
#pragma unroll
            for (int p = 0; p < 3; ++p) { const int o = 1024 * p + 16 * lane, row = (o * 21846) >> 22, col = o - row * 192; woff[p] = row * ldw2 + col; }
        } else woff[0] = (lane >> 4) * ldw2 + ((((lane & 15) ^ ((lane >> 4) << 2))) << 4);
    }
    DI void xp(int kc, int st, int j, int par) const { glds16(xbase + (size_t)j * 8 * 1280 + kc * 128, par ? xoff1 : xoff0, lds0 + st * K1_STAGE + j * 1024); }
    DI void wp(int kc, int st, int lw, int jj) const {
        if (HT) glds16(wbase + (size_t)(kc * 64 + 16 * lw) * ldw2, woff[jj], lds0 + st * K1_STAGE + 32768 + (3 * lw + jj) * 1024);
        else glds16(wbase + (size_t)(kc * 64 + 16 * lw + 4 * jj) * ldw2, woff[0], lds0 + st * K1_STAGE + 32768 + (4 * lw + jj) * 1024);
    }
    DI void first(int wid) const {
        const int kc = kact(0);
        if (wid >= 4) {
#pragma unroll
            for (int jj = 0; jj < NW; ++jj) wp(kc, 0, wid - 4, jj);
#pragma unroll
            for (int jj = 0; jj < 2; ++jj) xp(kc, 0, 2 * (wid - 4) + jj, jj & 1);
        } else {
#pragma unroll
            for (int jj = 0; jj < 6; ++jj) xp(kc, 0, 8 + 6 * wid + jj, jj & 1);
        }
    }
    DI void loader(int lw) const {
        auto issue = [&](int kc_, int st) {
            const int kc = kact(kc_);
#pragma unroll
            for (int jj = 0; jj < NW; ++jj) wp(kc, st, lw, jj);
#pragma unroll
            for (int jj = 0; jj < 8; ++jj) xp(kc, st, 8 * lw + jj, jj & 1);
        };
        issue(1, 1);
        if (HT) asm volatile("s_waitcnt vmcnt(11)\n\ts_barrier" ::: "memory"); else asm volatile("s_waitcnt vmcnt(12)\n\ts_barrier" ::: "memory");
        issue(2, 2);
        int st = 0;
        for (int kc = 0; kc < 9; ++kc) {
            if (kc == 8) asm volatile("s_waitcnt vmcnt(0)\n\ts_barrier" ::: "memory");
            else if (HT) asm volatile("s_waitcnt vmcnt(11)\n\ts_barrier" ::: "memory");
            else asm volatile("s_waitcnt vmcnt(12)\n\ts_barrier" ::: "memory");
            if (kc + 3 < 10) issue(kc + 3, st);
            st = st == 2 ? 0 : st + 1;
        }
    }
};

DI void rt_load(const unsigned short* w2, const unsigned short* wo, int u, unsigned ldsb, int piece, int lane) {
    const int sig = u >> 2, s = (u >> 1) & 1, cb = u & 1;
    const char* src = (sig < 48 ? (const char*)w2 + (size_t)sig * 32 * 1280 : (const char*)wo + (size_t)(sig - 48) * 32 * 1280) + s * 16 * 1280 + cb * 640;
    const int o = piece * 1024 + lane * 16, row = (o * 6554) >> 22;
    glds16(src, row * 1280 + (o - row * 640), ldsb + piece * 1024);
}
DI void rt_store(rsrc_t rs, int u, const char* ldsb, int i, int lane) {
    const int sig = u >> 2, s = (u >> 1) & 1, cb = u & 1;
    const int nl = 2 * i + (lane >> 5), hh = (lane >> 4) & 1, q = (lane & 15) >> 2, p = lane & 3;
    lds_cptr a = (lds_cptr)ldsb + (4 * hh + q) * 640 + (16 * nl + 4 * p) * 2;
    const s16x4 lo = ldstr(a), hi = ldstr(a + 8 * 640);
    const bf16x8 f = __builtin_shufflevector(lo, hi, 0, 1, 2, 3, 4, 5, 6, 7);
    const int nu = cb * 20 + nl, ng = (nu * 205) >> 10;
    st16(rs, (unsigned)WS_BF + (unsigned)((ng * NSIG + sig) * 5 + (nu - 5 * ng)) * 1024 + (s * 32 + (lane & 31)) * 16, __builtin_bit_cast(u32x4, f));
}
template <int TYPE, int MT, int NTL>
DI void k1_mainloop(f32x16 (&acc)[MT][NTL], char* lds, int wid, int lane, char* ws) {
    const int h = lane >> 5, r = lane & 31;
    constexpr bool HT = TYPE == 2;
    unsigned xa[4];
    { const int row = (HT ? wid * 64 : (wid >> 1) * 128) + r; const int sw = (row >> 1) & 7;
#pragma unroll
      for (int ks = 0; ks < 4; ++ks) xa[ks] = row * 128 + (((2 * ks + h) ^ sw) << 4); }
    unsigned wa[2];
    { const int blk = (lane >> 4) & 1, q = (lane & 15) >> 2, p = lane & 3;
      if (HT) wa[0] = wa[1] = 32768 + (8 * h + q) * 192 + blk * 32 + p * 8;
      else {
#pragma unroll
          for (int nt = 0; nt < 2; ++nt) { const int ch = (wid & 1) * 8 + nt * 4 + 2 * blk + (p >> 1); wa[nt] = 32768 + (8 * h + q) * 256 + ((ch ^ (q << 2)) << 4) + 8 * (p & 1); } } }
    bf16x8 xf[2][MT], wf[2][NTL];
    auto rdw = [&](int buf, int nt, lds_cptr sb, int ks) {
        lds_cptr a = HT ? sb + wa[0] + ks * 16 * 192 + nt * 64 : sb + wa[nt & 1] + ks * 4096;
        const s16x4 lo = ldstr(a), hi = ldstr(a + (HT ? 4 * 192 : 1024));
        wf[buf][nt] = __builtin_shufflevector(lo, hi, 0, 1, 2, 3, 4, 5, 6, 7);
    };
    auto rdx = [&](int buf, int mt, lds_cptr sb, int ks) { xf[buf][mt] = lds128(sb + xa[ks] + mt * 4096); };
    auto mm = [&](int buf, int mt, int nt) { acc[mt][nt] = TYPE == 1 ? MFMA32(xf[buf][mt], wf[buf][nt], acc[mt][nt]) : MFMA32(wf[buf][nt], xf[buf][mt], acc[mt][nt]); };
    asm volatile("s_waitcnt vmcnt(0)\n\ts_barrier" ::: "memory");
    { lds_cptr sb = (lds_cptr)lds; rdw(0, 0, sb, 0); rdx(0, 0, sb, 0); rdw(0, 1, sb, 0); rdx(0, 1, sb, 0);
      if (HT) rdw(0, 2, sb, 0); else { rdx(0, 2 % MT, sb, 0); rdx(0, 3 % MT, sb, 0); } }
    int st = 0;
#define K1_KSTEP(ks, NEXT) do { const int b = (ks) & 1, n = b ^ 1; lds_cptr np = (ks) < 3 ? sb : sn; const int nk = ((ks) + 1) & 3; \
        SBAR(); \
        if constexpr (HT) { \
            mm(b, 0, 0); SBAR(); if (NEXT) { rdw(n, 0, np, nk); } SBAR(); \
            mm(b, 0, 1); SBAR(); if (NEXT) { rdx(n, 0, np, nk); } SBAR(); \
            mm(b, 1, 0); SBAR(); if (NEXT) { rdw(n, 1, np, nk); } SBAR(); \
            mm(b, 1, 1); SBAR(); if (NEXT) { rdx(n, 1, np, nk); } SBAR(); \
            mm(b, 0, 2); SBAR(); if (NEXT) { rdw(n, 2, np, nk); } SBAR(); \
            mm(b, 1, 2); SBAR(); \
        } else { \
            mm(b, 0, 0); SBAR(); if (NEXT) { rdw(n, 0, np, nk); } SBAR(); \
            mm(b, 0, 1); SBAR(); if (NEXT) { rdx(n, 0, np, nk); } SBAR(); \
            mm(b, 1, 0); SBAR(); if (NEXT) { rdw(n, 1, np, nk); } SBAR(); \
            mm(b, 1, 1); SBAR(); if (NEXT) { rdx(n, 1, np, nk); } SBAR(); \
            mm(b, 2 % MT, 0); SBAR(); if (NEXT) { rdx(n, 2 % MT, np, nk); } SBAR(); \
            mm(b, 2 % MT, 1); SBAR(); if (NEXT) { rdx(n, 3 % MT, np, nk); } SBAR(); \
            mm(b, 3 % MT, 0); SBAR(); \
            mm(b, 3 % MT, 1); SBAR(); \
        } } while (0)
#pragma unroll 1
    for (int kc = 0; kc < 9; ++kc) {
        lds_cptr sb = (lds_cptr)lds + st * K1_STAGE;
        st = st == 2 ? 0 : st + 1;
        lds_cptr sn = (lds_cptr)lds + st * K1_STAGE;
        K1_KSTEP(0, true); K1_KSTEP(1, true); K1_KSTEP(2, true);
        asm volatile("s_waitcnt lgkmcnt(0)\n\ts_barrier" ::: "memory");
        K1_KSTEP(3, true);
    }
    { lds_cptr sb = (lds_cptr)lds + st * K1_STAGE; lds_cptr sn = sb;
      K1_KSTEP(0, true); K1_KSTEP(1, true); K1_KSTEP(2, true); K1_KSTEP(3, false); }
}

__global__ __launch_bounds__(512, 2) void k1_proj(const unsigned short* __restrict__ x, const unsigned short* __restrict__ wqkv, const unsigned short* __restrict__ wo,
                                                  const unsigned short* __restrict__ w1, const unsigned short* __restrict__ w2, char* __restrict__ ws) {
    extern __shared__ __attribute__((aligned(16))) char lds[];
    const int tid = threadIdx.x, lane = tid & 63, wid = rfl(tid >> 6);
    const int xcd = blockIdx.x & 7, slot = blockIdx.x >> 3;
    const int rgrp = xcd >> 2, cgrp = xcd & 3, nq = cgrp < 3 ? 4 : 3;
    const int h = lane >> 5, r = lane & 31;
    const rsrc_t rs = ws_rsrc(ws);
    const int rt = 4 * rgrp + (slot & 3), ci = slot >> 2;
    const char* xbase = (const char*)x + (size_t)rt * 256 * 1280;
    int ty = 0;
    STAMP(0, 0);
    if (ci < nq) {
        const int ct = 4 * cgrp + ci;
        const bool isV = ct >= 10;
        ty = isV ? 2 : 1;
        const int wm = (wid >> 1) & 1, wn = wid & 1;
        K1Dma<false> dma; dma.init(xbase, (const char*)wqkv + ct * 256, 3 * DM * 2, lds, lane, K1ROT);
        dma.first(wid);
        if (wid < 4) { for (int pc = wid; pc < 10; pc += 4) rt_load(w2, wo, blockIdx.x, (unsigned)(uintptr_t)lds + K1_RT, pc, lane); }
        if (wid >= 4) { dma.loader(wid - 4); for (int i = wid - 4; i < 10; i += 4) rt_store(rs, blockIdx.x, lds + K1_RT, i, lane); }
        else {
            f32x16 acc[4][2] = {};
            if (isV) {
                k1_mainloop<1, 4, 2>(acc, lds, wid, lane, ws);
                STAMP(0, 1);
#pragma unroll
                for (int nt = 0; nt < 2; ++nt) {
                    const int phi = (ct - 10) * 128 + wn * 64 + nt * 32 + r;
                    const int head = (phi * 3277) >> 16, d = phi - head * 20;
                    const unsigned dst = (unsigned)WS_KV + (unsigned)(head * NT + rt * 8 + wm * 4) * KVTILE_B + V_OFF + (h * V_ROWS + d) * 16;
#pragma unroll
                    for (int mt = 0; mt < 4; ++mt)
#pragma unroll
                        for (int s = 0; s < 2; ++s) {
                            const f32x16& a = acc[mt][nt];
                            const u32x4 w = {cvtpk(a[8 * s], a[8 * s + 1]), cvtpk(a[8 * s + 2], a[8 * s + 3]), cvtpk(a[8 * s + 4], a[8 * s + 5]), cvtpk(a[8 * s + 6], a[8 * s + 7])};
                            st16(rs, dst + mt * KVTILE_B + s * V_STEP, w);
                        }
                }
            } else {
                k1_mainloop<0, 4, 2>(acc, lds, wid, lane, ws);
                STAMP(0, 1);
                char* sb = lds + K1_STAGE + (wm * 4 * 32 + wn * 16 + h) * 256 + r * 8;
                if (ct < 5) {
#pragma unroll
                    for (int nt = 0; nt < 2; ++nt)
#pragma unroll
                        for (int g = 0; g < 4; ++g)
#pragma unroll
                            for (int mt = 0; mt < 4; ++mt) {
                                const f32x16& a = acc[mt][nt];
                                *(u32x2*)(sb + (mt * 32 + nt * 8 + 2 * g) * 256) = (u32x2){cvtpk(a[4 * g] * QSCALE, a[4 * g + 1] * QSCALE), cvtpk(a[4 * g + 2] * QSCALE, a[4 * g + 3] * QSCALE)};
                            }
                } else {
                    float nrm[8];
#pragma unroll
                    for (int nt = 0; nt < 2; ++nt)
#pragma unroll
                        for (int g = 0; g < 4; ++g) {
                            float m = 0.f;
#pragma unroll
                            for (int mt = 0; mt < 4; ++mt) {
                                const f32x16& a = acc[mt][nt];
                                *(u32x2*)(sb + (mt * 32 + nt * 8 + 2 * g) * 256) = (u32x2){cvtpk(a[4 * g], a[4 * g + 1]), cvtpk(a[4 * g + 2], a[4 * g + 3])};
                                m = fmaxf(m, __builtin_fmaf(a[4 * g], a[4 * g], a[4 * g + 1] * a[4 * g + 1]) + __builtin_fmaf(a[4 * g + 2], a[4 * g + 2], a[4 * g + 3] * a[4 * g + 3]));
                            }
                            nrm[nt * 4 + g] = m;
                        }
#pragma unroll
                    for (int i = 0; i < 8; ++i) { float m = nrm[i]; m = dpp_max<0xB1>(m); m = dpp_max<0x4E>(m); m = dpp_max<0x141>(m); m = dpp_max<0x140>(m);
                        auto rr = __builtin_amdgcn_permlane16_swap(__float_as_uint(m), __float_as_uint(m), false, false); nrm[i] = fmaxf(__uint_as_float(rr[0]), __uint_as_float(rr[1])); }
                    float mine = nrm[0];
#pragma unroll
                    for (int i = 1; i < 8; ++i) mine = r == i ? nrm[i] : mine;
                    if (r < 8) ((float*)(ws + WS_KMAX))[((ct - 5) * 32 + wn * 16 + (r >> 2) * 8 + 2 * (r & 3) + h) * 16 + rt * 2 + wm] = mine;
                }
            }
        }
        if (!isV) {
            __syncthreads();
            const bool isQ = ct < 5;
            const int ctl = isQ ? ct : ct - 5;
            const unsigned img = (unsigned)(isQ ? WS_Q : WS_KV);
            const unsigned tileb = isQ ? QTILE_B : KVTILE_B;
            const int T = rt * 8 + wid, G0 = ctl * 32;
            const int hd0 = (G0 * 205) >> 10, id0 = 3 * hd0 + ((G0 - 5 * hd0) >> 1);
            const char* lb = lds + K1_STAGE + wid * 8192;
            u32x2 pa[10], pb[10];
#pragma unroll
            for (int it = 0; it < 10; ++it) {
                const int id = id0 + 2 * it + h, head = (id * 171) >> 9, ty_ = id - 3 * head, la = 5 * head + 2 * ty_ - G0;
                const int lc = la < 0 ? 0 : (la > 31 ? 31 : la);
                const int aA = ty_ < 2 ? lc * 256 + r * 8 : lc * 256 + (r & 15) * 16;
                const int aB = ty_ < 2 ? (la > 30 ? 31 : la + 1) * 256 + r * 8 : aA + 8;
                pa[it] = *(const u32x2*)(lb + aA); pb[it] = *(const u32x2*)(lb + aB);
            }
#pragma unroll
            for (int it = 0; it < 10; ++it) {
                const int id = id0 + 2 * it + h, head = (id * 171) >> 9, ty_ = id - 3 * head, la = 5 * head + 2 * ty_ - G0;
                const unsigned dst = img + (unsigned)(head * NT + T) * tileb + ty_ * 512 + r * 16;
                const bool okA = (unsigned)la < 32u, okB = ty_ == 2 ? okA : (unsigned)(la + 1) < 32u;
                if (okA && okB) { if (ty_ < 2 || r < 16) st16(rs, dst, (u32x4){pa[it][0], pa[it][1], pb[it][0], pb[it][1]}); }
                else if (okA) st8(rs, dst, pa[it]);
                else if (okB) st8(rs, dst + 8, pb[it]);
            }
        }
    } else if (ci < nq + 4) {
        const int ch = 4 * cgrp + (ci - nq);
        ty = 3;
        K1Dma<true> dma; dma.init(xbase, (const char*)w1 + ch * 192, FF * 2, lds, lane, K1ROT);
        dma.first(wid);
        if (wid < 4) { for (int pc = wid; pc < 10; pc += 4) rt_load(w2, wo, blockIdx.x, (unsigned)(uintptr_t)lds + K1_RT, pc, lane); }
        if (wid >= 4) { dma.loader(wid - 4); for (int i = wid - 4; i < 10; i += 4) rt_store(rs, blockIdx.x, lds + K1_RT, i, lane); }
        else {
            f32x16 acc[2][3] = {};
            k1_mainloop<2, 2, 3>(acc, lds, wid, lane, ws);
            STAMP(0, 1);
#pragma unroll
            for (int mt = 0; mt < 2; ++mt) {
                const int tok = rt * 256 + wid * 64 + mt * 32 + r, rho = tok >> 4;
#pragma unroll
                for (int nt = 0; nt < 3; ++nt) {
                    const int sig = ch * 3 + nt;
                    const unsigned dst = (unsigned)WS_AF + (unsigned)(rho * NSIG + sig) * 1024 + (h * 16 + (r & 15)) * 16;
                    float sv[16];
#pragma unroll
                    for (int i = 0; i < 16; ++i) { const float v = acc[mt][nt][i]; sv[i] = v * __builtin_amdgcn_rcpf(1.f + __builtin_amdgcn_exp2f(v * -1.4426950408889634f)); }
#pragma unroll
                    for (int s = 0; s < 2; ++s) {
                        const u32x4 w = {cvtpk(sv[8 * s], sv[8 * s + 1]), cvtpk(sv[8 * s + 2], sv[8 * s + 3]), cvtpk(sv[8 * s + 4], sv[8 * s + 5]), cvtpk(sv[8 * s + 6], sv[8 * s + 7])};
                        st16(rs, dst + s * 512, w);
                    }
                }
            }
        }
    } else {
        const int aidx = rgrp * 4 + (slot & 3);
        const unsigned lds0 = (unsigned)(uintptr_t)lds;
        const int un[3] = {(int)blockIdx.x, 256 + 2 * aidx, 257 + 2 * aidx};
#pragma unroll
        for (int k = 0; k < 3; ++k) for (int pc = wid; pc < 10; pc += 8) rt_load(w2, wo, un[k], lds0 + k * 10240, pc, lane);
#pragma unroll
        for (int k = 0; k < 4; ++k) { const int i = (aidx * 4 + k) * 512 + tid;
          const unsigned f = (i & 1) ? 0u : ONES2; st16(rs, (unsigned)WS_KV + (unsigned)(i >> 3) * KVTILE_B + V_OFF + ((i >> 2) & 1) * V_STEP + (((i >> 1) & 1) * V_ROWS + 20 + (i & 1)) * 16, (u32x4){f, f, f, f}); }
        asm volatile("s_waitcnt vmcnt(0)\n\ts_barrier" ::: "memory");
#pragma unroll
        for (int k = 0; k < 3; ++k) for (int i = wid; i < 10; i += 8) rt_store(rs, un[k], lds + k * 10240, i, lane);
    }
}

DI float swap32_add(float x) { auto rr = __builtin_amdgcn_permlane32_swap(__float_as_uint(x), __float_as_uint(x), false, false); return __uint_as_float(rr[0]) + __uint_as_float(rr[1]); }
DI float swap32_max(float x) { auto rr = __builtin_amdgcn_permlane32_swap(__float_as_uint(x), __float_as_uint(x), false, false); return fmaxf(__uint_as_float(rr[0]), __uint_as_float(rr[1])); }
DI s16x4 make_qb(unsigned q0, unsigned q1, float M, int h) {
    const float mh = bf_hi(__float_as_uint(M)), r1 = M - mh, mm = bf_hi(__float_as_uint(r1)), r2 = r1 - mm, ml = bf_hi(__float_as_uint(r2));
    u32x2 w;
    w[0] = h ? ((__float_as_uint(mh) >> 16) | (__float_as_uint(mm) & 0xffff0000u)) ^ 0x80008000u : q0;
    w[1] = h ? (__float_as_uint(ml) >> 16) ^ 0x8000u : q1;
    return __builtin_bit_cast(s16x4, w);
}

DI void attn_fallback(f32x16& o, const char* kv, bf16x8 qa, unsigned q0, unsigned q1, int lane, int kboff, int voff) {
    const int h = lane >> 5;
    const f32x16 zero = {};
    float m = -INFINITY;
    const s16x4 qb0 = make_qb(q0, q1, 0.f, h);
    for (int t = 0; t < NT; ++t) {
        const char* tb = kv + t * KVTILE_B;
        const bf16x8 ka = *(const bf16x8*)(tb + lane * 16); const s16x4 kb = *(const s16x4*)(tb + kboff);
        f32x16 s = MFMA32(ka, qa, zero); s = MFMA8(kb, qb0, s);
#pragma unroll
        for (int i = 0; i < 16; ++i) m = fmaxf(m, s[i]);
    }
    m = swap32_max(m);
    const s16x4 qb = make_qb(q0, q1, m, h);
    o = zero;
    for (int t = 0; t < NT; ++t) {
        const char* tb = kv + t * KVTILE_B;
        const bf16x8 ka = *(const bf16x8*)(tb + lane * 16); const s16x4 kb = *(const s16x4*)(tb + kboff);
        const bf16x8 va = *(const bf16x8*)(tb + voff), vb = *(const bf16x8*)(tb + voff + V_STEP);
        f32x16 s = MFMA32(ka, qa, zero); s = MFMA8(kb, qb, s);
        u32x4 p0, p1;
#pragma unroll
        for (int i = 0; i < 4; ++i) { p0[i] = cvtpk(__builtin_amdgcn_exp2f(s[2 * i]), __builtin_amdgcn_exp2f(s[2 * i + 1])); p1[i] = cvtpk(__builtin_amdgcn_exp2f(s[8 + 2 * i]), __builtin_amdgcn_exp2f(s[9 + 2 * i])); }
        o = MFMA32(va, __builtin_bit_cast(bf16x8, p0), o);
        o = MFMA32(vb, __builtin_bit_cast(bf16x8, p1), o);
    }
}

constexpr int K2_BIG = 16 * KVTILE_B;
constexpr int K2_LDS = 3 * K2_BIG + 1024;
__global__ __launch_bounds__(576, 3) void k2_attn(char* __restrict__ ws, const unsigned short* __restrict__ x, const unsigned short* __restrict__ gw) {
    extern __shared__ __attribute__((aligned(16))) char lds[];
    const int bid = blockIdx.x;
    const int tid = threadIdx.x, lane = tid & 63, wid = rfl(tid >> 6);
    const int h = lane >> 5, r = lane & 31;
    const int xcd = bid & 7, slot = bid >> 3;
    const int head = xcd * 4 + (slot & 3), qblk = slot >> 2;
    STAMP(1, 0);
    const char* kv = ws + WS_KV + (size_t)head * NT * KVTILE_B;
    const unsigned lds0 = (unsigned)(uintptr_t)lds;
    const unsigned lane16 = lane * 16;
    if (wid == 8) {
        const char* pfa; {
            const int u0 = slot * 96, ng0 = (xcd & 1) * 4, bt0 = (xcd >> 1) * 8;
            if (u0 < 1536) { const int rho = u0 / 48; pfa = ws + WS_AF + (size_t)(bt0 * 4 + rho) * NSIG * 1024; }
            else { int v = u0 - 1536; if (v > 1360 - 96) v = 1360 - 96; pfa = ws + WS_BF + ((size_t)ng0 * NSIG * 5 + v) * 1024; }
        }
        const int pstep = slot < 16 ? (NSIG - 48) * 1024 : 0;
        auto pieces = [&](int first, int n, int st) {
            for (int j = 0; j < n; ++j) glds16(kv + (size_t)(first + j) * 1024, lane16, lds0 + st * K2_BIG + ((first + j) % 42) * 1024);
        };
        auto warm = [&](int u0, int n) {
            for (int j = 0; j < n; ++j) { const int u = u0 + j; glds16(pfa + (size_t)u * 1024 + (u >= 48 ? pstep : 0), lane16, lds0 + 3 * K2_BIG); }
        };
        pieces(21, 21, 0);
        asm volatile("s_barrier" ::: "memory");
        pieces(42, 42, 1);
        asm volatile("s_waitcnt vmcnt(42)\n\ts_barrier" ::: "memory");
        asm volatile("s_waitcnt vmcnt(0)\n\ts_barrier" ::: "memory");
        pieces(84, 42, 2);
        warm(0, 20);
        asm volatile("s_waitcnt vmcnt(0)\n\ts_barrier" ::: "memory");
        pieces(126, 42, 0); warm(20, 20);
        asm volatile("s_waitcnt vmcnt(0)\n\ts_barrier" ::: "memory");
        warm(40, 40);
        asm volatile("s_waitcnt vmcnt(20)" ::: "memory");
        warm(80, 16);
        asm volatile("s_waitcnt vmcnt(0)\n\ts_barrier" ::: "memory");
        return;
    }
    const char* kvw = kv + wid * 1024;
    const unsigned ldw = lds0 + wid * 1024;
    const int Tq = qblk * 8 + wid;
    const char* qt = ws + WS_Q + ((size_t)head * NT + Tq) * QTILE_B;
    const bf16x8 qa = *(const bf16x8*)(qt + lane * 16);
    const u32x2 qbr = *(const u32x2*)(qt + 1024 + r * 8);
    f32x4 km4 = *(const f32x4*)(ws + WS_KMAX + (size_t)head * 320 + (lane < 20 ? lane : 0) * 16);
    glds16(kvw, lane16, ldw); glds16(kvw + 8192, lane16, ldw + 8192); if (wid < 5) glds16(kvw + 16384, lane16, ldw + 16384);
    {
        const unsigned* xr = (const unsigned*)(x + (size_t)(bid * 8 + wid) * DM) + lane; const unsigned* gr = (const unsigned*)gw + lane;
        float sm = 0.f;
#pragma unroll
        for (int i = 0; i < 5; ++i) { const unsigned a_ = xr[64 * i], b_ = gr[64 * i]; sm = __builtin_amdgcn_fdot2_f32_bf16(__builtin_bit_cast(bf16x2_t, a_), __builtin_bit_cast(bf16x2_t, b_), sm, false); }
        sm = dpp_add<0xB1>(sm); sm = dpp_add<0x4E>(sm); sm = dpp_add<0x141>(sm); sm = dpp_add<0x140>(sm);
        sm = (rdl(sm, 0) + rdl(sm, 16)) + (rdl(sm, 32) + rdl(sm, 48));
        const float lg = bf_round(sm);
        const float g = bf_round(__builtin_amdgcn_rcpf(1.f + __builtin_amdgcn_exp2f(lg * -1.4426950408889634f)));
        if (lane == 0) *(f32x2*)(ws + WS_GATE + (bid * 8 + wid) * 8) = (f32x2){g, bf_round(1.f - g)};
    }
    float km = fmaxf(fmaxf(km4[0], km4[1]), fmaxf(km4[2], km4[3]));
    km = dpp_max<0xB1>(km); km = dpp_max<0x4E>(km);
    km = __builtin_amdgcn_sqrtf(km);
    float M;
    { const float k0 = rdl(km, 0), k1 = rdl(km, 4), k2 = rdl(km, 8), k3 = rdl(km, 12), k4 = rdl(km, 16);
      const u32x4 qw = __builtin_bit_cast(u32x4, qa);
      float n0, n1, n2;
      { const float a = bf_lo(qw[0]), b = bf_hi(qw[0]), c = bf_lo(qw[1]), d = bf_hi(qw[1]); n0 = (a * a + b * b) + (c * c + d * d); }
      { const float a = bf_lo(qw[2]), b = bf_hi(qw[2]), c = bf_lo(qw[3]), d = bf_hi(qw[3]); n1 = (a * a + b * b) + (c * c + d * d); }
      { const float a = bf_lo(qbr[0]), b = bf_hi(qbr[0]), c = bf_lo(qbr[1]), d = bf_hi(qbr[1]); n2 = (a * a + b * b) + (c * c + d * d); }
      float part = __builtin_amdgcn_sqrtf(n0) * (h ? k2 : k0) + __builtin_amdgcn_sqrtf(n1) * (h ? k3 : k1);
      if (!h) part += __builtin_amdgcn_sqrtf(n2) * k4;
      M = swap32_add(part) * 1.01f; }
    const s16x4 qb = make_qb(qbr[0], qbr[1], M, h);
    const int vrow = r <= 20 ? r : (r < 28 ? 21 : r - 12);
    const int voff = V_OFF + (h * V_ROWS + vrow) * 16, kboff = h ? V_OFF + 20 * 16 : 1024 + r * 8;
    lds_cptr kaC = (lds_cptr)lds + lane * 16, kbC = (lds_cptr)lds + kboff, vC = (lds_cptr)lds + voff;
    lds_cptr kaN = kaC + K2_BIG, kbN = kbC + K2_BIG, vN = vC + K2_BIG;
    const f32x16 zero = {};
    f32x16 o = {}, sA, sB;
    bf16x8 ka, va, vb; s16x4 kb;
    u32x4 plo, phi;
    asm volatile("s_waitcnt vmcnt(0)\n\ts_barrier" ::: "memory");
    STAMP(1, 1);
    ka = lds128(kaC); kb = lds64(kbC);
    sA = MFMA32(ka, qa, zero); sA = MFMA8(kb, qb, sA);
    ka = lds128(kaC + KVTILE_B); kb = lds64(kbC + KVTILE_B);
    va = lds128(vC);
    vb = (bf16x8){}; phi = (u32x4){};
    f32x4 e3 = {};
#define EXP4(X, i) do { X[i] = __builtin_amdgcn_exp2f(X[i]); X[i + 1] = __builtin_amdgcn_exp2f(X[i + 1]); X[i + 2] = __builtin_amdgcn_exp2f(X[i + 2]); X[i + 3] = __builtin_amdgcn_exp2f(X[i + 3]); } while (0)
#define KADDR(A, i) (((i) < 16) ? (A##C + (i) * KVTILE_B) : (A##N + ((i) - 16) * KVTILE_B))
#define STEP(C, N, i) do { \
        bf16x8 ka2, va2, vb2; s16x4 kb2; \
        N = MFMA32(ka, qa, zero); SBAR(); \
        ka2 = lds128(KADDR(ka, (i) + 2)); \
        phi[2] = cvtpk(e3[0], e3[1]); phi[3] = cvtpk(e3[2], e3[3]); PIN(phi); SBAR(); EXP4(C, 0); PIN(C); SBAR(); \
        o = MFMA32(vb, __builtin_bit_cast(bf16x8, phi), o); SBAR(); \
        vb2 = lds128(vC + (i) * KVTILE_B + V_STEP); \
        plo[0] = cvtpk(C[0], C[1]); plo[1] = cvtpk(C[2], C[3]); PIN(plo); SBAR(); EXP4(C, 4); PIN(C); SBAR(); \
        N = MFMA8(kb, qb, N); SBAR(); \
        kb2 = lds64(KADDR(kb, (i) + 2)); \
        plo[2] = cvtpk(C[4], C[5]); plo[3] = cvtpk(C[6], C[7]); PIN(plo); SBAR(); EXP4(C, 8); PIN(C); SBAR(); \
        o = MFMA32(va, __builtin_bit_cast(bf16x8, plo), o); SBAR(); \
        va2 = lds128(KADDR(v, (i) + 1)); \
        phi[0] = cvtpk(C[8], C[9]); phi[1] = cvtpk(C[10], C[11]); PIN(phi); SBAR(); \
        e3[0] = __builtin_amdgcn_exp2f(C[12]); e3[1] = __builtin_amdgcn_exp2f(C[13]); e3[2] = __builtin_amdgcn_exp2f(C[14]); e3[3] = __builtin_amdgcn_exp2f(C[15]); PIN(e3); SBAR(); \
        ka = ka2; kb = kb2; va = va2; vb = vb2; \
    } while (0)
#pragma unroll 1
    for (int c = 0; c < 4; ++c) {
        STEP(sA, sB, 0); STEP(sB, sA, 1); STEP(sA, sB, 2); STEP(sB, sA, 3); STEP(sA, sB, 4);
        if (c == 0) asm volatile("s_barrier" ::: "memory");
        STEP(sB, sA, 5); STEP(sA, sB, 6); STEP(sB, sA, 7); STEP(sA, sB, 8); STEP(sB, sA, 9); STEP(sA, sB, 10); STEP(sB, sA, 11); STEP(sA, sB, 12);
        asm volatile("s_barrier" ::: "memory");
        STEP(sB, sA, 13); STEP(sA, sB, 14); STEP(sB, sA, 15);
        const int d = (c == 1) ? -2 * K2_BIG : K2_BIG;
        kaC = kaN; kbC = kbN; vC = vN; kaN += d; kbN += d; vN += d;
    }
    phi[2] = cvtpk(e3[0], e3[1]); phi[3] = cvtpk(e3[2], e3[3]);
    o = MFMA32(vb, __builtin_bit_cast(bf16x8, phi), o);
    STAMP(1, 2);
    float l; { auto rr = __builtin_amdgcn_permlane32_swap(__float_as_uint(o[8]), __float_as_uint(o[8]), false, false); l = __uint_as_float(rr[1]); }
    if (__builtin_expect(__any(!(l >= 8.6736174e-19f && l <= 1.0e30f)), 0)) {
        attn_fallback(o, kv, qa, qbr[0], qbr[1], lane, kboff, voff);
        auto rr = __builtin_amdgcn_permlane32_swap(__float_as_uint(o[8]), __float_as_uint(o[8]), false, false); l = __uint_as_float(rr[1]);
    }
    const float f = __builtin_amdgcn_rcpf(l);
    const int tok = Tq * 32 + r;
    char* ab = ws + WS_AF + (size_t)(tok >> 4) * NSIG * 1024 + (tok & 15) * 16;
#pragma unroll
    for (int g = 0; g < 3; ++g) {
        const int gam = head * 5 + 2 * g + h;
        const int sig = 48 + (gam >> 3), c = ((gam >> 2) & 1) * 2 + (gam & 1), half = (gam >> 1) & 1;
        const u32x2 w = {cvtpk(o[4 * g] * f, o[4 * g + 1] * f), cvtpk(o[4 * g + 2] * f, o[4 * g + 3] * f)};
        if (g < 2 || h == 0) *(u32x2*)(ab + sig * 1024 + c * 256 + half * 8) = w;
    }
    STAMP(1, 3);
}

constexpr int K3_LDS = 8 * 20 * 1024;
DI void gld128(bf16x8& d, const char* sbase, unsigned voff) { asm volatile("global_load_dwordx4 %0, %1, %2" : "=v"(d) : "v"(voff), "s"(sbase) : "memory"); }
template <int N> DI void wait9(bf16x8 (&a)[4], bf16x8 (&b)[5]) {
    asm volatile("s_waitcnt vmcnt(%9)" : "+v"(a[0]), "+v"(a[1]), "+v"(a[2]), "+v"(a[3]), "+v"(b[0]), "+v"(b[1]), "+v"(b[2]), "+v"(b[3]), "+v"(b[4]) : "i"(N) : "memory");
}
template <int NS, int I> struct K3Step {
    template <class L> static DI void run(f32x4 (&acc)[4][5], bf16x8 (&a)[4][4], bf16x8 (&b)[4][5], L& load) {
        constexpr int left = NS - 1 - I, younger = (left < 3 ? left : 3) * 9;
        wait9<younger>(a[I & 3], b[I & 3]);
        SBAR();
#pragma unroll
        for (int nt = 0; nt < 5; ++nt)
#pragma unroll
            for (int mt = 0; mt < 4; ++mt) acc[mt][nt] = MFMA16(b[I & 3][nt], a[I & 3][mt], acc[mt][nt]);
        SBAR();
        if constexpr (I + 4 < NS) load(I + 4, a[I & 3], b[I & 3]);
        if constexpr (I + 1 < NS) K3Step<NS, I + 1>::run(acc, a, b, load);
    }
};
template <int NS>
DI void k3_stream(f32x4 (&acc)[4][5], const char* ap, const char* bp, unsigned lane16, int rot) {
    bf16x8 a[4][4], b[4][5];
    auto load = [&](int i, bf16x8 (&da)[4], bf16x8 (&db)[5]) {
        int st = i + rot; if (st >= NS) st -= NS;
#pragma unroll
        for (int nt = 0; nt < 5; ++nt) gld128(db[nt], bp + st * 5120 + nt * 1024, lane16);
#pragma unroll
        for (int mt = 0; mt < 4; ++mt) gld128(da[mt], ap + (size_t)mt * NSIG * 1024 + st * 1024, lane16);
    };
#pragma unroll
    for (int i = 0; i < 4; ++i) load(i, a[i], b[i]);
    K3Step<NS, 0>::run(acc, a, b, load);
}
__global__ __launch_bounds__(512, 2) void k3_out(const char* __restrict__ ws, unsigned short* __restrict__ out) {
    extern __shared__ __attribute__((aligned(16))) char lds[];
    const int tid = threadIdx.x, lane = tid & 63, wid = rfl(tid >> 6);
    const int xcd = blockIdx.x & 7, slot = blockIdx.x >> 3;
    const int ng = (xcd & 1) * 4 + (slot & 3), bt = (xcd >> 1) * 8 + (slot >> 2);
    STAMP(2, 0);
    const int s0 = wid < 6 ? wid * 8 : 48 + (wid - 6) * 10;
    const char* ap = ws + WS_AF + ((size_t)bt * 4 * NSIG + s0) * 1024;
    const char* bp = ws + WS_BF + ((size_t)ng * NSIG + s0) * 5 * 1024;
    const unsigned lane16 = lane * 16;
    float fac[4];
#pragma unroll
    for (int mt = 0; mt < 4; ++mt) asm volatile("global_load_dword %0, %1, %2" : "=v"(fac[mt]) : "v"((unsigned)((bt * 64 + mt * 16 + (lane & 15)) * 8 + (wid < 6 ? 4 : 0))), "s"(ws + WS_GATE) : "memory");
    f32x4 acc[4][5] = {};
    const int rr = (slot >> 2) + 3 * (slot & 3);
    if (wid < 6) k3_stream<8>(acc, ap, bp, lane16, rr & 7); else k3_stream<10>(acc, ap, bp, lane16, rr % 10);
    asm volatile("" : "+v"(fac[0]), "+v"(fac[1]), "+v"(fac[2]), "+v"(fac[3]));
    STAMP(2, 1);
#pragma unroll
    for (int mt = 0; mt < 4; ++mt)
#pragma unroll
        for (int nt = 0; nt < 5; ++nt) *(f32x4*)(lds + (wid * 20 + mt * 5 + nt) * 1024 + lane16) = acc[mt][nt] * fac[mt];
    __syncthreads();
#pragma unroll
    for (int i = 0; i < 3; ++i) { const int t = wid + 8 * i;
        if (t < 20) {
            f32x4 sum = *(const f32x4*)(lds + t * 1024 + lane16);
#pragma unroll
            for (int w = 1; w < 8; ++w) sum += *(const f32x4*)(lds + (w * 20 + t) * 1024 + lane16);
            const int m4 = t / 5, nt = t - m4 * 5;
            const int tok = bt * 64 + m4 * 16 + (lane & 15);
            *(u32x2*)(out + (size_t)tok * DM + ng * 80 + nt * 16 + (lane >> 4) * 4) = (u32x2){cvtpk(sum[0], sum[1]), cvtpk(sum[2], sum[3])};
        } }
    STAMP(2, 2);
}

extern "C" void kernel_launch(void* const* d_in, const int* in_sizes, int n_in, void* d_out, int out_size, void* d_ws, size_t ws_size, hipStream_t stream) {
    const unsigned short* x = (const unsigned short*)d_in[0];
    const unsigned short* wqkv = (const unsigned short*)d_in[1];
    const unsigned short* wo = (const unsigned short*)d_in[2];
    const unsigned short* w1 = (const unsigned short*)d_in[4];
    const unsigned short* w2 = (const unsigned short*)d_in[5];
    const unsigned short* gw = (const unsigned short*)d_in[6];
    char* ws = (char*)d_ws;
    (void)hipFuncSetAttribute((const void*)k1_proj, hipFuncAttributeMaxDynamicSharedMemorySize, K1_LDS);
    (void)hipFuncSetAttribute((const void*)k2_attn, hipFuncAttributeMaxDynamicSharedMemorySize, K2_LDS);
    (void)hipFuncSetAttribute((const void*)k3_out, hipFuncAttributeMaxDynamicSharedMemorySize, K3_LDS);
    k1_proj<<<256, 512, K1_LDS, stream>>>(x, wqkv, wo, w1, w2, ws);
    k2_attn<<<256, 576, K2_LDS, stream>>>(ws, x, gw);
    k3_out<<<256, 512, K3_LDS, stream>>>(ws, (unsigned short*)d_out);
}
```

```cpp
#include <hip/hip_runtime.h>
#include <hip/hip_bf16.h>
#include <stdint.h>

typedef short bf16x8 __attribute__((ext_vector_type(8)));
typedef short s16x4 __attribute__((ext_vector_type(4)));
typedef float f32x16 __attribute__((ext_vector_type(16)));
typedef float f32x4 __attribute__((ext_vector_type(4)));
typedef float f32x2 __attribute__((ext_vector_type(2)));
typedef unsigned u32x4 __attribute__((ext_vector_type(4)));
typedef unsigned u32x2 __attribute__((ext_vector_type(2)));
typedef __bf16 bf16x2_t __attribute__((ext_vector_type(2)));
#define LAS __attribute__((address_space(3)))
typedef LAS const char* lds_cptr;
#define DI __device__ __forceinline__
#define SBAR() __builtin_amdgcn_sched_barrier(0)
#define PIN(x) asm volatile("" : "+v"(x))
#define MFMA32(a, b, c) __builtin_amdgcn_mfma_f32_32x32x16_bf16(a, b, c, 0, 0, 0)
#define MFMA16(a, b, c) __builtin_amdgcn_mfma_f32_16x16x32_bf16(a, b, c, 0, 0, 0)
#define MFMA8(a, b, c) __builtin_amdgcn_mfma_f32_32x32x8bf16_1k(a, b, c, 0, 0, 0)

constexpr int S = 2048, DM = 640, NH = 32, DH = 20, FF = 1536;
constexpr int NT = S / 32;
constexpr int QTILE_B = 1280;
constexpr int KVTILE_B = 2688;
constexpr int V_OFF = 1280, V_STEP = 704, V_ROWS = 22;
constexpr int NSIG = 68;
constexpr size_t WS_Q = 0;
constexpr size_t WS_KV = WS_Q + (size_t)NH * NT * QTILE_B;
constexpr size_t WS_AF = WS_KV + (size_t)NH * NT * KVTILE_B;
constexpr size_t WS_BF = WS_AF + (size_t)128 * NSIG * 1024;
constexpr size_t WS_KMAX = WS_BF + (size_t)8 * NSIG * 5 * 1024;
constexpr size_t WS_GATE = WS_KMAX + (size_t)160 * 32 * 4;
constexpr size_t WS_F1 = WS_GATE + (size_t)S * 4;
constexpr size_t WS_F2 = WS_F1 + 2048;
constexpr size_t WS_F3 = WS_F2 + 2048;
constexpr size_t WS_EPOCH = WS_F3 + 2048;
constexpr size_t WS_END = WS_EPOCH + 256;
constexpr float QSCALE = 0.22360679774997896f * 1.4426950408889634f;
constexpr unsigned ONES2 = 0x3F803F80u;

DI unsigned cvtpk(float lo, float hi) { f32x2 v = {lo, hi}; bf16x2_t b = __builtin_convertvector(v, bf16x2_t); return __builtin_bit_cast(unsigned, b); }
DI float bf_lo(unsigned u) { return __uint_as_float(u << 16); }
DI float bf_hi(unsigned u) { return __uint_as_float(u & 0xffff0000u); }
DI float bf_round(float x) { return bf_lo(cvtpk(x, x)); }
DI void glds16(const void* sbase, unsigned voff, unsigned lds_base) {
    unsigned sv;
    asm volatile("s_mov_b32 %0, m0\n\ts_mov_b32 m0, %3\n\ts_nop 2\n\tglobal_load_lds_dwordx4 %1, %2\n\ts_mov_b32 m0, %0"
                 : "=&s"(sv) : "v"(voff), "s"(sbase), "s"(lds_base) : "memory");
}
typedef short v4i16_t __attribute__((ext_vector_type(4)));
DI s16x4 ldstr(lds_cptr p) { return __builtin_bit_cast(s16x4, __builtin_amdgcn_ds_read_tr16_b64_v4i16((LAS v4i16_t*)p)); }
DI bf16x8 lds128(lds_cptr p) { return *(const LAS bf16x8*)p; }
DI s16x4 lds64(lds_cptr p) { return *(const LAS s16x4*)p; }
DI unsigned rfl(unsigned x) { return __builtin_amdgcn_readfirstlane(x); }
#define STAMP(k, i) do { } while (0)

DI float rdl(float x, int l) { return __uint_as_float(__builtin_amdgcn_readlane(__float_as_uint(x), l)); }
typedef __amdgpu_buffer_rsrc_t rsrc_t;
typedef unsigned long long u64;
DI rsrc_t ws_rsrc(char* ws) { return __builtin_amdgcn_make_buffer_rsrc(ws, 0, (int)WS_END, 0x00020000); }
DI void st16(rsrc_t rs, unsigned off, u32x4 v) { __builtin_amdgcn_raw_buffer_store_b128(v, rs, off, 0, 16); }
DI void st8(rsrc_t rs, unsigned off, u32x2 v) { __builtin_amdgcn_raw_buffer_store_b64(v, rs, off, 0, 16); }
DI void st4f(char* p, float v) { __hip_atomic_store((float*)p, v, __ATOMIC_RELAXED, __HIP_MEMORY_SCOPE_AGENT); }
DI void flag_set(char* p, u64 tag) { __hip_atomic_store((u64*)p, tag, __ATOMIC_RELAXED, __HIP_MEMORY_SCOPE_AGENT); }
DI u64 flag_get(const char* p) { return __hip_atomic_load((const u64*)p, __ATOMIC_RELAXED, __HIP_MEMORY_SCOPE_AGENT); }
template <int CTRL> DI float dpp_max(float x) { const float y = __uint_as_float(__builtin_amdgcn_update_dpp(0u, __float_as_uint(x), CTRL, 0xf, 0xf, true)); return fmaxf(x, y); }
template <int CTRL> DI float dpp_add(float x) { const float y = __uint_as_float(__builtin_amdgcn_update_dpp(0u, __float_as_uint(x), CTRL, 0xf, 0xf, true)); return x + y; }
DI void gld128(bf16x8& d, const char* sbase, unsigned voff) { asm volatile("s_nop 4\n\tglobal_load_dwordx4 %0, %1, %2" : "=v"(d) : "v"(voff), "s"(sbase) : "memory"); }
DI void gld128_sc1(bf16x8& d, const char* sbase, unsigned voff) { asm volatile("s_nop 4\n\tglobal_load_dwordx4 %0, %1, %2 sc1" : "=v"(d) : "v"(voff), "s"(sbase) : "memory"); }
DI void gld128_rw(bf16x8& d, const char* sbase, unsigned voff) { asm volatile("s_nop 4\n\tglobal_load_dwordx4 %0, %1, %2" : "+v"(d) : "v"(voff), "s"(sbase) : "memory"); }
DI void gld128_sc1_rw(bf16x8& d, const char* sbase, unsigned voff) { asm volatile("s_nop 4\n\tglobal_load_dwordx4 %0, %1, %2 sc1" : "+v"(d) : "v"(voff), "s"(sbase) : "memory"); }
template <int N> DI void wait9(bf16x8 (&a)[4], bf16x8 (&b)[5]) {
    asm volatile("s_waitcnt vmcnt(%9)" : "+v"(a[0]), "+v"(a[1]), "+v"(a[2]), "+v"(a[3]), "+v"(b[0]), "+v"(b[1]), "+v"(b[2]), "+v"(b[3]), "+v"(b[4]) : "i"(N) : "memory");
}
#define WAITB(n) asm volatile("s_waitcnt vmcnt(" #n ")\n\ts_barrier" ::: "memory")
#define BARL() asm volatile("s_waitcnt lgkmcnt(0)\n\ts_barrier" ::: "memory")

constexpr int P1_STG = 32768;
constexpr int RT_OFF = 131072;
constexpr int P2_STG = 28672;
constexpr int CTL_OFF = 163584;
DI constexpr int p2_base(int s) { return CTL_OFF - (s + 1) * P2_STG; }
constexpr int K2_BIG = 16 * KVTILE_B;
constexpr int LDS_ALL = 163840;

template <bool N96>
struct GDma {
    static constexpr int NW = N96 ? 3 : 4;
    const char* xbase; const char* wbase; int ldw2, k0; unsigned lds0, xoff0, xoff1, woff[3];
    DI int kact(int kc) const { kc += k0; return kc >= 10 ? kc - 10 : kc; }
    DI static unsigned sbase(int st) { return N96 ? (unsigned)(CTL_OFF - (st + 1) * P2_STG) : (unsigned)(st * P1_STG); }
    DI void init(const char* xb, const char* wb, int ld, char* lds, int lane, int k0_) {
        xbase = xb; wbase = wb; ldw2 = ld; k0 = k0_; lds0 = (unsigned)(uintptr_t)lds;
        const int rr = lane >> 3;
        xoff0 = rr * 1280 + (((lane & 7) ^ ((lane >> 4) & 7)) << 4); xoff1 = rr * 1280 + (((lane & 7) ^ ((4 + (lane >> 4)) & 7)) << 4);
        if (N96) {
#pragma unroll
            for (int p = 0; p < 3; ++p) { const int o = 1024 * p + 16 * lane, row = (o * 21846) >> 22, col = o - row * 192; woff[p] = row * ldw2 + col; }
        } else woff[0] = (lane >> 4) * ldw2 + ((((lane & 15) ^ ((lane >> 4) << 2))) << 4);
    }
    DI void xp(int kc, int st, int j, int par) const { glds16(xbase + (size_t)j * 8 * 1280 + kc * 128, par ? xoff1 : xoff0, lds0 + sbase(st) + j * 1024); }
    DI void wp(int kc, int st, int lw, int jj) const {
        if (N96) glds16(wbase + (size_t)(kc * 64 + 16 * lw) * ldw2, woff[jj], lds0 + sbase(st) + 16384 + (3 * lw + jj) * 1024);
        else glds16(wbase + (size_t)(kc * 64 + 16 * lw + 4 * jj) * ldw2, woff[0], lds0 + sbase(st) + 16384 + (4 * lw + jj) * 1024);
    }
    DI void issue(int kc_, int st, int lw) const {
        const int kc = kact(kc_);
#pragma unroll
        for (int jj = 0; jj < NW; ++jj) wp(kc, st, lw, jj);
#pragma unroll
        for (int jj = 0; jj < 4; ++jj) xp(kc, st, 4 * lw + jj, jj & 1);
    }
};

DI void rt_load(const unsigned short* w2, const unsigned short* wo, int u, unsigned ldsb, int piece, int lane) {
    const int sig = u >> 2, s = (u >> 1) & 1, cb = u & 1;
    const char* src = (sig < 48 ? (const char*)w2 + (size_t)sig * 32 * 1280 : (const char*)wo + (size_t)(sig - 48) * 32 * 1280) + s * 16 * 1280 + cb * 640;
    const int o = piece * 1024 + lane * 16, row = (o * 6554) >> 22;
    glds16(src, row * 1280 + (o - row * 640), ldsb + piece * 1024);
}
DI void rt_store(rsrc_t rs, int u, const char* ldsb, int i, int lane) {
    const int sig = u >> 2, s = (u >> 1) & 1, cb = u & 1;
    const int nl = 2 * i + (lane >> 5), hh = (lane >> 4) & 1, q = (lane & 15) >> 2, p = lane & 3;
    lds_cptr a = (lds_cptr)ldsb + (4 * hh + q) * 640 + (16 * nl + 4 * p) * 2;
    const s16x4 lo = ldstr(a), hi = ldstr(a + 8 * 640);
    const bf16x8 f = __builtin_shufflevector(lo, hi, 0, 1, 2, 3, 4, 5, 6, 7);
    const int nu = cb * 20 + nl, ng = (nu * 205) >> 10;
    st16(rs, (unsigned)WS_BF + (unsigned)((ng * NSIG + sig) * 5 + (nu - 5 * ng)) * 1024 + (s * 32 + (lane & 31)) * 16, __builtin_bit_cast(u32x4, f));
}

template <bool N96, bool VT, int MT, int NTL, class HOOK>
DI void gemm_loop(f32x16 (&acc)[MT][NTL], char* lds, int wid, int lane, HOOK hook) {
    const int h = lane >> 5, r = lane & 31;
    constexpr int NST = N96 ? 3 : 4;
    unsigned xa[4];
    { const int row = (N96 ? wid * 32 : (wid >> 1) * 64) + r; const int sw = (row >> 1) & 7;
#pragma unroll
      for (int ks = 0; ks < 4; ++ks) xa[ks] = row * 128 + (((2 * ks + h) ^ sw) << 4); }
    unsigned wa[2];
    { const int blk = (lane >> 4) & 1, q = (lane & 15) >> 2, p = lane & 3;
      if (N96) wa[0] = wa[1] = 16384 + (8 * h + q) * 192 + blk * 32 + p * 8;
      else {
#pragma unroll
          for (int nt = 0; nt < 2; ++nt) { const int ch = (wid & 1) * 8 + nt * 4 + 2 * blk + (p >> 1); wa[nt] = 16384 + (8 * h + q) * 256 + ((ch ^ (q << 2)) << 4) + 8 * (p & 1); } } }
    bf16x8 xf[2][MT], wf[2][NTL];
    auto rdw = [&](int buf, int nt, lds_cptr sb, int ks) {
        lds_cptr a = N96 ? sb + wa[0] + ks * 16 * 192 + nt * 64 : sb + wa[nt & 1] + ks * 4096;
        const s16x4 lo = ldstr(a), hi = ldstr(a + (N96 ? 4 * 192 : 1024));
        wf[buf][nt] = __builtin_shufflevector(lo, hi, 0, 1, 2, 3, 4, 5, 6, 7);
    };
    auto rdx = [&](int buf, int mt, lds_cptr sb, int ks) { xf[buf][mt] = lds128(sb + xa[ks] + mt * 4096); };
    auto mm = [&](int buf, int mt, int nt) { acc[mt][nt] = VT ? MFMA32(xf[buf][mt], wf[buf][nt], acc[mt][nt]) : MFMA32(wf[buf][nt], xf[buf][mt], acc[mt][nt]); };
    { lds_cptr sb = (lds_cptr)lds + GDma<N96>::sbase(0); rdw(0, 0, sb, 0); rdx(0, 0, sb, 0); rdw(0, 1, sb, 0); if (N96) rdw(0, 2 % NTL, sb, 0); else rdx(0, 1 % MT, sb, 0); }
    int st = 0;
#define G_KSTEP(ks, NEXT) do { const int b = (ks) & 1, n = b ^ 1; lds_cptr np = (ks) < 3 ? sb : sn; const int nk = ((ks) + 1) & 3; \
        SBAR(); \
        if constexpr (N96) { \
            mm(b, 0, 0); SBAR(); if (NEXT) { rdw(n, 0, np, nk); rdx(n, 0, np, nk); } SBAR(); \
            mm(b, 0, 1); SBAR(); if (NEXT) { rdw(n, 1, np, nk); } SBAR(); \
            mm(b, 0, 2 % NTL); SBAR(); if (NEXT) { rdw(n, 2 % NTL, np, nk); } SBAR(); \
        } else { \
            mm(b, 0, 0); SBAR(); if (NEXT) { rdw(n, 0, np, nk); } SBAR(); \
            mm(b, 0, 1); SBAR(); if (NEXT) { rdx(n, 0, np, nk); } SBAR(); \
            mm(b, 1 % MT, 0); SBAR(); if (NEXT) { rdw(n, 1, np, nk); } SBAR(); \
            mm(b, 1 % MT, 1); SBAR(); if (NEXT) { rdx(n, 1 % MT, np, nk); } SBAR(); \
        } } while (0)
#pragma unroll 1
    for (int kc = 0; kc < 9; ++kc) {
        lds_cptr sb = (lds_cptr)lds + GDma<N96>::sbase(st);
        st = st == NST - 1 ? 0 : st + 1;
        lds_cptr sn = (lds_cptr)lds + GDma<N96>::sbase(st);
        G_KSTEP(0, true); G_KSTEP(1, true); G_KSTEP(2, true);
        hook(kc, true);
        BARL();
        hook(kc, false);
        G_KSTEP(3, true);
    }
    { lds_cptr sb = (lds_cptr)lds + GDma<N96>::sbase(st); lds_cptr sn = sb;
      G_KSTEP(0, true); G_KSTEP(1, true); G_KSTEP(2, true); G_KSTEP(3, false); }
}

DI void qk_copyout(rsrc_t rs, const char* lb, unsigned img, unsigned tileb, int T, int G0, int lane) {
    const int h = lane >> 5, r = lane & 31;
    const int hd0 = (G0 * 205) >> 10, id0 = 3 * hd0 + ((G0 - 5 * hd0) >> 1);
#pragma unroll
    for (int half = 0; half < 2; ++half) {
        u32x2 pa[5], pb[5];
#pragma unroll
        for (int i = 0; i < 5; ++i) { const int it = half * 5 + i;
            const int id = id0 + 2 * it + h, head = (id * 171) >> 9, ty = id - 3 * head, la = 5 * head + 2 * ty - G0;
            const int lc = la < 0 ? 0 : (la > 31 ? 31 : la), ld = la > 30 ? 31 : la + 1;
            const int aA = lc * 256 + (ty < 2 ? r * 8 : (r & 15) * 16);
            const int aB = ty < 2 ? ld * 256 + r * 8 : aA + 8;
            pa[i] = *(const u32x2*)(lb + aA); pb[i] = *(const u32x2*)(lb + aB);
        }
#pragma unroll
        for (int i = 0; i < 5; ++i) { const int it = half * 5 + i;
            const int id = id0 + 2 * it + h, head = (id * 171) >> 9, ty = id - 3 * head, la = 5 * head + 2 * ty - G0;
            const unsigned dst = img + (unsigned)(head * NT + T) * tileb + ty * 512 + r * 16;
            const bool okA = (unsigned)la < 32u, okB = ty == 2 ? okA : (unsigned)(la + 1) < 32u;
            if (okA && okB) { if (ty < 2 || r < 16) st16(rs, dst, (u32x4){pa[i][0], pa[i][1], pb[i][0], pb[i][1]}); }
            else if (okA) st8(rs, dst, pa[i]);
            else if (okB) st8(rs, dst + 8, pb[i]);
        }
    }
}

DI float swap32_add(float x) { auto rr = __builtin_amdgcn_permlane32_swap(__float_as_uint(x), __float_as_uint(x), false, false); return __uint_as_float(rr[0]) + __uint_as_float(rr[1]); }
DI float swap32_max(float x) { auto rr = __builtin_amdgcn_permlane32_swap(__float_as_uint(x), __float_as_uint(x), false, false); return fmaxf(__uint_as_float(rr[0]), __uint_as_float(rr[1])); }
DI s16x4 make_qb(unsigned q0, unsigned q1, float M, int h) {
    const float mh = bf_hi(__float_as_uint(M)), r1 = M - mh, mm = bf_hi(__float_as_uint(r1)), r2 = r1 - mm, ml = bf_hi(__float_as_uint(r2));
    u32x2 w;
    w[0] = h ? ((__float_as_uint(mh) >> 16) | (__float_as_uint(mm) & 0xffff0000u)) ^ 0x80008000u : q0;
    w[1] = h ? (__float_as_uint(ml) >> 16) ^ 0x8000u : q1;
    return __builtin_bit_cast(s16x4, w);
}

DI void attn_fallback(f32x16& o, const char* kv, bf16x8 qa, unsigned q0, unsigned q1, int lane, int kboff, int voff) {
    const int h = lane >> 5;
    const f32x16 zero = {};
    float m = -INFINITY;
    const s16x4 qb0 = make_qb(q0, q1, 0.f, h);
    for (int t = 0; t < NT; ++t) {
        const char* tb = kv + t * KVTILE_B;
        const bf16x8 ka = *(const bf16x8*)(tb + lane * 16); const s16x4 kb = *(const s16x4*)(tb + kboff);
        f32x16 s = MFMA32(ka, qa, zero); s = MFMA8(kb, qb0, s);
#pragma unroll
        for (int i = 0; i < 16; ++i) m = fmaxf(m, s[i]);
    }
    m = swap32_max(m);
    const s16x4 qb = make_qb(q0, q1, m, h);
    o = zero;
    for (int t = 0; t < NT; ++t) {
        const char* tb = kv + t * KVTILE_B;
        const bf16x8 ka = *(const bf16x8*)(tb + lane * 16); const s16x4 kb = *(const s16x4*)(tb + kboff);
        const bf16x8 va = *(const bf16x8*)(tb + voff), vb = *(const bf16x8*)(tb + voff + V_STEP);
        f32x16 s = MFMA32(ka, qa, zero); s = MFMA8(kb, qb, s);
        u32x4 p0, p1;
#pragma unroll
        for (int i = 0; i < 4; ++i) { p0[i] = cvtpk(__builtin_amdgcn_exp2f(s[2 * i]), __builtin_amdgcn_exp2f(s[2 * i + 1])); p1[i] = cvtpk(__builtin_amdgcn_exp2f(s[8 + 2 * i]), __builtin_amdgcn_exp2f(s[9 + 2 * i])); }
        o = MFMA32(va, __builtin_bit_cast(bf16x8, p0), o);
        o = MFMA32(vb, __builtin_bit_cast(bf16x8, p1), o);
    }
}

constexpr int SPIN_MAX = 1 << 20;
__global__ __launch_bounds__(512, 2) void gated_mix_mega(const unsigned short* __restrict__ x, const unsigned short* __restrict__ wqkv, const unsigned short* __restrict__ wo, const unsigned short* __restrict__ w1,
                                                         const unsigned short* __restrict__ w2, const unsigned short* __restrict__ gw, char* __restrict__ ws, unsigned short* __restrict__ out) {
    extern __shared__ __attribute__((aligned(16))) char lds[];
    const int tid = threadIdx.x, lane = tid & 63, wid = rfl(tid >> 6);
    const int bid = blockIdx.x, xcd = bid & 7, slot = bid >> 3;
    const int h = lane >> 5, r = lane & 31;
    const rsrc_t rs = ws_rsrc(ws);
    const unsigned lds0 = (unsigned)(uintptr_t)lds, lane16 = lane * 16;
    volatile LAS unsigned* ctl = (volatile LAS unsigned*)(lds + CTL_OFF);
    const int rt = 4 * (xcd >> 1) + (slot & 3), ci = slot >> 2, ct = 8 * (xcd & 1) + ci;
    const bool tile1 = ct < 15;
    const char* xbase = (const char*)x + (size_t)rt * 128 * 1280;
    const int k0 = (ci + 3 * (slot & 3)) % 10;
    const int head = xcd * 4 + (slot & 3), qblk = slot >> 2;
    const char* kv = ws + WS_KV + (size_t)head * NT * KVTILE_B;
    const int Tq = qblk * 8 + wid;
    const char* qt = ws + WS_Q + ((size_t)head * NT + Tq) * QTILE_B;
    bf16x8 qa; u32x2 qbr; f32x4 km4; float gate;
    auto q_loads = [&]() {
        asm volatile("s_nop 4\n\tglobal_load_dwordx4 %0, %1, %2" : "=v"(qa) : "v"(lane16), "s"(qt) : "memory");
        asm volatile("s_nop 4\n\tglobal_load_dwordx2 %0, %1, %2" : "=v"(qbr) : "v"((unsigned)(1024 + r * 8)), "s"(qt) : "memory");
        asm volatile("s_nop 4\n\tglobal_load_dwordx4 %0, %1, %2" : "=v"(km4) : "v"((unsigned)((lane < 40 ? lane : 0) * 16)), "s"(ws + WS_KMAX + (size_t)head * 640) : "memory");
        asm volatile("s_nop 4\n\tglobal_load_dword %0, %1, %2" : "=v"(gate) : "v"((unsigned)((Tq * 32 + r) * 4)), "s"(ws + WS_GATE) : "memory");
    };
    auto wait_seen = [&]() { for (int it = 0; it < SPIN_MAX; ++it) { if (ctl[1] != 0u) break; __builtin_amdgcn_s_sleep(1); } };
    auto gates = [&]() {
        const int aidx = (xcd >> 1) * 4 + (slot & 3);
        const u32x4* gr = (const u32x4*)gw + (lane & 7);
#pragma unroll 1
        for (int ps = 0; ps < 2; ++ps) {
            const int tok = aidx * 128 + wid * 16 + ps * 8 + (lane >> 3);
            const u32x4* xr = (const u32x4*)(x + (size_t)tok * DM) + (lane & 7);
            float sm = 0.f;
#pragma unroll
            for (int i = 0; i < 10; ++i) { const u32x4 xv = xr[i * 8], gv = gr[i * 8];
#pragma unroll
                for (int k = 0; k < 4; ++k) { const unsigned a_ = xv[k], b_ = gv[k]; sm = __builtin_amdgcn_fdot2_f32_bf16(__builtin_bit_cast(bf16x2_t, a_), __builtin_bit_cast(bf16x2_t, b_), sm, false); } }
            sm = dpp_add<0xB1>(sm); sm = dpp_add<0x4E>(sm); sm = dpp_add<0x141>(sm);
            const float lg = bf_round(sm);
            const float g = bf_round(__builtin_amdgcn_rcpf(1.f + __builtin_amdgcn_exp2f(lg * -1.4426950408889634f)));
            if ((lane & 7) == 0) st4f(ws + WS_GATE + tok * 4, g);
        }
    };
    GDma<true> d2; d2.init(xbase, (const char*)w1 + ct * 192, FF * 2, lds, lane, k0);
    STAMP(0, 0);
    if (wid < 4) {
        GDma<false> d1; d1.init(xbase, (const char*)wqkv + ct * 256, 3 * DM * 2, lds, lane, k0);
        if (tile1) {
#pragma unroll
            for (int jj = 0; jj < 4; ++jj) d1.xp(d1.kact(0), 0, 4 * wid + jj, jj & 1);
        }
        for (int pc = wid; pc < 10; pc += 4) rt_load(w2, wo, bid, lds0 + RT_OFF, pc, lane);
        if (!tile1) for (int pc = wid; pc < 10; pc += 4) rt_load(w2, wo, 256 + (xcd >> 1) * 4 + (slot & 3), lds0 + RT_OFF + 10240, pc, lane);
        if (wid == 0) {
            const u64 tg = flag_get(ws + WS_EPOCH) + 1ull;
            if (lane == 0) { ctl[0] = 0u; ctl[1] = 0u; ctl[2] = (unsigned)tg; ctl[3] = (unsigned)(tg >> 32); }
        }
        asm volatile("s_waitcnt vmcnt(0) lgkmcnt(0)\n\ts_barrier" ::: "memory");
        if (tile1) {
            const int wm = wid >> 1, wn = wid & 1;
            f32x16 acc[2][2] = {};
            auto nohook = [](int, bool) {};
            if (ct >= 10) {
                gemm_loop<false, true, 2, 2>(acc, lds, wid, lane, nohook);
                STAMP(0, 1);
#pragma unroll
                for (int nt = 0; nt < 2; ++nt) {
                    const int phi = (ct - 10) * 128 + wn * 64 + nt * 32 + r;
                    const int hd = (phi * 3277) >> 16, d = phi - hd * 20;
                    const unsigned dst = (unsigned)WS_KV + (unsigned)(hd * NT + rt * 4 + wm * 2) * KVTILE_B + V_OFF + (h * V_ROWS + d) * 16;
#pragma unroll
                    for (int mt = 0; mt < 2; ++mt)
#pragma unroll
                        for (int s = 0; s < 2; ++s) {
                            const f32x16& a = acc[mt][nt];
                            const u32x4 w = {cvtpk(a[8 * s], a[8 * s + 1]), cvtpk(a[8 * s + 2], a[8 * s + 3]), cvtpk(a[8 * s + 4], a[8 * s + 5]), cvtpk(a[8 * s + 6], a[8 * s + 7])};
                            st16(rs, dst + mt * KVTILE_B + s * V_STEP, w);
                        }
                }
                BARL();
            } else {
                gemm_loop<false, false, 2, 2>(acc, lds, wid, lane, nohook);
                STAMP(0, 1);
                const bool isQ = ct < 5;
                const float sc = isQ ? QSCALE : 1.f;
                char* sb = lds + (wm * 2 * 32 + wn * 16 + h) * 256 + r * 8;
                float nrm[8];
#pragma unroll
                for (int nt = 0; nt < 2; ++nt)
#pragma unroll
                    for (int g = 0; g < 4; ++g) {
                        float m = 0.f;
#pragma unroll
                        for (int mt = 0; mt < 2; ++mt) {
                            const f32x16& a = acc[mt][nt];
                            *(u32x2*)(sb + (mt * 32 + nt * 8 + 2 * g) * 256) = (u32x2){cvtpk(a[4 * g] * sc, a[4 * g + 1] * sc), cvtpk(a[4 * g + 2] * sc, a[4 * g + 3] * sc)};
                            m = fmaxf(m, __builtin_fmaf(a[4 * g], a[4 * g], a[4 * g + 1] * a[4 * g + 1]) + __builtin_fmaf(a[4 * g + 2], a[4 * g + 2], a[4 * g + 3] * a[4 * g + 3]));
                        }
                        nrm[nt * 4 + g] = m;
                    }
                if (!isQ) {
#pragma unroll
                    for (int i = 0; i < 8; ++i) { float m = nrm[i]; m = dpp_max<0xB1>(m); m = dpp_max<0x4E>(m); m = dpp_max<0x141>(m); m = dpp_max<0x140>(m);
                        auto rr = __builtin_amdgcn_permlane16_swap(__float_as_uint(m), __float_as_uint(m), false, false); nrm[i] = fmaxf(__uint_as_float(rr[0]), __uint_as_float(rr[1])); }
                    float mine = nrm[0];
#pragma unroll
                    for (int i = 1; i < 8; ++i) mine = r == i ? nrm[i] : mine;
                    if (r < 8) st4f(ws + WS_KMAX + (size_t)(((ct - 5) * 32 + wn * 16 + (r >> 2) * 8 + 2 * (r & 3) + h) * 32 + rt * 2 + wm) * 4, mine);
                }
                BARL();
                qk_copyout(rs, lds + wid * 8192, (unsigned)(isQ ? WS_Q : WS_KV), isQ ? QTILE_B : KVTILE_B, rt * 4 + wid, (isQ ? ct : ct - 5) * 32, lane);
            }
        } else { gates(); BARL(); }
        STAMP(0, 2);
        BARL();
        f32x16 acc2[1][3] = {};
        auto hook2 = [&](int kc, bool before) {
            if (kc == 2) { if (before) asm volatile("s_waitcnt vmcnt(0)" ::: "memory");
                           else if (tid == 0) flag_set(ws + WS_F1 + bid * 8, ((u64)ctl[3] << 32) | ctl[2]); }
        };
        gemm_loop<true, false, 1, 3>(acc2, lds, wid, lane, hook2);
        STAMP(0, 3);
        wait_seen();
        q_loads();
        {   const int tok = rt * 128 + wid * 32 + r, rho = tok >> 4;
#pragma unroll
            for (int nt = 0; nt < 3; ++nt) {
                const unsigned dst = (unsigned)WS_AF + (unsigned)(rho * NSIG + ct * 3 + nt) * 1024 + (h * 16 + (r & 15)) * 16;
                float sv[16];
#pragma unroll
                for (int i = 0; i < 16; ++i) { const float v = acc2[0][nt][i]; sv[i] = v * __builtin_amdgcn_rcpf(1.f + __builtin_amdgcn_exp2f(v * -1.4426950408889634f)); }
#pragma unroll
                for (int s = 0; s < 2; ++s) {
                    const u32x4 w = {cvtpk(sv[8 * s], sv[8 * s + 1]), cvtpk(sv[8 * s + 2], sv[8 * s + 3]), cvtpk(sv[8 * s + 4], sv[8 * s + 5]), cvtpk(sv[8 * s + 6], sv[8 * s + 7])};
                    st16(rs, dst + s * 512, w);
                }
            }
        }
        asm volatile("s_waitcnt vmcnt(6)" : "+v"(qa), "+v"(qbr), "+v"(km4), "+v"(gate) :: "memory");
    } else {
        const int lw = wid - 4;
        auto side_stores = [&]() {
            for (int i = lw; i < 10; i += 4) rt_store(rs, bid, lds + RT_OFF, i, lane);
            if (!tile1) for (int i = lw; i < 10; i += 4) rt_store(rs, 256 + (xcd >> 1) * 4 + (slot & 3), lds + RT_OFF + 10240, i, lane);
            if (lw == 0) { const int i = bid * 64 + lane;
                const unsigned f = (i & 1) ? 0u : ONES2; st16(rs, (unsigned)WS_KV + (unsigned)(i >> 3) * KVTILE_B + V_OFF + ((i >> 2) & 1) * V_STEP + (((i >> 1) & 1) * V_ROWS + 20 + (i & 1)) * 16, (u32x4){f, f, f, f}); }
        };
        if (tile1) {
            GDma<false> d1; d1.init(xbase, (const char*)wqkv + ct * 256, 3 * DM * 2, lds, lane, k0);
#pragma unroll
            for (int jj = 0; jj < 4; ++jj) d1.wp(d1.kact(0), 0, lw, jj);
            d1.issue(1, 1, lw);
            WAITB(8);
            d1.issue(2, 2, lw); d1.issue(3, 3, lw);
            for (int kc = 0; kc < 9; ++kc) {
                if (kc < 7) WAITB(16); else if (kc == 7) WAITB(15); else WAITB(14);
                if (kc + 4 < 10) d1.issue(kc + 4, kc & 3, lw);
                if (kc == 4) side_stores();
                if (kc >= 6) d2.issue(kc - 6, kc - 6, lw);
            }
            asm volatile("s_barrier" ::: "memory");
        } else {
            asm volatile("s_barrier" ::: "memory");
            side_stores(); gates();
            BARL();
            d2.issue(0, 0, lw); d2.issue(1, 1, lw); d2.issue(2, 2, lw);
        }
        WAITB(14);
        { int st = 0;
          for (int kc = 0; kc < 9; ++kc) {
              if (kc < 8) WAITB(7); else WAITB(0);
              if (kc + 3 < 10) d2.issue(kc + 3, st, lw);
              st = st == 2 ? 0 : st + 1;
          } }
        if (lw == 0) {
            const u64 tag = ((u64)ctl[3] << 32) | ctl[2];
            for (int it = 0; it < SPIN_MAX; ++it) {
                bool ok = true;
#pragma unroll
                for (int i = 0; i < 4; ++i) ok &= flag_get(ws + WS_F1 + (lane + 64 * i) * 8) == tag;
                if (__all(ok)) break;
                __builtin_amdgcn_s_sleep(2);
            }
            __builtin_amdgcn_fence(__ATOMIC_ACQUIRE, "agent");
            asm volatile("s_waitcnt vmcnt(0)" ::: "memory");
            if (lane == 0) { ctl[1] = 1u; if (bid == 0) flag_set(ws + WS_EPOCH, tag); }
        } else wait_seen();
        q_loads();
        for (int j = 0; j < 21; ++j) glds16(kv + (size_t)(lw + 4 * j) * 1024, lane16, lds0 + (lw + 4 * j) * 1024);
        asm volatile("s_waitcnt vmcnt(21)" : "+v"(qa), "+v"(qbr), "+v"(km4), "+v"(gate) :: "memory");
    }
    STAMP(1, 0);
    float km = fmaxf(fmaxf(km4[0], km4[1]), fmaxf(km4[2], km4[3]));
    km = dpp_max<0xB1>(km); km = dpp_max<0x4E>(km); km = dpp_max<0x141>(km);
    km = __builtin_amdgcn_sqrtf(km);
    float M;
    { const float c0 = rdl(km, 0), c1 = rdl(km, 8), c2 = rdl(km, 16), c3 = rdl(km, 24), c4 = rdl(km, 32);
      const u32x4 qw = __builtin_bit_cast(u32x4, qa);
      float n0, n1, n2;
      { const float a = bf_lo(qw[0]), b = bf_hi(qw[0]), c = bf_lo(qw[1]), d = bf_hi(qw[1]); n0 = (a * a + b * b) + (c * c + d * d); }
      { const float a = bf_lo(qw[2]), b = bf_hi(qw[2]), c = bf_lo(qw[3]), d = bf_hi(qw[3]); n1 = (a * a + b * b) + (c * c + d * d); }
      { const unsigned q0 = qbr[0], q1 = qbr[1]; const float a = bf_lo(q0), b = bf_hi(q0), c = bf_lo(q1), d = bf_hi(q1); n2 = (a * a + b * b) + (c * c + d * d); }
      float part = __builtin_amdgcn_sqrtf(n0) * (h ? c2 : c0) + __builtin_amdgcn_sqrtf(n1) * (h ? c3 : c1);
      if (!h) part += __builtin_amdgcn_sqrtf(n2) * c4;
      M = swap32_add(part) * 1.01f; }
    const s16x4 qb = make_qb(qbr[0], qbr[1], M, h);
    const int vrow = r <= 20 ? r : (r < 28 ? 21 : r - 12);
    const int voff = V_OFF + (h * V_ROWS + vrow) * 16, kboff = h ? V_OFF + 20 * 16 : 1024 + r * 8;
    lds_cptr kaC = (lds_cptr)lds + lane * 16, kbC = (lds_cptr)lds + kboff, vC = (lds_cptr)lds + voff;
    lds_cptr kaN = kaC + K2_BIG, kbN = kbC + K2_BIG, vN = vC + K2_BIG;
    const f32x16 zero = {};
    f32x16 o = {}, sA, sB;
    bf16x8 ka, va, vb; s16x4 kb;
    u32x4 plo, phi;
    if (wid >= 4) asm volatile("s_waitcnt vmcnt(15)" ::: "memory");
    asm volatile("s_barrier" ::: "memory");
    STAMP(1, 1);
    ka = lds128(kaC); kb = lds64(kbC);
    sA = MFMA32(ka, qa, zero); sA = MFMA8(kb, qb, sA);
    ka = lds128(kaC + KVTILE_B); kb = lds64(kbC + KVTILE_B);
    va = lds128(vC);
    vb = (bf16x8){}; phi = (u32x4){};
    f32x4 e3 = {};
    const int ng = (xcd & 1) * 4 + (slot & 3), bt = (xcd >> 1) * 8 + (slot >> 2);
    const char* ap = ws + WS_AF + (size_t)bt * 4 * NSIG * 1024;
    const char* bp = ws + WS_BF + (size_t)ng * NSIG * 5 * 1024;
    const int rot = ((slot >> 2) + 3 * (slot & 3)) % 6;
    auto hs = [&](int i) { int st = i + rot; if (st >= 6) st -= 6; return wid * 6 + st; };
    bf16x8 a[3][4], b[3][5];
    auto ldA1 = [&](int sg, int mt, bf16x8& d) { gld128_sc1(d, ap + ((size_t)mt * NSIG + sg) * 1024, lane16); };
    auto ldB1 = [&](int sg, int nt, bf16x8& d) { gld128(d, bp + sg * 5120 + nt * 1024, lane16); };
    const u64 tag = ((u64)ctl[3] << 32) | ctl[2];
#define EXP4(X, i) do { X[i] = __builtin_amdgcn_exp2f(X[i]); X[i + 1] = __builtin_amdgcn_exp2f(X[i + 1]); X[i + 2] = __builtin_amdgcn_exp2f(X[i + 2]); X[i + 3] = __builtin_amdgcn_exp2f(X[i + 3]); } while (0)
#define KADDR(A, i) (((i) < 16) ? (A##C + (i) * KVTILE_B) : (A##N + ((i) - 16) * KVTILE_B))
#define STEP(C, N, i, X0, X1) do { \
        bf16x8 ka2, va2, vb2; s16x4 kb2; \
        N = MFMA32(ka, qa, zero); SBAR(); \
        ka2 = lds128(KADDR(ka, (i) + 2)); \
        phi[2] = cvtpk(e3[0], e3[1]); phi[3] = cvtpk(e3[2], e3[3]); PIN(phi); SBAR(); EXP4(C, 0); PIN(C); SBAR(); \
        o = MFMA32(vb, __builtin_bit_cast(bf16x8, phi), o); SBAR(); \
        X0; \
        vb2 = lds128(vC + (i) * KVTILE_B + V_STEP); \
        plo[0] = cvtpk(C[0], C[1]); plo[1] = cvtpk(C[2], C[3]); PIN(plo); SBAR(); EXP4(C, 4); PIN(C); SBAR(); \
        N = MFMA8(kb, qb, N); SBAR(); \
        kb2 = lds64(KADDR(kb, (i) + 2)); \
        plo[2] = cvtpk(C[4], C[5]); plo[3] = cvtpk(C[6], C[7]); PIN(plo); SBAR(); EXP4(C, 8); PIN(C); SBAR(); \
        o = MFMA32(va, __builtin_bit_cast(bf16x8, plo), o); SBAR(); \
        X1; \
        va2 = lds128(KADDR(v, (i) + 1)); \
        phi[0] = cvtpk(C[8], C[9]); phi[1] = cvtpk(C[10], C[11]); PIN(phi); SBAR(); \
        e3[0] = __builtin_amdgcn_exp2f(C[12]); e3[1] = __builtin_amdgcn_exp2f(C[13]); e3[2] = __builtin_amdgcn_exp2f(C[14]); e3[3] = __builtin_amdgcn_exp2f(C[15]); PIN(e3); SBAR(); \
        ka = ka2; kb = kb2; va = va2; vb = vb2; \
    } while (0)
#define PIECE(j) do { if (8 * (j) + 7 < 42 || 8 * (j) + wid < 42) glds16(src + (j) * 8192, lane16, dst + (j) * 8192); } while (0)
    const char* src = kv + (84 + wid) * 1024; unsigned dst = lds0 + wid * 1024 + 2 * K2_BIG;
#pragma unroll 1
    for (int c = 0; c < 3; ++c) {
        STEP(sA, sB, 0, , ); STEP(sB, sA, 1, , ); STEP(sA, sB, 2, , ); STEP(sB, sA, 3, , ); STEP(sA, sB, 4, , );
        if (c == 0) { if (wid >= 4) asm volatile("s_waitcnt vmcnt(10)" ::: "memory"); asm volatile("s_barrier" ::: "memory"); }
        STEP(sB, sA, 5, , ); STEP(sA, sB, 6, , ); STEP(sB, sA, 7, , ); STEP(sA, sB, 8, , ); STEP(sB, sA, 9, , ); STEP(sA, sB, 10, , ); STEP(sB, sA, 11, , ); STEP(sA, sB, 12, , );
        if (c == 2 && wid == 0) {
            for (int it = 0; it < SPIN_MAX; ++it) {
                if (__all(flag_get(ws + WS_F2 + ((bt >> 1) * 16 + (lane & 15)) * 8) == tag)) break;
                __builtin_amdgcn_s_sleep(2);
            }
        }
        WAITB(0);
        if (c == 0 && tid == 0) flag_set(ws + WS_F2 + (rt * 16 + ct) * 8, tag);
        if (c < 2) { STEP(sB, sA, 13, PIECE(0), PIECE(1)); STEP(sA, sB, 14, PIECE(2), PIECE(3)); STEP(sB, sA, 15, PIECE(4), PIECE(5)); }
        else { STEP(sB, sA, 13, , ); STEP(sA, sB, 14, , ); STEP(sB, sA, 15, , ); }
        const int d = (c == 1) ? -2 * K2_BIG : K2_BIG;
        kaC = kaN; kbC = kbN; vC = vN; kaN += d; kbN += d; vN += d;
        src += 42 * 1024; dst -= 2 * K2_BIG;
    }
#define PA(s, m) ldA1(hs(s), m, a[s][m])
#define PB(s, n) ldB1(hs(s), n, b[s][n])
    STEP(sA, sB, 0, PA(0, 0), PA(0, 1)); STEP(sB, sA, 1, PA(0, 2), PA(0, 3)); STEP(sA, sB, 2, PB(0, 0), PB(0, 1)); STEP(sB, sA, 3, PB(0, 2), PB(0, 3));
    STEP(sA, sB, 4, PB(0, 4), PA(1, 0)); STEP(sB, sA, 5, PA(1, 1), PA(1, 2)); STEP(sA, sB, 6, PA(1, 3), PB(1, 0)); STEP(sB, sA, 7, PB(1, 1), PB(1, 2));
    STEP(sA, sB, 8, PB(1, 3), PB(1, 4)); STEP(sB, sA, 9, PA(2, 0), PA(2, 1)); STEP(sA, sB, 10, PA(2, 2), PA(2, 3)); STEP(sB, sA, 11, PB(2, 0), PB(2, 1));
    STEP(sA, sB, 12, PB(2, 2), PB(2, 3)); STEP(sB, sA, 13, PB(2, 4), ); STEP(sA, sB, 14, , ); STEP(sB, sA, 15, , );
    phi[2] = cvtpk(e3[0], e3[1]); phi[3] = cvtpk(e3[2], e3[3]);
    o = MFMA32(vb, __builtin_bit_cast(bf16x8, phi), o);
    STAMP(1, 2);
    float l; { auto rr = __builtin_amdgcn_permlane32_swap(__float_as_uint(o[8]), __float_as_uint(o[8]), false, false); l = __uint_as_float(rr[1]); }
    if (__builtin_expect(__any(!(l >= 8.6736174e-19f && l <= 1.0e30f)), 0)) {
        asm volatile("s_waitcnt vmcnt(0)" ::: "memory");
        attn_fallback(o, kv, qa, qbr[0], qbr[1], lane, kboff, voff);
        auto rr = __builtin_amdgcn_permlane32_swap(__float_as_uint(o[8]), __float_as_uint(o[8]), false, false); l = __uint_as_float(rr[1]);
    }
    const float f = gate * __builtin_amdgcn_rcpf(l);
    const int tok = Tq * 32 + r;
    char* ab = ws + WS_AF + (size_t)(tok >> 4) * NSIG * 1024 + (tok & 15) * 16;
#pragma unroll
    for (int g = 0; g < 3; ++g) {
        const int gam = head * 5 + 2 * g + h;
        const int sig = 48 + (gam >> 3), c = ((gam >> 2) & 1) * 2 + (gam & 1), half = (gam >> 1) & 1;
        const u32x2 w = {cvtpk(o[4 * g] * f, o[4 * g + 1] * f), cvtpk(o[4 * g + 2] * f, o[4 * g + 3] * f)};
        if (g < 2 || h == 0) __hip_atomic_store((u64*)(ab + sig * 1024 + c * 256 + half * 8), ((u64)w[1] << 32) | w[0], __ATOMIC_RELAXED, __HIP_MEMORY_SCOPE_AGENT);
    }
    STAMP(1, 3);
    {
        f32x4 acc[4][5] = {};
        auto loadA = [&](int sg, bf16x8 (&da)[4]) {
#pragma unroll
            for (int mt = 0; mt < 4; ++mt) gld128_sc1(da[mt], ap + ((size_t)mt * NSIG + sg) * 1024, lane16);
        };
        auto loadB = [&](int sg, bf16x8 (&db)[5]) {
#pragma unroll
            for (int nt = 0; nt < 5; ++nt) gld128(db[nt], bp + sg * 5120 + nt * 1024, lane16);
        };
        auto mma = [&](bf16x8 (&da)[4], bf16x8 (&db)[5]) {
            SBAR();
#pragma unroll
            for (int nt = 0; nt < 5; ++nt)
#pragma unroll
                for (int mt = 0; mt < 4; ++mt) acc[mt][nt] = MFMA16(db[nt], da[mt], acc[mt][nt]);
            SBAR();
        };
        wait9<3>(a[0], b[0]);
        asm volatile("" : "+v"(a[1][0]), "+v"(a[1][1]), "+v"(a[1][2]), "+v"(a[1][3]), "+v"(b[1][0]), "+v"(b[1][1]), "+v"(b[1][2]), "+v"(b[1][3]), "+v"(b[1][4]));
        asm volatile("" : "+v"(a[2][0]), "+v"(a[2][1]), "+v"(a[2][2]), "+v"(a[2][3]), "+v"(b[2][0]), "+v"(b[2][1]), "+v"(b[2][2]), "+v"(b[2][3]), "+v"(b[2][4]));
        mma(a[0], b[0]); loadA(hs(3), a[0]); loadB(hs(3), b[0]);
        mma(a[1], b[1]); loadA(hs(4), a[1]); loadB(hs(4), b[1]);
        asm volatile("s_waitcnt vmcnt(18)" ::: "memory");
        if (lane == 0) {
            const unsigned old = __hip_atomic_fetch_add((unsigned*)(lds + CTL_OFF), 1u, __ATOMIC_RELAXED, __HIP_MEMORY_SCOPE_WORKGROUP);
            if (old == 7u) flag_set(ws + WS_F3 + (qblk * 32 + head) * 8, tag);
        }
        mma(a[2], b[2]); loadA(hs(5), a[2]); loadB(hs(5), b[2]);
        float og[4];
#pragma unroll
        for (int mt = 0; mt < 4; ++mt) asm volatile("s_nop 4\n\tglobal_load_dword %0, %1, %2" : "=v"(og[mt]) : "v"((unsigned)((bt * 64 + mt * 16 + (lane & 15)) * 4)), "s"(ws + WS_GATE) : "memory");
        wait9<22>(a[0], b[0]); mma(a[0], b[0]); loadB(48 + wid, b[0]);
        wait9<18>(a[1], b[1]); mma(a[1], b[1]); loadB(56 + wid, b[1]);
        wait9<14>(a[2], b[2]); mma(a[2], b[2]);
        if (wid < 4) {
#pragma unroll
            for (int nt = 0; nt < 5; ++nt) gld128_rw(b[2][nt], bp + (64 + wid) * 5120 + nt * 1024, lane16);
        }
        STAMP(2, 0);
        asm volatile("s_waitcnt vmcnt(0)" : "+v"(og[0]), "+v"(og[1]), "+v"(og[2]), "+v"(og[3]) :: "memory");
#pragma unroll
        for (int mt = 0; mt < 4; ++mt) { const float f1 = bf_round(1.f - og[mt]);
#pragma unroll
            for (int nt = 0; nt < 5; ++nt) acc[mt][nt] *= f1; }
        if (wid == 0) {
            for (int it = 0; it < SPIN_MAX; ++it) {
                if (__all(flag_get(ws + WS_F3 + ((bt >> 2) * 32 + r) * 8) == tag)) break;
                __builtin_amdgcn_s_sleep(2);
            }
        }
        asm volatile("s_barrier" ::: "memory");
        STAMP(2, 1);
        loadA(48 + wid, a[0]); loadA(56 + wid, a[1]);
        a[2][0] = (bf16x8){}; a[2][1] = (bf16x8){}; a[2][2] = (bf16x8){}; a[2][3] = (bf16x8){};
        if (wid < 4) {
#pragma unroll
            for (int mt = 0; mt < 4; ++mt) gld128_sc1_rw(a[2][mt], ap + ((size_t)mt * NSIG + 64 + wid) * 1024, lane16);
        }
        wait9<4>(a[0], b[0]); mma(a[0], b[0]);
        wait9<0>(a[1], b[1]); wait9<0>(a[2], b[2]); mma(a[1], b[1]); mma(a[2], b[2]);
        STAMP(2, 2);
#pragma unroll
        for (int mt = 0; mt < 4; ++mt)
#pragma unroll
            for (int nt = 0; nt < 5; ++nt) *(f32x4*)(lds + (wid * 20 + mt * 5 + nt) * 1024 + lane16) = acc[mt][nt];
        BARL();
#pragma unroll
        for (int i = 0; i < 3; ++i) { const int t = wid + 8 * i;
            if (t < 20) {
                f32x4 sum = *(const f32x4*)(lds + t * 1024 + lane16);
#pragma unroll
                for (int w = 1; w < 8; ++w) sum += *(const f32x4*)(lds + (w * 20 + t) * 1024 + lane16);
                const int m4 = t / 5, nt = t - m4 * 5;
                const int tok2 = bt * 64 + m4 * 16 + (lane & 15);
                *(u32x2*)(out + (size_t)tok2 * DM + ng * 80 + nt * 16 + (lane >> 4) * 4) = (u32x2){cvtpk(sum[0], sum[1]), cvtpk(sum[2], sum[3])};
            } }
        STAMP(2, 3);
    }
}

extern "C" void kernel_launch(void* const* d_in, const int* in_sizes, int n_in, void* d_out, int out_size, void* d_ws, size_t ws_size, hipStream_t stream) {
    const unsigned short* x = (const unsigned short*)d_in[0];
    const unsigned short* wqkv = (const unsigned short*)d_in[1];
    const unsigned short* wo = (const unsigned short*)d_in[2];
    const unsigned short* w1 = (const unsigned short*)d_in[4];
    const unsigned short* w2 = (const unsigned short*)d_in[5];
    const unsigned short* gw = (const unsigned short*)d_in[6];
    (void)hipFuncSetAttribute((const void*)gated_mix_mega, hipFuncAttributeMaxDynamicSharedMemorySize, LDS_ALL);
    gated_mix_mega<<<256, 512, LDS_ALL, stream>>>(x, wqkv, wo, w1, w2, gw, (char*)d_ws, (unsigned short*)d_out);
}
```

```cpp
#include <hip/hip_runtime.h>
#include <hip/hip_bf16.h>
#include <stdint.h>

typedef short bf16x8 __attribute__((ext_vector_type(8)));
typedef short s16x4 __attribute__((ext_vector_type(4)));
typedef float f32x16 __attribute__((ext_vector_type(16)));
typedef float f32x4 __attribute__((ext_vector_type(4)));
typedef float f32x2 __attribute__((ext_vector_type(2)));
typedef unsigned u32x4 __attribute__((ext_vector_type(4)));
typedef unsigned u32x2 __attribute__((ext_vector_type(2)));
typedef __bf16 bf16x2_t __attribute__((ext_vector_type(2)));
#define LAS __attribute__((address_space(3)))
typedef LAS const char* lds_cptr;
#define DI __device__ __forceinline__
#define SBAR() __builtin_amdgcn_sched_barrier(0)
#define PIN(x) asm volatile("" : "+v"(x))
#define MFMA32(a, b, c) __builtin_amdgcn_mfma_f32_32x32x16_bf16(a, b, c, 0, 0, 0)
#define MFMA16(a, b, c) __builtin_amdgcn_mfma_f32_16x16x32_bf16(a, b, c, 0, 0, 0)
#define MFMA8(a, b, c) __builtin_amdgcn_mfma_f32_32x32x8bf16_1k(a, b, c, 0, 0, 0)

constexpr int S = 2048, DM = 640, NH = 32, DH = 20, FF = 1536;
constexpr int NT = S / 32;
constexpr int QTILE_B = 1280;
constexpr int KVTILE_B = 2688;
constexpr int V_OFF = 1280, V_STEP = 704, V_ROWS = 22;
constexpr int NSIG = 68;
constexpr size_t WS_Q = 0;
constexpr size_t WS_KV = WS_Q + (size_t)NH * NT * QTILE_B;
constexpr size_t WS_AF = WS_KV + (size_t)NH * NT * KVTILE_B;
constexpr size_t WS_BF = WS_AF + (size_t)128 * NSIG * 1024;
constexpr size_t WS_KMAX = WS_BF + (size_t)8 * NSIG * 5 * 1024;
constexpr size_t WS_GATE = WS_KMAX + (size_t)160 * 32 * 4;
constexpr size_t WS_CNT = WS_GATE + (size_t)S * 8;
constexpr size_t WS_END = WS_CNT + 8 * 256;
constexpr float QSCALE = 0.22360679774997896f * 1.4426950408889634f;
constexpr unsigned ONES2 = 0x3F803F80u;

DI unsigned cvtpk(float lo, float hi) { f32x2 v = {lo, hi}; bf16x2_t b = __builtin_convertvector(v, bf16x2_t); return __builtin_bit_cast(unsigned, b); }
DI float bf_lo(unsigned u) { return __uint_as_float(u << 16); }
DI float bf_hi(unsigned u) { return __uint_as_float(u & 0xffff0000u); }
DI float bf_round(float x) { return bf_lo(cvtpk(x, x)); }
DI void glds16(const void* sbase, unsigned voff, unsigned lds_base) {
    unsigned sv;
    asm volatile("s_mov_b32 %0, m0\n\ts_mov_b32 m0, %3\n\ts_nop 0\n\tglobal_load_lds_dwordx4 %1, %2\n\ts_mov_b32 m0, %0"
                 : "=&s"(sv) : "v"(voff), "s"(sbase), "s"(lds_base) : "memory");
}
typedef short v4i16_t __attribute__((ext_vector_type(4)));
DI s16x4 ldstr(lds_cptr p) { return __builtin_bit_cast(s16x4, __builtin_amdgcn_ds_read_tr16_b64_v4i16((LAS v4i16_t*)p)); }
DI bf16x8 lds128(lds_cptr p) { return *(const LAS bf16x8*)p; }
DI s16x4 lds64(lds_cptr p) { return *(const LAS s16x4*)p; }
DI unsigned rfl(unsigned x) { return __builtin_amdgcn_readfirstlane(x); }
#define STAMP(k, i) do { } while (0)

constexpr int K1_LDS = 157696;

DI float rdl(float x, int l) { return __uint_as_float(__builtin_amdgcn_readlane(__float_as_uint(x), l)); }
#define WT 1
typedef __amdgpu_buffer_rsrc_t rsrc_t;
DI rsrc_t ws_rsrc(char* ws) { return __builtin_amdgcn_make_buffer_rsrc(ws, 0, (int)WS_END, 0x00020000); }
DI void st16(rsrc_t rs, unsigned off, u32x4 v) { __builtin_amdgcn_raw_buffer_store_b128(v, rs, off, 0, WT ? 16 : 0); }
DI void st8(rsrc_t rs, unsigned off, u32x2 v) { __builtin_amdgcn_raw_buffer_store_b64(v, rs, off, 0, WT ? 16 : 0); }
template <int CTRL> DI float dpp_max(float x) { const float y = __uint_as_float(__builtin_amdgcn_update_dpp(0u, __float_as_uint(x), CTRL, 0xf, 0xf, true)); return fmaxf(x, y); }
template <int CTRL> DI float dpp_add(float x) { const float y = __uint_as_float(__builtin_amdgcn_update_dpp(0u, __float_as_uint(x), CTRL, 0xf, 0xf, true)); return x + y; }

template <bool N96>
struct K1Dma {
    static constexpr int NW = N96 ? 3 : 4;
    static constexpr int STG = N96 ? 45056 : 49152;
    const char* xbase; const char* wbase; int ldw2, k0; unsigned lds0, xoff0, xoff1, woff[3];
    DI int kact(int kc) const { kc += k0; return kc >= 10 ? kc - 10 : kc; }
    DI void init(const char* xb, const char* wb, int ld, char* lds, int lane, int k0_) {
        xbase = xb; wbase = wb; ldw2 = ld; k0 = k0_; lds0 = (unsigned)(uintptr_t)lds;
        const int rr = lane >> 3;
        xoff0 = rr * 1280 + (((lane & 7) ^ ((lane >> 4) & 7)) << 4); xoff1 = rr * 1280 + (((lane & 7) ^ ((4 + (lane >> 4)) & 7)) << 4);
        if (N96) {
#pragma unroll
            for (int p = 0; p < 3; ++p) { const int o = 1024 * p + 16 * lane, row = (o * 21846) >> 22, col = o - row * 192; woff[p] = row * ldw2 + col; }
        } else woff[0] = (lane >> 4) * ldw2 + ((((lane & 15) ^ ((lane >> 4) << 2))) << 4);
    }
    DI void xp(int kc, int st, int j, int par) const { glds16(xbase + (size_t)j * 8 * 1280 + kc * 128, par ? xoff1 : xoff0, lds0 + st * STG + j * 1024); }
    DI void wp(int kc, int st, int lw, int jj) const {
        if (N96) glds16(wbase + (size_t)(kc * 64 + 16 * lw) * ldw2, woff[jj], lds0 + st * STG + 32768 + (3 * lw + jj) * 1024);
        else glds16(wbase + (size_t)(kc * 64 + 16 * lw + 4 * jj) * ldw2, woff[0], lds0 + st * STG + 32768 + (4 * lw + jj) * 1024);
    }
    DI void first(int wid) const {
        const int kc = kact(0);
        if (wid >= 4) {
#pragma unroll
            for (int jj = 0; jj < NW; ++jj) wp(kc, 0, wid - 4, jj);
#pragma unroll
            for (int jj = 0; jj < 2; ++jj) xp(kc, 0, 2 * (wid - 4) + jj, jj & 1);
        } else {
#pragma unroll
            for (int jj = 0; jj < 6; ++jj) xp(kc, 0, 8 + 6 * wid + jj, jj & 1);
        }
    }
    DI void loader(int lw, char* g_ws) const {
        auto issue = [&](int kc_, int st) {
            const int kc = kact(kc_);
#pragma unroll
            for (int jj = 0; jj < NW; ++jj) wp(kc, st, lw, jj);
#pragma unroll
            for (int jj = 0; jj < 8; ++jj) xp(kc, st, 8 * lw + jj, jj & 1);
        };
        issue(1, 1);
        if (N96) asm volatile("s_waitcnt vmcnt(11)\n\ts_barrier" ::: "memory"); else asm volatile("s_waitcnt vmcnt(12)\n\ts_barrier" ::: "memory");
        issue(2, 2);
        int st = 0;
        for (int kc = 0; kc < 9; ++kc) {
            if (kc == 8) asm volatile("s_waitcnt vmcnt(0)\n\ts_barrier" ::: "memory");
            else if (N96) asm volatile("s_waitcnt vmcnt(11)\n\ts_barrier" ::: "memory");
            else asm volatile("s_waitcnt vmcnt(12)\n\ts_barrier" ::: "memory");
            if (kc + 3 < 10) issue(kc + 3, st);
            st = st == 2 ? 0 : st + 1;
        }
    }
};

DI void rt_load(const unsigned short* w2, const unsigned short* wo, int u, unsigned ldsb, int piece, int lane) {
    const int sig = u >> 2, s = (u >> 1) & 1, cb = u & 1;
    const char* src = (sig < 48 ? (const char*)w2 + (size_t)sig * 32 * 1280 : (const char*)wo + (size_t)(sig - 48) * 32 * 1280) + s * 16 * 1280 + cb * 640;
    const int o = piece * 1024 + lane * 16, row = (o * 6554) >> 22;
    glds16(src, row * 1280 + (o - row * 640), ldsb + piece * 1024);
}
DI void rt_store(rsrc_t rs, int u, const char* ldsb, int i, int lane) {
    const int sig = u >> 2, s = (u >> 1) & 1, cb = u & 1;
    const int nl = 2 * i + (lane >> 5), hh = (lane >> 4) & 1, q = (lane & 15) >> 2, p = lane & 3;
    lds_cptr a = (lds_cptr)ldsb + (4 * hh + q) * 640 + (16 * nl + 4 * p) * 2;
    const s16x4 lo = ldstr(a), hi = ldstr(a + 8 * 640);
    const bf16x8 f = __builtin_shufflevector(lo, hi, 0, 1, 2, 3, 4, 5, 6, 7);
    const int nu = cb * 20 + nl, ng = (nu * 205) >> 10;
    st16(rs, (unsigned)WS_BF + (unsigned)((ng * NSIG + sig) * 5 + (nu - 5 * ng)) * 1024 + (s * 32 + (lane & 31)) * 16, __builtin_bit_cast(u32x4, f));
}
template <bool N96, int VMASK, int MT, int NTL>
DI void k1_mainloop(f32x16 (&acc)[MT][NTL], char* lds, int wid, int lane) {
    const int h = lane >> 5, r = lane & 31;
    constexpr int STG = N96 ? 45056 : 49152;
    unsigned xa[4];
    { const int row = (N96 ? wid * 64 : (wid >> 1) * 128) + r; const int sw = (row >> 1) & 7;
#pragma unroll
      for (int ks = 0; ks < 4; ++ks) xa[ks] = row * 128 + (((2 * ks + h) ^ sw) << 4); }
    unsigned wa[2];
    { const int blk = (lane >> 4) & 1, q = (lane & 15) >> 2, p = lane & 3;
      if (N96) wa[0] = wa[1] = 32768 + (8 * h + q) * 192 + blk * 32 + p * 8;
      else {
#pragma unroll
          for (int nt = 0; nt < 2; ++nt) { const int ch = (wid & 1) * 8 + nt * 4 + 2 * blk + (p >> 1); wa[nt] = 32768 + (8 * h + q) * 256 + ((ch ^ (q << 2)) << 4) + 8 * (p & 1); } } }
    bf16x8 xf[2][MT], wf[2][NTL];
    auto rdw = [&](int buf, int nt, lds_cptr sb, int ks) {
        lds_cptr a = N96 ? sb + wa[0] + ks * 16 * 192 + nt * 64 : sb + wa[nt & 1] + ks * 4096;
        const s16x4 lo = ldstr(a), hi = ldstr(a + (N96 ? 4 * 192 : 1024));
        wf[buf][nt] = __builtin_shufflevector(lo, hi, 0, 1, 2, 3, 4, 5, 6, 7);
    };
    auto rdx = [&](int buf, int mt, lds_cptr sb, int ks) { xf[buf][mt] = lds128(sb + xa[ks] + mt * 4096); };
    auto mm = [&](int buf, int mt, int nt) { acc[mt][nt] = ((VMASK >> nt) & 1) ? MFMA32(xf[buf][mt], wf[buf][nt], acc[mt][nt]) : MFMA32(wf[buf][nt], xf[buf][mt], acc[mt][nt]); };
    asm volatile("s_waitcnt vmcnt(0)\n\ts_barrier" ::: "memory");
    { lds_cptr sb = (lds_cptr)lds; rdw(0, 0, sb, 0); rdx(0, 0, sb, 0); rdw(0, 1, sb, 0); rdx(0, 1, sb, 0);
      if (N96) rdw(0, 2 % NTL, sb, 0); else { rdx(0, 2 % MT, sb, 0); rdx(0, 3 % MT, sb, 0); } }
    int st = 0;
#define K1_KSTEP(ks, NEXT) do { const int b = (ks) & 1, n = b ^ 1; lds_cptr np = (ks) < 3 ? sb : sn; const int nk = ((ks) + 1) & 3; \
        SBAR(); \
        if constexpr (N96) { \
            mm(b, 0, 0); SBAR(); if (NEXT) { rdw(n, 0, np, nk); } SBAR(); \
            mm(b, 0, 1); SBAR(); if (NEXT) { rdx(n, 0, np, nk); } SBAR(); \
            mm(b, 1, 0); SBAR(); if (NEXT) { rdw(n, 1, np, nk); } SBAR(); \
            mm(b, 1, 1); SBAR(); if (NEXT) { rdx(n, 1, np, nk); } SBAR(); \
            mm(b, 0, 2 % NTL); SBAR(); if (NEXT) { rdw(n, 2 % NTL, np, nk); } SBAR(); \
            mm(b, 1, 2 % NTL); SBAR(); \
        } else { \
            mm(b, 0, 0); SBAR(); if (NEXT) { rdw(n, 0, np, nk); } SBAR(); \
            mm(b, 0, 1); SBAR(); if (NEXT) { rdx(n, 0, np, nk); } SBAR(); \
            mm(b, 1, 0); SBAR(); if (NEXT) { rdw(n, 1, np, nk); } SBAR(); \
            mm(b, 1, 1); SBAR(); if (NEXT) { rdx(n, 1, np, nk); } SBAR(); \
            mm(b, 2 % MT, 0); SBAR(); if (NEXT) { rdx(n, 2 % MT, np, nk); } SBAR(); \
            mm(b, 2 % MT, 1); SBAR(); if (NEXT) { rdx(n, 3 % MT, np, nk); } SBAR(); \
            mm(b, 3 % MT, 0); SBAR(); \
            mm(b, 3 % MT, 1); SBAR(); \
        } } while (0)
#pragma unroll 1
    for (int kc = 0; kc < 9; ++kc) {
        lds_cptr sb = (lds_cptr)lds + st * STG;
        st = st == 2 ? 0 : st + 1;
        lds_cptr sn = (lds_cptr)lds + st * STG;
        K1_KSTEP(0, true); K1_KSTEP(1, true); K1_KSTEP(2, true);
        asm volatile("s_waitcnt lgkmcnt(0)\n\ts_barrier" ::: "memory");
        K1_KSTEP(3, true);
    }
    { lds_cptr sb = (lds_cptr)lds + st * STG; lds_cptr sn = sb;
      K1_KSTEP(0, true); K1_KSTEP(1, true); K1_KSTEP(2, true); K1_KSTEP(3, false); }
}

template <int VMASK>
DI void qkv_epilogue(f32x16 (&acc)[2][3], int j, int rt, int wid, int lane, char* lds, char* ws, rsrc_t rs) {
    const int h = lane >> 5, r = lane & 31;
    char* sb = lds + 45056 + (wid * 2 * 24 + h) * 256 + r * 8;
#pragma unroll
    for (int nt = 0; nt < 3; ++nt) {
        const int u = 3 * j + nt;
        if ((VMASK >> nt) & 1) {
            const int phi = (u - 40) * 32 + r, head = (phi * 3277) >> 16, d = phi - head * 20;
            const unsigned dst = (unsigned)WS_KV + (unsigned)(head * NT + rt * 8 + wid * 2) * KVTILE_B + V_OFF + (h * V_ROWS + d) * 16;
#pragma unroll
            for (int mt = 0; mt < 2; ++mt)
#pragma unroll
                for (int s = 0; s < 2; ++s) {
                    const f32x16& a = acc[mt][nt];
                    const u32x4 w = {cvtpk(a[8 * s], a[8 * s + 1]), cvtpk(a[8 * s + 2], a[8 * s + 3]), cvtpk(a[8 * s + 4], a[8 * s + 5]), cvtpk(a[8 * s + 6], a[8 * s + 7])};
                    st16(rs, dst + mt * KVTILE_B + s * V_STEP, w);
                }
        } else {
            const float sc = u < 20 ? QSCALE : 1.f;
            float nrm[4];
#pragma unroll
            for (int g = 0; g < 4; ++g) {
                float m = 0.f;
#pragma unroll
                for (int mt = 0; mt < 2; ++mt) {
                    const f32x16& a = acc[mt][nt];
                    *(u32x2*)(sb + (mt * 24 + nt * 8 + 2 * g) * 256) = (u32x2){cvtpk(a[4 * g] * sc, a[4 * g + 1] * sc), cvtpk(a[4 * g + 2] * sc, a[4 * g + 3] * sc)};
                    m = fmaxf(m, __builtin_fmaf(a[4 * g], a[4 * g], a[4 * g + 1] * a[4 * g + 1]) + __builtin_fmaf(a[4 * g + 2], a[4 * g + 2], a[4 * g + 3] * a[4 * g + 3]));
                }
                nrm[g] = m;
            }
            if (u >= 20) {
#pragma unroll
                for (int g = 0; g < 4; ++g) { float m = nrm[g]; m = dpp_max<0xB1>(m); m = dpp_max<0x4E>(m); m = dpp_max<0x141>(m); m = dpp_max<0x140>(m);
                    auto rr = __builtin_amdgcn_permlane16_swap(__float_as_uint(m), __float_as_uint(m), false, false); nrm[g] = fmaxf(__uint_as_float(rr[0]), __uint_as_float(rr[1])); }
                const float mine = r == 0 ? nrm[0] : r == 1 ? nrm[1] : r == 2 ? nrm[2] : nrm[3];
                if (r < 4) ((float*)(ws + WS_KMAX))[((u - 20) * 8 + 2 * r + h) * 32 + rt * 4 + wid] = mine;
            }
        }
    }
}
DI void qk_copyout(rsrc_t rs, const char* lb, unsigned img, unsigned tileb, int T, int G0, int n, int L0, int lane) {
    const int h = lane >> 5, r = lane & 31;
    const int hd0 = (G0 * 205) >> 10, id0 = 3 * hd0 + ((G0 - 5 * hd0) >> 1);
    u32x2 pa[8], pb[8];
#pragma unroll
    for (int it = 0; it < 8; ++it) {
        const int id = id0 + 2 * it + h, head = (id * 171) >> 9, ty = id - 3 * head, la = 5 * head + 2 * ty - G0;
        const int lc = la < 0 ? 0 : (la >= n ? n - 1 : la), ld = la + 1 >= n ? n - 1 : la + 1;
        const int aA = (L0 + lc) * 256 + (ty < 2 ? r * 8 : (r & 15) * 16);
        const int aB = ty < 2 ? (L0 + ld) * 256 + r * 8 : aA + 8;
        pa[it] = *(const u32x2*)(lb + aA); pb[it] = *(const u32x2*)(lb + aB);
    }
#pragma unroll
    for (int it = 0; it < 8; ++it) {
        const int id = id0 + 2 * it + h, head = (id * 171) >> 9, ty = id - 3 * head, la = 5 * head + 2 * ty - G0;
        const unsigned dst = img + (unsigned)(head * NT + T) * tileb + ty * 512 + r * 16;
        const bool okA = (unsigned)la < (unsigned)n, okB = ty == 2 ? okA : (unsigned)(la + 1) < (unsigned)n;
        if (okA && okB) { if (ty < 2 || r < 16) st16(rs, dst, (u32x4){pa[it][0], pa[it][1], pb[it][0], pb[it][1]}); }
        else if (okA) st8(rs, dst, pa[it]);
        else if (okB) st8(rs, dst + 8, pb[it]);
    }
}

__global__ __launch_bounds__(512, 2) void k1_proj(const unsigned short* __restrict__ x, const unsigned short* __restrict__ wqkv, const unsigned short* __restrict__ wo,
                                                  const unsigned short* __restrict__ w1, const unsigned short* __restrict__ w2, const unsigned short* __restrict__ gw, char* __restrict__ ws) {
    extern __shared__ __attribute__((aligned(16))) char lds[];
    const int tid = threadIdx.x, lane = tid & 63, wid = rfl(tid >> 6);
    const int bid = blockIdx.x, xcd = bid & 7, slot = bid >> 3;
    const int rgrp = xcd >> 2, cgrp = xcd & 3;
    const int h = lane >> 5, r = lane & 31;
    const rsrc_t rs = ws_rsrc(ws);
    const int rt = 4 * rgrp + (slot & 3), ci = slot >> 2;
    const char* xbase = (const char*)x + (size_t)rt * 256 * 1280;
    const unsigned lds0 = (unsigned)(uintptr_t)lds;
    const int k0 = (ci + 3 * (slot & 3)) % 10;
    const bool isH = ci >= 5;
    const unsigned rtl = isH ? 3 * 49152 : 3 * 45056;
    const int u2 = (!isH && rgrp == 0 && ci == 0) ? 256 + cgrp * 4 + (slot & 3) : -1;
    STAMP(0, 0);
    auto side_start = [&]() {
        for (int pc = wid; pc < 10; pc += 4) rt_load(w2, wo, bid, lds0 + rtl, pc, lane);
        if (u2 >= 0) for (int pc = wid; pc < 10; pc += 4) rt_load(w2, wo, u2, lds0 + rtl + 10240, pc, lane);
        const unsigned* gr = (const unsigned*)gw + lane;
#pragma unroll
        for (int t = 0; t < 2; ++t) {
            const int tok = bid * 8 + wid * 2 + t;
            const unsigned* xr = (const unsigned*)(x + (size_t)tok * DM) + lane;
            float sm = 0.f;
#pragma unroll
            for (int i = 0; i < 5; ++i) { const unsigned a_ = xr[64 * i], b_ = gr[64 * i]; sm = __builtin_amdgcn_fdot2_f32_bf16(__builtin_bit_cast(bf16x2_t, a_), __builtin_bit_cast(bf16x2_t, b_), sm, false); }
            sm = dpp_add<0xB1>(sm); sm = dpp_add<0x4E>(sm); sm = dpp_add<0x141>(sm); sm = dpp_add<0x140>(sm);
            sm = (rdl(sm, 0) + rdl(sm, 16)) + (rdl(sm, 32) + rdl(sm, 48));
            const float lg = bf_round(sm);
            const float g = bf_round(__builtin_amdgcn_rcpf(1.f + __builtin_amdgcn_exp2f(lg * -1.4426950408889634f)));
            if (lane == 0) *(float*)(ws + WS_GATE + tok * 4) = g;
        }
    };
    auto side_end = [&]() {
        for (int i = wid - 4; i < 10; i += 4) rt_store(rs, bid, lds + rtl, i, lane);
        if (u2 >= 0) for (int i = wid - 4; i < 10; i += 4) rt_store(rs, u2, lds + rtl + 10240, i, lane);
        if (wid == 4) { const int i = bid * 64 + lane;
            const unsigned f = (i & 1) ? 0u : ONES2; st16(rs, (unsigned)WS_KV + (unsigned)(i >> 3) * KVTILE_B + V_OFF + ((i >> 2) & 1) * V_STEP + (((i >> 1) & 1) * V_ROWS + 20 + (i & 1)) * 16, (u32x4){f, f, f, f}); }
        if (bid == 0 && wid == 5 && lane < 8) *(unsigned*)(ws + WS_CNT + lane * 256) = 0u;
    };
    if (!isH) {
        const int j = 5 * cgrp + ci;
        K1Dma<true> dma; dma.init(xbase, (const char*)wqkv + j * 192, 3 * DM * 2, lds, lane, k0);
        dma.first(wid);
        if (wid >= 4) { dma.loader(wid - 4, ws); side_end(); }
        else {
            side_start();
            f32x16 acc[2][3] = {};
            if (j < 13) { k1_mainloop<true, 0, 2, 3>(acc, lds, wid, lane); STAMP(0, 1); qkv_epilogue<0>(acc, j, rt, wid, lane, lds, ws, rs); }
            else if (j == 13) { k1_mainloop<true, 6, 2, 3>(acc, lds, wid, lane); STAMP(0, 1); qkv_epilogue<6>(acc, j, rt, wid, lane, lds, ws, rs); }
            else { k1_mainloop<true, 7, 2, 3>(acc, lds, wid, lane); STAMP(0, 1); qkv_epilogue<7>(acc, j, rt, wid, lane, lds, ws, rs); }
        }
        if (j <= 13) {
            __syncthreads();
            const char* lb = lds + 45056 + wid * 24 * 256;
            const int T = rt * 8 + wid;
            if (j < 6) qk_copyout(rs, lb, (unsigned)WS_Q, QTILE_B, T, 24 * j, 24, 0, lane);
            else if (j == 6) { qk_copyout(rs, lb, (unsigned)WS_Q, QTILE_B, T, 144, 16, 0, lane); qk_copyout(rs, lb, (unsigned)WS_KV, KVTILE_B, T, 0, 8, 16, lane); }
            else qk_copyout(rs, lb, (unsigned)WS_KV, KVTILE_B, T, 24 * j - 160, j == 13 ? 8 : 24, 0, lane);
        }
    } else {
        const int ch = 3 * cgrp + (ci - 5);
        K1Dma<false> dma; dma.init(xbase, (const char*)w1 + ch * 256, FF * 2, lds, lane, k0);
        dma.first(wid);
        if (wid >= 4) { dma.loader(wid - 4, ws); side_end(); }
        else {
            side_start();
            f32x16 acc[4][2] = {};
            k1_mainloop<false, 0, 4, 2>(acc, lds, wid, lane);
            STAMP(0, 1);
            const int wm = wid >> 1, wn = wid & 1;
#pragma unroll
            for (int mt = 0; mt < 4; ++mt) {
                const int tok = rt * 256 + wm * 128 + mt * 32 + r, rho = tok >> 4;
#pragma unroll
                for (int nt = 0; nt < 2; ++nt) {
                    const int sig = ch * 4 + wn * 2 + nt;
                    const unsigned dst = (unsigned)WS_AF + (unsigned)(rho * NSIG + sig) * 1024 + (h * 16 + (r & 15)) * 16;
                    float sv[16];
#pragma unroll
                    for (int i = 0; i < 16; ++i) { const float v = acc[mt][nt][i]; sv[i] = v * __builtin_amdgcn_rcpf(1.f + __builtin_amdgcn_exp2f(v * -1.4426950408889634f)); }
#pragma unroll
                    for (int s = 0; s < 2; ++s) {
                        const u32x4 w = {cvtpk(sv[8 * s], sv[8 * s + 1]), cvtpk(sv[8 * s + 2], sv[8 * s + 3]), cvtpk(sv[8 * s + 4], sv[8 * s + 5]), cvtpk(sv[8 * s + 6], sv[8 * s + 7])};
                        st16(rs, dst + s * 512, w);
                    }
                }
            }
        }
    }
}

DI float swap32_add(float x) { auto rr = __builtin_amdgcn_permlane32_swap(__float_as_uint(x), __float_as_uint(x), false, false); return __uint_as_float(rr[0]) + __uint_as_float(rr[1]); }
DI float swap32_max(float x) { auto rr = __builtin_amdgcn_permlane32_swap(__float_as_uint(x), __float_as_uint(x), false, false); return fmaxf(__uint_as_float(rr[0]), __uint_as_float(rr[1])); }
DI s16x4 make_qb(unsigned q0, unsigned q1, float M, int h) {
    const float mh = bf_hi(__float_as_uint(M)), r1 = M - mh, mm = bf_hi(__float_as_uint(r1)), r2 = r1 - mm, ml = bf_hi(__float_as_uint(r2));
    u32x2 w;
    w[0] = h ? ((__float_as_uint(mh) >> 16) | (__float_as_uint(mm) & 0xffff0000u)) ^ 0x80008000u : q0;
    w[1] = h ? (__float_as_uint(ml) >> 16) ^ 0x8000u : q1;
    return __builtin_bit_cast(s16x4, w);
}

DI void attn_fallback(f32x16& o, const char* kv, bf16x8 qa, unsigned q0, unsigned q1, int lane, int kboff, int voff) {
    const int h = lane >> 5;
    const f32x16 zero = {};
    float m = -INFINITY;
    const s16x4 qb0 = make_qb(q0, q1, 0.f, h);
    for (int t = 0; t < NT; ++t) {
        const char* tb = kv + t * KVTILE_B;
        const bf16x8 ka = *(const bf16x8*)(tb + lane * 16); const s16x4 kb = *(const s16x4*)(tb + kboff);
        f32x16 s = MFMA32(ka, qa, zero); s = MFMA8(kb, qb0, s);
#pragma unroll
        for (int i = 0; i < 16; ++i) m = fmaxf(m, s[i]);
    }
    m = swap32_max(m);
    const s16x4 qb = make_qb(q0, q1, m, h);
    o = zero;
    for (int t = 0; t < NT; ++t) {
        const char* tb = kv + t * KVTILE_B;
        const bf16x8 ka = *(const bf16x8*)(tb + lane * 16); const s16x4 kb = *(const s16x4*)(tb + kboff);
        const bf16x8 va = *(const bf16x8*)(tb + voff), vb = *(const bf16x8*)(tb + voff + V_STEP);
        f32x16 s = MFMA32(ka, qa, zero); s = MFMA8(kb, qb, s);
        u32x4 p0, p1;
#pragma unroll
        for (int i = 0; i < 4; ++i) { p0[i] = cvtpk(__builtin_amdgcn_exp2f(s[2 * i]), __builtin_amdgcn_exp2f(s[2 * i + 1])); p1[i] = cvtpk(__builtin_amdgcn_exp2f(s[8 + 2 * i]), __builtin_amdgcn_exp2f(s[9 + 2 * i])); }
        o = MFMA32(va, __builtin_bit_cast(bf16x8, p0), o);
        o = MFMA32(vb, __builtin_bit_cast(bf16x8, p1), o);
    }
}

constexpr int K2_BIG = 16 * KVTILE_B;
constexpr int K2_ARR = 3 * K2_BIG;
constexpr int K2_LDS = 163840;
DI void gld128(bf16x8& d, const char* sbase, unsigned voff) { asm volatile("global_load_dwordx4 %0, %1, %2" : "=v"(d) : "v"(voff), "s"(sbase) : "memory"); }
DI void gld128_rw(bf16x8& d, const char* sbase, unsigned voff) { asm volatile("global_load_dwordx4 %0, %1, %2" : "+v"(d) : "v"(voff), "s"(sbase) : "memory"); }
DI void gld128_sc1_rw(bf16x8& d, const char* sbase, unsigned voff) { asm volatile("global_load_dwordx4 %0, %1, %2 sc1" : "+v"(d) : "v"(voff), "s"(sbase) : "memory"); }
DI void gld128_sc1(bf16x8& d, const char* sbase, unsigned voff) { asm volatile("global_load_dwordx4 %0, %1, %2 sc1" : "=v"(d) : "v"(voff), "s"(sbase) : "memory"); }
template <int N> DI void wait9(bf16x8 (&a)[4], bf16x8 (&b)[5]) {
    asm volatile("s_waitcnt vmcnt(%9)" : "+v"(a[0]), "+v"(a[1]), "+v"(a[2]), "+v"(a[3]), "+v"(b[0]), "+v"(b[1]), "+v"(b[2]), "+v"(b[3]), "+v"(b[4]) : "i"(N) : "memory");
}
__global__ __launch_bounds__(512, 2) void k2_attn(char* __restrict__ ws, unsigned short* __restrict__ out) {
    extern __shared__ __attribute__((aligned(16))) char lds[];
    const int bid = blockIdx.x;
    const int tid = threadIdx.x, lane = tid & 63, wid = rfl(tid >> 6);
    const int h = lane >> 5, r = lane & 31;
    const int xcd = bid & 7, slot = bid >> 3;
    const int head = xcd * 4 + (slot & 3), qblk = slot >> 2;
    STAMP(1, 0);
    if (tid == 0) *(unsigned*)(lds + K2_ARR) = 0u;
    const char* kv = ws + WS_KV + (size_t)head * NT * KVTILE_B;
    const unsigned lds0 = (unsigned)(uintptr_t)lds;
    const unsigned lane16 = lane * 16;
    const char* kvw = kv + wid * 1024;
    const unsigned ldw = lds0 + wid * 1024;
    const int Tq = qblk * 8 + wid;
    const char* qt = ws + WS_Q + ((size_t)head * NT + Tq) * QTILE_B;
    const bf16x8 qa = *(const bf16x8*)(qt + lane * 16);
    const u32x2 qbr = *(const u32x2*)(qt + 1024 + r * 8);
    f32x4 km4 = *(const f32x4*)(ws + WS_KMAX + (size_t)head * 640 + (lane < 40 ? lane : 0) * 16);
    const float gate = *(const float*)(ws + WS_GATE + (Tq * 32 + r) * 4);
#pragma unroll
    for (int j = 0; j < 11; ++j) { const int p = 8 * j; if (p + 7 < 84 || p + wid < 84) glds16(kvw + p * 1024, lane16, ldw + p * 1024); }
    float km = fmaxf(fmaxf(km4[0], km4[1]), fmaxf(km4[2], km4[3]));
    km = dpp_max<0xB1>(km); km = dpp_max<0x4E>(km); km = dpp_max<0x141>(km);
    km = __builtin_amdgcn_sqrtf(km);
    float M;
    { const float k0 = rdl(km, 0), k1 = rdl(km, 8), k2 = rdl(km, 16), k3 = rdl(km, 24), k4 = rdl(km, 32);
      const u32x4 qw = __builtin_bit_cast(u32x4, qa);
      float n0, n1, n2;
      { const float a = bf_lo(qw[0]), b = bf_hi(qw[0]), c = bf_lo(qw[1]), d = bf_hi(qw[1]); n0 = (a * a + b * b) + (c * c + d * d); }
      { const float a = bf_lo(qw[2]), b = bf_hi(qw[2]), c = bf_lo(qw[3]), d = bf_hi(qw[3]); n1 = (a * a + b * b) + (c * c + d * d); }
      { const float a = bf_lo(qbr[0]), b = bf_hi(qbr[0]), c = bf_lo(qbr[1]), d = bf_hi(qbr[1]); n2 = (a * a + b * b) + (c * c + d * d); }
      float part = __builtin_amdgcn_sqrtf(n0) * (h ? k2 : k0) + __builtin_amdgcn_sqrtf(n1) * (h ? k3 : k1);
      if (!h) part += __builtin_amdgcn_sqrtf(n2) * k4;
      M = swap32_add(part) * 1.01f; }
    const s16x4 qb = make_qb(qbr[0], qbr[1], M, h);
    const int vrow = r <= 20 ? r : (r < 28 ? 21 : r - 12);
    const int voff = V_OFF + (h * V_ROWS + vrow) * 16, kboff = h ? V_OFF + 20 * 16 : 1024 + r * 8;
    lds_cptr kaC = (lds_cptr)lds + lane * 16, kbC = (lds_cptr)lds + kboff, vC = (lds_cptr)lds + voff;
    lds_cptr kaN = kaC + K2_BIG, kbN = kbC + K2_BIG, vN = vC + K2_BIG;
    const f32x16 zero = {};
    f32x16 o = {}, sA, sB;
    bf16x8 ka, va, vb; s16x4 kb;
    u32x4 plo, phi;
    asm volatile("s_waitcnt vmcnt(7) lgkmcnt(0)\n\ts_barrier" ::: "memory");
    STAMP(1, 1);
    ka = lds128(kaC); kb = lds64(kbC);
    sA = MFMA32(ka, qa, zero); sA = MFMA8(kb, qb, sA);
    ka = lds128(kaC + KVTILE_B); kb = lds64(kbC + KVTILE_B);
    va = lds128(vC);
    vb = (bf16x8){}; phi = (u32x4){};
    f32x4 e3 = {};
    const int ng = (xcd & 1) * 4 + (slot & 3), bt = (xcd >> 1) * 8 + (slot >> 2);
    const char* ap = ws + WS_AF + (size_t)bt * 4 * NSIG * 1024;
    const char* bp = ws + WS_BF + (size_t)ng * NSIG * 5 * 1024;
    const int rot = ((slot >> 2) + 3 * (slot & 3)) % 6;
    auto hs = [&](int i) { int st = i + rot; if (st >= 6) st -= 6; return wid * 6 + st; };
    bf16x8 a[3][4], b[3][5];
    auto ldA1 = [&](int sg, int mt, bf16x8& d) { gld128(d, ap + ((size_t)mt * NSIG + sg) * 1024, lane16); };
    auto ldB1 = [&](int sg, int nt, bf16x8& d) { gld128(d, bp + sg * 5120 + nt * 1024, lane16); };
#define EXP4(X, i) do { X[i] = __builtin_amdgcn_exp2f(X[i]); X[i + 1] = __builtin_amdgcn_exp2f(X[i + 1]); X[i + 2] = __builtin_amdgcn_exp2f(X[i + 2]); X[i + 3] = __builtin_amdgcn_exp2f(X[i + 3]); } while (0)
#define KADDR(A, i) (((i) < 16) ? (A##C + (i) * KVTILE_B) : (A##N + ((i) - 16) * KVTILE_B))
#define STEP(C, N, i, X0, X1) do { \
        bf16x8 ka2, va2, vb2; s16x4 kb2; \
        N = MFMA32(ka, qa, zero); SBAR(); \
        ka2 = lds128(KADDR(ka, (i) + 2)); \
        phi[2] = cvtpk(e3[0], e3[1]); phi[3] = cvtpk(e3[2], e3[3]); PIN(phi); SBAR(); EXP4(C, 0); PIN(C); SBAR(); \
        o = MFMA32(vb, __builtin_bit_cast(bf16x8, phi), o); SBAR(); \
        X0; \
        vb2 = lds128(vC + (i) * KVTILE_B + V_STEP); \
        plo[0] = cvtpk(C[0], C[1]); plo[1] = cvtpk(C[2], C[3]); PIN(plo); SBAR(); EXP4(C, 4); PIN(C); SBAR(); \
        N = MFMA8(kb, qb, N); SBAR(); \
        kb2 = lds64(KADDR(kb, (i) + 2)); \
        plo[2] = cvtpk(C[4], C[5]); plo[3] = cvtpk(C[6], C[7]); PIN(plo); SBAR(); EXP4(C, 8); PIN(C); SBAR(); \
        o = MFMA32(va, __builtin_bit_cast(bf16x8, plo), o); SBAR(); \
        X1; \
        va2 = lds128(KADDR(v, (i) + 1)); \
        phi[0] = cvtpk(C[8], C[9]); phi[1] = cvtpk(C[10], C[11]); PIN(phi); SBAR(); \
        e3[0] = __builtin_amdgcn_exp2f(C[12]); e3[1] = __builtin_amdgcn_exp2f(C[13]); e3[2] = __builtin_amdgcn_exp2f(C[14]); e3[3] = __builtin_amdgcn_exp2f(C[15]); PIN(e3); SBAR(); \
        ka = ka2; kb = kb2; va = va2; vb = vb2; \
    } while (0)
#define PIECE(j) do { if (8 * (j) + 7 < 42 || 8 * (j) + wid < 42) glds16(src + (j) * 8192, lane16, dst + (j) * 8192); } while (0)
    const char* src = kvw + 84 * 1024; unsigned dst = ldw + 2 * K2_BIG;
#pragma unroll 1
    for (int c = 0; c < 3; ++c) {
        STEP(sA, sB, 0, , ); STEP(sB, sA, 1, , ); STEP(sA, sB, 2, , ); STEP(sB, sA, 3, , ); STEP(sA, sB, 4, , );
        if (c == 0) asm volatile("s_waitcnt vmcnt(4)\n\ts_barrier" ::: "memory");
        STEP(sB, sA, 5, , ); STEP(sA, sB, 6, , ); STEP(sB, sA, 7, , ); STEP(sA, sB, 8, , ); STEP(sB, sA, 9, , ); STEP(sA, sB, 10, , ); STEP(sB, sA, 11, , ); STEP(sA, sB, 12, , );
        asm volatile("s_waitcnt vmcnt(0)\n\ts_barrier" ::: "memory");
        if (c < 2) { STEP(sB, sA, 13, PIECE(0), PIECE(1)); STEP(sA, sB, 14, PIECE(2), PIECE(3)); STEP(sB, sA, 15, PIECE(4), PIECE(5)); }
        else { STEP(sB, sA, 13, , ); STEP(sA, sB, 14, , ); STEP(sB, sA, 15, , ); }
        const int d = (c == 1) ? -2 * K2_BIG : K2_BIG;
        kaC = kaN; kbC = kbN; vC = vN; kaN += d; kbN += d; vN += d;
        src += 42 * 1024; dst -= 2 * K2_BIG;
    }
#define PA(s, m) ldA1(hs(s), m, a[s][m])
#define PB(s, n) ldB1(hs(s), n, b[s][n])
    STEP(sA, sB, 0, PA(0, 0), PA(0, 1)); STEP(sB, sA, 1, PA(0, 2), PA(0, 3)); STEP(sA, sB, 2, PB(0, 0), PB(0, 1)); STEP(sB, sA, 3, PB(0, 2), PB(0, 3));
    STEP(sA, sB, 4, PB(0, 4), PA(1, 0)); STEP(sB, sA, 5, PA(1, 1), PA(1, 2)); STEP(sA, sB, 6, PA(1, 3), PB(1, 0)); STEP(sB, sA, 7, PB(1, 1), PB(1, 2));
    STEP(sA, sB, 8, PB(1, 3), PB(1, 4)); STEP(sB, sA, 9, PA(2, 0), PA(2, 1)); STEP(sA, sB, 10, PA(2, 2), PA(2, 3)); STEP(sB, sA, 11, PB(2, 0), PB(2, 1));
    STEP(sA, sB, 12, PB(2, 2), PB(2, 3)); STEP(sB, sA, 13, PB(2, 4), ); STEP(sA, sB, 14, , ); STEP(sB, sA, 15, , );
    phi[2] = cvtpk(e3[0], e3[1]); phi[3] = cvtpk(e3[2], e3[3]);
    o = MFMA32(vb, __builtin_bit_cast(bf16x8, phi), o);
    STAMP(1, 2);
    float l; { auto rr = __builtin_amdgcn_permlane32_swap(__float_as_uint(o[8]), __float_as_uint(o[8]), false, false); l = __uint_as_float(rr[1]); }
    if (__builtin_expect(__any(!(l >= 8.6736174e-19f && l <= 1.0e30f)), 0)) {
        asm volatile("s_waitcnt vmcnt(0)" ::: "memory");
        attn_fallback(o, kv, qa, qbr[0], qbr[1], lane, kboff, voff);
        auto rr = __builtin_amdgcn_permlane32_swap(__float_as_uint(o[8]), __float_as_uint(o[8]), false, false); l = __uint_as_float(rr[1]);
    }
    const float f = gate * __builtin_amdgcn_rcpf(l);
    const int tok = Tq * 32 + r;
    char* ab = ws + WS_AF + (size_t)(tok >> 4) * NSIG * 1024 + (tok & 15) * 16;
#pragma unroll
    for (int g = 0; g < 3; ++g) {
        const int gam = head * 5 + 2 * g + h;
        const int sig = 48 + (gam >> 3), c = ((gam >> 2) & 1) * 2 + (gam & 1), half = (gam >> 1) & 1;
        const u32x2 w = {cvtpk(o[4 * g] * f, o[4 * g + 1] * f), cvtpk(o[4 * g + 2] * f, o[4 * g + 3] * f)};
        if (g < 2 || h == 0) __hip_atomic_store((unsigned long long*)(ab + sig * 1024 + c * 256 + half * 8), ((unsigned long long)w[1] << 32) | w[0], __ATOMIC_RELAXED, __HIP_MEMORY_SCOPE_AGENT);
    }
    STAMP(1, 3);
    {
        f32x4 acc[4][5] = {};
        auto loadA = [&](int sg, bf16x8 (&da)[4], bool sc1) {
#pragma unroll
            for (int mt = 0; mt < 4; ++mt) { if (sc1) gld128_sc1(da[mt], ap + ((size_t)mt * NSIG + sg) * 1024, lane16); else gld128(da[mt], ap + ((size_t)mt * NSIG + sg) * 1024, lane16); }
        };
        auto loadB = [&](int sg, bf16x8 (&db)[5]) {
#pragma unroll
            for (int nt = 0; nt < 5; ++nt) gld128(db[nt], bp + sg * 5120 + nt * 1024, lane16);
        };
        auto mma = [&](bf16x8 (&da)[4], bf16x8 (&db)[5]) {
            SBAR();
#pragma unroll
            for (int nt = 0; nt < 5; ++nt)
#pragma unroll
                for (int mt = 0; mt < 4; ++mt) acc[mt][nt] = MFMA16(db[nt], da[mt], acc[mt][nt]);
            SBAR();
        };
        wait9<3>(a[0], b[0]);
        asm volatile("" : "+v"(a[1][0]), "+v"(a[1][1]), "+v"(a[1][2]), "+v"(a[1][3]), "+v"(b[1][0]), "+v"(b[1][1]), "+v"(b[1][2]), "+v"(b[1][3]), "+v"(b[1][4]));
        asm volatile("" : "+v"(a[2][0]), "+v"(a[2][1]), "+v"(a[2][2]), "+v"(a[2][3]), "+v"(b[2][0]), "+v"(b[2][1]), "+v"(b[2][2]), "+v"(b[2][3]), "+v"(b[2][4]));
        mma(a[0], b[0]); loadA(hs(3), a[0], false); loadB(hs(3), b[0]);
        mma(a[1], b[1]); loadA(hs(4), a[1], false); loadB(hs(4), b[1]);
        asm volatile("s_waitcnt vmcnt(18)" ::: "memory");
        if (lane == 0) {
            const unsigned old = __hip_atomic_fetch_add((unsigned*)(lds + K2_ARR), 1u, __ATOMIC_RELAXED, __HIP_MEMORY_SCOPE_WORKGROUP);
            if (old == 7u) __hip_atomic_fetch_add((unsigned*)(ws + WS_CNT + qblk * 256), 1u, __ATOMIC_RELAXED, __HIP_MEMORY_SCOPE_AGENT);
        }
        mma(a[2], b[2]); loadA(hs(5), a[2], false); loadB(hs(5), b[2]);
        float og[4];
#pragma unroll
        for (int mt = 0; mt < 4; ++mt) asm volatile("global_load_dword %0, %1, %2" : "=v"(og[mt]) : "v"((unsigned)((bt * 64 + mt * 16 + (lane & 15)) * 4)), "s"(ws + WS_GATE) : "memory");
        wait9<22>(a[0], b[0]); mma(a[0], b[0]); loadB(48 + wid, b[0]);
        wait9<18>(a[1], b[1]); mma(a[1], b[1]); loadB(56 + wid, b[1]);
        wait9<14>(a[2], b[2]); mma(a[2], b[2]);
        if (wid < 4) {
#pragma unroll
            for (int nt = 0; nt < 5; ++nt) gld128_rw(b[2][nt], bp + (64 + wid) * 5120 + nt * 1024, lane16);
        }
        STAMP(2, 0);
        asm volatile("s_waitcnt vmcnt(0)" : "+v"(og[0]), "+v"(og[1]), "+v"(og[2]), "+v"(og[3]) :: "memory");
#pragma unroll
        for (int mt = 0; mt < 4; ++mt) { const float f1 = bf_round(1.f - og[mt]);
#pragma unroll
            for (int nt = 0; nt < 5; ++nt) acc[mt][nt] *= f1; }
        if (wid == 0) {
            const unsigned* cnt = (const unsigned*)(ws + WS_CNT + (bt >> 2) * 256);
            for (int it = 0; it < (1 << 22); ++it) {
                if ((unsigned)rfl(__hip_atomic_load(cnt, __ATOMIC_RELAXED, __HIP_MEMORY_SCOPE_AGENT)) >= 32u) break;
                __builtin_amdgcn_s_sleep(2);
            }
        }
        asm volatile("s_barrier" ::: "memory");
        STAMP(2, 1);
        loadA(48 + wid, a[0], true); loadA(56 + wid, a[1], true);
        a[2][0] = (bf16x8){}; a[2][1] = (bf16x8){}; a[2][2] = (bf16x8){}; a[2][3] = (bf16x8){};
        if (wid < 4) {
#pragma unroll
            for (int mt = 0; mt < 4; ++mt) gld128_sc1_rw(a[2][mt], ap + ((size_t)mt * NSIG + 64 + wid) * 1024, lane16);
        }
        wait9<4>(a[0], b[0]); mma(a[0], b[0]);
        wait9<0>(a[1], b[1]); wait9<0>(a[2], b[2]); mma(a[1], b[1]); mma(a[2], b[2]);
        STAMP(2, 2);
#pragma unroll
        for (int mt = 0; mt < 4; ++mt)
#pragma unroll
            for (int nt = 0; nt < 5; ++nt) *(f32x4*)(lds + (wid * 20 + mt * 5 + nt) * 1024 + lane16) = acc[mt][nt];
        asm volatile("s_waitcnt lgkmcnt(0)\n\ts_barrier" ::: "memory");
#pragma unroll
        for (int i = 0; i < 3; ++i) { const int t = wid + 8 * i;
            if (t < 20) {
                f32x4 sum = *(const f32x4*)(lds + t * 1024 + lane16);
#pragma unroll
                for (int w = 1; w < 8; ++w) sum += *(const f32x4*)(lds + (w * 20 + t) * 1024 + lane16);
                const int m4 = t / 5, nt = t - m4 * 5;
                const int tok2 = bt * 64 + m4 * 16 + (lane & 15);
                *(u32x2*)(out + (size_t)tok2 * DM + ng * 80 + nt * 16 + (lane >> 4) * 4) = (u32x2){cvtpk(sum[0], sum[1]), cvtpk(sum[2], sum[3])};
            } }
        STAMP(2, 3);
    }
}

extern "C" void kernel_launch(void* const* d_in, const int* in_sizes, int n_in, void* d_out, int out_size, void* d_ws, size_t ws_size, hipStream_t stream) {
    const unsigned short* x = (const unsigned short*)d_in[0];
    const unsigned short* wqkv = (const unsigned short*)d_in[1];
    const unsigned short* wo = (const unsigned short*)d_in[2];
    const unsigned short* w1 = (const unsigned short*)d_in[4];
    const unsigned short* w2 = (const unsigned short*)d_in[5];
    const unsigned short* gw = (const unsigned short*)d_in[6];
    char* ws = (char*)d_ws;
    (void)hipFuncSetAttribute((const void*)k1_proj, hipFuncAttributeMaxDynamicSharedMemorySize, K1_LDS);
    (void)hipFuncSetAttribute((const void*)k2_attn, hipFuncAttributeMaxDynamicSharedMemorySize, K2_LDS);
    k1_proj<<<256, 512, K1_LDS, stream>>>(x, wqkv, wo, w1, w2, gw, ws);
    k2_attn<<<256, 512, K2_LDS, stream>>>(ws, (unsigned short*)d_out);
}
```

```cpp
#include <hip/hip_runtime.h>
#include <hip/hip_bf16.h>
#include <stdint.h>

typedef short bf16x8 __attribute__((ext_vector_type(8)));
typedef short s16x4 __attribute__((ext_vector_type(4)));
typedef float f32x16 __attribute__((ext_vector_type(16)));
typedef float f32x4 __attribute__((ext_vector_type(4)));
typedef float f32x2 __attribute__((ext_vector_type(2)));
typedef unsigned u32x4 __attribute__((ext_vector_type(4)));
typedef unsigned u32x2 __attribute__((ext_vector_type(2)));
typedef __bf16 bf16x2_t __attribute__((ext_vector_type(2)));
#define LAS __attribute__((address_space(3)))
typedef LAS const char* lds_cptr;
#define DI __device__ __forceinline__
#define SBAR() __builtin_amdgcn_sched_barrier(0)
#define PIN(x) asm volatile("" : "+v"(x))
#define MFMA32(a, b, c) __builtin_amdgcn_mfma_f32_32x32x16_bf16(a, b, c, 0, 0, 0)
#define MFMA16(a, b, c) __builtin_amdgcn_mfma_f32_16x16x32_bf16(a, b, c, 0, 0, 0)
#define MFMA8(a, b, c) __builtin_amdgcn_mfma_f32_32x32x8bf16_1k(a, b, c, 0, 0, 0)

constexpr int S = 2048, DM = 640, NH = 32, DH = 20, FF = 1536;
constexpr int NT = S / 32;
constexpr int QTILE_B = 1280;
constexpr int KVTILE_B = 2688;
constexpr int V_OFF = 1280, V_STEP = 704, V_ROWS = 22;
constexpr int NSIG = 68;
constexpr size_t WS_Q = 0;
constexpr size_t WS_KV = WS_Q + (size_t)NH * NT * QTILE_B;
constexpr size_t WS_AF = WS_KV + (size_t)NH * NT * KVTILE_B;
constexpr size_t WS_BF = WS_AF + (size_t)128 * NSIG * 1024;
constexpr size_t WS_KMAX = WS_BF + (size_t)8 * NSIG * 5 * 1024;
constexpr size_t WS_GATE = WS_KMAX + (size_t)160 * 32 * 4;
constexpr size_t WS_F1 = WS_GATE + (size_t)S * 4;
constexpr size_t WS_F2 = WS_F1 + 2048;
constexpr size_t WS_F3 = WS_F2 + 2048;
constexpr size_t WS_EPOCH = WS_F3 + 2048;
constexpr size_t WS_END = WS_EPOCH + 256;
constexpr float QSCALE = 0.22360679774997896f * 1.4426950408889634f;
constexpr unsigned ONES2 = 0x3F803F80u;

DI unsigned cvtpk(float lo, float hi) { f32x2 v = {lo, hi}; bf16x2_t b = __builtin_convertvector(v, bf16x2_t); return __builtin_bit_cast(unsigned, b); }
DI float bf_lo(unsigned u) { return __uint_as_float(u << 16); }
DI float bf_hi(unsigned u) { return __uint_as_float(u & 0xffff0000u); }
DI float bf_round(float x) { return bf_lo(cvtpk(x, x)); }
DI void glds16(const void* sbase, unsigned voff, unsigned lds_base) {
    unsigned sv;
    asm volatile("s_mov_b32 %0, m0\n\ts_mov_b32 m0, %3\n\ts_nop 2\n\tglobal_load_lds_dwordx4 %1, %2\n\ts_mov_b32 m0, %0"
                 : "=&s"(sv) : "v"(voff), "s"(sbase), "s"(lds_base) : "memory");
}
typedef short v4i16_t __attribute__((ext_vector_type(4)));
DI s16x4 ldstr(lds_cptr p) { return __builtin_bit_cast(s16x4, __builtin_amdgcn_ds_read_tr16_b64_v4i16((LAS v4i16_t*)p)); }
DI bf16x8 lds128(lds_cptr p) { return *(const LAS bf16x8*)p; }
DI s16x4 lds64(lds_cptr p) { return *(const LAS s16x4*)p; }
DI unsigned rfl(unsigned x) { return __builtin_amdgcn_readfirstlane(x); }
#define STAMP(k, i) do { } while (0)

DI float rdl(float x, int l) { return __uint_as_float(__builtin_amdgcn_readlane(__float_as_uint(x), l)); }
typedef __amdgpu_buffer_rsrc_t rsrc_t;
typedef unsigned long long u64;
DI rsrc_t ws_rsrc(char* ws) { return __builtin_amdgcn_make_buffer_rsrc(ws, 0, (int)WS_END, 0x00020000); }
DI void st16(rsrc_t rs, unsigned off, u32x4 v) { __builtin_amdgcn_raw_buffer_store_b128(v, rs, off, 0, 16); }
DI void st8(rsrc_t rs, unsigned off, u32x2 v) { __builtin_amdgcn_raw_buffer_store_b64(v, rs, off, 0, 16); }
DI void st4f(char* p, float v) { __hip_atomic_store((float*)p, v, __ATOMIC_RELAXED, __HIP_MEMORY_SCOPE_AGENT); }
DI void flag_set(char* p, u64 tag) { __hip_atomic_store((u64*)p, tag, __ATOMIC_RELAXED, __HIP_MEMORY_SCOPE_AGENT); }
DI u64 flag_get(const char* p) { return __hip_atomic_load((const u64*)p, __ATOMIC_RELAXED, __HIP_MEMORY_SCOPE_AGENT); }
template <int CTRL> DI float dpp_max(float x) { const float y = __uint_as_float(__builtin_amdgcn_update_dpp(0u, __float_as_uint(x), CTRL, 0xf, 0xf, true)); return fmaxf(x, y); }
template <int CTRL> DI float dpp_add(float x) { const float y = __uint_as_float(__builtin_amdgcn_update_dpp(0u, __float_as_uint(x), CTRL, 0xf, 0xf, true)); return x + y; }
DI void gld128(bf16x8& d, const char* sbase, unsigned voff) { asm volatile("s_nop 4\n\tglobal_load_dwordx4 %0, %1, %2" : "=v"(d) : "v"(voff), "s"(sbase) : "memory"); }
DI void gld128_sc1(bf16x8& d, const char* sbase, unsigned voff) { asm volatile("s_nop 4\n\tglobal_load_dwordx4 %0, %1, %2 sc1" : "=v"(d) : "v"(voff), "s"(sbase) : "memory"); }
DI void gld128_rw(bf16x8& d, const char* sbase, unsigned voff) { asm volatile("s_nop 4\n\tglobal_load_dwordx4 %0, %1, %2" : "+v"(d) : "v"(voff), "s"(sbase) : "memory"); }
DI void gld128_sc1_rw(bf16x8& d, const char* sbase, unsigned voff) { asm volatile("s_nop 4\n\tglobal_load_dwordx4 %0, %1, %2 sc1" : "+v"(d) : "v"(voff), "s"(sbase) : "memory"); }
template <int N> DI void wait9(bf16x8 (&a)[4], bf16x8 (&b)[5]) {
    asm volatile("s_waitcnt vmcnt(%9)" : "+v"(a[0]), "+v"(a[1]), "+v"(a[2]), "+v"(a[3]), "+v"(b[0]), "+v"(b[1]), "+v"(b[2]), "+v"(b[3]), "+v"(b[4]) : "i"(N) : "memory");
}
#define WAITB(n) asm volatile("s_waitcnt vmcnt(" #n ")\n\ts_barrier" ::: "memory")
#define BARL() asm volatile("s_waitcnt lgkmcnt(0)\n\ts_barrier" ::: "memory")

constexpr int P1_STG = 32768;
constexpr int RT_OFF = 131072;
constexpr int P2_STG = 28672;
constexpr int CTL_OFF = 163584;
DI constexpr int p2_base(int s) { return CTL_OFF - (s + 1) * P2_STG; }
constexpr int K2_BIG = 16 * KVTILE_B;
constexpr int LDS_ALL = 163840;

template <bool N96>
struct GDma {
    static constexpr int NW = N96 ? 3 : 4;
    const char* xbase; const char* wbase; int ldw2, k0; unsigned lds0, xoff0, xoff1, woff[3];
    DI int kact(int kc) const { kc += k0; return kc >= 10 ? kc - 10 : kc; }
    DI static unsigned sbase(int st) { return N96 ? (unsigned)(CTL_OFF - (st + 1) * P2_STG) : (unsigned)(st * P1_STG); }
    DI void init(const char* xb, const char* wb, int ld, char* lds, int lane, int k0_) {
        xbase = xb; wbase = wb; ldw2 = ld; k0 = k0_; lds0 = (unsigned)(uintptr_t)lds;
        const int rr = lane >> 3;
        xoff0 = rr * 1280 + (((lane & 7) ^ ((lane >> 4) & 7)) << 4); xoff1 = rr * 1280 + (((lane & 7) ^ ((4 + (lane >> 4)) & 7)) << 4);
        if (N96) {
#pragma unroll
            for (int p = 0; p < 3; ++p) { const int o = 1024 * p + 16 * lane, row = (o * 21846) >> 22, col = o - row * 192; woff[p] = row * ldw2 + col; }
        } else woff[0] = (lane >> 4) * ldw2 + ((((lane & 15) ^ ((lane >> 4) << 2))) << 4);
    }
    DI void xp(int kc, int st, int j, int par) const { glds16(xbase + (size_t)j * 8 * 1280 + kc * 128, par ? xoff1 : xoff0, lds0 + sbase(st) + j * 1024); }
    DI void wp(int kc, int st, int lw, int jj) const {
        if (N96) glds16(wbase + (size_t)(kc * 64 + 16 * lw) * ldw2, woff[jj], lds0 + sbase(st) + 16384 + (3 * lw + jj) * 1024);
        else glds16(wbase + (size_t)(kc * 64 + 16 * lw + 4 * jj) * ldw2, woff[0], lds0 + sbase(st) + 16384 + (4 * lw + jj) * 1024);
    }
    DI void issue(int kc_, int st, int lw) const {
        const int kc = kact(kc_);
#pragma unroll
        for (int jj = 0; jj < NW; ++jj) wp(kc, st, lw, jj);
#pragma unroll
        for (int jj = 0; jj < 4; ++jj) xp(kc, st, 4 * lw + jj, jj & 1);
    }
};

DI void rt_load(const unsigned short* w2, const unsigned short* wo, int u, unsigned ldsb, int piece, int lane) {
    const int sig = u >> 2, s = (u >> 1) & 1, cb = u & 1;
    const char* src = (sig < 48 ? (const char*)w2 + (size_t)sig * 32 * 1280 : (const char*)wo + (size_t)(sig - 48) * 32 * 1280) + s * 16 * 1280 + cb * 640;
    const int o = piece * 1024 + lane * 16, row = (o * 6554) >> 22;
    glds16(src, row * 1280 + (o - row * 640), ldsb + piece * 1024);
}
DI void rt_store(rsrc_t rs, int u, const char* ldsb, int i, int lane) {
    const int sig = u >> 2, s = (u >> 1) & 1, cb = u & 1;
    const int nl = 2 * i + (lane >> 5), hh = (lane >> 4) & 1, q = (lane & 15) >> 2, p = lane & 3;
    lds_cptr a = (lds_cptr)ldsb + (4 * hh + q) * 640 + (16 * nl + 4 * p) * 2;
    const s16x4 lo = ldstr(a), hi = ldstr(a + 8 * 640);
    const bf16x8 f = __builtin_shufflevector(lo, hi, 0, 1, 2, 3, 4, 5, 6, 7);
    const int nu = cb * 20 + nl, ng = (nu * 205) >> 10;
    st16(rs, (unsigned)WS_BF + (unsigned)((ng * NSIG + sig) * 5 + (nu - 5 * ng)) * 1024 + (s * 32 + (lane & 31)) * 16, __builtin_bit_cast(u32x4, f));
}

template <bool N96, bool VT, int MT, int NTL, class HOOK>
DI void gemm_loop(f32x16 (&acc)[MT][NTL], char* lds, int wid, int lane, HOOK hook) {
    const int h = lane >> 5, r = lane & 31;
    constexpr int NST = N96 ? 3 : 4;
    unsigned xa[4];
    { const int row = (N96 ? wid * 32 : (wid >> 1) * 64) + r; const int sw = (row >> 1) & 7;
#pragma unroll
      for (int ks = 0; ks < 4; ++ks) xa[ks] = row * 128 + (((2 * ks + h) ^ sw) << 4); }
    unsigned wa[2];
    { const int blk = (lane >> 4) & 1, q = (lane & 15) >> 2, p = lane & 3;
      if (N96) wa[0] = wa[1] = 16384 + (8 * h + q) * 192 + blk * 32 + p * 8;
      else {
#pragma unroll
          for (int nt = 0; nt < 2; ++nt) { const int ch = (wid & 1) * 8 + nt * 4 + 2 * blk + (p >> 1); wa[nt] = 16384 + (8 * h + q) * 256 + ((ch ^ (q << 2)) << 4) + 8 * (p & 1); } } }
    bf16x8 xf[2][MT], wf[2][NTL];
    auto rdw = [&](int buf, int nt, lds_cptr sb, int ks) {
        lds_cptr a = N96 ? sb + wa[0] + ks * 16 * 192 + nt * 64 : sb + wa[nt & 1] + ks * 4096;
        const s16x4 lo = ldstr(a), hi = ldstr(a + (N96 ? 4 * 192 : 1024));
        wf[buf][nt] = __builtin_shufflevector(lo, hi, 0, 1, 2, 3, 4, 5, 6, 7);
    };
    auto rdx = [&](int buf, int mt, lds_cptr sb, int ks) { xf[buf][mt] = lds128(sb + xa[ks] + mt * 4096); };
    auto mm = [&](int buf, int mt, int nt) { acc[mt][nt] = VT ? MFMA32(xf[buf][mt], wf[buf][nt], acc[mt][nt]) : MFMA32(wf[buf][nt], xf[buf][mt], acc[mt][nt]); };
    { lds_cptr sb = (lds_cptr)lds + GDma<N96>::sbase(0); rdw(0, 0, sb, 0); rdx(0, 0, sb, 0); rdw(0, 1, sb, 0); if (N96) rdw(0, 2 % NTL, sb, 0); else rdx(0, 1 % MT, sb, 0); }
    int st = 0;
#define G_KSTEP(ks, NEXT) do { const int b = (ks) & 1, n = b ^ 1; lds_cptr np = (ks) < 3 ? sb : sn; const int nk = ((ks) + 1) & 3; \
        SBAR(); \
        if constexpr (N96) { \
            mm(b, 0, 0); SBAR(); if (NEXT) { rdw(n, 0, np, nk); rdx(n, 0, np, nk); } SBAR(); \
            mm(b, 0, 1); SBAR(); if (NEXT) { rdw(n, 1, np, nk); } SBAR(); \
            mm(b, 0, 2 % NTL); SBAR(); if (NEXT) { rdw(n, 2 % NTL, np, nk); } SBAR(); \
        } else { \
            mm(b, 0, 0); SBAR(); if (NEXT) { rdw(n, 0, np, nk); } SBAR(); \
            mm(b, 0, 1); SBAR(); if (NEXT) { rdx(n, 0, np, nk); } SBAR(); \
            mm(b, 1 % MT, 0); SBAR(); if (NEXT) { rdw(n, 1, np, nk); } SBAR(); \
            mm(b, 1 % MT, 1); SBAR(); if (NEXT) { rdx(n, 1 % MT, np, nk); } SBAR(); \
        } } while (0)
#pragma unroll 1
    for (int kc = 0; kc < 9; ++kc) {
        lds_cptr sb = (lds_cptr)lds + GDma<N96>::sbase(st);
        st = st == NST - 1 ? 0 : st + 1;
        lds_cptr sn = (lds_cptr)lds + GDma<N96>::sbase(st);
        G_KSTEP(0, true); G_KSTEP(1, true); G_KSTEP(2, true);
        hook(kc, true);
        BARL();
        hook(kc, false);
        G_KSTEP(3, true);
    }
    { lds_cptr sb = (lds_cptr)lds + GDma<N96>::sbase(st); lds_cptr sn = sb;
      G_KSTEP(0, true); G_KSTEP(1, true); G_KSTEP(2, true); G_KSTEP(3, false); }
}

DI float swap32_add(float x) { auto rr = __builtin_amdgcn_permlane32_swap(__float_as_uint(x), __float_as_uint(x), false, false); return __uint_as_float(rr[0]) + __uint_as_float(rr[1]); }
DI float swap32_max(float x) { auto rr = __builtin_amdgcn_permlane32_swap(__float_as_uint(x), __float_as_uint(x), false, false); return fmaxf(__uint_as_float(rr[0]), __uint_as_float(rr[1])); }
DI s16x4 make_qb(unsigned q0, unsigned q1, float M, int h) {
    const float mh = bf_hi(__float_as_uint(M)), r1 = M - mh, mm = bf_hi(__float_as_uint(r1)), r2 = r1 - mm, ml = bf_hi(__float_as_uint(r2));
    u32x2 w;
    w[0] = h ? ((__float_as_uint(mh) >> 16) | (__float_as_uint(mm) & 0xffff0000u)) ^ 0x80008000u : q0;
    w[1] = h ? (__float_as_uint(ml) >> 16) ^ 0x8000u : q1;
    return __builtin_bit_cast(s16x4, w);
}

DI void attn_fallback(f32x16& o, const char* kv, bf16x8 qa, unsigned q0, unsigned q1, int lane, int kboff, int voff) {
    const int h = lane >> 5;
    const f32x16 zero = {};
    float m = -INFINITY;
    const s16x4 qb0 = make_qb(q0, q1, 0.f, h);
    for (int t = 0; t < NT; ++t) {
        const char* tb = kv + t * KVTILE_B;
        const bf16x8 ka = *(const bf16x8*)(tb + lane * 16); const s16x4 kb = *(const s16x4*)(tb + kboff);
        f32x16 s = MFMA32(ka, qa, zero); s = MFMA8(kb, qb0, s);
#pragma unroll
        for (int i = 0; i < 16; ++i) m = fmaxf(m, s[i]);
    }
    m = swap32_max(m);
    const s16x4 qb = make_qb(q0, q1, m, h);
    o = zero;
    for (int t = 0; t < NT; ++t) {
        const char* tb = kv + t * KVTILE_B;
        const bf16x8 ka = *(const bf16x8*)(tb + lane * 16); const s16x4 kb = *(const s16x4*)(tb + kboff);
        const bf16x8 va = *(const bf16x8*)(tb + voff), vb = *(const bf16x8*)(tb + voff + V_STEP);
        f32x16 s = MFMA32(ka, qa, zero); s = MFMA8(kb, qb, s);
        u32x4 p0, p1;
#pragma unroll
        for (int i = 0; i < 4; ++i) { p0[i] = cvtpk(__builtin_amdgcn_exp2f(s[2 * i]), __builtin_amdgcn_exp2f(s[2 * i + 1])); p1[i] = cvtpk(__builtin_amdgcn_exp2f(s[8 + 2 * i]), __builtin_amdgcn_exp2f(s[9 + 2 * i])); }
        o = MFMA32(va, __builtin_bit_cast(bf16x8, p0), o);
        o = MFMA32(vb, __builtin_bit_cast(bf16x8, p1), o);
    }
}

constexpr int SPIN_MAX = 1 << 20;
__global__ __launch_bounds__(512, 2) void gated_mix_mega(const unsigned short* __restrict__ x, const unsigned short* __restrict__ wqkv, const unsigned short* __restrict__ wo, const unsigned short* __restrict__ w1,
                                                         const unsigned short* __restrict__ w2, const unsigned short* __restrict__ gw, char* __restrict__ ws, unsigned short* __restrict__ out) {
    extern __shared__ __attribute__((aligned(16))) char lds[];
    const int tid = threadIdx.x, lane = tid & 63, wid = rfl(tid >> 6);
    const int bid = blockIdx.x, xcd = bid & 7, slot = bid >> 3;
    const int h = lane >> 5, r = lane & 31;
    const rsrc_t rs = ws_rsrc(ws);
    const unsigned lds0 = (unsigned)(uintptr_t)lds, lane16 = lane * 16;
    volatile LAS unsigned* ctl = (volatile LAS unsigned*)(lds + CTL_OFF);
    const int rt = 4 * (xcd >> 1) + (slot & 3), ci = slot >> 2, ct = 8 * (xcd & 1) + ci;
    const bool tile1 = ct < 15;
    const char* xbase = (const char*)x + (size_t)rt * 128 * 1280;
    const int k0 = (ci + 3 * (slot & 3)) % 10;
    const int head = xcd * 4 + (slot & 3), qblk = slot >> 2;
    const char* kv = ws + WS_KV + (size_t)head * NT * KVTILE_B;
    const int Tq = qblk * 8 + wid;
    const char* qt = ws + WS_Q + ((size_t)head * NT + Tq) * QTILE_B;
    bf16x8 qa; u32x2 qbr; f32x4 km4; float gate;
    auto q_loads = [&]() {
        asm volatile("s_nop 4\n\tglobal_load_dwordx4 %0, %1, %2" : "=v"(qa) : "v"(lane16), "s"(qt) : "memory");
        asm volatile("s_nop 4\n\tglobal_load_dwordx2 %0, %1, %2" : "=v"(qbr) : "v"((unsigned)(1024 + r * 8)), "s"(qt) : "memory");
        asm volatile("s_nop 4\n\tglobal_load_dwordx4 %0, %1, %2" : "=v"(km4) : "v"((unsigned)((lane < 40 ? lane : 0) * 16)), "s"(ws + WS_KMAX + (size_t)head * 640) : "memory");
        asm volatile("s_nop 4\n\tglobal_load_dword %0, %1, %2" : "=v"(gate) : "v"((unsigned)((Tq * 32 + r) * 4)), "s"(ws + WS_GATE) : "memory");
    };
    auto wait_seen = [&]() { for (int it = 0; it < SPIN_MAX; ++it) { if (ctl[1] != 0u) break; __builtin_amdgcn_s_sleep(1); } };
    auto gates = [&]() {
        const int aidx = (xcd >> 1) * 4 + (slot & 3);
        const u32x4* gr = (const u32x4*)gw + (lane & 7);
#pragma unroll 1
        for (int ps = 0; ps < 2; ++ps) {
            const int tok = aidx * 128 + wid * 16 + ps * 8 + (lane >> 3);
            const u32x4* xr = (const u32x4*)(x + (size_t)tok * DM) + (lane & 7);
            float sm = 0.f;
#pragma unroll
            for (int i = 0; i < 10; ++i) { const u32x4 xv = xr[i * 8], gv = gr[i * 8];
#pragma unroll
                for (int k = 0; k < 4; ++k) { const unsigned a_ = xv[k], b_ = gv[k]; sm = __builtin_amdgcn_fdot2_f32_bf16(__builtin_bit_cast(bf16x2_t, a_), __builtin_bit_cast(bf16x2_t, b_), sm, false); } }
            sm = dpp_add<0xB1>(sm); sm = dpp_add<0x4E>(sm); sm = dpp_add<0x141>(sm);
            const float lg = bf_round(sm);
            const float g = bf_round(__builtin_amdgcn_rcpf(1.f + __builtin_amdgcn_exp2f(lg * -1.4426950408889634f)));
            if ((lane & 7) == 0) st4f(ws + WS_GATE + tok * 4, g);
        }
    };
    GDma<true> d2; d2.init(xbase, (const char*)w1 + ct * 192, FF * 2, lds, lane, k0);
    {
        u64 pc; asm volatile("s_getpc_b64 %0" : "=s"(pc));
        const int kb = slot * 8 + wid;
        if (kb < 42) glds16((const char*)((pc + 15ull) & ~15ull) + kb * 1024, lane16, lds0 + 152 * 1024);
    }
    STAMP(0, 0);
    if (wid < 4) {
        GDma<false> d1; d1.init(xbase, (const char*)wqkv + ct * 256, 3 * DM * 2, lds, lane, k0);
        if (tile1) {
#pragma unroll
            for (int jj = 0; jj < 4; ++jj) d1.xp(d1.kact(0), 0, 4 * wid + jj, jj & 1);
        }
        for (int pc = wid; pc < 10; pc += 4) rt_load(w2, wo, bid, lds0 + RT_OFF, pc, lane);
        if (!tile1) for (int pc = wid; pc < 10; pc += 4) rt_load(w2, wo, 256 + (xcd >> 1) * 4 + (slot & 3), lds0 + RT_OFF + 10240, pc, lane);
        if (wid == 0) {
            const u64 tg = flag_get(ws + WS_EPOCH) + 1ull;
            if (lane == 0) { ctl[0] = 0u; ctl[1] = 0u; ctl[2] = (unsigned)tg; ctl[3] = (unsigned)(tg >> 32); }
        }
        asm volatile("s_waitcnt vmcnt(0) lgkmcnt(0)\n\ts_barrier" ::: "memory");
        if (tile1) {
            const int wm = wid >> 1, wn = wid & 1;
            f32x16 acc[2][2] = {};
            auto nohook = [](int, bool) {};
            if (ct >= 10) {
                gemm_loop<false, true, 2, 2>(acc, lds, wid, lane, nohook);
                STAMP(0, 1);
#pragma unroll
                for (int nt = 0; nt < 2; ++nt) {
                    const int phi = (ct - 10) * 128 + wn * 64 + nt * 32 + r;
                    const int hd = (phi * 3277) >> 16, d = phi - hd * 20;
                    const unsigned dst = (unsigned)WS_KV + (unsigned)(hd * NT + rt * 4 + wm * 2) * KVTILE_B + V_OFF + (h * V_ROWS + d) * 16;
#pragma unroll
                    for (int mt = 0; mt < 2; ++mt)
#pragma unroll
                        for (int s = 0; s < 2; ++s) {
                            const f32x16& a = acc[mt][nt];
                            const u32x4 w = {cvtpk(a[8 * s], a[8 * s + 1]), cvtpk(a[8 * s + 2], a[8 * s + 3]), cvtpk(a[8 * s + 4], a[8 * s + 5]), cvtpk(a[8 * s + 6], a[8 * s + 7])};
                            st16(rs, dst + mt * KVTILE_B + s * V_STEP, w);
                        }
                }
                BARL();
            } else {
                gemm_loop<false, false, 2, 2>(acc, lds, wid, lane, nohook);
                STAMP(0, 1);
                const bool isQ = ct < 5;
                const float sc = isQ ? QSCALE : 1.f;
                const unsigned img = (unsigned)(isQ ? WS_Q : WS_KV), tileb = isQ ? QTILE_B : KVTILE_B;
                const int G = (isQ ? ct : ct - 5) * 32 + wn * 16 + h;
                u32x2 P[2][8]; float nrm[8];
#pragma unroll
                for (int p = 0; p < 8; ++p) {
                    const int nt = p >> 2, g = p & 3; float m = 0.f;
#pragma unroll
                    for (int mt = 0; mt < 2; ++mt) {
                        const f32x16& a = acc[mt][nt];
                        P[mt][p] = (u32x2){cvtpk(a[4 * g] * sc, a[4 * g + 1] * sc), cvtpk(a[4 * g + 2] * sc, a[4 * g + 3] * sc)};
                        m = fmaxf(m, __builtin_fmaf(a[4 * g], a[4 * g], a[4 * g + 1] * a[4 * g + 1]) + __builtin_fmaf(a[4 * g + 2], a[4 * g + 2], a[4 * g + 3] * a[4 * g + 3]));
                    }
                    nrm[p] = m;
                }
#pragma unroll
                for (int p = 0; p < 8; ++p) {
                    const int gam = G + 2 * p, hd = (gam * 205) >> 10, dg = gam - 5 * hd;
                    const unsigned base = img + (unsigned)(hd * NT + rt * 4 + wm * 2) * tileb;
#pragma unroll
                    for (int mt = 0; mt < 2; ++mt) {
                        const unsigned bm = base + mt * tileb;
                        if (dg < 2) {
                            if (p < 7) st16(rs, bm + dg * 512 + r * 16, (u32x4){P[mt][p][0], P[mt][p][1], P[mt][(p + 1) & 7][0], P[mt][(p + 1) & 7][1]});
                            else st8(rs, bm + dg * 512 + r * 16, P[mt][p]);
                        } else if (dg == 4) st8(rs, bm + 1024 + r * 8, P[mt][p]);
                        else if (p == 0) st8(rs, bm + (dg - 2) * 512 + r * 16 + 8, P[mt][p]);
                    }
                }
                if (!isQ) {
#pragma unroll
                    for (int i = 0; i < 8; ++i) { float m = nrm[i]; m = dpp_max<0xB1>(m); m = dpp_max<0x4E>(m); m = dpp_max<0x141>(m); m = dpp_max<0x140>(m);
                        auto rr = __builtin_amdgcn_permlane16_swap(__float_as_uint(m), __float_as_uint(m), false, false); nrm[i] = fmaxf(__uint_as_float(rr[0]), __uint_as_float(rr[1])); }
                    float mine = nrm[0];
#pragma unroll
                    for (int i = 1; i < 8; ++i) mine = r == i ? nrm[i] : mine;
                    if (r < 8) st4f(ws + WS_KMAX + (size_t)((G + 2 * r) * 32 + rt * 2 + wm) * 4, mine);
                }
                BARL();
            }
        } else { gates(); BARL(); }
        STAMP(0, 2);
        BARL();
        f32x16 acc2[1][3] = {};
        auto hook2 = [&](int kc, bool before) {
            if (kc == 2) { if (before) asm volatile("s_waitcnt vmcnt(0)" ::: "memory");
                           else if (tid == 0) flag_set(ws + WS_F1 + bid * 8, ((u64)ctl[3] << 32) | ctl[2]); }
        };
        gemm_loop<true, false, 1, 3>(acc2, lds, wid, lane, hook2);
        STAMP(0, 3);
        wait_seen();
        q_loads();
        {   const int tok = rt * 128 + wid * 32 + r, rho = tok >> 4;
#pragma unroll
            for (int nt = 0; nt < 3; ++nt) {
                const unsigned dst = (unsigned)WS_AF + (unsigned)(rho * NSIG + ct * 3 + nt) * 1024 + (h * 16 + (r & 15)) * 16;
                float sv[16];
#pragma unroll
                for (int i = 0; i < 16; ++i) { const float v = acc2[0][nt][i]; sv[i] = v * __builtin_amdgcn_rcpf(1.f + __builtin_amdgcn_exp2f(v * -1.4426950408889634f)); }
#pragma unroll
                for (int s = 0; s < 2; ++s) {
                    const u32x4 w = {cvtpk(sv[8 * s], sv[8 * s + 1]), cvtpk(sv[8 * s + 2], sv[8 * s + 3]), cvtpk(sv[8 * s + 4], sv[8 * s + 5]), cvtpk(sv[8 * s + 6], sv[8 * s + 7])};
                    st16(rs, dst + s * 512, w);
                }
            }
        }
        asm volatile("s_waitcnt vmcnt(6)" : "+v"(qa), "+v"(qbr), "+v"(km4), "+v"(gate) :: "memory");
    } else {
        const int lw = wid - 4;
        auto side_stores = [&]() {
            for (int i = lw; i < 10; i += 4) rt_store(rs, bid, lds + RT_OFF, i, lane);
            if (!tile1) for (int i = lw; i < 10; i += 4) rt_store(rs, 256 + (xcd >> 1) * 4 + (slot & 3), lds + RT_OFF + 10240, i, lane);
            if (lw == 0) { const int i = bid * 64 + lane;
                const unsigned f = (i & 1) ? 0u : ONES2; st16(rs, (unsigned)WS_KV + (unsigned)(i >> 3) * KVTILE_B + V_OFF + ((i >> 2) & 1) * V_STEP + (((i >> 1) & 1) * V_ROWS + 20 + (i & 1)) * 16, (u32x4){f, f, f, f}); }
        };
        if (tile1) {
            GDma<false> d1; d1.init(xbase, (const char*)wqkv + ct * 256, 3 * DM * 2, lds, lane, k0);
#pragma unroll
            for (int jj = 0; jj < 4; ++jj) d1.wp(d1.kact(0), 0, lw, jj);
            d1.issue(1, 1, lw);
            WAITB(8);
            d1.issue(2, 2, lw); d1.issue(3, 3, lw);
            for (int kc = 0; kc < 9; ++kc) {
                if (kc < 7) WAITB(16); else if (kc == 7) WAITB(15); else WAITB(14);
                if (kc + 4 < 10) d1.issue(kc + 4, kc & 3, lw);
                if (kc == 4) side_stores();
                if (kc >= 6) d2.issue(kc - 6, kc - 6, lw);
            }
            asm volatile("s_barrier" ::: "memory");
        } else {
            asm volatile("s_barrier" ::: "memory");
            side_stores(); gates();
            BARL();
            d2.issue(0, 0, lw); d2.issue(1, 1, lw); d2.issue(2, 2, lw);
        }
        WAITB(14);
        { int st = 0;
          for (int kc = 0; kc < 9; ++kc) {
              if (kc < 8) WAITB(7); else WAITB(0);
              if (kc + 3 < 10) d2.issue(kc + 3, st, lw);
              st = st == 2 ? 0 : st + 1;
          } }
        if (lw == 0) {
            const u64 tag = ((u64)ctl[3] << 32) | ctl[2];
            for (int it = 0; it < SPIN_MAX; ++it) {
                bool ok = true;
#pragma unroll
                for (int i = 0; i < 4; ++i) ok &= flag_get(ws + WS_F1 + (lane + 64 * i) * 8) == tag;
                if (__all(ok)) break;
                __builtin_amdgcn_s_sleep(2);
            }
            __builtin_amdgcn_fence(__ATOMIC_ACQUIRE, "agent");
            asm volatile("s_waitcnt vmcnt(0)" ::: "memory");
            if (lane == 0) { ctl[1] = 1u; if (bid == 0) flag_set(ws + WS_EPOCH, tag); }
        } else wait_seen();
        q_loads();
        for (int j = 0; j < 21; ++j) glds16(kv + (size_t)(lw + 4 * j) * 1024, lane16, lds0 + (lw + 4 * j) * 1024);
        asm volatile("s_waitcnt vmcnt(21)" : "+v"(qa), "+v"(qbr), "+v"(km4), "+v"(gate) :: "memory");
    }
    STAMP(1, 0);
    float km = fmaxf(fmaxf(km4[0], km4[1]), fmaxf(km4[2], km4[3]));
    km = dpp_max<0xB1>(km); km = dpp_max<0x4E>(km); km = dpp_max<0x141>(km);
    km = __builtin_amdgcn_sqrtf(km);
    float M;
    { const float c0 = rdl(km, 0), c1 = rdl(km, 8), c2 = rdl(km, 16), c3 = rdl(km, 24), c4 = rdl(km, 32);
      const u32x4 qw = __builtin_bit_cast(u32x4, qa);
      float n0, n1, n2;
      { const float a = bf_lo(qw[0]), b = bf_hi(qw[0]), c = bf_lo(qw[1]), d = bf_hi(qw[1]); n0 = (a * a + b * b) + (c * c + d * d); }
      { const float a = bf_lo(qw[2]), b = bf_hi(qw[2]), c = bf_lo(qw[3]), d = bf_hi(qw[3]); n1 = (a * a + b * b) + (c * c + d * d); }
      { const unsigned q0 = qbr[0], q1 = qbr[1]; const float a = bf_lo(q0), b = bf_hi(q0), c = bf_lo(q1), d = bf_hi(q1); n2 = (a * a + b * b) + (c * c + d * d); }
      float part = __builtin_amdgcn_sqrtf(n0) * (h ? c1 : c0) + __builtin_amdgcn_sqrtf(n1) * (h ? c3 : c2);
      if (!h) part += __builtin_amdgcn_sqrtf(n2) * c4;
      M = swap32_add(part) * 1.01f; }
    const s16x4 qb = make_qb(qbr[0], qbr[1], M, h);
    const int vrow = r <= 20 ? r : (r < 28 ? 21 : r - 12);
    const int voff = V_OFF + (h * V_ROWS + vrow) * 16, kboff = h ? V_OFF + 20 * 16 : 1024 + r * 8;
    lds_cptr kaC = (lds_cptr)lds + lane * 16, kbC = (lds_cptr)lds + kboff, vC = (lds_cptr)lds + voff;
    lds_cptr kaN = kaC + K2_BIG, kbN = kbC + K2_BIG, vN = vC + K2_BIG;
    const f32x16 zero = {};
    f32x16 o = {}, sA, sB;
    bf16x8 ka, va, vb; s16x4 kb;
    u32x4 plo, phi;
    if (wid >= 4) asm volatile("s_waitcnt vmcnt(15)" ::: "memory");
    asm volatile("s_barrier" ::: "memory");
    STAMP(1, 1);
    ka = lds128(kaC); kb = lds64(kbC);
    sA = MFMA32(ka, qa, zero); sA = MFMA8(kb, qb, sA);
    ka = lds128(kaC + KVTILE_B); kb = lds64(kbC + KVTILE_B);
    va = lds128(vC);
    vb = (bf16x8){}; phi = (u32x4){};
    f32x4 e3 = {};
    const int ng = (xcd & 1) * 4 + (slot & 3), bt = (xcd >> 1) * 8 + (slot >> 2);
    const char* ap = ws + WS_AF + (size_t)bt * 4 * NSIG * 1024;
    const char* bp = ws + WS_BF + (size_t)ng * NSIG * 5 * 1024;
    const int rot = ((slot >> 2) + 3 * (slot & 3)) % 6;
    auto hs = [&](int i) { int st = i + rot; if (st >= 6) st -= 6; return wid * 6 + st; };
    bf16x8 a[3][4], b[3][5];
    auto ldA1 = [&](int sg, int mt, bf16x8& d) { gld128_sc1(d, ap + ((size_t)mt * NSIG + sg) * 1024, lane16); };
    auto ldB1 = [&](int sg, int nt, bf16x8& d) { gld128(d, bp + sg * 5120 + nt * 1024, lane16); };
    const u64 tag = ((u64)ctl[3] << 32) | ctl[2];
#define EXP4(X, i) do { X[i] = __builtin_amdgcn_exp2f(X[i]); X[i + 1] = __builtin_amdgcn_exp2f(X[i + 1]); X[i + 2] = __builtin_amdgcn_exp2f(X[i + 2]); X[i + 3] = __builtin_amdgcn_exp2f(X[i + 3]); } while (0)
#define KADDR(A, i) (((i) < 16) ? (A##C + (i) * KVTILE_B) : (A##N + ((i) - 16) * KVTILE_B))
#define STEP(C, N, i, X0, X1) do { \
        bf16x8 ka2, va2, vb2; s16x4 kb2; \
        N = MFMA32(ka, qa, zero); SBAR(); \
        ka2 = lds128(KADDR(ka, (i) + 2)); \
        phi[2] = cvtpk(e3[0], e3[1]); phi[3] = cvtpk(e3[2], e3[3]); PIN(phi); SBAR(); EXP4(C, 0); PIN(C); SBAR(); \
        o = MFMA32(vb, __builtin_bit_cast(bf16x8, phi), o); SBAR(); \
        X0; \
        vb2 = lds128(vC + (i) * KVTILE_B + V_STEP); \
        plo[0] = cvtpk(C[0], C[1]); plo[1] = cvtpk(C[2], C[3]); PIN(plo); SBAR(); EXP4(C, 4); PIN(C); SBAR(); \
        N = MFMA8(kb, qb, N); SBAR(); \
        kb2 = lds64(KADDR(kb, (i) + 2)); \
        plo[2] = cvtpk(C[4], C[5]); plo[3] = cvtpk(C[6], C[7]); PIN(plo); SBAR(); EXP4(C, 8); PIN(C); SBAR(); \
        o = MFMA32(va, __builtin_bit_cast(bf16x8, plo), o); SBAR(); \
        X1; \
        va2 = lds128(KADDR(v, (i) + 1)); \
        phi[0] = cvtpk(C[8], C[9]); phi[1] = cvtpk(C[10], C[11]); PIN(phi); SBAR(); \
        e3[0] = __builtin_amdgcn_exp2f(C[12]); e3[1] = __builtin_amdgcn_exp2f(C[13]); e3[2] = __builtin_amdgcn_exp2f(C[14]); e3[3] = __builtin_amdgcn_exp2f(C[15]); PIN(e3); SBAR(); \
        ka = ka2; kb = kb2; va = va2; vb = vb2; \
    } while (0)
#define PIECE(j) do { if (8 * (j) + 7 < 42 || 8 * (j) + wid < 42) glds16(src + (j) * 8192, lane16, dst + (j) * 8192); } while (0)
    const char* src = kv + (84 + wid) * 1024; unsigned dst = lds0 + wid * 1024 + 2 * K2_BIG;
#pragma unroll 1
    for (int c = 0; c < 3; ++c) {
        STEP(sA, sB, 0, , ); STEP(sB, sA, 1, , ); STEP(sA, sB, 2, , ); STEP(sB, sA, 3, , ); STEP(sA, sB, 4, , );
        if (c == 0) { if (wid >= 4) asm volatile("s_waitcnt vmcnt(10)" ::: "memory"); asm volatile("s_barrier" ::: "memory"); }
        STEP(sB, sA, 5, , ); STEP(sA, sB, 6, , ); STEP(sB, sA, 7, , ); STEP(sA, sB, 8, , ); STEP(sB, sA, 9, , ); STEP(sA, sB, 10, , ); STEP(sB, sA, 11, , ); STEP(sA, sB, 12, , );
        if (c == 2 && wid == 0) {
            for (int it = 0; it < SPIN_MAX; ++it) {
                if (__all(flag_get(ws + WS_F2 + ((bt >> 1) * 16 + (lane & 15)) * 8) == tag)) break;
                __builtin_amdgcn_s_sleep(2);
            }
        }
        WAITB(0);
        if (c == 0 && tid == 0) flag_set(ws + WS_F2 + (rt * 16 + ct) * 8, tag);
        if (c < 2) { STEP(sB, sA, 13, PIECE(0), PIECE(1)); STEP(sA, sB, 14, PIECE(2), PIECE(3)); STEP(sB, sA, 15, PIECE(4), PIECE(5)); }
        else { STEP(sB, sA, 13, , ); STEP(sA, sB, 14, , ); STEP(sB, sA, 15, , ); }
        const int d = (c == 1) ? -2 * K2_BIG : K2_BIG;
        kaC = kaN; kbC = kbN; vC = vN; kaN += d; kbN += d; vN += d;
        src += 42 * 1024; dst -= 2 * K2_BIG;
    }
#define PA(s, m) ldA1(hs(s), m, a[s][m])
#define PB(s, n) ldB1(hs(s), n, b[s][n])
    STEP(sA, sB, 0, PA(0, 0), PA(0, 1)); STEP(sB, sA, 1, PA(0, 2), PA(0, 3)); STEP(sA, sB, 2, PB(0, 0), PB(0, 1)); STEP(sB, sA, 3, PB(0, 2), PB(0, 3));
    STEP(sA, sB, 4, PB(0, 4), PA(1, 0)); STEP(sB, sA, 5, PA(1, 1), PA(1, 2)); STEP(sA, sB, 6, PA(1, 3), PB(1, 0)); STEP(sB, sA, 7, PB(1, 1), PB(1, 2));
    STEP(sA, sB, 8, PB(1, 3), PB(1, 4)); STEP(sB, sA, 9, PA(2, 0), PA(2, 1)); STEP(sA, sB, 10, PA(2, 2), PA(2, 3)); STEP(sB, sA, 11, PB(2, 0), PB(2, 1));
    STEP(sA, sB, 12, PB(2, 2), PB(2, 3)); STEP(sB, sA, 13, PB(2, 4), ); STEP(sA, sB, 14, , ); STEP(sB, sA, 15, , );
    phi[2] = cvtpk(e3[0], e3[1]); phi[3] = cvtpk(e3[2], e3[3]);
    o = MFMA32(vb, __builtin_bit_cast(bf16x8, phi), o);
    STAMP(1, 2);
    float l; { auto rr = __builtin_amdgcn_permlane32_swap(__float_as_uint(o[8]), __float_as_uint(o[8]), false, false); l = __uint_as_float(rr[1]); }
    if (__builtin_expect(__any(!(l >= 8.6736174e-19f && l <= 1.0e30f)), 0)) {
        asm volatile("s_waitcnt vmcnt(0)" ::: "memory");
        attn_fallback(o, kv, qa, qbr[0], qbr[1], lane, kboff, voff);
        auto rr = __builtin_amdgcn_permlane32_swap(__float_as_uint(o[8]), __float_as_uint(o[8]), false, false); l = __uint_as_float(rr[1]);
    }
    const float f = gate * __builtin_amdgcn_rcpf(l);
    const int tok = Tq * 32 + r;
    char* ab = ws + WS_AF + (size_t)(tok >> 4) * NSIG * 1024 + (tok & 15) * 16;
#pragma unroll
    for (int g = 0; g < 3; ++g) {
        const int gam = head * 5 + 2 * g + h;
        const int sig = 48 + (gam >> 3), c = ((gam >> 2) & 1) * 2 + (gam & 1), half = (gam >> 1) & 1;
        const u32x2 w = {cvtpk(o[4 * g] * f, o[4 * g + 1] * f), cvtpk(o[4 * g + 2] * f, o[4 * g + 3] * f)};
        if (g < 2 || h == 0) __hip_atomic_store((u64*)(ab + sig * 1024 + c * 256 + half * 8), ((u64)w[1] << 32) | w[0], __ATOMIC_RELAXED, __HIP_MEMORY_SCOPE_AGENT);
    }
    STAMP(1, 3);
    {
        f32x4 acc[4][5] = {};
        auto loadA = [&](int sg, bf16x8 (&da)[4]) {
#pragma unroll
            for (int mt = 0; mt < 4; ++mt) gld128_sc1(da[mt], ap + ((size_t)mt * NSIG + sg) * 1024, lane16);
        };
        auto loadB = [&](int sg, bf16x8 (&db)[5]) {
#pragma unroll
            for (int nt = 0; nt < 5; ++nt) gld128(db[nt], bp + sg * 5120 + nt * 1024, lane16);
        };
        auto mma = [&](bf16x8 (&da)[4], bf16x8 (&db)[5]) {
            SBAR();
#pragma unroll
            for (int nt = 0; nt < 5; ++nt)
#pragma unroll
                for (int mt = 0; mt < 4; ++mt) acc[mt][nt] = MFMA16(db[nt], da[mt], acc[mt][nt]);
            SBAR();
        };
        wait9<3>(a[0], b[0]);
        asm volatile("" : "+v"(a[1][0]), "+v"(a[1][1]), "+v"(a[1][2]), "+v"(a[1][3]), "+v"(b[1][0]), "+v"(b[1][1]), "+v"(b[1][2]), "+v"(b[1][3]), "+v"(b[1][4]));
        asm volatile("" : "+v"(a[2][0]), "+v"(a[2][1]), "+v"(a[2][2]), "+v"(a[2][3]), "+v"(b[2][0]), "+v"(b[2][1]), "+v"(b[2][2]), "+v"(b[2][3]), "+v"(b[2][4]));
        mma(a[0], b[0]); loadA(hs(3), a[0]); loadB(hs(3), b[0]);
        mma(a[1], b[1]); loadA(hs(4), a[1]); loadB(hs(4), b[1]);
        asm volatile("s_waitcnt vmcnt(18)" ::: "memory");
        if (lane == 0) {
            const unsigned old = __hip_atomic_fetch_add((unsigned*)(lds + CTL_OFF), 1u, __ATOMIC_RELAXED, __HIP_MEMORY_SCOPE_WORKGROUP);
            if (old == 7u) flag_set(ws + WS_F3 + (qblk * 32 + head) * 8, tag);
        }
        mma(a[2], b[2]); loadA(hs(5), a[2]); loadB(hs(5), b[2]);
        float og[4];
#pragma unroll
        for (int mt = 0; mt < 4; ++mt) asm volatile("s_nop 4\n\tglobal_load_dword %0, %1, %2" : "=v"(og[mt]) : "v"((unsigned)((bt * 64 + mt * 16 + (lane & 15)) * 4)), "s"(ws + WS_GATE) : "memory");
        wait9<22>(a[0], b[0]); mma(a[0], b[0]); loadB(48 + wid, b[0]);
        wait9<18>(a[1], b[1]); mma(a[1], b[1]); loadB(56 + wid, b[1]);
        wait9<14>(a[2], b[2]); mma(a[2], b[2]);
        if (wid < 4) {
#pragma unroll
            for (int nt = 0; nt < 5; ++nt) gld128_rw(b[2][nt], bp + (64 + wid) * 5120 + nt * 1024, lane16);
        }
        STAMP(2, 0);
        asm volatile("s_waitcnt vmcnt(0)" : "+v"(og[0]), "+v"(og[1]), "+v"(og[2]), "+v"(og[3]) :: "memory");
#pragma unroll
        for (int mt = 0; mt < 4; ++mt) { const float f1 = bf_round(1.f - og[mt]);
#pragma unroll
            for (int nt = 0; nt < 5; ++nt) acc[mt][nt] *= f1; }
        if (wid == 0) {
            for (int it = 0; it < SPIN_MAX; ++it) {
                if (__all(flag_get(ws + WS_F3 + ((bt >> 2) * 32 + r) * 8) == tag)) break;
                __builtin_amdgcn_s_sleep(2);
            }
        }
        asm volatile("s_barrier" ::: "memory");
        STAMP(2, 1);
        loadA(48 + wid, a[0]); loadA(56 + wid, a[1]);
        a[2][0] = (bf16x8){}; a[2][1] = (bf16x8){}; a[2][2] = (bf16x8){}; a[2][3] = (bf16x8){};
        if (wid < 4) {
#pragma unroll
            for (int mt = 0; mt < 4; ++mt) gld128_sc1_rw(a[2][mt], ap + ((size_t)mt * NSIG + 64 + wid) * 1024, lane16);
        }
        wait9<4>(a[0], b[0]); mma(a[0], b[0]);
        wait9<0>(a[1], b[1]); wait9<0>(a[2], b[2]); mma(a[1], b[1]); mma(a[2], b[2]);
        STAMP(2, 2);
#pragma unroll
        for (int mt = 0; mt < 4; ++mt)
#pragma unroll
            for (int nt = 0; nt < 5; ++nt) *(f32x4*)(lds + (wid * 20 + mt * 5 + nt) * 1024 + lane16) = acc[mt][nt];
        BARL();
#pragma unroll
        for (int i = 0; i < 3; ++i) { const int t = wid + 8 * i;
            if (t < 20) {
                f32x4 sum = *(const f32x4*)(lds + t * 1024 + lane16);
#pragma unroll
                for (int w = 1; w < 8; ++w) sum += *(const f32x4*)(lds + (w * 20 + t) * 1024 + lane16);
                const int m4 = t / 5, nt = t - m4 * 5;
                const int tok2 = bt * 64 + m4 * 16 + (lane & 15);
                *(u32x2*)(out + (size_t)tok2 * DM + ng * 80 + nt * 16 + (lane >> 4) * 4) = (u32x2){cvtpk(sum[0], sum[1]), cvtpk(sum[2], sum[3])};
            } }
        STAMP(2, 3);
    }
}

extern "C" void kernel_launch(void* const* d_in, const int* in_sizes, int n_in, void* d_out, int out_size, void* d_ws, size_t ws_size, hipStream_t stream) {
    const unsigned short* x = (const unsigned short*)d_in[0];
    const unsigned short* wqkv = (const unsigned short*)d_in[1];
    const unsigned short* wo = (const unsigned short*)d_in[2];
    const unsigned short* w1 = (const unsigned short*)d_in[4];
    const unsigned short* w2 = (const unsigned short*)d_in[5];
    const unsigned short* gw = (const unsigned short*)d_in[6];
    (void)hipFuncSetAttribute((const void*)gated_mix_mega, hipFuncAttributeMaxDynamicSharedMemorySize, LDS_ALL);
    gated_mix_mega<<<256, 512, LDS_ALL, stream>>>(x, wqkv, wo, w1, w2, gw, (char*)d_ws, (unsigned short*)d_out);
}
```

```cpp
#include <hip/hip_runtime.h>
#include <hip/hip_bf16.h>
#include <stdint.h>

typedef short bf16x8 __attribute__((ext_vector_type(8)));
typedef short s16x4 __attribute__((ext_vector_type(4)));
typedef float f32x16 __attribute__((ext_vector_type(16)));
typedef float f32x4 __attribute__((ext_vector_type(4)));
typedef float f32x2 __attribute__((ext_vector_type(2)));
typedef unsigned u32x4 __attribute__((ext_vector_type(4)));
typedef unsigned u32x2 __attribute__((ext_vector_type(2)));
typedef __bf16 bf16x2_t __attribute__((ext_vector_type(2)));
#define LAS __attribute__((address_space(3)))
typedef LAS const char* lds_cptr;
#define DI __device__ __forceinline__
#define SBAR() __builtin_amdgcn_sched_barrier(0)
#define PIN(x) asm volatile("" : "+v"(x))
#define MFMA32(a, b, c) __builtin_amdgcn_mfma_f32_32x32x16_bf16(a, b, c, 0, 0, 0)
#define MFMA16(a, b, c) __builtin_amdgcn_mfma_f32_16x16x32_bf16(a, b, c, 0, 0, 0)
#define MFMA8(a, b, c) __builtin_amdgcn_mfma_f32_32x32x8bf16_1k(a, b, c, 0, 0, 0)

constexpr int S = 2048, DM = 640, NH = 32, DH = 20, FF = 1536;
constexpr int NT = S / 32;
constexpr int QTILE_B = 1280;
constexpr int KVTILE_B = 2688;
constexpr int V_OFF = 1280, V_STEP = 704, V_ROWS = 22;
constexpr int NSIG = 68;
constexpr size_t WS_Q = 0;
constexpr size_t WS_KV = WS_Q + (size_t)NH * NT * QTILE_B;
constexpr size_t WS_AF = WS_KV + (size_t)NH * NT * KVTILE_B;
constexpr size_t WS_BF = WS_AF + (size_t)128 * NSIG * 1024;
constexpr size_t WS_KMAX = WS_BF + (size_t)8 * NSIG * 5 * 1024;
constexpr size_t WS_GATE = WS_KMAX + (size_t)160 * 32 * 4;
constexpr size_t WS_F1 = WS_GATE + (size_t)S * 4;
constexpr size_t WS_F2 = WS_F1 + 2048;
constexpr size_t WS_F3 = WS_F2 + 2048;
constexpr size_t WS_EPOCH = WS_F3 + 2048;
constexpr size_t WS_END = WS_EPOCH + 256;
constexpr float QSCALE = 0.22360679774997896f * 1.4426950408889634f;
constexpr unsigned ONES2 = 0x3F803F80u;

DI unsigned cvtpk(float lo, float hi) { f32x2 v = {lo, hi}; bf16x2_t b = __builtin_convertvector(v, bf16x2_t); return __builtin_bit_cast(unsigned, b); }
DI float bf_lo(unsigned u) { return __uint_as_float(u << 16); }
DI float bf_hi(unsigned u) { return __uint_as_float(u & 0xffff0000u); }
DI float bf_round(float x) { return bf_lo(cvtpk(x, x)); }
DI void glds16(const void* sbase, unsigned voff, unsigned lds_base) {
    unsigned sv;
    asm volatile("s_mov_b32 %0, m0\n\ts_mov_b32 m0, %3\n\ts_nop 2\n\tglobal_load_lds_dwordx4 %1, %2\n\ts_mov_b32 m0, %0"
                 : "=&s"(sv) : "v"(voff), "s"(sbase), "s"(lds_base) : "memory");
}
DI void glds16_sc1(const void* sbase, unsigned voff, unsigned lds_base) {
    unsigned sv;
    asm volatile("s_mov_b32 %0, m0\n\ts_mov_b32 m0, %3\n\ts_nop 2\n\tglobal_load_lds_dwordx4 %1, %2 sc1\n\ts_mov_b32 m0, %0"
                 : "=&s"(sv) : "v"(voff), "s"(sbase), "s"(lds_base) : "memory");
}
typedef short v4i16_t __attribute__((ext_vector_type(4)));
DI s16x4 ldstr(lds_cptr p) { return __builtin_bit_cast(s16x4, __builtin_amdgcn_ds_read_tr16_b64_v4i16((LAS v4i16_t*)p)); }
DI bf16x8 lds128(lds_cptr p) { return *(const LAS bf16x8*)p; }
DI s16x4 lds64(lds_cptr p) { return *(const LAS s16x4*)p; }
DI unsigned rfl(unsigned x) { return __builtin_amdgcn_readfirstlane(x); }
#define STAMP(k, i) do { } while (0)

DI float rdl(float x, int l) { return __uint_as_float(__builtin_amdgcn_readlane(__float_as_uint(x), l)); }
typedef __amdgpu_buffer_rsrc_t rsrc_t;
typedef unsigned long long u64;
DI rsrc_t ws_rsrc(char* ws) { return __builtin_amdgcn_make_buffer_rsrc(ws, 0, (int)WS_END, 0x00020000); }
DI void st16(rsrc_t rs, unsigned off, u32x4 v) { __builtin_amdgcn_raw_buffer_store_b128(v, rs, off, 0, 16); }
DI void st8(rsrc_t rs, unsigned off, u32x2 v) { __builtin_amdgcn_raw_buffer_store_b64(v, rs, off, 0, 16); }
DI void st4f(char* p, float v) { __hip_atomic_store((float*)p, v, __ATOMIC_RELAXED, __HIP_MEMORY_SCOPE_AGENT); }
DI void flag_set(char* p, u64 tag) { __hip_atomic_store((u64*)p, tag, __ATOMIC_RELAXED, __HIP_MEMORY_SCOPE_AGENT); }
DI u64 flag_get(const char* p) { return __hip_atomic_load((const u64*)p, __ATOMIC_RELAXED, __HIP_MEMORY_SCOPE_AGENT); }
template <int CTRL> DI float dpp_max(float x) { const float y = __uint_as_float(__builtin_amdgcn_update_dpp(0u, __float_as_uint(x), CTRL, 0xf, 0xf, true)); return fmaxf(x, y); }
template <int CTRL> DI float dpp_add(float x) { const float y = __uint_as_float(__builtin_amdgcn_update_dpp(0u, __float_as_uint(x), CTRL, 0xf, 0xf, true)); return x + y; }
DI void gld128(bf16x8& d, const char* sbase, unsigned voff) { asm volatile("s_nop 4\n\tglobal_load_dwordx4 %0, %1, %2" : "=v"(d) : "v"(voff), "s"(sbase) : "memory"); }
DI void gld128_sc1(bf16x8& d, const char* sbase, unsigned voff) { asm volatile("s_nop 4\n\tglobal_load_dwordx4 %0, %1, %2 sc1" : "=v"(d) : "v"(voff), "s"(sbase) : "memory"); }
DI void gld128_rw(bf16x8& d, const char* sbase, unsigned voff) { asm volatile("s_nop 4\n\tglobal_load_dwordx4 %0, %1, %2" : "+v"(d) : "v"(voff), "s"(sbase) : "memory"); }
DI void gld128_sc1_rw(bf16x8& d, const char* sbase, unsigned voff) { asm volatile("s_nop 4\n\tglobal_load_dwordx4 %0, %1, %2 sc1" : "+v"(d) : "v"(voff), "s"(sbase) : "memory"); }
template <int N> DI void wait9(bf16x8 (&a)[4], bf16x8 (&b)[5]) {
    asm volatile("s_waitcnt vmcnt(%9)" : "+v"(a[0]), "+v"(a[1]), "+v"(a[2]), "+v"(a[3]), "+v"(b[0]), "+v"(b[1]), "+v"(b[2]), "+v"(b[3]), "+v"(b[4]) : "i"(N) : "memory");
}
#define WAITB(n) asm volatile("s_waitcnt vmcnt(" #n ")\n\ts_barrier" ::: "memory")
#define BARL() asm volatile("s_waitcnt lgkmcnt(0)\n\ts_barrier" ::: "memory")

constexpr int P1_STG = 32768;
constexpr int RT_OFF = 131072;
constexpr int P2_STG = 28672;
constexpr int CTL_OFF = 163584;
DI constexpr int p2_base(int s) { return CTL_OFF - (s + 1) * P2_STG; }
constexpr int K2_BIG = 16 * KVTILE_B;
constexpr int EP_OFF = 151552;
constexpr int LDS_ALL = 163840;

template <bool N96>
struct GDma {
    static constexpr int NW = N96 ? 3 : 4;
    const char* xbase; const char* wbase; int ldw2, k0; unsigned lds0, xoff0, xoff1, woff[3];
    DI int kact(int kc) const { kc += k0; return kc >= 10 ? kc - 10 : kc; }
    DI static unsigned sbase(int st) { return N96 ? (unsigned)(CTL_OFF - (st + 1) * P2_STG) : (unsigned)(st * P1_STG); }
    DI void init(const char* xb, const char* wb, int ld, char* lds, int lane, int k0_) {
        xbase = xb; wbase = wb; ldw2 = ld; k0 = k0_; lds0 = (unsigned)(uintptr_t)lds;
        const int rr = lane >> 3;
        xoff0 = rr * 1280 + (((lane & 7) ^ ((lane >> 4) & 7)) << 4); xoff1 = rr * 1280 + (((lane & 7) ^ ((4 + (lane >> 4)) & 7)) << 4);
        if (N96) {
#pragma unroll
            for (int p = 0; p < 3; ++p) { const int o = 1024 * p + 16 * lane, row = (o * 21846) >> 22, col = o - row * 192; woff[p] = row * ldw2 + col; }
        } else woff[0] = (lane >> 4) * ldw2 + ((((lane & 15) ^ ((lane >> 4) << 2))) << 4);
    }
    DI void xp(int kc, int st, int j, int par) const { glds16(xbase + (size_t)j * 8 * 1280 + kc * 128, par ? xoff1 : xoff0, lds0 + sbase(st) + j * 1024); }
    DI void wp(int kc, int st, int lw, int jj) const {
        if (N96) glds16(wbase + (size_t)(kc * 64 + 16 * lw) * ldw2, woff[jj], lds0 + sbase(st) + 16384 + (3 * lw + jj) * 1024);
        else glds16(wbase + (size_t)(kc * 64 + 16 * lw + 4 * jj) * ldw2, woff[0], lds0 + sbase(st) + 16384 + (4 * lw + jj) * 1024);
    }
    DI void issue(int kc_, int st, int lw) const {
        const int kc = kact(kc_);
#pragma unroll
        for (int jj = 0; jj < NW; ++jj) wp(kc, st, lw, jj);
#pragma unroll
        for (int jj = 0; jj < 4; ++jj) xp(kc, st, 4 * lw + jj, jj & 1);
    }
};

DI void rt_load(const unsigned short* w2, const unsigned short* wo, int u, unsigned ldsb, int piece, int lane) {
    const int sig = u >> 2, s = (u >> 1) & 1, cb = u & 1;
    const char* src = (sig < 48 ? (const char*)w2 + (size_t)sig * 32 * 1280 : (const char*)wo + (size_t)(sig - 48) * 32 * 1280) + s * 16 * 1280 + cb * 640;
    const int o = piece * 1024 + lane * 16, row = (o * 6554) >> 22;
    glds16(src, row * 1280 + (o - row * 640), ldsb + piece * 1024);
}
DI void rt_store(rsrc_t rs, int u, const char* ldsb, int i, int lane) {
    const int sig = u >> 2, s = (u >> 1) & 1, cb = u & 1;
    const int nl = 2 * i + (lane >> 5), hh = (lane >> 4) & 1, q = (lane & 15) >> 2, p = lane & 3;
    lds_cptr a = (lds_cptr)ldsb + (4 * hh + q) * 640 + (16 * nl + 4 * p) * 2;
    const s16x4 lo = ldstr(a), hi = ldstr(a + 8 * 640);
    const bf16x8 f = __builtin_shufflevector(lo, hi, 0, 1, 2, 3, 4, 5, 6, 7);
    const int nu = cb * 20 + nl, ng = (nu * 205) >> 10;
    st16(rs, (unsigned)WS_BF + (unsigned)((ng * NSIG + sig) * 5 + (nu - 5 * ng)) * 1024 + (s * 32 + (lane & 31)) * 16, __builtin_bit_cast(u32x4, f));
}

template <bool N96, bool VT, int MT, int NTL, class HOOK>
DI void gemm_loop(f32x16 (&acc)[MT][NTL], char* lds, int wid, int lane, HOOK hook) {
    const int h = lane >> 5, r = lane & 31;
    constexpr int NST = N96 ? 3 : 4;
    unsigned xa[4];
    { const int row = (N96 ? wid * 32 : (wid >> 1) * 64) + r; const int sw = (row >> 1) & 7;
#pragma unroll
      for (int ks = 0; ks < 4; ++ks) xa[ks] = row * 128 + (((2 * ks + h) ^ sw) << 4); }
    unsigned wa[2];
    { const int blk = (lane >> 4) & 1, q = (lane & 15) >> 2, p = lane & 3;
      if (N96) wa[0] = wa[1] = 16384 + (8 * h + q) * 192 + blk * 32 + p * 8;
      else {
#pragma unroll
          for (int nt = 0; nt < 2; ++nt) { const int ch = (wid & 1) * 8 + nt * 4 + 2 * blk + (p >> 1); wa[nt] = 16384 + (8 * h + q) * 256 + ((ch ^ (q << 2)) << 4) + 8 * (p & 1); } } }
    bf16x8 xf[2][MT], wf[2][NTL];
    auto rdw = [&](int buf, int nt, lds_cptr sb, int ks) {
        lds_cptr a = N96 ? sb + wa[0] + ks * 16 * 192 + nt * 64 : sb + wa[nt & 1] + ks * 4096;
        const s16x4 lo = ldstr(a), hi = ldstr(a + (N96 ? 4 * 192 : 1024));
        wf[buf][nt] = __builtin_shufflevector(lo, hi, 0, 1, 2, 3, 4, 5, 6, 7);
    };
    auto rdx = [&](int buf, int mt, lds_cptr sb, int ks) { xf[buf][mt] = lds128(sb + xa[ks] + mt * 4096); };
    auto mm = [&](int buf, int mt, int nt) { acc[mt][nt] = VT ? MFMA32(xf[buf][mt], wf[buf][nt], acc[mt][nt]) : MFMA32(wf[buf][nt], xf[buf][mt], acc[mt][nt]); };
    { lds_cptr sb = (lds_cptr)lds + GDma<N96>::sbase(0); rdw(0, 0, sb, 0); rdx(0, 0, sb, 0); rdw(0, 1, sb, 0); if (N96) rdw(0, 2 % NTL, sb, 0); else rdx(0, 1 % MT, sb, 0); }
    int st = 0;
#define G_KSTEP(ks, NEXT) do { const int b = (ks) & 1, n = b ^ 1; lds_cptr np = (ks) < 3 ? sb : sn; const int nk = ((ks) + 1) & 3; \
        SBAR(); \
        if constexpr (N96) { \
            mm(b, 0, 0); SBAR(); if (NEXT) { rdw(n, 0, np, nk); rdx(n, 0, np, nk); } SBAR(); \
            mm(b, 0, 1); SBAR(); if (NEXT) { rdw(n, 1, np, nk); } SBAR(); \
            mm(b, 0, 2 % NTL); SBAR(); if (NEXT) { rdw(n, 2 % NTL, np, nk); } SBAR(); \
        } else { \
            mm(b, 0, 0); SBAR(); if (NEXT) { rdw(n, 0, np, nk); } SBAR(); \
            mm(b, 0, 1); SBAR(); if (NEXT) { rdx(n, 0, np, nk); } SBAR(); \
            mm(b, 1 % MT, 0); SBAR(); if (NEXT) { rdw(n, 1, np, nk); } SBAR(); \
            mm(b, 1 % MT, 1); SBAR(); if (NEXT) { rdx(n, 1 % MT, np, nk); } SBAR(); \
        } } while (0)
#pragma unroll 1
    for (int kc = 0; kc < 9; ++kc) {
        lds_cptr sb = (lds_cptr)lds + GDma<N96>::sbase(st);
        st = st == NST - 1 ? 0 : st + 1;
        lds_cptr sn = (lds_cptr)lds + GDma<N96>::sbase(st);
        G_KSTEP(0, true); G_KSTEP(1, true); G_KSTEP(2, true);
        hook(kc, true);
        BARL();
        hook(kc, false);
        G_KSTEP(3, true);
    }
    { lds_cptr sb = (lds_cptr)lds + GDma<N96>::sbase(st); lds_cptr sn = sb;
      G_KSTEP(0, true); G_KSTEP(1, true); G_KSTEP(2, true); G_KSTEP(3, false); }
}

DI float swap32_add(float x) { auto rr = __builtin_amdgcn_permlane32_swap(__float_as_uint(x), __float_as_uint(x), false, false); return __uint_as_float(rr[0]) + __uint_as_float(rr[1]); }
DI float swap32_max(float x) { auto rr = __builtin_amdgcn_permlane32_swap(__float_as_uint(x), __float_as_uint(x), false, false); return fmaxf(__uint_as_float(rr[0]), __uint_as_float(rr[1])); }
DI s16x4 make_qb(unsigned q0, unsigned q1, float M, int h) {
    const float mh = bf_hi(__float_as_uint(M)), r1 = M - mh, mm = bf_hi(__float_as_uint(r1)), r2 = r1 - mm, ml = bf_hi(__float_as_uint(r2));
    u32x2 w;
    w[0] = h ? ((__float_as_uint(mh) >> 16) | (__float_as_uint(mm) & 0xffff0000u)) ^ 0x80008000u : q0;
    w[1] = h ? (__float_as_uint(ml) >> 16) ^ 0x8000u : q1;
    return __builtin_bit_cast(s16x4, w);
}

DI void attn_fallback(f32x16& o, const char* kv, bf16x8 qa, unsigned q0, unsigned q1, int lane, int kboff, int voff) {
    const int h = lane >> 5;
    const f32x16 zero = {};
    float m = -INFINITY;
    const s16x4 qb0 = make_qb(q0, q1, 0.f, h);
    for (int t = 0; t < NT; ++t) {
        const char* tb = kv + t * KVTILE_B;
        const bf16x8 ka = *(const bf16x8*)(tb + lane * 16); const s16x4 kb = *(const s16x4*)(tb + kboff);
        f32x16 s = MFMA32(ka, qa, zero); s = MFMA8(kb, qb0, s);
#pragma unroll
        for (int i = 0; i < 16; ++i) m = fmaxf(m, s[i]);
    }
    m = swap32_max(m);
    const s16x4 qb = make_qb(q0, q1, m, h);
    o = zero;
    for (int t = 0; t < NT; ++t) {
        const char* tb = kv + t * KVTILE_B;
        const bf16x8 ka = *(const bf16x8*)(tb + lane * 16); const s16x4 kb = *(const s16x4*)(tb + kboff);
        const bf16x8 va = *(const bf16x8*)(tb + voff), vb = *(const bf16x8*)(tb + voff + V_STEP);
        f32x16 s = MFMA32(ka, qa, zero); s = MFMA8(kb, qb, s);
        u32x4 p0, p1;
#pragma unroll
        for (int i = 0; i < 4; ++i) { p0[i] = cvtpk(__builtin_amdgcn_exp2f(s[2 * i]), __builtin_amdgcn_exp2f(s[2 * i + 1])); p1[i] = cvtpk(__builtin_amdgcn_exp2f(s[8 + 2 * i]), __builtin_amdgcn_exp2f(s[9 + 2 * i])); }
        o = MFMA32(va, __builtin_bit_cast(bf16x8, p0), o);
        o = MFMA32(vb, __builtin_bit_cast(bf16x8, p1), o);
    }
}

constexpr int SPIN_MAX = 1 << 20;
__global__ __launch_bounds__(512, 2) void gated_mix_mega(const unsigned short* __restrict__ x, const unsigned short* __restrict__ wqkv, const unsigned short* __restrict__ wo, const unsigned short* __restrict__ w1,
                                                         const unsigned short* __restrict__ w2, const unsigned short* __restrict__ gw, char* __restrict__ ws, unsigned short* __restrict__ out) {
    extern __shared__ __attribute__((aligned(16))) char lds[];
    const int tid = threadIdx.x, lane = tid & 63, wid = rfl(tid >> 6);
    const int bid = blockIdx.x, xcd = bid & 7, slot = bid >> 3;
    const int h = lane >> 5, r = lane & 31;
    const rsrc_t rs = ws_rsrc(ws);
    const unsigned lds0 = (unsigned)(uintptr_t)lds, lane16 = lane * 16;
    volatile LAS unsigned* ctl = (volatile LAS unsigned*)(lds + CTL_OFF);
    const int rt = 4 * (xcd >> 1) + (slot & 3), ci = slot >> 2, ct = 8 * (xcd & 1) + ci;
    const bool tile1 = ct < 15;
    const char* xbase = (const char*)x + (size_t)rt * 128 * 1280;
#define K0EXPR 0
    const int k0 = K0EXPR;
    const int head = xcd * 4 + (slot & 3), qblk = slot >> 2;
    const char* kv = ws + WS_KV + (size_t)head * NT * KVTILE_B;
    const int Tq = qblk * 8 + wid;
    const char* qt = ws + WS_Q + ((size_t)head * NT + Tq) * QTILE_B;
    bf16x8 qa; u32x2 qbr; f32x4 km4; float gate;
    auto q_loads = [&]() {
        asm volatile("s_nop 4\n\tglobal_load_dwordx4 %0, %1, %2" : "=v"(qa) : "v"(lane16), "s"(qt) : "memory");
        asm volatile("s_nop 4\n\tglobal_load_dwordx2 %0, %1, %2" : "=v"(qbr) : "v"((unsigned)(1024 + r * 8)), "s"(qt) : "memory");
        asm volatile("s_nop 4\n\tglobal_load_dwordx4 %0, %1, %2" : "=v"(km4) : "v"((unsigned)((lane < 40 ? lane : 0) * 16)), "s"(ws + WS_KMAX + (size_t)head * 640) : "memory");
        asm volatile("s_nop 4\n\tglobal_load_dword %0, %1, %2" : "=v"(gate) : "v"((unsigned)((Tq * 32 + r) * 4)), "s"(ws + WS_GATE) : "memory");
    };
    auto wait_seen = [&]() { for (int it = 0; it < SPIN_MAX; ++it) { if (ctl[1] != 0u) break; __builtin_amdgcn_s_sleep(1); } };
    auto gates = [&]() {
        const int aidx = (xcd >> 1) * 4 + (slot & 3);
        const u32x4* gr = (const u32x4*)gw + (lane & 7);
#pragma unroll 1
        for (int ps = 0; ps < 2; ++ps) {
            const int tok = aidx * 128 + wid * 16 + ps * 8 + (lane >> 3);
            const u32x4* xr = (const u32x4*)(x + (size_t)tok * DM) + (lane & 7);
            float sm = 0.f;
#pragma unroll
            for (int i = 0; i < 10; ++i) { const u32x4 xv = xr[i * 8], gv = gr[i * 8];
#pragma unroll
                for (int k = 0; k < 4; ++k) { const unsigned a_ = xv[k], b_ = gv[k]; sm = __builtin_amdgcn_fdot2_f32_bf16(__builtin_bit_cast(bf16x2_t, a_), __builtin_bit_cast(bf16x2_t, b_), sm, false); } }
            sm = dpp_add<0xB1>(sm); sm = dpp_add<0x4E>(sm); sm = dpp_add<0x141>(sm);
            const float lg = bf_round(sm);
            const float g = bf_round(__builtin_amdgcn_rcpf(1.f + __builtin_amdgcn_exp2f(lg * -1.4426950408889634f)));
            if ((lane & 7) == 0) st4f(ws + WS_GATE + tok * 4, g);
        }
    };
    GDma<true> d2; d2.init(xbase, (const char*)w1 + ct * 192, FF * 2, lds, lane, k0);
    u64 pc0; asm volatile("s_getpc_b64 %0" : "=s"(pc0));
    STAMP(0, 0);
    if (wid < 4) {
        GDma<false> d1; d1.init(xbase, (const char*)wqkv + ct * 256, 3 * DM * 2, lds, lane, k0);
        if (tile1) {
#pragma unroll
            for (int jj = 0; jj < 4; ++jj) d1.xp(d1.kact(0), 0, 4 * wid + jj, jj & 1);
        }
        if (tid == 0) { ctl[0] = 0u; ctl[1] = 0u; }
        if (tile1) asm volatile("s_waitcnt vmcnt(0) lgkmcnt(0)\n\ts_barrier" ::: "memory");
        if (slot * 4 + wid < 42) glds16((const char*)((pc0 + 15ull) & ~15ull) + (slot * 4 + wid) * 1024, lane16, lds0 + 152 * 1024);
        for (int pc = wid; pc < 10; pc += 4) rt_load(w2, wo, bid, lds0 + RT_OFF, pc, lane);
        if (!tile1) for (int pc = wid; pc < 10; pc += 4) rt_load(w2, wo, 256 + (xcd >> 1) * 4 + (slot & 3), lds0 + RT_OFF + 10240, pc, lane);
        if (wid == 0) glds16_sc1(ws + WS_EPOCH, lane < 1 ? 0u : 0u, lds0 + EP_OFF);
        auto tag_ready = [&]() {
            if (tid == 0) { const u64 tg = *(const volatile LAS u64*)(lds + EP_OFF) + 1ull; ctl[2] = (unsigned)tg; ctl[3] = (unsigned)(tg >> 32); }
        };
        if (!tile1) { asm volatile("s_waitcnt vmcnt(0)" ::: "memory"); tag_ready(); asm volatile("s_waitcnt lgkmcnt(0)\n\ts_barrier" ::: "memory"); }
        if (tile1) {
            const int wm = wid >> 1, wn = wid & 1;
            f32x16 acc[2][2] = {};
                        auto nohook = [&](int kc, bool before) { if (kc == 1 && before) { asm volatile("s_waitcnt vmcnt(0)" ::: "memory"); tag_ready(); } };
            if (ct >= 10) {
                gemm_loop<false, true, 2, 2>(acc, lds, wid, lane, nohook);
                STAMP(0, 1);
#pragma unroll
                for (int nt = 0; nt < 2; ++nt) {
                    const int phi = (ct - 10) * 128 + wn * 64 + nt * 32 + r;
                    const int hd = (phi * 3277) >> 16, d = phi - hd * 20;
                    const unsigned dst = (unsigned)WS_KV + (unsigned)(hd * NT + rt * 4 + wm * 2) * KVTILE_B + V_OFF + (h * V_ROWS + d) * 16;
#pragma unroll
                    for (int mt = 0; mt < 2; ++mt)
#pragma unroll
                        for (int s = 0; s < 2; ++s) {
                            const f32x16& a = acc[mt][nt];
                            const u32x4 w = {cvtpk(a[8 * s], a[8 * s + 1]), cvtpk(a[8 * s + 2], a[8 * s + 3]), cvtpk(a[8 * s + 4], a[8 * s + 5]), cvtpk(a[8 * s + 6], a[8 * s + 7])};
                            st16(rs, dst + mt * KVTILE_B + s * V_STEP, w);
                        }
                }
                BARL();
            } else {
                gemm_loop<false, false, 2, 2>(acc, lds, wid, lane, nohook);
                STAMP(0, 1);
                const bool isQ = ct < 5;
                const float sc = isQ ? QSCALE : 1.f;
                const unsigned img = (unsigned)(isQ ? WS_Q : WS_KV), tileb = isQ ? QTILE_B : KVTILE_B;
                const int G = (isQ ? ct : ct - 5) * 32 + wn * 16 + h;
                u32x2 P[2][8]; float nrm[8];
#pragma unroll
                for (int p = 0; p < 8; ++p) {
                    const int nt = p >> 2, g = p & 3; float m = 0.f;
#pragma unroll
                    for (int mt = 0; mt < 2; ++mt) {
                        const f32x16& a = acc[mt][nt];
                        P[mt][p] = (u32x2){cvtpk(a[4 * g] * sc, a[4 * g + 1] * sc), cvtpk(a[4 * g + 2] * sc, a[4 * g + 3] * sc)};
                        m = fmaxf(m, __builtin_fmaf(a[4 * g], a[4 * g], a[4 * g + 1] * a[4 * g + 1]) + __builtin_fmaf(a[4 * g + 2], a[4 * g + 2], a[4 * g + 3] * a[4 * g + 3]));
                    }
                    nrm[p] = m;
                }
#pragma unroll
                for (int p = 0; p < 8; ++p) {
                    const int gam = G + 2 * p, hd = (gam * 205) >> 10, dg = gam - 5 * hd;
                    const unsigned base = img + (unsigned)(hd * NT + rt * 4 + wm * 2) * tileb;
#pragma unroll
                    for (int mt = 0; mt < 2; ++mt) {
                        const unsigned bm = base + mt * tileb;
                        if (dg < 2) {
                            if (p < 7) st16(rs, bm + dg * 512 + r * 16, (u32x4){P[mt][p][0], P[mt][p][1], P[mt][(p + 1) & 7][0], P[mt][(p + 1) & 7][1]});
                            else st8(rs, bm + dg * 512 + r * 16, P[mt][p]);
                        } else if (dg == 4) st8(rs, bm + 1024 + r * 8, P[mt][p]);
                        else if (p == 0) st8(rs, bm + (dg - 2) * 512 + r * 16 + 8, P[mt][p]);
                    }
                }
                if (!isQ) {
#pragma unroll
                    for (int i = 0; i < 8; ++i) { float m = nrm[i]; m = dpp_max<0xB1>(m); m = dpp_max<0x4E>(m); m = dpp_max<0x141>(m); m = dpp_max<0x140>(m);
                        auto rr = __builtin_amdgcn_permlane16_swap(__float_as_uint(m), __float_as_uint(m), false, false); nrm[i] = fmaxf(__uint_as_float(rr[0]), __uint_as_float(rr[1])); }
                    float mine = nrm[0];
#pragma unroll
                    for (int i = 1; i < 8; ++i) mine = r == i ? nrm[i] : mine;
                    if (r < 8) st4f(ws + WS_KMAX + (size_t)((G + 2 * r) * 32 + rt * 2 + wm) * 4, mine);
                }
                BARL();
            }
        } else { gates(); BARL(); }
        STAMP(0, 2);
        BARL();
        f32x16 acc2[1][3] = {};
        auto hook2 = [&](int kc, bool before) {
            if (kc == 2) { if (before) asm volatile("s_waitcnt vmcnt(0)" ::: "memory");
                           else if (tid == 0) flag_set(ws + WS_F1 + bid * 8, ((u64)ctl[3] << 32) | ctl[2]); }
        };
        gemm_loop<true, false, 1, 3>(acc2, lds, wid, lane, hook2);
        STAMP(0, 3);
        wait_seen();
        q_loads();
        {   const int tok = rt * 128 + wid * 32 + r, rho = tok >> 4;
#pragma unroll
            for (int nt = 0; nt < 3; ++nt) {
                const unsigned dst = (unsigned)WS_AF + (unsigned)(rho * NSIG + ct * 3 + nt) * 1024 + (h * 16 + (r & 15)) * 16;
                float sv[16];
#pragma unroll
                for (int i = 0; i < 16; ++i) { const float v = acc2[0][nt][i]; sv[i] = v * __builtin_amdgcn_rcpf(1.f + __builtin_amdgcn_exp2f(v * -1.4426950408889634f)); }
#pragma unroll
                for (int s = 0; s < 2; ++s) {
                    const u32x4 w = {cvtpk(sv[8 * s], sv[8 * s + 1]), cvtpk(sv[8 * s + 2], sv[8 * s + 3]), cvtpk(sv[8 * s + 4], sv[8 * s + 5]), cvtpk(sv[8 * s + 6], sv[8 * s + 7])};
                    st16(rs, dst + s * 512, w);
                }
            }
        }
        asm volatile("s_waitcnt vmcnt(6)" : "+v"(qa), "+v"(qbr), "+v"(km4), "+v"(gate) :: "memory");
    } else {
        const int lw = wid - 4;
        auto side_stores = [&]() {
            for (int i = lw; i < 10; i += 4) rt_store(rs, bid, lds + RT_OFF, i, lane);
            if (!tile1) for (int i = lw; i < 10; i += 4) rt_store(rs, 256 + (xcd >> 1) * 4 + (slot & 3), lds + RT_OFF + 10240, i, lane);
            if (lw == 0) { const int i = bid * 64 + lane;
                const unsigned f = (i & 1) ? 0u : ONES2; st16(rs, (unsigned)WS_KV + (unsigned)(i >> 3) * KVTILE_B + V_OFF + ((i >> 2) & 1) * V_STEP + (((i >> 1) & 1) * V_ROWS + 20 + (i & 1)) * 16, (u32x4){f, f, f, f}); }
        };
        if (tile1) {
            GDma<false> d1; d1.init(xbase, (const char*)wqkv + ct * 256, 3 * DM * 2, lds, lane, k0);
#pragma unroll
            for (int jj = 0; jj < 4; ++jj) d1.wp(d1.kact(0), 0, lw, jj);
            d1.issue(1, 1, lw);
            WAITB(8);
            d1.issue(2, 2, lw); d1.issue(3, 3, lw);
            for (int kc = 0; kc < 9; ++kc) {
                if (kc < 7) WAITB(16); else if (kc == 7) WAITB(15); else WAITB(14);
                if (kc + 4 < 10) d1.issue(kc + 4, kc & 3, lw);
                if (kc == 4) side_stores();
                if (kc >= 6) d2.issue(kc - 6, kc - 6, lw);
            }
            asm volatile("s_barrier" ::: "memory");
        } else {
            asm volatile("s_barrier" ::: "memory");
            side_stores(); gates();
            BARL();
            d2.issue(0, 0, lw); d2.issue(1, 1, lw); d2.issue(2, 2, lw);
        }
        WAITB(14);
        { int st = 0;
          for (int kc = 0; kc < 9; ++kc) {
              if (kc < 8) WAITB(7); else WAITB(0);
              if (kc + 3 < 10) d2.issue(kc + 3, st, lw);
              st = st == 2 ? 0 : st + 1;
          } }
        if (lw == 0) {
            const u64 tag = ((u64)ctl[3] << 32) | ctl[2];
            for (int it = 0; it < SPIN_MAX; ++it) {
                bool ok = true;
#pragma unroll
                for (int i = 0; i < 4; ++i) ok &= flag_get(ws + WS_F1 + (lane + 64 * i) * 8) == tag;
                if (__all(ok)) break;
                __builtin_amdgcn_s_sleep(2);
            }
            __builtin_amdgcn_fence(__ATOMIC_ACQUIRE, "agent");
            asm volatile("s_waitcnt vmcnt(0)" ::: "memory");
            if (lane == 0) { ctl[1] = 1u; if (bid == 0) flag_set(ws + WS_EPOCH, tag); }
        } else wait_seen();
        q_loads();
        for (int j = 0; j < 21; ++j) glds16(kv + (size_t)(lw + 4 * j) * 1024, lane16, lds0 + (lw + 4 * j) * 1024);
        asm volatile("s_waitcnt vmcnt(21)" : "+v"(qa), "+v"(qbr), "+v"(km4), "+v"(gate) :: "memory");
    }
    STAMP(1, 0);
    float km = fmaxf(fmaxf(km4[0], km4[1]), fmaxf(km4[2], km4[3]));
    km = dpp_max<0xB1>(km); km = dpp_max<0x4E>(km); km = dpp_max<0x141>(km);
    km = __builtin_amdgcn_sqrtf(km);
    float M;
    { const float c0 = rdl(km, 0), c1 = rdl(km, 8), c2 = rdl(km, 16), c3 = rdl(km, 24), c4 = rdl(km, 32);
      const u32x4 qw = __builtin_bit_cast(u32x4, qa);
      float n0, n1, n2;
      { const float a = bf_lo(qw[0]), b = bf_hi(qw[0]), c = bf_lo(qw[1]), d = bf_hi(qw[1]); n0 = (a * a + b * b) + (c * c + d * d); }
      { const float a = bf_lo(qw[2]), b = bf_hi(qw[2]), c = bf_lo(qw[3]), d = bf_hi(qw[3]); n1 = (a * a + b * b) + (c * c + d * d); }
      { const unsigned q0 = qbr[0], q1 = qbr[1]; const float a = bf_lo(q0), b = bf_hi(q0), c = bf_lo(q1), d = bf_hi(q1); n2 = (a * a + b * b) + (c * c + d * d); }
      float part = __builtin_amdgcn_sqrtf(n0) * (h ? c1 : c0) + __builtin_amdgcn_sqrtf(n1) * (h ? c3 : c2);
      if (!h) part += __builtin_amdgcn_sqrtf(n2) * c4;
      M = swap32_add(part) * 1.01f; }
    const s16x4 qb = make_qb(qbr[0], qbr[1], M, h);
    const int vrow = r <= 20 ? r : (r < 28 ? 21 : r - 12);
    const int voff = V_OFF + (h * V_ROWS + vrow) * 16, kboff = h ? V_OFF + 20 * 16 : 1024 + r * 8;
    lds_cptr kaC = (lds_cptr)lds + lane * 16, kbC = (lds_cptr)lds + kboff, vC = (lds_cptr)lds + voff;
    lds_cptr kaN = kaC + K2_BIG, kbN = kbC + K2_BIG, vN = vC + K2_BIG;
    const f32x16 zero = {};
    f32x16 o = {}, sA, sB;
    bf16x8 ka, va, vb; s16x4 kb;
    u32x4 plo, phi;
    if (wid >= 4) asm volatile("s_waitcnt vmcnt(15)" ::: "memory");
    asm volatile("s_barrier" ::: "memory");
    STAMP(1, 1);
    ka = lds128(kaC); kb = lds64(kbC);
    sA = MFMA32(ka, qa, zero); sA = MFMA8(kb, qb, sA);
    ka = lds128(kaC + KVTILE_B); kb = lds64(kbC + KVTILE_B);
    va = lds128(vC);
    vb = (bf16x8){}; phi = (u32x4){};
    f32x4 e3 = {};
    const int ng = (xcd & 1) * 4 + (slot & 3), bt = (xcd >> 1) * 8 + (slot >> 2);
    const char* ap = ws + WS_AF + (size_t)bt * 4 * NSIG * 1024;
    const char* bp = ws + WS_BF + (size_t)ng * NSIG * 5 * 1024;
#define ROTEXPR (((slot >> 2) + 3 * (slot & 3)) % 6)
    const int rot = ROTEXPR;
    auto hs = [&](int i) { int st = i + rot; if (st >= 6) st -= 6; return wid * 6 + st; };
    bf16x8 a[3][4], b[3][5];
    auto ldA1 = [&](int sg, int mt, bf16x8& d) { gld128_sc1(d, ap + ((size_t)mt * NSIG + sg) * 1024, lane16); };
    auto ldB1 = [&](int sg, int nt, bf16x8& d) { gld128(d, bp + sg * 5120 + nt * 1024, lane16); };
    const u64 tag = ((u64)ctl[3] << 32) | ctl[2];
#define EXP4(X, i) do { X[i] = __builtin_amdgcn_exp2f(X[i]); X[i + 1] = __builtin_amdgcn_exp2f(X[i + 1]); X[i + 2] = __builtin_amdgcn_exp2f(X[i + 2]); X[i + 3] = __builtin_amdgcn_exp2f(X[i + 3]); } while (0)
#define KADDR(A, i) (((i) < 16) ? (A##C + (i) * KVTILE_B) : (A##N + ((i) - 16) * KVTILE_B))
#define STEP(C, N, i, X0, X1) do { \
        bf16x8 ka2, va2, vb2; s16x4 kb2; \
        N = MFMA32(ka, qa, zero); SBAR(); \
        ka2 = lds128(KADDR(ka, (i) + 2)); \
        phi[2] = cvtpk(e3[0], e3[1]); phi[3] = cvtpk(e3[2], e3[3]); PIN(phi); SBAR(); EXP4(C, 0); PIN(C); SBAR(); \
        o = MFMA32(vb, __builtin_bit_cast(bf16x8, phi), o); SBAR(); \
        X0; \
        vb2 = lds128(vC + (i) * KVTILE_B + V_STEP); \
        plo[0] = cvtpk(C[0], C[1]); plo[1] = cvtpk(C[2], C[3]); PIN(plo); SBAR(); EXP4(C, 4); PIN(C); SBAR(); \
        N = MFMA8(kb, qb, N); SBAR(); \
        kb2 = lds64(KADDR(kb, (i) + 2)); \
        plo[2] = cvtpk(C[4], C[5]); plo[3] = cvtpk(C[6], C[7]); PIN(plo); SBAR(); EXP4(C, 8); PIN(C); SBAR(); \
        o = MFMA32(va, __builtin_bit_cast(bf16x8, plo), o); SBAR(); \
        X1; \
        va2 = lds128(KADDR(v, (i) + 1)); \
        phi[0] = cvtpk(C[8], C[9]); phi[1] = cvtpk(C[10], C[11]); PIN(phi); SBAR(); \
        e3[0] = __builtin_amdgcn_exp2f(C[12]); e3[1] = __builtin_amdgcn_exp2f(C[13]); e3[2] = __builtin_amdgcn_exp2f(C[14]); e3[3] = __builtin_amdgcn_exp2f(C[15]); PIN(e3); SBAR(); \
        ka = ka2; kb = kb2; va = va2; vb = vb2; \
    } while (0)
#define PIECE(j) do { if (8 * (j) + 7 < 42 || 8 * (j) + wid < 42) glds16(src + (j) * 8192, lane16, dst + (j) * 8192); } while (0)
    const char* src = kv + (84 + wid) * 1024; unsigned dst = lds0 + wid * 1024 + 2 * K2_BIG;
#pragma unroll 1
    for (int c = 0; c < 3; ++c) {
        STEP(sA, sB, 0, , ); STEP(sB, sA, 1, , ); STEP(sA, sB, 2, , ); STEP(sB, sA, 3, , ); STEP(sA, sB, 4, , );
        if (c == 0) { if (wid >= 4) asm volatile("s_waitcnt vmcnt(10)" ::: "memory"); asm volatile("s_barrier" ::: "memory"); }
        STEP(sB, sA, 5, , ); STEP(sA, sB, 6, , ); STEP(sB, sA, 7, , ); STEP(sA, sB, 8, , ); STEP(sB, sA, 9, , ); STEP(sA, sB, 10, , ); STEP(sB, sA, 11, , ); STEP(sA, sB, 12, , );
        if (c == 2 && wid == 0) {
            for (int it = 0; it < SPIN_MAX; ++it) {
                if (__all(flag_get(ws + WS_F2 + ((bt >> 1) * 16 + (lane & 15)) * 8) == tag)) break;
                __builtin_amdgcn_s_sleep(2);
            }
        }
        WAITB(0);
        if (c == 0 && tid == 0) flag_set(ws + WS_F2 + (rt * 16 + ct) * 8, tag);
        if (c < 2) { STEP(sB, sA, 13, PIECE(0), PIECE(1)); STEP(sA, sB, 14, PIECE(2), PIECE(3)); STEP(sB, sA, 15, PIECE(4), PIECE(5)); }
        else { STEP(sB, sA, 13, , ); STEP(sA, sB, 14, , ); STEP(sB, sA, 15, , ); }
        const int d = (c == 1) ? -2 * K2_BIG : K2_BIG;
        kaC = kaN; kbC = kbN; vC = vN; kaN += d; kbN += d; vN += d;
        src += 42 * 1024; dst -= 2 * K2_BIG;
    }
#define PA(s, m) ldA1(hs(s), m, a[s][m])
#define PB(s, n) ldB1(hs(s), n, b[s][n])
    STEP(sA, sB, 0, PA(0, 0), PA(0, 1)); STEP(sB, sA, 1, PA(0, 2), PA(0, 3)); STEP(sA, sB, 2, PB(0, 0), PB(0, 1)); STEP(sB, sA, 3, PB(0, 2), PB(0, 3));
    STEP(sA, sB, 4, PB(0, 4), PA(1, 0)); STEP(sB, sA, 5, PA(1, 1), PA(1, 2)); STEP(sA, sB, 6, PA(1, 3), PB(1, 0)); STEP(sB, sA, 7, PB(1, 1), PB(1, 2));
    STEP(sA, sB, 8, PB(1, 3), PB(1, 4)); STEP(sB, sA, 9, PA(2, 0), PA(2, 1)); STEP(sA, sB, 10, PA(2, 2), PA(2, 3)); STEP(sB, sA, 11, PB(2, 0), PB(2, 1));
    STEP(sA, sB, 12, PB(2, 2), PB(2, 3)); STEP(sB, sA, 13, PB(2, 4), ); STEP(sA, sB, 14, , ); STEP(sB, sA, 15, , );
    phi[2] = cvtpk(e3[0], e3[1]); phi[3] = cvtpk(e3[2], e3[3]);
    o = MFMA32(vb, __builtin_bit_cast(bf16x8, phi), o);
    STAMP(1, 2);
    float l; { auto rr = __builtin_amdgcn_permlane32_swap(__float_as_uint(o[8]), __float_as_uint(o[8]), false, false); l = __uint_as_float(rr[1]); }
    if (__builtin_expect(__any(!(l >= 8.6736174e-19f && l <= 1.0e30f)), 0)) {
        asm volatile("s_waitcnt vmcnt(0)" ::: "memory");
        attn_fallback(o, kv, qa, qbr[0], qbr[1], lane, kboff, voff);
        auto rr = __builtin_amdgcn_permlane32_swap(__float_as_uint(o[8]), __float_as_uint(o[8]), false, false); l = __uint_as_float(rr[1]);
    }
    const float f = gate * __builtin_amdgcn_rcpf(l);
    const int tok = Tq * 32 + r;
    char* ab = ws + WS_AF + (size_t)(tok >> 4) * NSIG * 1024 + (tok & 15) * 16;
#pragma unroll
    for (int g = 0; g < 3; ++g) {
        const int gam = head * 5 + 2 * g + h;
        const int sig = 48 + (gam >> 3), c = ((gam >> 2) & 1) * 2 + (gam & 1), half = (gam >> 1) & 1;
        const u32x2 w = {cvtpk(o[4 * g] * f, o[4 * g + 1] * f), cvtpk(o[4 * g + 2] * f, o[4 * g + 3] * f)};
        if (g < 2 || h == 0) __hip_atomic_store((u64*)(ab + sig * 1024 + c * 256 + half * 8), ((u64)w[1] << 32) | w[0], __ATOMIC_RELAXED, __HIP_MEMORY_SCOPE_AGENT);
    }
    STAMP(1, 3);
    {
        f32x4 acc[4][5] = {};
        auto loadA = [&](int sg, bf16x8 (&da)[4]) {
#pragma unroll
            for (int mt = 0; mt < 4; ++mt) gld128_sc1(da[mt], ap + ((size_t)mt * NSIG + sg) * 1024, lane16);
        };
        auto loadB = [&](int sg, bf16x8 (&db)[5]) {
#pragma unroll
            for (int nt = 0; nt < 5; ++nt) gld128(db[nt], bp + sg * 5120 + nt * 1024, lane16);
        };
        auto mma = [&](bf16x8 (&da)[4], bf16x8 (&db)[5]) {
            SBAR();
#pragma unroll
            for (int nt = 0; nt < 5; ++nt)
#pragma unroll
                for (int mt = 0; mt < 4; ++mt) acc[mt][nt] = MFMA16(db[nt], da[mt], acc[mt][nt]);
            SBAR();
        };
        wait9<3>(a[0], b[0]);
        asm volatile("" : "+v"(a[1][0]), "+v"(a[1][1]), "+v"(a[1][2]), "+v"(a[1][3]), "+v"(b[1][0]), "+v"(b[1][1]), "+v"(b[1][2]), "+v"(b[1][3]), "+v"(b[1][4]));
        asm volatile("" : "+v"(a[2][0]), "+v"(a[2][1]), "+v"(a[2][2]), "+v"(a[2][3]), "+v"(b[2][0]), "+v"(b[2][1]), "+v"(b[2][2]), "+v"(b[2][3]), "+v"(b[2][4]));
        mma(a[0], b[0]); loadA(hs(3), a[0]); loadB(hs(3), b[0]);
        mma(a[1], b[1]); loadA(hs(4), a[1]); loadB(hs(4), b[1]);
        asm volatile("s_waitcnt vmcnt(18)" ::: "memory");
        if (lane == 0) {
            const unsigned old = __hip_atomic_fetch_add((unsigned*)(lds + CTL_OFF), 1u, __ATOMIC_RELAXED, __HIP_MEMORY_SCOPE_WORKGROUP);
            if (old == 7u) flag_set(ws + WS_F3 + (qblk * 32 + head) * 8, tag);
        }
        mma(a[2], b[2]); loadA(hs(5), a[2]); loadB(hs(5), b[2]);
        float og[4];
#pragma unroll
        for (int mt = 0; mt < 4; ++mt) asm volatile("s_nop 4\n\tglobal_load_dword %0, %1, %2" : "=v"(og[mt]) : "v"((unsigned)((bt * 64 + mt * 16 + (lane & 15)) * 4)), "s"(ws + WS_GATE) : "memory");
        wait9<22>(a[0], b[0]); mma(a[0], b[0]); loadB(48 + wid, b[0]);
        wait9<18>(a[1], b[1]); mma(a[1], b[1]); loadB(56 + wid, b[1]);
        wait9<14>(a[2], b[2]); mma(a[2], b[2]);
        if (wid < 4) {
#pragma unroll
            for (int nt = 0; nt < 5; ++nt) gld128_rw(b[2][nt], bp + (64 + wid) * 5120 + nt * 1024, lane16);
        }
        STAMP(2, 0);
        asm volatile("s_waitcnt vmcnt(0)" : "+v"(og[0]), "+v"(og[1]), "+v"(og[2]), "+v"(og[3]) :: "memory");
#pragma unroll
        for (int mt = 0; mt < 4; ++mt) { const float f1 = bf_round(1.f - og[mt]);
#pragma unroll
            for (int nt = 0; nt < 5; ++nt) acc[mt][nt] *= f1; }
        if (wid == 0) {
            for (int it = 0; it < SPIN_MAX; ++it) {
                if (__all(flag_get(ws + WS_F3 + ((bt >> 2) * 32 + r) * 8) == tag)) break;
                __builtin_amdgcn_s_sleep(2);
            }
        }
        asm volatile("s_barrier" ::: "memory");
        STAMP(2, 1);
        loadA(48 + wid, a[0]); loadA(56 + wid, a[1]);
        a[2][0] = (bf16x8){}; a[2][1] = (bf16x8){}; a[2][2] = (bf16x8){}; a[2][3] = (bf16x8){};
        if (wid < 4) {
#pragma unroll
            for (int mt = 0; mt < 4; ++mt) gld128_sc1_rw(a[2][mt], ap + ((size_t)mt * NSIG + 64 + wid) * 1024, lane16);
        }
        wait9<4>(a[0], b[0]); mma(a[0], b[0]);
        wait9<0>(a[1], b[1]); wait9<0>(a[2], b[2]); mma(a[1], b[1]); mma(a[2], b[2]);
        STAMP(2, 2);
#pragma unroll
        for (int mt = 0; mt < 4; ++mt)
#pragma unroll
            for (int nt = 0; nt < 5; ++nt) *(f32x4*)(lds + (wid * 20 + mt * 5 + nt) * 1024 + lane16) = acc[mt][nt];
        BARL();
#pragma unroll
        for (int i = 0; i < 3; ++i) { const int t = wid + 8 * i;
            if (t < 20) {
                f32x4 sum = *(const f32x4*)(lds + t * 1024 + lane16);
#pragma unroll
                for (int w = 1; w < 8; ++w) sum += *(const f32x4*)(lds + (w * 20 + t) * 1024 + lane16);
                const int m4 = t / 5, nt = t - m4 * 5;
                const int tok2 = bt * 64 + m4 * 16 + (lane & 15);
                *(u32x2*)(out + (size_t)tok2 * DM + ng * 80 + nt * 16 + (lane >> 4) * 4) = (u32x2){cvtpk(sum[0], sum[1]), cvtpk(sum[2], sum[3])};
            } }
        STAMP(2, 3);
    }
}

extern "C" void kernel_launch(void* const* d_in, const int* in_sizes, int n_in, void* d_out, int out_size, void* d_ws, size_t ws_size, hipStream_t stream) {
    const unsigned short* x = (const unsigned short*)d_in[0];
    const unsigned short* wqkv = (const unsigned short*)d_in[1];
    const unsigned short* wo = (const unsigned short*)d_in[2];
    const unsigned short* w1 = (const unsigned short*)d_in[4];
    const unsigned short* w2 = (const unsigned short*)d_in[5];
    const unsigned short* gw = (const unsigned short*)d_in[6];
    (void)hipFuncSetAttribute((const void*)gated_mix_mega, hipFuncAttributeMaxDynamicSharedMemorySize, LDS_ALL);
    gated_mix_mega<<<256, 512, LDS_ALL, stream>>>(x, wqkv, wo, w1, w2, gw, (char*)d_ws, (unsigned short*)d_out);
}
```

```cpp
#include <hip/hip_runtime.h>
#include <hip/hip_bf16.h>
#include <stdint.h>

typedef short bf16x8 __attribute__((ext_vector_type(8)));
typedef short s16x4 __attribute__((ext_vector_type(4)));
typedef float f32x16 __attribute__((ext_vector_type(16)));
typedef float f32x4 __attribute__((ext_vector_type(4)));
typedef float f32x2 __attribute__((ext_vector_type(2)));
typedef unsigned u32x4 __attribute__((ext_vector_type(4)));
typedef unsigned u32x2 __attribute__((ext_vector_type(2)));
typedef __bf16 bf16x2_t __attribute__((ext_vector_type(2)));
#define LAS __attribute__((address_space(3)))
typedef LAS const char* lds_cptr;
#define DI __device__ __forceinline__
#define SBAR() __builtin_amdgcn_sched_barrier(0)
#define PIN(x) asm volatile("" : "+v"(x))
#define MFMA32(a, b, c) __builtin_amdgcn_mfma_f32_32x32x16_bf16(a, b, c, 0, 0, 0)
#define MFMA16(a, b, c) __builtin_amdgcn_mfma_f32_16x16x32_bf16(a, b, c, 0, 0, 0)
#define MFMA8(a, b, c) __builtin_amdgcn_mfma_f32_32x32x8bf16_1k(a, b, c, 0, 0, 0)

constexpr int S = 2048, DM = 640, NH = 32, DH = 20, FF = 1536;
constexpr int NT = S / 32;
constexpr int QTILE_B = 1280;
constexpr int KVTILE_B = 2688;
constexpr int V_OFF = 1280, V_STEP = 704, V_ROWS = 22;
constexpr int NSIG = 68;
constexpr size_t WS_Q = 0;
constexpr size_t WS_KV = WS_Q + (size_t)NH * NT * QTILE_B;
constexpr size_t WS_AF = WS_KV + (size_t)NH * NT * KVTILE_B;
constexpr size_t WS_BF = WS_AF + (size_t)128 * NSIG * 1024;
constexpr size_t WS_KMAX = WS_BF + (size_t)8 * NSIG * 5 * 1024;
constexpr size_t WS_GATE = WS_KMAX + (size_t)160 * 32 * 4;
constexpr size_t WS_F1 = WS_GATE + (size_t)S * 4;
constexpr size_t WS_F2 = WS_F1 + 2048;
constexpr size_t WS_F3 = WS_F2 + 2048;
constexpr size_t WS_EPOCH = WS_F3 + 2048;
constexpr size_t WS_END = WS_EPOCH + 256;
constexpr float QSCALE = 0.22360679774997896f * 1.4426950408889634f;
constexpr unsigned ONES2 = 0x3F803F80u;

DI unsigned cvtpk(float lo, float hi) { f32x2 v = {lo, hi}; bf16x2_t b = __builtin_convertvector(v, bf16x2_t); return __builtin_bit_cast(unsigned, b); }
DI float bf_lo(unsigned u) { return __uint_as_float(u << 16); }
DI float bf_hi(unsigned u) { return __uint_as_float(u & 0xffff0000u); }
DI float bf_round(float x) { return bf_lo(cvtpk(x, x)); }
DI void glds16(const void* sbase, unsigned voff, unsigned lds_base) {
    unsigned sv;
    asm volatile("s_mov_b32 %0, m0\n\ts_mov_b32 m0, %3\n\ts_nop 2\n\tglobal_load_lds_dwordx4 %1, %2\n\ts_mov_b32 m0, %0"
                 : "=&s"(sv) : "v"(voff), "s"(sbase), "s"(lds_base) : "memory");
}
DI void glds16_sc1(const void* sbase, unsigned voff, unsigned lds_base) {
    unsigned sv;
    asm volatile("s_mov_b32 %0, m0\n\ts_mov_b32 m0, %3\n\ts_nop 2\n\tglobal_load_lds_dwordx4 %1, %2 sc1\n\ts_mov_b32 m0, %0"
                 : "=&s"(sv) : "v"(voff), "s"(sbase), "s"(lds_base) : "memory");
}
typedef short v4i16_t __attribute__((ext_vector_type(4)));
DI s16x4 ldstr(lds_cptr p) { return __builtin_bit_cast(s16x4, __builtin_amdgcn_ds_read_tr16_b64_v4i16((LAS v4i16_t*)p)); }
DI bf16x8 lds128(lds_cptr p) { return *(const LAS bf16x8*)p; }
DI s16x4 lds64(lds_cptr p) { return *(const LAS s16x4*)p; }
DI unsigned rfl(unsigned x) { return __builtin_amdgcn_readfirstlane(x); }
#define STAMP(k, i) do { } while (0)

DI float rdl(float x, int l) { return __uint_as_float(__builtin_amdgcn_readlane(__float_as_uint(x), l)); }
typedef __amdgpu_buffer_rsrc_t rsrc_t;
typedef unsigned long long u64;
DI rsrc_t ws_rsrc(char* ws) { return __builtin_amdgcn_make_buffer_rsrc(ws, 0, (int)WS_END, 0x00020000); }
DI void st16(rsrc_t rs, unsigned off, u32x4 v) { __builtin_amdgcn_raw_buffer_store_b128(v, rs, off, 0, 16); }
DI void st8(rsrc_t rs, unsigned off, u32x2 v) { __builtin_amdgcn_raw_buffer_store_b64(v, rs, off, 0, 16); }
DI void st4f(char* p, float v) { __hip_atomic_store((float*)p, v, __ATOMIC_RELAXED, __HIP_MEMORY_SCOPE_AGENT); }
DI void flag_set(char* p, u64 tag) { __hip_atomic_store((u64*)p, tag, __ATOMIC_RELAXED, __HIP_MEMORY_SCOPE_AGENT); }
DI u64 flag_get(const char* p) { return __hip_atomic_load((const u64*)p, __ATOMIC_RELAXED, __HIP_MEMORY_SCOPE_AGENT); }
template <int CTRL> DI float dpp_max(float x) { const float y = __uint_as_float(__builtin_amdgcn_update_dpp(0u, __float_as_uint(x), CTRL, 0xf, 0xf, true)); return fmaxf(x, y); }
template <int CTRL> DI float dpp_add(float x) { const float y = __uint_as_float(__builtin_amdgcn_update_dpp(0u, __float_as_uint(x), CTRL, 0xf, 0xf, true)); return x + y; }
DI void gld128(bf16x8& d, const char* sbase, unsigned voff) { asm volatile("s_nop 4\n\tglobal_load_dwordx4 %0, %1, %2" : "=v"(d) : "v"(voff), "s"(sbase) : "memory"); }
DI void gld128_sc1(bf16x8& d, const char* sbase, unsigned voff) { asm volatile("s_nop 4\n\tglobal_load_dwordx4 %0, %1, %2 sc1" : "=v"(d) : "v"(voff), "s"(sbase) : "memory"); }
DI void gld128_rw(bf16x8& d, const char* sbase, unsigned voff) { asm volatile("s_nop 4\n\tglobal_load_dwordx4 %0, %1, %2" : "+v"(d) : "v"(voff), "s"(sbase) : "memory"); }
DI void gld128_sc1_rw(bf16x8& d, const char* sbase, unsigned voff) { asm volatile("s_nop 4\n\tglobal_load_dwordx4 %0, %1, %2 sc1" : "+v"(d) : "v"(voff), "s"(sbase) : "memory"); }
template <int N> DI void wait9(bf16x8 (&a)[4], bf16x8 (&b)[5]) {
    asm volatile("s_waitcnt vmcnt(%9)" : "+v"(a[0]), "+v"(a[1]), "+v"(a[2]), "+v"(a[3]), "+v"(b[0]), "+v"(b[1]), "+v"(b[2]), "+v"(b[3]), "+v"(b[4]) : "i"(N) : "memory");
}
#define WAITB(n) asm volatile("s_waitcnt vmcnt(" #n ")\n\ts_barrier" ::: "memory")
#define BARL() asm volatile("s_waitcnt lgkmcnt(0)\n\ts_barrier" ::: "memory")

constexpr int P1_STG = 32768;
constexpr int RT_OFF = 131072;
constexpr int P2_STG = 28672;
constexpr int CTL_OFF = 163584;
DI constexpr int p2_base(int s) { return CTL_OFF - (s + 1) * P2_STG; }
constexpr int K2_BIG = 16 * KVTILE_B;
constexpr int EP_OFF = 151552;
constexpr int LDS_ALL = 163840;

template <bool N96>
struct GDma {
    static constexpr int NW = N96 ? 3 : 4;
    const char* xbase; const char* wbase; int ldw2, k0; unsigned lds0, xoff0, xoff1, woff[3];
    DI int kact(int kc) const { kc += k0; return kc >= 10 ? kc - 10 : kc; }
    DI static unsigned sbase(int st) { return N96 ? (unsigned)(CTL_OFF - (st + 1) * P2_STG) : (unsigned)(st * P1_STG); }
    DI void init(const char* xb, const char* wb, int ld, char* lds, int lane, int k0_) {
        xbase = xb; wbase = wb; ldw2 = ld; k0 = k0_; lds0 = (unsigned)(uintptr_t)lds;
        const int rr = lane >> 3;
        xoff0 = rr * 1280 + (((lane & 7) ^ ((lane >> 4) & 7)) << 4); xoff1 = rr * 1280 + (((lane & 7) ^ ((4 + (lane >> 4)) & 7)) << 4);
        if (N96) {
#pragma unroll
            for (int p = 0; p < 3; ++p) { const int o = 1024 * p + 16 * lane, row = (o * 21846) >> 22, col = o - row * 192; woff[p] = row * ldw2 + col; }
        } else woff[0] = (lane >> 4) * ldw2 + ((((lane & 15) ^ ((lane >> 4) << 2))) << 4);
    }
    DI void xp(int kc, int st, int j, int par) const { glds16(xbase + (size_t)j * 8 * 1280 + kc * 128, par ? xoff1 : xoff0, lds0 + sbase(st) + j * 1024); }
    DI void wp(int kc, int st, int lw, int jj) const {
        if (N96) glds16(wbase + (size_t)(kc * 64 + 16 * lw) * ldw2, woff[jj], lds0 + sbase(st) + 16384 + (3 * lw + jj) * 1024);
        else glds16(wbase + (size_t)(kc * 64 + 16 * lw + 4 * jj) * ldw2, woff[0], lds0 + sbase(st) + 16384 + (4 * lw + jj) * 1024);
    }
    DI void issue(int kc_, int st, int lw) const {
        const int kc = kact(kc_);
#pragma unroll
        for (int jj = 0; jj < NW; ++jj) wp(kc, st, lw, jj);
#pragma unroll
        for (int jj = 0; jj < 4; ++jj) xp(kc, st, 4 * lw + jj, jj & 1);
    }
};

DI void rt_load(const unsigned short* w2, const unsigned short* wo, int u, unsigned ldsb, int piece, int lane) {
    const int sig = u >> 2, s = (u >> 1) & 1, cb = u & 1;
    const char* src = (sig < 48 ? (const char*)w2 + (size_t)sig * 32 * 1280 : (const char*)wo + (size_t)(sig - 48) * 32 * 1280) + s * 16 * 1280 + cb * 640;
    const int o = piece * 1024 + lane * 16, row = (o * 6554) >> 22;
    glds16(src, row * 1280 + (o - row * 640), ldsb + piece * 1024);
}
DI void rt_store(rsrc_t rs, int u, const char* ldsb, int i, int lane) {
    const int sig = u >> 2, s = (u >> 1) & 1, cb = u & 1;
    const int nl = 2 * i + (lane >> 5), hh = (lane >> 4) & 1, q = (lane & 15) >> 2, p = lane & 3;
    lds_cptr a = (lds_cptr)ldsb + (4 * hh + q) * 640 + (16 * nl + 4 * p) * 2;
    const s16x4 lo = ldstr(a), hi = ldstr(a + 8 * 640);
    const bf16x8 f = __builtin_shufflevector(lo, hi, 0, 1, 2, 3, 4, 5, 6, 7);
    const int nu = cb * 20 + nl, ng = (nu * 205) >> 10;
    st16(rs, (unsigned)WS_BF + (unsigned)((ng * NSIG + sig) * 5 + (nu - 5 * ng)) * 1024 + (s * 32 + (lane & 31)) * 16, __builtin_bit_cast(u32x4, f));
}

template <bool N96, bool VT, int MT, int NTL, class HOOK>
DI void gemm_loop(f32x16 (&acc)[MT][NTL], char* lds, int wid, int lane, HOOK hook) {
    const int h = lane >> 5, r = lane & 31;
    constexpr int NST = N96 ? 3 : 4;
    unsigned xa[4];
    { const int row = (N96 ? wid * 32 : (wid >> 1) * 64) + r; const int sw = (row >> 1) & 7;
#pragma unroll
      for (int ks = 0; ks < 4; ++ks) xa[ks] = row * 128 + (((2 * ks + h) ^ sw) << 4); }
    unsigned wa[2];
    { const int blk = (lane >> 4) & 1, q = (lane & 15) >> 2, p = lane & 3;
      if (N96) wa[0] = wa[1] = 16384 + (8 * h + q) * 192 + blk * 32 + p * 8;
      else {
#pragma unroll
          for (int nt = 0; nt < 2; ++nt) { const int ch = (wid & 1) * 8 + nt * 4 + 2 * blk + (p >> 1); wa[nt] = 16384 + (8 * h + q) * 256 + ((ch ^ (q << 2)) << 4) + 8 * (p & 1); } } }
    bf16x8 xf[2][MT], wf[2][NTL];
    auto rdw = [&](int buf, int nt, lds_cptr sb, int ks) {
        lds_cptr a = N96 ? sb + wa[0] + ks * 16 * 192 + nt * 64 : sb + wa[nt & 1] + ks * 4096;
        const s16x4 lo = ldstr(a), hi = ldstr(a + (N96 ? 4 * 192 : 1024));
        wf[buf][nt] = __builtin_shufflevector(lo, hi, 0, 1, 2, 3, 4, 5, 6, 7);
    };
    auto rdx = [&](int buf, int mt, lds_cptr sb, int ks) { xf[buf][mt] = lds128(sb + xa[ks] + mt * 4096); };
    auto mm = [&](int buf, int mt, int nt) { acc[mt][nt] = VT ? MFMA32(xf[buf][mt], wf[buf][nt], acc[mt][nt]) : MFMA32(wf[buf][nt], xf[buf][mt], acc[mt][nt]); };
    { lds_cptr sb = (lds_cptr)lds + GDma<N96>::sbase(0); rdw(0, 0, sb, 0); rdx(0, 0, sb, 0); rdw(0, 1, sb, 0); if (N96) rdw(0, 2 % NTL, sb, 0); else rdx(0, 1 % MT, sb, 0); }
    int st = 0;
#define G_KSTEP(ks, NEXT) do { const int b = (ks) & 1, n = b ^ 1; lds_cptr np = (ks) < 3 ? sb : sn; const int nk = ((ks) + 1) & 3; \
        SBAR(); \
        if constexpr (N96) { \
            mm(b, 0, 0); SBAR(); if (NEXT) { rdw(n, 0, np, nk); rdx(n, 0, np, nk); } SBAR(); \
            mm(b, 0, 1); SBAR(); if (NEXT) { rdw(n, 1, np, nk); } SBAR(); \
            mm(b, 0, 2 % NTL); SBAR(); if (NEXT) { rdw(n, 2 % NTL, np, nk); } SBAR(); \
        } else { \
            mm(b, 0, 0); SBAR(); if (NEXT) { rdw(n, 0, np, nk); } SBAR(); \
            mm(b, 0, 1); SBAR(); if (NEXT) { rdx(n, 0, np, nk); } SBAR(); \
            mm(b, 1 % MT, 0); SBAR(); if (NEXT) { rdw(n, 1, np, nk); } SBAR(); \
            mm(b, 1 % MT, 1); SBAR(); if (NEXT) { rdx(n, 1 % MT, np, nk); } SBAR(); \
        } } while (0)
#pragma unroll 1
    for (int kc = 0; kc < 9; ++kc) {
        lds_cptr sb = (lds_cptr)lds + GDma<N96>::sbase(st);
        st = st == NST - 1 ? 0 : st + 1;
        lds_cptr sn = (lds_cptr)lds + GDma<N96>::sbase(st);
        G_KSTEP(0, true); G_KSTEP(1, true); G_KSTEP(2, true);
        hook(kc, true);
        BARL();
        hook(kc, false);
        G_KSTEP(3, true);
    }
    { lds_cptr sb = (lds_cptr)lds + GDma<N96>::sbase(st); lds_cptr sn = sb;
      G_KSTEP(0, true); G_KSTEP(1, true); G_KSTEP(2, true); G_KSTEP(3, false); }
}

DI float swap32_add(float x) { auto rr = __builtin_amdgcn_permlane32_swap(__float_as_uint(x), __float_as_uint(x), false, false); return __uint_as_float(rr[0]) + __uint_as_float(rr[1]); }
DI float swap32_max(float x) { auto rr = __builtin_amdgcn_permlane32_swap(__float_as_uint(x), __float_as_uint(x), false, false); return fmaxf(__uint_as_float(rr[0]), __uint_as_float(rr[1])); }
DI s16x4 make_qb(unsigned q0, unsigned q1, float M, int h) {
    const float mh = bf_hi(__float_as_uint(M)), r1 = M - mh, mm = bf_hi(__float_as_uint(r1)), r2 = r1 - mm, ml = bf_hi(__float_as_uint(r2));
    u32x2 w;
    w[0] = h ? ((__float_as_uint(mh) >> 16) | (__float_as_uint(mm) & 0xffff0000u)) ^ 0x80008000u : q0;
    w[1] = h ? (__float_as_uint(ml) >> 16) ^ 0x8000u : q1;
    return __builtin_bit_cast(s16x4, w);
}

DI void attn_fallback(f32x16& o, const char* kv, bf16x8 qa, unsigned q0, unsigned q1, int lane, int kboff, int voff) {
    const int h = lane >> 5;
    const f32x16 zero = {};
    float m = -INFINITY;
    const s16x4 qb0 = make_qb(q0, q1, 0.f, h);
    for (int t = 0; t < NT; ++t) {
        const char* tb = kv + t * KVTILE_B;
        const bf16x8 ka = *(const bf16x8*)(tb + lane * 16); const s16x4 kb = *(const s16x4*)(tb + kboff);
        f32x16 s = MFMA32(ka, qa, zero); s = MFMA8(kb, qb0, s);
#pragma unroll
        for (int i = 0; i < 16; ++i) m = fmaxf(m, s[i]);
    }
    m = swap32_max(m);
    const s16x4 qb = make_qb(q0, q1, m, h);
    o = zero;
    for (int t = 0; t < NT; ++t) {
        const char* tb = kv + t * KVTILE_B;
        const bf16x8 ka = *(const bf16x8*)(tb + lane * 16); const s16x4 kb = *(const s16x4*)(tb + kboff);
        const bf16x8 va = *(const bf16x8*)(tb + voff), vb = *(const bf16x8*)(tb + voff + V_STEP);
        f32x16 s = MFMA32(ka, qa, zero); s = MFMA8(kb, qb, s);
        u32x4 p0, p1;
#pragma unroll
        for (int i = 0; i < 4; ++i) { p0[i] = cvtpk(__builtin_amdgcn_exp2f(s[2 * i]), __builtin_amdgcn_exp2f(s[2 * i + 1])); p1[i] = cvtpk(__builtin_amdgcn_exp2f(s[8 + 2 * i]), __builtin_amdgcn_exp2f(s[9 + 2 * i])); }
        o = MFMA32(va, __builtin_bit_cast(bf16x8, p0), o);
        o = MFMA32(vb, __builtin_bit_cast(bf16x8, p1), o);
    }
}

constexpr int SPIN_MAX = 1 << 20;
__global__ __launch_bounds__(512, 2) void gated_mix_mega(const unsigned short* __restrict__ x, const unsigned short* __restrict__ wqkv, const unsigned short* __restrict__ wo, const unsigned short* __restrict__ w1,
                                                         const unsigned short* __restrict__ w2, const unsigned short* __restrict__ gw, char* __restrict__ ws, unsigned short* __restrict__ out) {
    extern __shared__ __attribute__((aligned(16))) char lds[];
    const int tid = threadIdx.x, lane = tid & 63, wid = rfl(tid >> 6);
    const int bid = blockIdx.x, xcd = bid & 7, slot = bid >> 3;
    const int h = lane >> 5, r = lane & 31;
    const rsrc_t rs = ws_rsrc(ws);
    const unsigned lds0 = (unsigned)(uintptr_t)lds, lane16 = lane * 16;
    volatile LAS unsigned* ctl = (volatile LAS unsigned*)(lds + CTL_OFF);
    const int rt = 4 * (xcd >> 1) + (slot & 3), ci = slot >> 2, ct = 8 * (xcd & 1) + ci;
    const bool tile1 = ct < 15;
    const char* xbase = (const char*)x + (size_t)rt * 128 * 1280;
#define K0EXPR 0
    const int k0 = K0EXPR;
    const int head = xcd * 4 + (slot & 3), qblk = slot >> 2;
    const char* kv = ws + WS_KV + (size_t)head * NT * KVTILE_B;
    const int Tq = qblk * 8 + wid;
    const char* qt = ws + WS_Q + ((size_t)head * NT + Tq) * QTILE_B;
    bf16x8 qa; u32x2 qbr; f32x4 km4; float gate;
    auto q_loads = [&]() {
        asm volatile("s_nop 4\n\tglobal_load_dwordx4 %0, %1, %2" : "=v"(qa) : "v"(lane16), "s"(qt) : "memory");
        asm volatile("s_nop 4\n\tglobal_load_dwordx2 %0, %1, %2" : "=v"(qbr) : "v"((unsigned)(1024 + r * 8)), "s"(qt) : "memory");
        asm volatile("s_nop 4\n\tglobal_load_dwordx4 %0, %1, %2" : "=v"(km4) : "v"((unsigned)((lane < 40 ? lane : 0) * 16)), "s"(ws + WS_KMAX + (size_t)head * 640) : "memory");
        asm volatile("s_nop 4\n\tglobal_load_dword %0, %1, %2" : "=v"(gate) : "v"((unsigned)((Tq * 32 + r) * 4)), "s"(ws + WS_GATE) : "memory");
    };
    auto wait_seen = [&]() { for (int it = 0; it < SPIN_MAX; ++it) { if (ctl[1] != 0u) break; __builtin_amdgcn_s_sleep(1); } };
    auto gates = [&]() {
        const int aidx = (xcd >> 1) * 4 + (slot & 3);
        const u32x4* gr = (const u32x4*)gw + (lane & 7);
#pragma unroll 1
        for (int ps = 0; ps < 2; ++ps) {
            const int tok = aidx * 128 + wid * 16 + ps * 8 + (lane >> 3);
            const u32x4* xr = (const u32x4*)(x + (size_t)tok * DM) + (lane & 7);
            float sm = 0.f;
#pragma unroll
            for (int i = 0; i < 10; ++i) { const u32x4 xv = xr[i * 8], gv = gr[i * 8];
#pragma unroll
                for (int k = 0; k < 4; ++k) { const unsigned a_ = xv[k], b_ = gv[k]; sm = __builtin_amdgcn_fdot2_f32_bf16(__builtin_bit_cast(bf16x2_t, a_), __builtin_bit_cast(bf16x2_t, b_), sm, false); } }
            sm = dpp_add<0xB1>(sm); sm = dpp_add<0x4E>(sm); sm = dpp_add<0x141>(sm);
            const float lg = bf_round(sm);
            const float g = bf_round(__builtin_amdgcn_rcpf(1.f + __builtin_amdgcn_exp2f(lg * -1.4426950408889634f)));
            if ((lane & 7) == 0) st4f(ws + WS_GATE + tok * 4, g);
        }
    };
    GDma<true> d2; d2.init(xbase, (const char*)w1 + ct * 192, FF * 2, lds, lane, k0);
    u64 pc0; asm volatile("s_getpc_b64 %0" : "=s"(pc0));
    STAMP(0, 0);
    if (wid < 4) {
        GDma<false> d1; d1.init(xbase, (const char*)wqkv + ct * 256, 3 * DM * 2, lds, lane, k0);
        if (tile1) {
#pragma unroll
            for (int jj = 0; jj < 4; ++jj) d1.xp(d1.kact(0), 0, 4 * wid + jj, jj & 1);
        }
        if (tid == 0) { ctl[0] = 0u; ctl[1] = 0u; }
        if (tile1) asm volatile("s_waitcnt vmcnt(0) lgkmcnt(0)\n\ts_barrier" ::: "memory");
        if (slot * 4 + wid < 42) glds16((const char*)((pc0 + 15ull) & ~15ull) + (slot * 4 + wid) * 1024, lane16, lds0 + 152 * 1024);
        for (int pc = wid; pc < 10; pc += 4) rt_load(w2, wo, bid, lds0 + RT_OFF, pc, lane);
        if (!tile1) for (int pc = wid; pc < 10; pc += 4) rt_load(w2, wo, 256 + (xcd >> 1) * 4 + (slot & 3), lds0 + RT_OFF + 10240, pc, lane);
        if (wid == 0) glds16_sc1(ws + WS_EPOCH, lane < 1 ? 0u : 0u, lds0 + EP_OFF);
        auto tag_ready = [&]() {
            if (tid == 0) { const u64 tg = *(const volatile LAS u64*)(lds + EP_OFF) + 1ull; ctl[2] = (unsigned)tg; ctl[3] = (unsigned)(tg >> 32); }
        };
        if (!tile1) { asm volatile("s_waitcnt vmcnt(0)" ::: "memory"); tag_ready(); asm volatile("s_waitcnt lgkmcnt(0)\n\ts_barrier" ::: "memory"); }
        if (tile1) {
            const int wm = wid >> 1, wn = wid & 1;
            f32x16 acc[2][2] = {};
            const char* w1w = (const char*)w1 + (size_t)(slot * 20) * 3072 + (xcd & 1) * 1536;
            auto nohook = [&](int kc, bool before) {
                if (kc == 1 && before) { asm volatile("s_waitcnt vmcnt(0)" ::: "memory"); tag_ready(); }
                if (!before && kc < 4) {
#pragma unroll
                    for (int e = 0; e < 2; ++e) { const int u = kc * 8 + wid * 2 + e;
                        if (u < 30) { const int o = u * 1024 + (int)lane16, row = o / 1536; glds16(w1w, (unsigned)(row * 3072 + (o - row * 1536)), lds0 + 152 * 1024); } } }
                if (kc == 5 && before) asm volatile("s_waitcnt vmcnt(0)" ::: "memory");
            };
            if (ct >= 10) {
                gemm_loop<false, true, 2, 2>(acc, lds, wid, lane, nohook);
                STAMP(0, 1);
#pragma unroll
                for (int nt = 0; nt < 2; ++nt) {
                    const int phi = (ct - 10) * 128 + wn * 64 + nt * 32 + r;
                    const int hd = (phi * 3277) >> 16, d = phi - hd * 20;
                    const unsigned dst = (unsigned)WS_KV + (unsigned)(hd * NT + rt * 4 + wm * 2) * KVTILE_B + V_OFF + (h * V_ROWS + d) * 16;
#pragma unroll
                    for (int mt = 0; mt < 2; ++mt)
#pragma unroll
                        for (int s = 0; s < 2; ++s) {
                            const f32x16& a = acc[mt][nt];
                            const u32x4 w = {cvtpk(a[8 * s], a[8 * s + 1]), cvtpk(a[8 * s + 2], a[8 * s + 3]), cvtpk(a[8 * s + 4], a[8 * s + 5]), cvtpk(a[8 * s + 6], a[8 * s + 7])};
                            st16(rs, dst + mt * KVTILE_B + s * V_STEP, w);
                        }
                }
                BARL();
            } else {
                gemm_loop<false, false, 2, 2>(acc, lds, wid, lane, nohook);
                STAMP(0, 1);
                const bool isQ = ct < 5;
                const float sc = isQ ? QSCALE : 1.f;
                const unsigned img = (unsigned)(isQ ? WS_Q : WS_KV), tileb = isQ ? QTILE_B : KVTILE_B;
                const int G = (isQ ? ct : ct - 5) * 32 + wn * 16 + h;
                u32x2 P[2][8]; float nrm[8];
#pragma unroll
                for (int p = 0; p < 8; ++p) {
                    const int nt = p >> 2, g = p & 3; float m = 0.f;
#pragma unroll
                    for (int mt = 0; mt < 2; ++mt) {
                        const f32x16& a = acc[mt][nt];
                        P[mt][p] = (u32x2){cvtpk(a[4 * g] * sc, a[4 * g + 1] * sc), cvtpk(a[4 * g + 2] * sc, a[4 * g + 3] * sc)};
                        m = fmaxf(m, __builtin_fmaf(a[4 * g], a[4 * g], a[4 * g + 1] * a[4 * g + 1]) + __builtin_fmaf(a[4 * g + 2], a[4 * g + 2], a[4 * g + 3] * a[4 * g + 3]));
                    }
                    nrm[p] = m;
                }
#pragma unroll
                for (int p = 0; p < 8; ++p) {
                    const int gam = G + 2 * p, hd = (gam * 205) >> 10, dg = gam - 5 * hd;
                    const unsigned base = img + (unsigned)(hd * NT + rt * 4 + wm * 2) * tileb;
#pragma unroll
                    for (int mt = 0; mt < 2; ++mt) {
                        const unsigned bm = base + mt * tileb;
                        if (dg < 2) {
                            if (p < 7) st16(rs, bm + dg * 512 + r * 16, (u32x4){P[mt][p][0], P[mt][p][1], P[mt][(p + 1) & 7][0], P[mt][(p + 1) & 7][1]});
                            else st8(rs, bm + dg * 512 + r * 16, P[mt][p]);
                        } else if (dg == 4) st8(rs, bm + 1024 + r * 8, P[mt][p]);
                        else if (p == 0) st8(rs, bm + (dg - 2) * 512 + r * 16 + 8, P[mt][p]);
                    }
                }
                if (!isQ) {
#pragma unroll
                    for (int i = 0; i < 8; ++i) { float m = nrm[i]; m = dpp_max<0xB1>(m); m = dpp_max<0x4E>(m); m = dpp_max<0x141>(m); m = dpp_max<0x140>(m);
                        auto rr = __builtin_amdgcn_permlane16_swap(__float_as_uint(m), __float_as_uint(m), false, false); nrm[i] = fmaxf(__uint_as_float(rr[0]), __uint_as_float(rr[1])); }
                    float mine = nrm[0];
#pragma unroll
                    for (int i = 1; i < 8; ++i) mine = r == i ? nrm[i] : mine;
                    if (r < 8) st4f(ws + WS_KMAX + (size_t)((G + 2 * r) * 32 + rt * 2 + wm) * 4, mine);
                }
                BARL();
            }
        } else { gates(); BARL(); }
        STAMP(0, 2);
        BARL();
        f32x16 acc2[1][3] = {};
        auto hook2 = [&](int kc, bool before) {
            if (kc == 2) { if (before) asm volatile("s_waitcnt vmcnt(0)" ::: "memory");
                           else if (tid == 0) flag_set(ws + WS_F1 + bid * 8, ((u64)ctl[3] << 32) | ctl[2]); }
        };
        gemm_loop<true, false, 1, 3>(acc2, lds, wid, lane, hook2);
        STAMP(0, 3);
        wait_seen();
        q_loads();
        {   const int tok = rt * 128 + wid * 32 + r, rho = tok >> 4;
#pragma unroll
            for (int nt = 0; nt < 3; ++nt) {
                const unsigned dst = (unsigned)WS_AF + (unsigned)(rho * NSIG + ct * 3 + nt) * 1024 + (h * 16 + (r & 15)) * 16;
                float sv[16];
#pragma unroll
                for (int i = 0; i < 16; ++i) { const float v = acc2[0][nt][i]; sv[i] = v * __builtin_amdgcn_rcpf(1.f + __builtin_amdgcn_exp2f(v * -1.4426950408889634f)); }
#pragma unroll
                for (int s = 0; s < 2; ++s) {
                    const u32x4 w = {cvtpk(sv[8 * s], sv[8 * s + 1]), cvtpk(sv[8 * s + 2], sv[8 * s + 3]), cvtpk(sv[8 * s + 4], sv[8 * s + 5]), cvtpk(sv[8 * s + 6], sv[8 * s + 7])};
                    st16(rs, dst + s * 512, w);
                }
            }
        }
        asm volatile("s_waitcnt vmcnt(6)" : "+v"(qa), "+v"(qbr), "+v"(km4), "+v"(gate) :: "memory");
    } else {
        const int lw = wid - 4;
        auto side_stores = [&]() {
            for (int i = lw; i < 10; i += 4) rt_store(rs, bid, lds + RT_OFF, i, lane);
            if (!tile1) for (int i = lw; i < 10; i += 4) rt_store(rs, 256 + (xcd >> 1) * 4 + (slot & 3), lds + RT_OFF + 10240, i, lane);
            if (lw == 0) { const int i = bid * 64 + lane;
                const unsigned f = (i & 1) ? 0u : ONES2; st16(rs, (unsigned)WS_KV + (unsigned)(i >> 3) * KVTILE_B + V_OFF + ((i >> 2) & 1) * V_STEP + (((i >> 1) & 1) * V_ROWS + 20 + (i & 1)) * 16, (u32x4){f, f, f, f}); }
        };
        if (tile1) {
            GDma<false> d1; d1.init(xbase, (const char*)wqkv + ct * 256, 3 * DM * 2, lds, lane, k0);
#pragma unroll
            for (int jj = 0; jj < 4; ++jj) d1.wp(d1.kact(0), 0, lw, jj);
            d1.issue(1, 1, lw);
            WAITB(8);
            d1.issue(2, 2, lw); d1.issue(3, 3, lw);
            for (int kc = 0; kc < 9; ++kc) {
                if (kc < 7) WAITB(16); else if (kc == 7) WAITB(15); else WAITB(14);
                if (kc + 4 < 10) d1.issue(kc + 4, kc & 3, lw);
                if (kc == 4) side_stores();
                if (kc >= 6) d2.issue(kc - 6, kc - 6, lw);
            }
            asm volatile("s_barrier" ::: "memory");
        } else {
            asm volatile("s_barrier" ::: "memory");
            side_stores(); gates();
            BARL();
            d2.issue(0, 0, lw); d2.issue(1, 1, lw); d2.issue(2, 2, lw);
        }
        WAITB(14);
        { int st = 0;
          for (int kc = 0; kc < 9; ++kc) {
              if (kc < 8) WAITB(7); else WAITB(0);
              if (kc + 3 < 10) d2.issue(kc + 3, st, lw);
              st = st == 2 ? 0 : st + 1;
          } }
        if (lw == 0) {
            const u64 tag = ((u64)ctl[3] << 32) | ctl[2];
            for (int it = 0; it < SPIN_MAX; ++it) {
                bool ok = true;
#pragma unroll
                for (int i = 0; i < 4; ++i) ok &= flag_get(ws + WS_F1 + (lane + 64 * i) * 8) == tag;
                if (__all(ok)) break;
                __builtin_amdgcn_s_sleep(2);
            }
            __builtin_amdgcn_fence(__ATOMIC_ACQUIRE, "agent");
            asm volatile("s_waitcnt vmcnt(0)" ::: "memory");
            if (lane == 0) { ctl[1] = 1u; if (bid == 0) flag_set(ws + WS_EPOCH, tag); }
        } else wait_seen();
        q_loads();
        for (int j = 0; j < 21; ++j) glds16(kv + (size_t)(lw + 4 * j) * 1024, lane16, lds0 + (lw + 4 * j) * 1024);
        asm volatile("s_waitcnt vmcnt(21)" : "+v"(qa), "+v"(qbr), "+v"(km4), "+v"(gate) :: "memory");
    }
    STAMP(1, 0);
    float km = fmaxf(fmaxf(km4[0], km4[1]), fmaxf(km4[2], km4[3]));
    km = dpp_max<0xB1>(km); km = dpp_max<0x4E>(km); km = dpp_max<0x141>(km);
    km = __builtin_amdgcn_sqrtf(km);
    float M;
    { const float c0 = rdl(km, 0), c1 = rdl(km, 8), c2 = rdl(km, 16), c3 = rdl(km, 24), c4 = rdl(km, 32);
      const u32x4 qw = __builtin_bit_cast(u32x4, qa);
      float n0, n1, n2;
      { const float a = bf_lo(qw[0]), b = bf_hi(qw[0]), c = bf_lo(qw[1]), d = bf_hi(qw[1]); n0 = (a * a + b * b) + (c * c + d * d); }
      { const float a = bf_lo(qw[2]), b = bf_hi(qw[2]), c = bf_lo(qw[3]), d = bf_hi(qw[3]); n1 = (a * a + b * b) + (c * c + d * d); }
      { const unsigned q0 = qbr[0], q1 = qbr[1]; const float a = bf_lo(q0), b = bf_hi(q0), c = bf_lo(q1), d = bf_hi(q1); n2 = (a * a + b * b) + (c * c + d * d); }
      float part = __builtin_amdgcn_sqrtf(n0) * (h ? c1 : c0) + __builtin_amdgcn_sqrtf(n1) * (h ? c3 : c2);
      if (!h) part += __builtin_amdgcn_sqrtf(n2) * c4;
      M = swap32_add(part) * 1.01f; }
    const s16x4 qb = make_qb(qbr[0], qbr[1], M, h);
    const int vrow = r <= 20 ? r : (r < 28 ? 21 : r - 12);
    const int voff = V_OFF + (h * V_ROWS + vrow) * 16, kboff = h ? V_OFF + 20 * 16 : 1024 + r * 8;
    lds_cptr kaC = (lds_cptr)lds + lane * 16, kbC = (lds_cptr)lds + kboff, vC = (lds_cptr)lds + voff;
    lds_cptr kaN = kaC + K2_BIG, kbN = kbC + K2_BIG, vN = vC + K2_BIG;
    const f32x16 zero = {};
    f32x16 o = {}, sA, sB;
    bf16x8 ka, va, vb; s16x4 kb;
    u32x4 plo, phi;
    if (wid >= 4) asm volatile("s_waitcnt vmcnt(15)" ::: "memory");
    asm volatile("s_barrier" ::: "memory");
    STAMP(1, 1);
    ka = lds128(kaC); kb = lds64(kbC);
    sA = MFMA32(ka, qa, zero); sA = MFMA8(kb, qb, sA);
    ka = lds128(kaC + KVTILE_B); kb = lds64(kbC + KVTILE_B);
    va = lds128(vC);
    vb = (bf16x8){}; phi = (u32x4){};
    f32x4 e3 = {};
    const int ng = (xcd & 1) * 4 + (slot & 3), bt = (xcd >> 1) * 8 + (slot >> 2);
    const char* ap = ws + WS_AF + (size_t)bt * 4 * NSIG * 1024;
    const char* bp = ws + WS_BF + (size_t)ng * NSIG * 5 * 1024;
#define ROTEXPR (((slot >> 2) + 3 * (slot & 3)) % 6)
    const int rot = ROTEXPR;
    auto hs = [&](int i) { int st = i + rot; if (st >= 6) st -= 6; return wid * 6 + st; };
    bf16x8 a[3][4], b[3][5];
    auto ldA1 = [&](int sg, int mt, bf16x8& d) { gld128_sc1(d, ap + ((size_t)mt * NSIG + sg) * 1024, lane16); };
    auto ldB1 = [&](int sg, int nt, bf16x8& d) { gld128(d, bp + sg * 5120 + nt * 1024, lane16); };
    const u64 tag = ((u64)ctl[3] << 32) | ctl[2];
#define EXP4(X, i) do { X[i] = __builtin_amdgcn_exp2f(X[i]); X[i + 1] = __builtin_amdgcn_exp2f(X[i + 1]); X[i + 2] = __builtin_amdgcn_exp2f(X[i + 2]); X[i + 3] = __builtin_amdgcn_exp2f(X[i + 3]); } while (0)
#define KADDR(A, i) (((i) < 16) ? (A##C + (i) * KVTILE_B) : (A##N + ((i) - 16) * KVTILE_B))
#define STEP(C, N, i, X0, X1) do { \
        bf16x8 ka2, va2, vb2; s16x4 kb2; \
        N = MFMA32(ka, qa, zero); SBAR(); \
        ka2 = lds128(KADDR(ka, (i) + 2)); \
        phi[2] = cvtpk(e3[0], e3[1]); phi[3] = cvtpk(e3[2], e3[3]); PIN(phi); SBAR(); EXP4(C, 0); PIN(C); SBAR(); \
        o = MFMA32(vb, __builtin_bit_cast(bf16x8, phi), o); SBAR(); \
        X0; \
        vb2 = lds128(vC + (i) * KVTILE_B + V_STEP); \
        plo[0] = cvtpk(C[0], C[1]); plo[1] = cvtpk(C[2], C[3]); PIN(plo); SBAR(); EXP4(C, 4); PIN(C); SBAR(); \
        N = MFMA8(kb, qb, N); SBAR(); \
        kb2 = lds64(KADDR(kb, (i) + 2)); \
        plo[2] = cvtpk(C[4], C[5]); plo[3] = cvtpk(C[6], C[7]); PIN(plo); SBAR(); EXP4(C, 8); PIN(C); SBAR(); \
        o = MFMA32(va, __builtin_bit_cast(bf16x8, plo), o); SBAR(); \
        X1; \
        va2 = lds128(KADDR(v, (i) + 1)); \
        phi[0] = cvtpk(C[8], C[9]); phi[1] = cvtpk(C[10], C[11]); PIN(phi); SBAR(); \
        e3[0] = __builtin_amdgcn_exp2f(C[12]); e3[1] = __builtin_amdgcn_exp2f(C[13]); e3[2] = __builtin_amdgcn_exp2f(C[14]); e3[3] = __builtin_amdgcn_exp2f(C[15]); PIN(e3); SBAR(); \
        ka = ka2; kb = kb2; va = va2; vb = vb2; \
    } while (0)
#define PIECE(j) do { if (8 * (j) + 7 < 42 || 8 * (j) + wid < 42) glds16(src + (j) * 8192, lane16, dst + (j) * 8192); } while (0)
    const char* src = kv + (84 + wid) * 1024; unsigned dst = lds0 + wid * 1024 + 2 * K2_BIG;
#pragma unroll 1
    for (int c = 0; c < 3; ++c) {
        STEP(sA, sB, 0, , ); STEP(sB, sA, 1, , ); STEP(sA, sB, 2, , ); STEP(sB, sA, 3, , ); STEP(sA, sB, 4, , );
        if (c == 0) { if (wid >= 4) asm volatile("s_waitcnt vmcnt(10)" ::: "memory"); asm volatile("s_barrier" ::: "memory"); }
        STEP(sB, sA, 5, , ); STEP(sA, sB, 6, , ); STEP(sB, sA, 7, , ); STEP(sA, sB, 8, , ); STEP(sB, sA, 9, , ); STEP(sA, sB, 10, , ); STEP(sB, sA, 11, , ); STEP(sA, sB, 12, , );
        if (c == 2 && wid == 0) {
            for (int it = 0; it < SPIN_MAX; ++it) {
                if (__all(flag_get(ws + WS_F2 + ((bt >> 1) * 16 + (lane & 15)) * 8) == tag)) break;
                __builtin_amdgcn_s_sleep(2);
            }
        }
        WAITB(0);
        if (c == 0 && tid == 0) flag_set(ws + WS_F2 + (rt * 16 + ct) * 8, tag);
        if (c < 2) { STEP(sB, sA, 13, PIECE(0), PIECE(1)); STEP(sA, sB, 14, PIECE(2), PIECE(3)); STEP(sB, sA, 15, PIECE(4), PIECE(5)); }
        else { STEP(sB, sA, 13, , ); STEP(sA, sB, 14, , ); STEP(sB, sA, 15, , ); }
        const int d = (c == 1) ? -2 * K2_BIG : K2_BIG;
        kaC = kaN; kbC = kbN; vC = vN; kaN += d; kbN += d; vN += d;
        src += 42 * 1024; dst -= 2 * K2_BIG;
    }
#define PA(s, m) ldA1(hs(s), m, a[s][m])
#define PB(s, n) ldB1(hs(s), n, b[s][n])
    STEP(sA, sB, 0, PA(0, 0), PA(0, 1)); STEP(sB, sA, 1, PA(0, 2), PA(0, 3)); STEP(sA, sB, 2, PB(0, 0), PB(0, 1)); STEP(sB, sA, 3, PB(0, 2), PB(0, 3));
    STEP(sA, sB, 4, PB(0, 4), PA(1, 0)); STEP(sB, sA, 5, PA(1, 1), PA(1, 2)); STEP(sA, sB, 6, PA(1, 3), PB(1, 0)); STEP(sB, sA, 7, PB(1, 1), PB(1, 2));
    STEP(sA, sB, 8, PB(1, 3), PB(1, 4)); STEP(sB, sA, 9, PA(2, 0), PA(2, 1)); STEP(sA, sB, 10, PA(2, 2), PA(2, 3)); STEP(sB, sA, 11, PB(2, 0), PB(2, 1));
    STEP(sA, sB, 12, PB(2, 2), PB(2, 3)); STEP(sB, sA, 13, PB(2, 4), ); STEP(sA, sB, 14, , ); STEP(sB, sA, 15, , );
    phi[2] = cvtpk(e3[0], e3[1]); phi[3] = cvtpk(e3[2], e3[3]);
    o = MFMA32(vb, __builtin_bit_cast(bf16x8, phi), o);
    STAMP(1, 2);
    float l; { auto rr = __builtin_amdgcn_permlane32_swap(__float_as_uint(o[8]), __float_as_uint(o[8]), false, false); l = __uint_as_float(rr[1]); }
    if (__builtin_expect(__any(!(l >= 8.6736174e-19f && l <= 1.0e30f)), 0)) {
        asm volatile("s_waitcnt vmcnt(0)" ::: "memory");
        attn_fallback(o, kv, qa, qbr[0], qbr[1], lane, kboff, voff);
        auto rr = __builtin_amdgcn_permlane32_swap(__float_as_uint(o[8]), __float_as_uint(o[8]), false, false); l = __uint_as_float(rr[1]);
    }
    const float f = gate * __builtin_amdgcn_rcpf(l);
    const int tok = Tq * 32 + r;
    char* ab = ws + WS_AF + (size_t)(tok >> 4) * NSIG * 1024 + (tok & 15) * 16;
#pragma unroll
    for (int g = 0; g < 3; ++g) {
        const int gam = head * 5 + 2 * g + h;
        const int sig = 48 + (gam >> 3), c = ((gam >> 2) & 1) * 2 + (gam & 1), half = (gam >> 1) & 1;
        const u32x2 w = {cvtpk(o[4 * g] * f, o[4 * g + 1] * f), cvtpk(o[4 * g + 2] * f, o[4 * g + 3] * f)};
        if (g < 2 || h == 0) __hip_atomic_store((u64*)(ab + sig * 1024 + c * 256 + half * 8), ((u64)w[1] << 32) | w[0], __ATOMIC_RELAXED, __HIP_MEMORY_SCOPE_AGENT);
    }
    STAMP(1, 3);
    {
        f32x4 acc[4][5] = {};
        auto loadA = [&](int sg, bf16x8 (&da)[4]) {
#pragma unroll
            for (int mt = 0; mt < 4; ++mt) gld128_sc1(da[mt], ap + ((size_t)mt * NSIG + sg) * 1024, lane16);
        };
        auto loadB = [&](int sg, bf16x8 (&db)[5]) {
#pragma unroll
            for (int nt = 0; nt < 5; ++nt) gld128(db[nt], bp + sg * 5120 + nt * 1024, lane16);
        };
        auto mma = [&](bf16x8 (&da)[4], bf16x8 (&db)[5]) {
            SBAR();
#pragma unroll
            for (int nt = 0; nt < 5; ++nt)
#pragma unroll
                for (int mt = 0; mt < 4; ++mt) acc[mt][nt] = MFMA16(db[nt], da[mt], acc[mt][nt]);
            SBAR();
        };
        wait9<3>(a[0], b[0]);
        asm volatile("" : "+v"(a[1][0]), "+v"(a[1][1]), "+v"(a[1][2]), "+v"(a[1][3]), "+v"(b[1][0]), "+v"(b[1][1]), "+v"(b[1][2]), "+v"(b[1][3]), "+v"(b[1][4]));
        asm volatile("" : "+v"(a[2][0]), "+v"(a[2][1]), "+v"(a[2][2]), "+v"(a[2][3]), "+v"(b[2][0]), "+v"(b[2][1]), "+v"(b[2][2]), "+v"(b[2][3]), "+v"(b[2][4]));
        mma(a[0], b[0]); loadA(hs(3), a[0]); loadB(hs(3), b[0]);
        mma(a[1], b[1]); loadA(hs(4), a[1]); loadB(hs(4), b[1]);
        asm volatile("s_waitcnt vmcnt(18)" ::: "memory");
        if (lane == 0) {
            const unsigned old = __hip_atomic_fetch_add((unsigned*)(lds + CTL_OFF), 1u, __ATOMIC_RELAXED, __HIP_MEMORY_SCOPE_WORKGROUP);
            if (old == 7u) flag_set(ws + WS_F3 + (qblk * 32 + head) * 8, tag);
        }
        mma(a[2], b[2]); loadA(hs(5), a[2]); loadB(hs(5), b[2]);
        float og[4];
#pragma unroll
        for (int mt = 0; mt < 4; ++mt) asm volatile("s_nop 4\n\tglobal_load_dword %0, %1, %2" : "=v"(og[mt]) : "v"((unsigned)((bt * 64 + mt * 16 + (lane & 15)) * 4)), "s"(ws + WS_GATE) : "memory");
        wait9<22>(a[0], b[0]); mma(a[0], b[0]); loadB(48 + wid, b[0]);
        wait9<18>(a[1], b[1]); mma(a[1], b[1]); loadB(56 + wid, b[1]);
        wait9<14>(a[2], b[2]); mma(a[2], b[2]);
        if (wid < 4) {
#pragma unroll
            for (int nt = 0; nt < 5; ++nt) gld128_rw(b[2][nt], bp + (64 + wid) * 5120 + nt * 1024, lane16);
        }
        STAMP(2, 0);
        asm volatile("s_waitcnt vmcnt(0)" : "+v"(og[0]), "+v"(og[1]), "+v"(og[2]), "+v"(og[3]) :: "memory");
#pragma unroll
        for (int mt = 0; mt < 4; ++mt) { const float f1 = bf_round(1.f - og[mt]);
#pragma unroll
            for (int nt = 0; nt < 5; ++nt) acc[mt][nt] *= f1; }
        if (wid == 0) {
            for (int it = 0; it < SPIN_MAX; ++it) {
                if (__all(flag_get(ws + WS_F3 + ((bt >> 2) * 32 + r) * 8) == tag)) break;
                __builtin_amdgcn_s_sleep(2);
            }
        }
        asm volatile("s_barrier" ::: "memory");
        STAMP(2, 1);
        loadA(48 + wid, a[0]); loadA(56 + wid, a[1]);
        a[2][0] = (bf16x8){}; a[2][1] = (bf16x8){}; a[2][2] = (bf16x8){}; a[2][3] = (bf16x8){};
        if (wid < 4) {
#pragma unroll
            for (int mt = 0; mt < 4; ++mt) gld128_sc1_rw(a[2][mt], ap + ((size_t)mt * NSIG + 64 + wid) * 1024, lane16);
        }
        wait9<4>(a[0], b[0]); mma(a[0], b[0]);
        wait9<0>(a[1], b[1]); wait9<0>(a[2], b[2]); mma(a[1], b[1]); mma(a[2], b[2]);
        STAMP(2, 2);
#pragma unroll
        for (int mt = 0; mt < 4; ++mt)
#pragma unroll
            for (int nt = 0; nt < 5; ++nt) *(f32x4*)(lds + (wid * 20 + mt * 5 + nt) * 1024 + lane16) = acc[mt][nt];
        BARL();
#pragma unroll
        for (int i = 0; i < 3; ++i) { const int t = wid + 8 * i;
            if (t < 20) {
                f32x4 sum = *(const f32x4*)(lds + t * 1024 + lane16);
#pragma unroll
                for (int w = 1; w < 8; ++w) sum += *(const f32x4*)(lds + (w * 20 + t) * 1024 + lane16);
                const int m4 = t / 5, nt = t - m4 * 5;
                const int tok2 = bt * 64 + m4 * 16 + (lane & 15);
                *(u32x2*)(out + (size_t)tok2 * DM + ng * 80 + nt * 16 + (lane >> 4) * 4) = (u32x2){cvtpk(sum[0], sum[1]), cvtpk(sum[2], sum[3])};
            } }
        STAMP(2, 3);
    }
}

extern "C" void kernel_launch(void* const* d_in, const int* in_sizes, int n_in, void* d_out, int out_size, void* d_ws, size_t ws_size, hipStream_t stream) {
    const unsigned short* x = (const unsigned short*)d_in[0];
    const unsigned short* wqkv = (const unsigned short*)d_in[1];
    const unsigned short* wo = (const unsigned short*)d_in[2];
    const unsigned short* w1 = (const unsigned short*)d_in[4];
    const unsigned short* w2 = (const unsigned short*)d_in[5];
    const unsigned short* gw = (const unsigned short*)d_in[6];
    (void)hipFuncSetAttribute((const void*)gated_mix_mega, hipFuncAttributeMaxDynamicSharedMemorySize, LDS_ALL);
    gated_mix_mega<<<256, 512, LDS_ALL, stream>>>(x, wqkv, wo, w1, w2, gw, (char*)d_ws, (unsigned short*)d_out);
}
```

```cpp
#include <hip/hip_runtime.h>
#include <hip/hip_bf16.h>
#include <stdint.h>

typedef short bf16x8 __attribute__((ext_vector_type(8)));
typedef short s16x4 __attribute__((ext_vector_type(4)));
typedef float f32x16 __attribute__((ext_vector_type(16)));
typedef float f32x4 __attribute__((ext_vector_type(4)));
typedef float f32x2 __attribute__((ext_vector_type(2)));
typedef unsigned u32x4 __attribute__((ext_vector_type(4)));
typedef unsigned u32x2 __attribute__((ext_vector_type(2)));
typedef __bf16 bf16x2_t __attribute__((ext_vector_type(2)));
#define LAS __attribute__((address_space(3)))
typedef LAS const char* lds_cptr;
#define DI __device__ __forceinline__
#define SBAR() __builtin_amdgcn_sched_barrier(0)
#define PIN(x) asm volatile("" : "+v"(x))
#define MFMA32(a, b, c) __builtin_amdgcn_mfma_f32_32x32x16_bf16(a, b, c, 0, 0, 0)
#define MFMA16(a, b, c) __builtin_amdgcn_mfma_f32_16x16x32_bf16(a, b, c, 0, 0, 0)
#define MFMA8(a, b, c) __builtin_amdgcn_mfma_f32_32x32x8bf16_1k(a, b, c, 0, 0, 0)

constexpr int S = 2048, DM = 640, NH = 32, DH = 20, FF = 1536;
constexpr int NT = S / 32;
constexpr int QTILE_B = 1280;
constexpr int KVTILE_B = 2688;
constexpr int V_OFF = 1280, V_STEP = 704, V_ROWS = 22;
constexpr int NSIG = 68;
constexpr size_t WS_Q = 0;
constexpr size_t WS_KV = WS_Q + (size_t)NH * NT * QTILE_B;
constexpr size_t WS_AF = WS_KV + (size_t)NH * NT * KVTILE_B;
constexpr size_t WS_BF = WS_AF + (size_t)128 * NSIG * 1024;
constexpr size_t WS_KMAX = WS_BF + (size_t)8 * NSIG * 5 * 1024;
constexpr size_t WS_GATE = WS_KMAX + (size_t)160 * 32 * 4;
constexpr size_t WS_F1 = WS_GATE + (size_t)S * 4;
constexpr size_t WS_F2 = WS_F1 + 2048;
constexpr size_t WS_F3 = WS_F2 + 2048;
constexpr size_t WS_EPOCH = WS_F3 + 2048;
constexpr size_t WS_END = WS_EPOCH + 256;
constexpr float QSCALE = 0.22360679774997896f * 1.4426950408889634f;
constexpr unsigned ONES2 = 0x3F803F80u;

DI unsigned cvtpk(float lo, float hi) { f32x2 v = {lo, hi}; bf16x2_t b = __builtin_convertvector(v, bf16x2_t); return __builtin_bit_cast(unsigned, b); }
DI float bf_lo(unsigned u) { return __uint_as_float(u << 16); }
DI float bf_hi(unsigned u) { return __uint_as_float(u & 0xffff0000u); }
DI float bf_round(float x) { return bf_lo(cvtpk(x, x)); }
DI void glds16(const void* sbase, unsigned voff, unsigned lds_base) {
    unsigned sv;
    asm volatile("s_mov_b32 %0, m0\n\ts_mov_b32 m0, %3\n\ts_nop 2\n\tglobal_load_lds_dwordx4 %1, %2\n\ts_mov_b32 m0, %0"
                 : "=&s"(sv) : "v"(voff), "s"(sbase), "s"(lds_base) : "memory");
}
DI void glds16_sc1(const void* sbase, unsigned voff, unsigned lds_base) {
    unsigned sv;
    asm volatile("s_mov_b32 %0, m0\n\ts_mov_b32 m0, %3\n\ts_nop 2\n\tglobal_load_lds_dwordx4 %1, %2 sc1\n\ts_mov_b32 m0, %0"
                 : "=&s"(sv) : "v"(voff), "s"(sbase), "s"(lds_base) : "memory");
}
typedef short v4i16_t __attribute__((ext_vector_type(4)));
DI s16x4 ldstr(lds_cptr p) { return __builtin_bit_cast(s16x4, __builtin_amdgcn_ds_read_tr16_b64_v4i16((LAS v4i16_t*)p)); }
DI bf16x8 lds128(lds_cptr p) { return *(const LAS bf16x8*)p; }
DI s16x4 lds64(lds_cptr p) { return *(const LAS s16x4*)p; }
DI unsigned rfl(unsigned x) { return __builtin_amdgcn_readfirstlane(x); }
#define STAMP(k, i) do { } while (0)

DI float rdl(float x, int l) { return __uint_as_float(__builtin_amdgcn_readlane(__float_as_uint(x), l)); }
typedef __amdgpu_buffer_rsrc_t rsrc_t;
typedef unsigned long long u64;
DI rsrc_t ws_rsrc(char* ws) { return __builtin_amdgcn_make_buffer_rsrc(ws, 0, (int)WS_END, 0x00020000); }
DI void st16(rsrc_t rs, unsigned off, u32x4 v) { __builtin_amdgcn_raw_buffer_store_b128(v, rs, off, 0, 16); }
DI void st8(rsrc_t rs, unsigned off, u32x2 v) { __builtin_amdgcn_raw_buffer_store_b64(v, rs, off, 0, 16); }
DI void st4f(char* p, float v) { __hip_atomic_store((float*)p, v, __ATOMIC_RELAXED, __HIP_MEMORY_SCOPE_AGENT); }
DI void flag_set(char* p, u64 tag) { __hip_atomic_store((u64*)p, tag, __ATOMIC_RELAXED, __HIP_MEMORY_SCOPE_AGENT); }
DI u64 flag_get(const char* p) { return __hip_atomic_load((const u64*)p, __ATOMIC_RELAXED, __HIP_MEMORY_SCOPE_AGENT); }
template <int CTRL> DI float dpp_max(float x) { const float y = __uint_as_float(__builtin_amdgcn_update_dpp(0u, __float_as_uint(x), CTRL, 0xf, 0xf, true)); return fmaxf(x, y); }
template <int CTRL> DI float dpp_add(float x) { const float y = __uint_as_float(__builtin_amdgcn_update_dpp(0u, __float_as_uint(x), CTRL, 0xf, 0xf, true)); return x + y; }
DI void gld128(bf16x8& d, const char* sbase, unsigned voff) { asm volatile("s_nop 4\n\tglobal_load_dwordx4 %0, %1, %2" : "=v"(d) : "v"(voff), "s"(sbase) : "memory"); }
DI void gld128_sc1(bf16x8& d, const char* sbase, unsigned voff) { asm volatile("s_nop 4\n\tglobal_load_dwordx4 %0, %1, %2 sc1" : "=v"(d) : "v"(voff), "s"(sbase) : "memory"); }
DI void gld128_rw(bf16x8& d, const char* sbase, unsigned voff) { asm volatile("s_nop 4\n\tglobal_load_dwordx4 %0, %1, %2" : "+v"(d) : "v"(voff), "s"(sbase) : "memory"); }
DI void gld128_sc1_rw(bf16x8& d, const char* sbase, unsigned voff) { asm volatile("s_nop 4\n\tglobal_load_dwordx4 %0, %1, %2 sc1" : "+v"(d) : "v"(voff), "s"(sbase) : "memory"); }
template <int N> DI void wait9(bf16x8 (&a)[4], bf16x8 (&b)[5]) {
    asm volatile("s_waitcnt vmcnt(%9)" : "+v"(a[0]), "+v"(a[1]), "+v"(a[2]), "+v"(a[3]), "+v"(b[0]), "+v"(b[1]), "+v"(b[2]), "+v"(b[3]), "+v"(b[4]) : "i"(N) : "memory");
}
#define WAITB(n) asm volatile("s_waitcnt vmcnt(" #n ")\n\ts_barrier" ::: "memory")
#define BARL() asm volatile("s_waitcnt lgkmcnt(0)\n\ts_barrier" ::: "memory")

constexpr int P1_STG = 32768;
constexpr int RT_OFF = 131072;
constexpr int P2_STG = 28672;
constexpr int CTL_OFF = 163584;
DI constexpr int p2_base(int s) { return CTL_OFF - (s + 1) * P2_STG; }
constexpr int K2_BIG = 16 * KVTILE_B;
constexpr int EP_OFF = 151552;
constexpr int LDS_ALL = 163840;

template <bool N96>
struct GDma {
    static constexpr int NW = N96 ? 3 : 4;
    const char* xbase; const char* wbase; int ldw2, k0; unsigned lds0, xoff0, xoff1, woff[3];
    DI int kact(int kc) const { kc += k0; return kc >= 10 ? kc - 10 : kc; }
    DI static unsigned sbase(int st) { return N96 ? (unsigned)(CTL_OFF - (st + 1) * P2_STG) : (unsigned)(st * P1_STG); }
    DI void init(const char* xb, const char* wb, int ld, char* lds, int lane, int k0_) {
        xbase = xb; wbase = wb; ldw2 = ld; k0 = k0_; lds0 = (unsigned)(uintptr_t)lds;
        const int rr = lane >> 3;
        xoff0 = rr * 1280 + (((lane & 7) ^ ((lane >> 4) & 7)) << 4); xoff1 = rr * 1280 + (((lane & 7) ^ ((4 + (lane >> 4)) & 7)) << 4);
        if (N96) {
#pragma unroll
            for (int p = 0; p < 3; ++p) { const int o = 1024 * p + 16 * lane, row = (o * 21846) >> 22, col = o - row * 192; woff[p] = row * ldw2 + col; }
        } else woff[0] = (lane >> 4) * ldw2 + ((((lane & 15) ^ ((lane >> 4) << 2))) << 4);
    }
    DI void xp(int kc, int st, int j, int par) const { glds16(xbase + (size_t)j * 8 * 1280 + kc * 128, par ? xoff1 : xoff0, lds0 + sbase(st) + j * 1024); }
    DI void wp(int kc, int st, int lw, int jj) const {
        if (N96) glds16(wbase + (size_t)(kc * 64 + 16 * lw) * ldw2, woff[jj], lds0 + sbase(st) + 16384 + (3 * lw + jj) * 1024);
        else glds16(wbase + (size_t)(kc * 64 + 16 * lw + 4 * jj) * ldw2, woff[0], lds0 + sbase(st) + 16384 + (4 * lw + jj) * 1024);
    }
    DI void issue(int kc_, int st, int lw) const {
        const int kc = kact(kc_);
#pragma unroll
        for (int jj = 0; jj < NW; ++jj) wp(kc, st, lw, jj);
#pragma unroll
        for (int jj = 0; jj < 4; ++jj) xp(kc, st, 4 * lw + jj, jj & 1);
    }
};

DI void rt_load(const unsigned short* w2, const unsigned short* wo, int u, unsigned ldsb, int piece, int lane) {
    const int sig = u >> 2, s = (u >> 1) & 1, cb = u & 1;
    const char* src = (sig < 48 ? (const char*)w2 + (size_t)sig * 32 * 1280 : (const char*)wo + (size_t)(sig - 48) * 32 * 1280) + s * 16 * 1280 + cb * 640;
    const int o = piece * 1024 + lane * 16, row = (o * 6554) >> 22;
    glds16(src, row * 1280 + (o - row * 640), ldsb + piece * 1024);
}
DI void rt_store(rsrc_t rs, int u, const char* ldsb, int i, int lane) {
    const int sig = u >> 2, s = (u >> 1) & 1, cb = u & 1;
    const int nl = 2 * i + (lane >> 5), hh = (lane >> 4) & 1, q = (lane & 15) >> 2, p = lane & 3;
    lds_cptr a = (lds_cptr)ldsb + (4 * hh + q) * 640 + (16 * nl + 4 * p) * 2;
    const s16x4 lo = ldstr(a), hi = ldstr(a + 8 * 640);
    const bf16x8 f = __builtin_shufflevector(lo, hi, 0, 1, 2, 3, 4, 5, 6, 7);
    const int nu = cb * 20 + nl, ng = (nu * 205) >> 10;
    st16(rs, (unsigned)WS_BF + (unsigned)((ng * NSIG + sig) * 5 + (nu - 5 * ng)) * 1024 + (s * 32 + (lane & 31)) * 16, __builtin_bit_cast(u32x4, f));
}

template <bool N96, bool VT, int MT, int NTL, class HOOK>
DI void gemm_loop(f32x16 (&acc)[MT][NTL], char* lds, int wid, int lane, HOOK hook) {
    const int h = lane >> 5, r = lane & 31;
    constexpr int NST = N96 ? 3 : 4;
    unsigned xa[4];
    { const int row = (N96 ? wid * 32 : (wid >> 1) * 64) + r; const int sw = (row >> 1) & 7;
#pragma unroll
      for (int ks = 0; ks < 4; ++ks) xa[ks] = row * 128 + (((2 * ks + h) ^ sw) << 4); }
    unsigned wa[2];
    { const int blk = (lane >> 4) & 1, q = (lane & 15) >> 2, p = lane & 3;
      if (N96) wa[0] = wa[1] = 16384 + (8 * h + q) * 192 + blk * 32 + p * 8;
      else {
#pragma unroll
          for (int nt = 0; nt < 2; ++nt) { const int ch = (wid & 1) * 8 + nt * 4 + 2 * blk + (p >> 1); wa[nt] = 16384 + (8 * h + q) * 256 + ((ch ^ (q << 2)) << 4) + 8 * (p & 1); } } }
    bf16x8 xf[2][MT], wf[2][NTL];
    auto rdw = [&](int buf, int nt, lds_cptr sb, int ks) {
        lds_cptr a = N96 ? sb + wa[0] + ks * 16 * 192 + nt * 64 : sb + wa[nt & 1] + ks * 4096;
        const s16x4 lo = ldstr(a), hi = ldstr(a + (N96 ? 4 * 192 : 1024));
        wf[buf][nt] = __builtin_shufflevector(lo, hi, 0, 1, 2, 3, 4, 5, 6, 7);
    };
    auto rdx = [&](int buf, int mt, lds_cptr sb, int ks) { xf[buf][mt] = lds128(sb + xa[ks] + mt * 4096); };
    auto mm = [&](int buf, int mt, int nt) { acc[mt][nt] = VT ? MFMA32(xf[buf][mt], wf[buf][nt], acc[mt][nt]) : MFMA32(wf[buf][nt], xf[buf][mt], acc[mt][nt]); };
    { lds_cptr sb = (lds_cptr)lds + GDma<N96>::sbase(0); rdw(0, 0, sb, 0); rdx(0, 0, sb, 0); rdw(0, 1, sb, 0); if (N96) rdw(0, 2 % NTL, sb, 0); else rdx(0, 1 % MT, sb, 0); }
    int st = 0;
#define G_KSTEP(ks, NEXT) do { const int b = (ks) & 1, n = b ^ 1; lds_cptr np = (ks) < 3 ? sb : sn; const int nk = ((ks) + 1) & 3; \
        SBAR(); \
        if constexpr (N96) { \
            mm(b, 0, 0); SBAR(); if (NEXT) { rdw(n, 0, np, nk); rdx(n, 0, np, nk); } SBAR(); \
            mm(b, 0, 1); SBAR(); if (NEXT) { rdw(n, 1, np, nk); } SBAR(); \
            mm(b, 0, 2 % NTL); SBAR(); if (NEXT) { rdw(n, 2 % NTL, np, nk); } SBAR(); \
        } else { \
            mm(b, 0, 0); SBAR(); if (NEXT) { rdw(n, 0, np, nk); } SBAR(); \
            mm(b, 0, 1); SBAR(); if (NEXT) { rdx(n, 0, np, nk); } SBAR(); \
            mm(b, 1 % MT, 0); SBAR(); if (NEXT) { rdw(n, 1, np, nk); } SBAR(); \
            mm(b, 1 % MT, 1); SBAR(); if (NEXT) { rdx(n, 1 % MT, np, nk); } SBAR(); \
        } } while (0)
#pragma unroll 1
    for (int kc = 0; kc < 9; ++kc) {
        lds_cptr sb = (lds_cptr)lds + GDma<N96>::sbase(st);
        st = st == NST - 1 ? 0 : st + 1;
        lds_cptr sn = (lds_cptr)lds + GDma<N96>::sbase(st);
        G_KSTEP(0, true); G_KSTEP(1, true); G_KSTEP(2, true);
        hook(kc, true);
        BARL();
        hook(kc, false);
        G_KSTEP(3, true);
    }
    { lds_cptr sb = (lds_cptr)lds + GDma<N96>::sbase(st); lds_cptr sn = sb;
      G_KSTEP(0, true); G_KSTEP(1, true); G_KSTEP(2, true); G_KSTEP(3, false); }
}

DI float swap32_add(float x) { auto rr = __builtin_amdgcn_permlane32_swap(__float_as_uint(x), __float_as_uint(x), false, false); return __uint_as_float(rr[0]) + __uint_as_float(rr[1]); }
DI float swap32_max(float x) { auto rr = __builtin_amdgcn_permlane32_swap(__float_as_uint(x), __float_as_uint(x), false, false); return fmaxf(__uint_as_float(rr[0]), __uint_as_float(rr[1])); }
DI s16x4 make_qb(unsigned q0, unsigned q1, float M, int h) {
    const float mh = bf_hi(__float_as_uint(M)), r1 = M - mh, mm = bf_hi(__float_as_uint(r1)), r2 = r1 - mm, ml = bf_hi(__float_as_uint(r2));
    u32x2 w;
    w[0] = h ? ((__float_as_uint(mh) >> 16) | (__float_as_uint(mm) & 0xffff0000u)) ^ 0x80008000u : q0;
    w[1] = h ? (__float_as_uint(ml) >> 16) ^ 0x8000u : q1;
    return __builtin_bit_cast(s16x4, w);
}

DI void attn_fallback(f32x16& o, const char* kv, bf16x8 qa, unsigned q0, unsigned q1, int lane, int kboff, int voff) {
    const int h = lane >> 5;
    const f32x16 zero = {};
    float m = -INFINITY;
    const s16x4 qb0 = make_qb(q0, q1, 0.f, h);
    for (int t = 0; t < NT; ++t) {
        const char* tb = kv + t * KVTILE_B;
        const bf16x8 ka = *(const bf16x8*)(tb + lane * 16); const s16x4 kb = *(const s16x4*)(tb + kboff);
        f32x16 s = MFMA32(ka, qa, zero); s = MFMA8(kb, qb0, s);
#pragma unroll
        for (int i = 0; i < 16; ++i) m = fmaxf(m, s[i]);
    }
    m = swap32_max(m);
    const s16x4 qb = make_qb(q0, q1, m, h);
    o = zero;
    for (int t = 0; t < NT; ++t) {
        const char* tb = kv + t * KVTILE_B;
        const bf16x8 ka = *(const bf16x8*)(tb + lane * 16); const s16x4 kb = *(const s16x4*)(tb + kboff);
        const bf16x8 va = *(const bf16x8*)(tb + voff), vb = *(const bf16x8*)(tb + voff + V_STEP);
        f32x16 s = MFMA32(ka, qa, zero); s = MFMA8(kb, qb, s);
        u32x4 p0, p1;
#pragma unroll
        for (int i = 0; i < 4; ++i) { p0[i] = cvtpk(__builtin_amdgcn_exp2f(s[2 * i]), __builtin_amdgcn_exp2f(s[2 * i + 1])); p1[i] = cvtpk(__builtin_amdgcn_exp2f(s[8 + 2 * i]), __builtin_amdgcn_exp2f(s[9 + 2 * i])); }
        o = MFMA32(va, __builtin_bit_cast(bf16x8, p0), o);
        o = MFMA32(vb, __builtin_bit_cast(bf16x8, p1), o);
    }
}

constexpr int SPIN_MAX = 1 << 20;
__global__ __launch_bounds__(512, 2) void gated_mix_mega(const unsigned short* __restrict__ x, const unsigned short* __restrict__ wqkv, const unsigned short* __restrict__ wo, const unsigned short* __restrict__ w1,
                                                         const unsigned short* __restrict__ w2, const unsigned short* __restrict__ gw, char* __restrict__ ws, unsigned short* __restrict__ out) {
    extern __shared__ __attribute__((aligned(16))) char lds[];
    const int tid = threadIdx.x, lane = tid & 63, wid = rfl(tid >> 6);
    const int bid = blockIdx.x, xcd = bid & 7, slot = bid >> 3;
    const int h = lane >> 5, r = lane & 31;
    const rsrc_t rs = ws_rsrc(ws);
    const unsigned lds0 = (unsigned)(uintptr_t)lds, lane16 = lane * 16;
    volatile LAS unsigned* ctl = (volatile LAS unsigned*)(lds + CTL_OFF);
    const int rt = 4 * (xcd >> 1) + (slot & 3), ci = slot >> 2, ct = 8 * (xcd & 1) + ci;
    const bool tile1 = ct < 15;
    const char* xbase = (const char*)x + (size_t)rt * 128 * 1280;
#define K0EXPR 0
    const int k0 = K0EXPR;
    const int head = xcd * 4 + (slot & 3), qblk = slot >> 2;
    const char* kv = ws + WS_KV + (size_t)head * NT * KVTILE_B;
    const int Tq = qblk * 8 + wid;
    const char* qt = ws + WS_Q + ((size_t)head * NT + Tq) * QTILE_B;
    bf16x8 qa; u32x2 qbr; f32x4 km4; float gate;
    auto q_loads = [&]() {
        asm volatile("s_nop 4\n\tglobal_load_dwordx4 %0, %1, %2" : "=v"(qa) : "v"(lane16), "s"(qt) : "memory");
        asm volatile("s_nop 4\n\tglobal_load_dwordx2 %0, %1, %2" : "=v"(qbr) : "v"((unsigned)(1024 + r * 8)), "s"(qt) : "memory");
        asm volatile("s_nop 4\n\tglobal_load_dwordx4 %0, %1, %2" : "=v"(km4) : "v"((unsigned)((lane < 40 ? lane : 0) * 16)), "s"(ws + WS_KMAX + (size_t)head * 640) : "memory");
        asm volatile("s_nop 4\n\tglobal_load_dword %0, %1, %2" : "=v"(gate) : "v"((unsigned)((Tq * 32 + r) * 4)), "s"(ws + WS_GATE) : "memory");
    };
    auto wait_seen = [&]() { for (int it = 0; it < SPIN_MAX; ++it) { if (ctl[1] != 0u) break; __builtin_amdgcn_s_sleep(1); } };
    auto gates = [&]() {
        const int aidx = (xcd >> 1) * 4 + (slot & 3);
        const u32x4* gr = (const u32x4*)gw + (lane & 7);
#pragma unroll 1
        for (int ps = 0; ps < 2; ++ps) {
            const int tok = aidx * 128 + wid * 16 + ps * 8 + (lane >> 3);
            const u32x4* xr = (const u32x4*)(x + (size_t)tok * DM) + (lane & 7);
            float sm = 0.f;
#pragma unroll
            for (int i = 0; i < 10; ++i) { const u32x4 xv = xr[i * 8], gv = gr[i * 8];
#pragma unroll
                for (int k = 0; k < 4; ++k) { const unsigned a_ = xv[k], b_ = gv[k]; sm = __builtin_amdgcn_fdot2_f32_bf16(__builtin_bit_cast(bf16x2_t, a_), __builtin_bit_cast(bf16x2_t, b_), sm, false); } }
            sm = dpp_add<0xB1>(sm); sm = dpp_add<0x4E>(sm); sm = dpp_add<0x141>(sm);
            const float lg = bf_round(sm);
            const float g = bf_round(__builtin_amdgcn_rcpf(1.f + __builtin_amdgcn_exp2f(lg * -1.4426950408889634f)));
            if ((lane & 7) == 0) st4f(ws + WS_GATE + tok * 4, g);
        }
    };
    GDma<true> d2; d2.init(xbase, (const char*)w1 + ct * 192, FF * 2, lds, lane, k0);
    u64 pc0; asm volatile("s_getpc_b64 %0" : "=s"(pc0));
    STAMP(0, 0);
    if (wid < 4) {
        GDma<false> d1; d1.init(xbase, (const char*)wqkv + ct * 256, 3 * DM * 2, lds, lane, k0);
        if (tile1) {
#pragma unroll
            for (int jj = 0; jj < 4; ++jj) d1.xp(d1.kact(0), 0, 4 * wid + jj, jj & 1);
        }
        if (tid == 0) { ctl[0] = 0u; ctl[1] = 0u; }
        {
#pragma unroll
            for (int i = 0; i < 7; ++i) {
                int u = (bid * 4 + wid) * 7 + i; if (u > 6879) u = 6879;
                const char* src = u < 2560 ? (const char*)x + (size_t)u * 1024 : u < 4960 ? (const char*)wqkv + (size_t)(u - 2560) * 1024 : (const char*)w1 + (size_t)(u - 4960) * 1024;
                glds16(src, lane16, lds0 + 152 * 1024);
            }
        }
        if (tile1) asm volatile("s_waitcnt vmcnt(7) lgkmcnt(0)\n\ts_barrier" ::: "memory");
        if (slot * 4 + wid < 42) glds16((const char*)((pc0 + 15ull) & ~15ull) + (slot * 4 + wid) * 1024, lane16, lds0 + 152 * 1024);
        for (int pc = wid; pc < 10; pc += 4) rt_load(w2, wo, bid, lds0 + RT_OFF, pc, lane);
        if (!tile1) for (int pc = wid; pc < 10; pc += 4) rt_load(w2, wo, 256 + (xcd >> 1) * 4 + (slot & 3), lds0 + RT_OFF + 10240, pc, lane);
        if (wid == 0) glds16_sc1(ws + WS_EPOCH, lane < 1 ? 0u : 0u, lds0 + EP_OFF);
        auto tag_ready = [&]() {
            if (tid == 0) { const u64 tg = *(const volatile LAS u64*)(lds + EP_OFF) + 1ull; ctl[2] = (unsigned)tg; ctl[3] = (unsigned)(tg >> 32); }
        };
        if (!tile1) { asm volatile("s_waitcnt vmcnt(0)" ::: "memory"); tag_ready(); asm volatile("s_waitcnt lgkmcnt(0)\n\ts_barrier" ::: "memory"); }
        if (tile1) {
            const int wm = wid >> 1, wn = wid & 1;
            f32x16 acc[2][2] = {};
            const char* w1w = (const char*)w1 + (size_t)(slot * 20) * 3072 + (xcd & 1) * 1536;
            auto nohook = [&](int kc, bool before) {
                if (kc == 1 && before) { asm volatile("s_waitcnt vmcnt(0)" ::: "memory"); tag_ready(); }
                if (!before && kc < 4) {
#pragma unroll
                    for (int e = 0; e < 2; ++e) { const int u = kc * 8 + wid * 2 + e;
                        if (u < 30) { const int o = u * 1024 + (int)lane16, row = o / 1536; glds16(w1w, (unsigned)(row * 3072 + (o - row * 1536)), lds0 + 152 * 1024); } } }
                if (kc == 5 && before) asm volatile("s_waitcnt vmcnt(0)" ::: "memory");
            };
            if (ct >= 10) {
                gemm_loop<false, true, 2, 2>(acc, lds, wid, lane, nohook);
                STAMP(0, 1);
#pragma unroll
                for (int nt = 0; nt < 2; ++nt) {
                    const int phi = (ct - 10) * 128 + wn * 64 + nt * 32 + r;
                    const int hd = (phi * 3277) >> 16, d = phi - hd * 20;
                    const unsigned dst = (unsigned)WS_KV + (unsigned)(hd * NT + rt * 4 + wm * 2) * KVTILE_B + V_OFF + (h * V_ROWS + d) * 16;
#pragma unroll
                    for (int mt = 0; mt < 2; ++mt)
#pragma unroll
                        for (int s = 0; s < 2; ++s) {
                            const f32x16& a = acc[mt][nt];
                            const u32x4 w = {cvtpk(a[8 * s], a[8 * s + 1]), cvtpk(a[8 * s + 2], a[8 * s + 3]), cvtpk(a[8 * s + 4], a[8 * s + 5]), cvtpk(a[8 * s + 6], a[8 * s + 7])};
                            st16(rs, dst + mt * KVTILE_B + s * V_STEP, w);
                        }
                }
                BARL();
            } else {
                gemm_loop<false, false, 2, 2>(acc, lds, wid, lane, nohook);
                STAMP(0, 1);
                const bool isQ = ct < 5;
                const float sc = isQ ? QSCALE : 1.f;
                const unsigned img = (unsigned)(isQ ? WS_Q : WS_KV), tileb = isQ ? QTILE_B : KVTILE_B;
                const int G = (isQ ? ct : ct - 5) * 32 + wn * 16 + h;
                u32x2 P[2][8]; float nrm[8];
#pragma unroll
                for (int p = 0; p < 8; ++p) {
                    const int nt = p >> 2, g = p & 3; float m = 0.f;
#pragma unroll
                    for (int mt = 0; mt < 2; ++mt) {
                        const f32x16& a = acc[mt][nt];
                        P[mt][p] = (u32x2){cvtpk(a[4 * g] * sc, a[4 * g + 1] * sc), cvtpk(a[4 * g + 2] * sc, a[4 * g + 3] * sc)};
                        m = fmaxf(m, __builtin_fmaf(a[4 * g], a[4 * g], a[4 * g + 1] * a[4 * g + 1]) + __builtin_fmaf(a[4 * g + 2], a[4 * g + 2], a[4 * g + 3] * a[4 * g + 3]));
                    }
                    nrm[p] = m;
                }
#pragma unroll
                for (int p = 0; p < 8; ++p) {
                    const int gam = G + 2 * p, hd = (gam * 205) >> 10, dg = gam - 5 * hd;
                    const unsigned base = img + (unsigned)(hd * NT + rt * 4 + wm * 2) * tileb;
#pragma unroll
                    for (int mt = 0; mt < 2; ++mt) {
                        const unsigned bm = base + mt * tileb;
                        if (dg < 2) {
                            if (p < 7) st16(rs, bm + dg * 512 + r * 16, (u32x4){P[mt][p][0], P[mt][p][1], P[mt][(p + 1) & 7][0], P[mt][(p + 1) & 7][1]});
                            else st8(rs, bm + dg * 512 + r * 16, P[mt][p]);
                        } else if (dg == 4) st8(rs, bm + 1024 + r * 8, P[mt][p]);
                        else if (p == 0) st8(rs, bm + (dg - 2) * 512 + r * 16 + 8, P[mt][p]);
                    }
                }
                if (!isQ) {
#pragma unroll
                    for (int i = 0; i < 8; ++i) { float m = nrm[i]; m = dpp_max<0xB1>(m); m = dpp_max<0x4E>(m); m = dpp_max<0x141>(m); m = dpp_max<0x140>(m);
                        auto rr = __builtin_amdgcn_permlane16_swap(__float_as_uint(m), __float_as_uint(m), false, false); nrm[i] = fmaxf(__uint_as_float(rr[0]), __uint_as_float(rr[1])); }
                    float mine = nrm[0];
#pragma unroll
                    for (int i = 1; i < 8; ++i) mine = r == i ? nrm[i] : mine;
                    if (r < 8) st4f(ws + WS_KMAX + (size_t)((G + 2 * r) * 32 + rt * 2 + wm) * 4, mine);
                }
                BARL();
            }
        } else { gates(); BARL(); }
        STAMP(0, 2);
        BARL();
        f32x16 acc2[1][3] = {};
        auto hook2 = [&](int kc, bool before) {
            if (kc == 2) { if (before) asm volatile("s_waitcnt vmcnt(0)" ::: "memory");
                           else if (tid == 0) flag_set(ws + WS_F1 + bid * 8, ((u64)ctl[3] << 32) | ctl[2]); }
        };
        gemm_loop<true, false, 1, 3>(acc2, lds, wid, lane, hook2);
        STAMP(0, 3);
        wait_seen();
        q_loads();
        {   const int tok = rt * 128 + wid * 32 + r, rho = tok >> 4;
#pragma unroll
            for (int nt = 0; nt < 3; ++nt) {
                const unsigned dst = (unsigned)WS_AF + (unsigned)(rho * NSIG + ct * 3 + nt) * 1024 + (h * 16 + (r & 15)) * 16;
                float sv[16];
#pragma unroll
                for (int i = 0; i < 16; ++i) { const float v = acc2[0][nt][i]; sv[i] = v * __builtin_amdgcn_rcpf(1.f + __builtin_amdgcn_exp2f(v * -1.4426950408889634f)); }
#pragma unroll
                for (int s = 0; s < 2; ++s) {
                    const u32x4 w = {cvtpk(sv[8 * s], sv[8 * s + 1]), cvtpk(sv[8 * s + 2], sv[8 * s + 3]), cvtpk(sv[8 * s + 4], sv[8 * s + 5]), cvtpk(sv[8 * s + 6], sv[8 * s + 7])};
                    st16(rs, dst + s * 512, w);
                }
            }
        }
        asm volatile("s_waitcnt vmcnt(6)" : "+v"(qa), "+v"(qbr), "+v"(km4), "+v"(gate) :: "memory");
    } else {
        const int lw = wid - 4;
        auto side_stores = [&]() {
            for (int i = lw; i < 10; i += 4) rt_store(rs, bid, lds + RT_OFF, i, lane);
            if (!tile1) for (int i = lw; i < 10; i += 4) rt_store(rs, 256 + (xcd >> 1) * 4 + (slot & 3), lds + RT_OFF + 10240, i, lane);
            if (lw == 0) { const int i = bid * 64 + lane;
                const unsigned f = (i & 1) ? 0u : ONES2; st16(rs, (unsigned)WS_KV + (unsigned)(i >> 3) * KVTILE_B + V_OFF + ((i >> 2) & 1) * V_STEP + (((i >> 1) & 1) * V_ROWS + 20 + (i & 1)) * 16, (u32x4){f, f, f, f}); }
        };
        if (tile1) {
            GDma<false> d1; d1.init(xbase, (const char*)wqkv + ct * 256, 3 * DM * 2, lds, lane, k0);
#pragma unroll
            for (int jj = 0; jj < 4; ++jj) d1.wp(d1.kact(0), 0, lw, jj);
            d1.issue(1, 1, lw);
            WAITB(8);
            d1.issue(2, 2, lw); d1.issue(3, 3, lw);
            for (int kc = 0; kc < 9; ++kc) {
                if (kc < 7) WAITB(16); else if (kc == 7) WAITB(15); else WAITB(14);
                if (kc + 4 < 10) d1.issue(kc + 4, kc & 3, lw);
                if (kc == 4) side_stores();
                if (kc >= 6) d2.issue(kc - 6, kc - 6, lw);
            }
            asm volatile("s_barrier" ::: "memory");
        } else {
            asm volatile("s_barrier" ::: "memory");
            side_stores(); gates();
            BARL();
            d2.issue(0, 0, lw); d2.issue(1, 1, lw); d2.issue(2, 2, lw);
        }
        WAITB(14);
        { int st = 0;
          for (int kc = 0; kc < 9; ++kc) {
              if (kc < 8) WAITB(7); else WAITB(0);
              if (kc + 3 < 10) d2.issue(kc + 3, st, lw);
              st = st == 2 ? 0 : st + 1;
          } }
        if (lw == 0) {
            const u64 tag = ((u64)ctl[3] << 32) | ctl[2];
            for (int it = 0; it < SPIN_MAX; ++it) {
                bool ok = true;
#pragma unroll
                for (int i = 0; i < 4; ++i) ok &= flag_get(ws + WS_F1 + (lane + 64 * i) * 8) == tag;
                if (__all(ok)) break;
                __builtin_amdgcn_s_sleep(2);
            }
            __builtin_amdgcn_fence(__ATOMIC_ACQUIRE, "agent");
            asm volatile("s_waitcnt vmcnt(0)" ::: "memory");
            if (lane == 0) { ctl[1] = 1u; if (bid == 0) flag_set(ws + WS_EPOCH, tag); }
        } else wait_seen();
        q_loads();
        for (int j = 0; j < 21; ++j) glds16(kv + (size_t)(lw + 4 * j) * 1024, lane16, lds0 + (lw + 4 * j) * 1024);
        asm volatile("s_waitcnt vmcnt(21)" : "+v"(qa), "+v"(qbr), "+v"(km4), "+v"(gate) :: "memory");
    }
    STAMP(1, 0);
    float km = fmaxf(fmaxf(km4[0], km4[1]), fmaxf(km4[2], km4[3]));
    km = dpp_max<0xB1>(km); km = dpp_max<0x4E>(km); km = dpp_max<0x141>(km);
    km = __builtin_amdgcn_sqrtf(km);
    float M;
    { const float c0 = rdl(km, 0), c1 = rdl(km, 8), c2 = rdl(km, 16), c3 = rdl(km, 24), c4 = rdl(km, 32);
      const u32x4 qw = __builtin_bit_cast(u32x4, qa);
      float n0, n1, n2;
      { const float a = bf_lo(qw[0]), b = bf_hi(qw[0]), c = bf_lo(qw[1]), d = bf_hi(qw[1]); n0 = (a * a + b * b) + (c * c + d * d); }
      { const float a = bf_lo(qw[2]), b = bf_hi(qw[2]), c = bf_lo(qw[3]), d = bf_hi(qw[3]); n1 = (a * a + b * b) + (c * c + d * d); }
      { const unsigned q0 = qbr[0], q1 = qbr[1]; const float a = bf_lo(q0), b = bf_hi(q0), c = bf_lo(q1), d = bf_hi(q1); n2 = (a * a + b * b) + (c * c + d * d); }
      float part = __builtin_amdgcn_sqrtf(n0) * (h ? c1 : c0) + __builtin_amdgcn_sqrtf(n1) * (h ? c3 : c2);
      if (!h) part += __builtin_amdgcn_sqrtf(n2) * c4;
      M = swap32_add(part) * 1.01f; }
    const s16x4 qb = make_qb(qbr[0], qbr[1], M, h);
    const int vrow = r <= 20 ? r : (r < 28 ? 21 : r - 12);
    const int voff = V_OFF + (h * V_ROWS + vrow) * 16, kboff = h ? V_OFF + 20 * 16 : 1024 + r * 8;
    lds_cptr kaC = (lds_cptr)lds + lane * 16, kbC = (lds_cptr)lds + kboff, vC = (lds_cptr)lds + voff;
    lds_cptr kaN = kaC + K2_BIG, kbN = kbC + K2_BIG, vN = vC + K2_BIG;
    const f32x16 zero = {};
    f32x16 o = {}, sA, sB;
    bf16x8 ka, va, vb; s16x4 kb;
    u32x4 plo, phi;
    if (wid >= 4) asm volatile("s_waitcnt vmcnt(15)" ::: "memory");
    asm volatile("s_barrier" ::: "memory");
    STAMP(1, 1);
    ka = lds128(kaC); kb = lds64(kbC);
    sA = MFMA32(ka, qa, zero); sA = MFMA8(kb, qb, sA);
    ka = lds128(kaC + KVTILE_B); kb = lds64(kbC + KVTILE_B);
    va = lds128(vC);
    vb = (bf16x8){}; phi = (u32x4){};
    f32x4 e3 = {};
    const int ng = (xcd & 1) * 4 + (slot & 3), bt = (xcd >> 1) * 8 + (slot >> 2);
    const char* ap = ws + WS_AF + (size_t)bt * 4 * NSIG * 1024;
    const char* bp = ws + WS_BF + (size_t)ng * NSIG * 5 * 1024;
#define ROTEXPR (((slot >> 2) + 3 * (slot & 3)) % 6)
    const int rot = ROTEXPR;
    auto hs = [&](int i) { int st = i + rot; if (st >= 6) st -= 6; return wid * 6 + st; };
    bf16x8 a[3][4], b[3][5];
    auto ldA1 = [&](int sg, int mt, bf16x8& d) { gld128_sc1(d, ap + ((size_t)mt * NSIG + sg) * 1024, lane16); };
    auto ldB1 = [&](int sg, int nt, bf16x8& d) { gld128(d, bp + sg * 5120 + nt * 1024, lane16); };
    const u64 tag = ((u64)ctl[3] << 32) | ctl[2];
#define EXP4(X, i) do { X[i] = __builtin_amdgcn_exp2f(X[i]); X[i + 1] = __builtin_amdgcn_exp2f(X[i + 1]); X[i + 2] = __builtin_amdgcn_exp2f(X[i + 2]); X[i + 3] = __builtin_amdgcn_exp2f(X[i + 3]); } while (0)
#define KADDR(A, i) (((i) < 16) ? (A##C + (i) * KVTILE_B) : (A##N + ((i) - 16) * KVTILE_B))
#define STEP(C, N, i, X0, X1) do { \
        bf16x8 ka2, va2, vb2; s16x4 kb2; \
        N = MFMA32(ka, qa, zero); SBAR(); \
        ka2 = lds128(KADDR(ka, (i) + 2)); \
        phi[2] = cvtpk(e3[0], e3[1]); phi[3] = cvtpk(e3[2], e3[3]); PIN(phi); SBAR(); EXP4(C, 0); PIN(C); SBAR(); \
        o = MFMA32(vb, __builtin_bit_cast(bf16x8, phi), o); SBAR(); \
        X0; \
        vb2 = lds128(vC + (i) * KVTILE_B + V_STEP); \
        plo[0] = cvtpk(C[0], C[1]); plo[1] = cvtpk(C[2], C[3]); PIN(plo); SBAR(); EXP4(C, 4); PIN(C); SBAR(); \
        N = MFMA8(kb, qb, N); SBAR(); \
        kb2 = lds64(KADDR(kb, (i) + 2)); \
        plo[2] = cvtpk(C[4], C[5]); plo[3] = cvtpk(C[6], C[7]); PIN(plo); SBAR(); EXP4(C, 8); PIN(C); SBAR(); \
        o = MFMA32(va, __builtin_bit_cast(bf16x8, plo), o); SBAR(); \
        X1; \
        va2 = lds128(KADDR(v, (i) + 1)); \
        phi[0] = cvtpk(C[8], C[9]); phi[1] = cvtpk(C[10], C[11]); PIN(phi); SBAR(); \
        e3[0] = __builtin_amdgcn_exp2f(C[12]); e3[1] = __builtin_amdgcn_exp2f(C[13]); e3[2] = __builtin_amdgcn_exp2f(C[14]); e3[3] = __builtin_amdgcn_exp2f(C[15]); PIN(e3); SBAR(); \
        ka = ka2; kb = kb2; va = va2; vb = vb2; \
    } while (0)
#define PIECE(j) do { if (8 * (j) + 7 < 42 || 8 * (j) + wid < 42) glds16(src + (j) * 8192, lane16, dst + (j) * 8192); } while (0)
    const char* src = kv + (84 + wid) * 1024; unsigned dst = lds0 + wid * 1024 + 2 * K2_BIG;
#pragma unroll 1
    for (int c = 0; c < 3; ++c) {
        STEP(sA, sB, 0, , ); STEP(sB, sA, 1, , ); STEP(sA, sB, 2, , ); STEP(sB, sA, 3, , ); STEP(sA, sB, 4, , );
        if (c == 0) { if (wid >= 4) asm volatile("s_waitcnt vmcnt(10)" ::: "memory"); asm volatile("s_barrier" ::: "memory"); }
        STEP(sB, sA, 5, , ); STEP(sA, sB, 6, , ); STEP(sB, sA, 7, , ); STEP(sA, sB, 8, , ); STEP(sB, sA, 9, , ); STEP(sA, sB, 10, , ); STEP(sB, sA, 11, , ); STEP(sA, sB, 12, , );
        if (c == 2 && wid == 0) {
            for (int it = 0; it < SPIN_MAX; ++it) {
                if (__all(flag_get(ws + WS_F2 + ((bt >> 1) * 16 + (lane & 15)) * 8) == tag)) break;
                __builtin_amdgcn_s_sleep(2);
            }
        }
        WAITB(0);
        if (c == 0 && tid == 0) flag_set(ws + WS_F2 + (rt * 16 + ct) * 8, tag);
        if (c < 2) { STEP(sB, sA, 13, PIECE(0), PIECE(1)); STEP(sA, sB, 14, PIECE(2), PIECE(3)); STEP(sB, sA, 15, PIECE(4), PIECE(5)); }
        else { STEP(sB, sA, 13, , ); STEP(sA, sB, 14, , ); STEP(sB, sA, 15, , ); }
        const int d = (c == 1) ? -2 * K2_BIG : K2_BIG;
        kaC = kaN; kbC = kbN; vC = vN; kaN += d; kbN += d; vN += d;
        src += 42 * 1024; dst -= 2 * K2_BIG;
    }
#define PA(s, m) ldA1(hs(s), m, a[s][m])
#define PB(s, n) ldB1(hs(s), n, b[s][n])
    STEP(sA, sB, 0, PA(0, 0), PA(0, 1)); STEP(sB, sA, 1, PA(0, 2), PA(0, 3)); STEP(sA, sB, 2, PB(0, 0), PB(0, 1)); STEP(sB, sA, 3, PB(0, 2), PB(0, 3));
    STEP(sA, sB, 4, PB(0, 4), PA(1, 0)); STEP(sB, sA, 5, PA(1, 1), PA(1, 2)); STEP(sA, sB, 6, PA(1, 3), PB(1, 0)); STEP(sB, sA, 7, PB(1, 1), PB(1, 2));
    STEP(sA, sB, 8, PB(1, 3), PB(1, 4)); STEP(sB, sA, 9, PA(2, 0), PA(2, 1)); STEP(sA, sB, 10, PA(2, 2), PA(2, 3)); STEP(sB, sA, 11, PB(2, 0), PB(2, 1));
    STEP(sA, sB, 12, PB(2, 2), PB(2, 3)); STEP(sB, sA, 13, PB(2, 4), ); STEP(sA, sB, 14, , ); STEP(sB, sA, 15, , );
    phi[2] = cvtpk(e3[0], e3[1]); phi[3] = cvtpk(e3[2], e3[3]);
    o = MFMA32(vb, __builtin_bit_cast(bf16x8, phi), o);
    STAMP(1, 2);
    float l; { auto rr = __builtin_amdgcn_permlane32_swap(__float_as_uint(o[8]), __float_as_uint(o[8]), false, false); l = __uint_as_float(rr[1]); }
    if (__builtin_expect(__any(!(l >= 8.6736174e-19f && l <= 1.0e30f)), 0)) {
        asm volatile("s_waitcnt vmcnt(0)" ::: "memory");
        attn_fallback(o, kv, qa, qbr[0], qbr[1], lane, kboff, voff);
        auto rr = __builtin_amdgcn_permlane32_swap(__float_as_uint(o[8]), __float_as_uint(o[8]), false, false); l = __uint_as_float(rr[1]);
    }
    const float f = gate * __builtin_amdgcn_rcpf(l);
    const int tok = Tq * 32 + r;
    char* ab = ws + WS_AF + (size_t)(tok >> 4) * NSIG * 1024 + (tok & 15) * 16;
#pragma unroll
    for (int g = 0; g < 3; ++g) {
        const int gam = head * 5 + 2 * g + h;
        const int sig = 48 + (gam >> 3), c = ((gam >> 2) & 1) * 2 + (gam & 1), half = (gam >> 1) & 1;
        const u32x2 w = {cvtpk(o[4 * g] * f, o[4 * g + 1] * f), cvtpk(o[4 * g + 2] * f, o[4 * g + 3] * f)};
        if (g < 2 || h == 0) __hip_atomic_store((u64*)(ab + sig * 1024 + c * 256 + half * 8), ((u64)w[1] << 32) | w[0], __ATOMIC_RELAXED, __HIP_MEMORY_SCOPE_AGENT);
    }
    STAMP(1, 3);
    {
        f32x4 acc[4][5] = {};
        auto loadA = [&](int sg, bf16x8 (&da)[4]) {
#pragma unroll
            for (int mt = 0; mt < 4; ++mt) gld128_sc1(da[mt], ap + ((size_t)mt * NSIG + sg) * 1024, lane16);
        };
        auto loadB = [&](int sg, bf16x8 (&db)[5]) {
#pragma unroll
            for (int nt = 0; nt < 5; ++nt) gld128(db[nt], bp + sg * 5120 + nt * 1024, lane16);
        };
        auto mma = [&](bf16x8 (&da)[4], bf16x8 (&db)[5]) {
            SBAR();
#pragma unroll
            for (int nt = 0; nt < 5; ++nt)
#pragma unroll
                for (int mt = 0; mt < 4; ++mt) acc[mt][nt] = MFMA16(db[nt], da[mt], acc[mt][nt]);
            SBAR();
        };
        wait9<3>(a[0], b[0]);
        asm volatile("" : "+v"(a[1][0]), "+v"(a[1][1]), "+v"(a[1][2]), "+v"(a[1][3]), "+v"(b[1][0]), "+v"(b[1][1]), "+v"(b[1][2]), "+v"(b[1][3]), "+v"(b[1][4]));
        asm volatile("" : "+v"(a[2][0]), "+v"(a[2][1]), "+v"(a[2][2]), "+v"(a[2][3]), "+v"(b[2][0]), "+v"(b[2][1]), "+v"(b[2][2]), "+v"(b[2][3]), "+v"(b[2][4]));
        mma(a[0], b[0]); loadA(hs(3), a[0]); loadB(hs(3), b[0]);
        mma(a[1], b[1]); loadA(hs(4), a[1]); loadB(hs(4), b[1]);
        asm volatile("s_waitcnt vmcnt(18)" ::: "memory");
        if (lane == 0) {
            const unsigned old = __hip_atomic_fetch_add((unsigned*)(lds + CTL_OFF), 1u, __ATOMIC_RELAXED, __HIP_MEMORY_SCOPE_WORKGROUP);
            if (old == 7u) flag_set(ws + WS_F3 + (qblk * 32 + head) * 8, tag);
        }
        mma(a[2], b[2]); loadA(hs(5), a[2]); loadB(hs(5), b[2]);
        float og[4];
#pragma unroll
        for (int mt = 0; mt < 4; ++mt) asm volatile("s_nop 4\n\tglobal_load_dword %0, %1, %2" : "=v"(og[mt]) : "v"((unsigned)((bt * 64 + mt * 16 + (lane & 15)) * 4)), "s"(ws + WS_GATE) : "memory");
        wait9<22>(a[0], b[0]); mma(a[0], b[0]); loadB(48 + wid, b[0]);
        wait9<18>(a[1], b[1]); mma(a[1], b[1]); loadB(56 + wid, b[1]);
        wait9<14>(a[2], b[2]); mma(a[2], b[2]);
        if (wid < 4) {
#pragma unroll
            for (int nt = 0; nt < 5; ++nt) gld128_rw(b[2][nt], bp + (64 + wid) * 5120 + nt * 1024, lane16);
        }
        STAMP(2, 0);
        asm volatile("s_waitcnt vmcnt(0)" : "+v"(og[0]), "+v"(og[1]), "+v"(og[2]), "+v"(og[3]) :: "memory");
#pragma unroll
        for (int mt = 0; mt < 4; ++mt) { const float f1 = bf_round(1.f - og[mt]);
#pragma unroll
            for (int nt = 0; nt < 5; ++nt) acc[mt][nt] *= f1; }
        if (wid == 0) {
            for (int it = 0; it < SPIN_MAX; ++it) {
                if (__all(flag_get(ws + WS_F3 + ((bt >> 2) * 32 + r) * 8) == tag)) break;
                __builtin_amdgcn_s_sleep(2);
            }
        }
        asm volatile("s_barrier" ::: "memory");
        STAMP(2, 1);
        loadA(48 + wid, a[0]); loadA(56 + wid, a[1]);
        a[2][0] = (bf16x8){}; a[2][1] = (bf16x8){}; a[2][2] = (bf16x8){}; a[2][3] = (bf16x8){};
        if (wid < 4) {
#pragma unroll
            for (int mt = 0; mt < 4; ++mt) gld128_sc1_rw(a[2][mt], ap + ((size_t)mt * NSIG + 64 + wid) * 1024, lane16);
        }
        wait9<4>(a[0], b[0]); mma(a[0], b[0]);
        wait9<0>(a[1], b[1]); wait9<0>(a[2], b[2]); mma(a[1], b[1]); mma(a[2], b[2]);
        STAMP(2, 2);
#pragma unroll
        for (int mt = 0; mt < 4; ++mt)
#pragma unroll
            for (int nt = 0; nt < 5; ++nt) *(f32x4*)(lds + (wid * 20 + mt * 5 + nt) * 1024 + lane16) = acc[mt][nt];
        BARL();
#pragma unroll
        for (int i = 0; i < 3; ++i) { const int t = wid + 8 * i;
            if (t < 20) {
                f32x4 sum = *(const f32x4*)(lds + t * 1024 + lane16);
#pragma unroll
                for (int w = 1; w < 8; ++w) sum += *(const f32x4*)(lds + (w * 20 + t) * 1024 + lane16);
                const int m4 = t / 5, nt = t - m4 * 5;
                const int tok2 = bt * 64 + m4 * 16 + (lane & 15);
                *(u32x2*)(out + (size_t)tok2 * DM + ng * 80 + nt * 16 + (lane >> 4) * 4) = (u32x2){cvtpk(sum[0], sum[1]), cvtpk(sum[2], sum[3])};
            } }
        STAMP(2, 3);
    }
}

extern "C" void kernel_launch(void* const* d_in, const int* in_sizes, int n_in, void* d_out, int out_size, void* d_ws, size_t ws_size, hipStream_t stream) {
    const unsigned short* x = (const unsigned short*)d_in[0];
    const unsigned short* wqkv = (const unsigned short*)d_in[1];
    const unsigned short* wo = (const unsigned short*)d_in[2];
    const unsigned short* w1 = (const unsigned short*)d_in[4];
    const unsigned short* w2 = (const unsigned short*)d_in[5];
    const unsigned short* gw = (const unsigned short*)d_in[6];
    (void)hipFuncSetAttribute((const void*)gated_mix_mega, hipFuncAttributeMaxDynamicSharedMemorySize, LDS_ALL);
    gated_mix_mega<<<256, 512, LDS_ALL, stream>>>(x, wqkv, wo, w1, w2, gw, (char*)d_ws, (unsigned short*)d_out);
}
```

```cpp
#include <hip/hip_runtime.h>
#include <hip/hip_bf16.h>
#include <stdint.h>

typedef short bf16x8 __attribute__((ext_vector_type(8)));
typedef short s16x4 __attribute__((ext_vector_type(4)));
typedef float f32x16 __attribute__((ext_vector_type(16)));
typedef float f32x4 __attribute__((ext_vector_type(4)));
typedef float f32x2 __attribute__((ext_vector_type(2)));
typedef unsigned u32x4 __attribute__((ext_vector_type(4)));
typedef unsigned u32x2 __attribute__((ext_vector_type(2)));
typedef __bf16 bf16x2_t __attribute__((ext_vector_type(2)));
#define LAS __attribute__((address_space(3)))
typedef LAS const char* lds_cptr;
#define DI __device__ __forceinline__
#define SBAR() __builtin_amdgcn_sched_barrier(0)
#define PIN(x) asm volatile("" : "+v"(x))
#define MFMA32(a, b, c) __builtin_amdgcn_mfma_f32_32x32x16_bf16(a, b, c, 0, 0, 0)
#define MFMA16(a, b, c) __builtin_amdgcn_mfma_f32_16x16x32_bf16(a, b, c, 0, 0, 0)
#define MFMA8(a, b, c) __builtin_amdgcn_mfma_f32_32x32x8bf16_1k(a, b, c, 0, 0, 0)

constexpr int S = 2048, DM = 640, NH = 32, DH = 20, FF = 1536;
constexpr int NT = S / 32;
constexpr int QTILE_B = 1280;
constexpr int KVTILE_B = 2688;
constexpr int V_OFF = 1280, V_STEP = 704, V_ROWS = 22;
constexpr int NSIG = 68;
constexpr size_t WS_Q = 0;
constexpr size_t WS_KV = WS_Q + (size_t)NH * NT * QTILE_B;
constexpr size_t WS_AF = WS_KV + (size_t)NH * NT * KVTILE_B;
constexpr size_t WS_BF = WS_AF + (size_t)128 * NSIG * 1024;
constexpr size_t WS_KMAX = WS_BF + (size_t)8 * NSIG * 5 * 1024;
constexpr size_t WS_GATE = WS_KMAX + (size_t)160 * 32 * 4;
constexpr size_t WS_F1 = WS_GATE + (size_t)S * 4;
constexpr size_t WS_F2 = WS_F1 + 2048;
constexpr size_t WS_F3 = WS_F2 + 2048;
constexpr size_t WS_EPOCH = WS_F3 + 2048;
constexpr size_t WS_END = WS_EPOCH + 256;
constexpr float QSCALE = 0.22360679774997896f * 1.4426950408889634f;
constexpr unsigned ONES2 = 0x3F803F80u;

DI unsigned cvtpk(float lo, float hi) { f32x2 v = {lo, hi}; bf16x2_t b = __builtin_convertvector(v, bf16x2_t); return __builtin_bit_cast(unsigned, b); }
DI float bf_lo(unsigned u) { return __uint_as_float(u << 16); }
DI float bf_hi(unsigned u) { return __uint_as_float(u & 0xffff0000u); }
DI float bf_round(float x) { return bf_lo(cvtpk(x, x)); }
DI void glds16(const void* sbase, unsigned voff, unsigned lds_base) {
    unsigned sv;
    asm volatile("s_mov_b32 %0, m0\n\ts_mov_b32 m0, %3\n\ts_nop 2\n\tglobal_load_lds_dwordx4 %1, %2\n\ts_mov_b32 m0, %0"
                 : "=&s"(sv) : "v"(voff), "s"(sbase), "s"(lds_base) : "memory");
}
DI void glds16_sc1(const void* sbase, unsigned voff, unsigned lds_base) {
    unsigned sv;
    asm volatile("s_mov_b32 %0, m0\n\ts_mov_b32 m0, %3\n\ts_nop 2\n\tglobal_load_lds_dwordx4 %1, %2 sc1\n\ts_mov_b32 m0, %0"
                 : "=&s"(sv) : "v"(voff), "s"(sbase), "s"(lds_base) : "memory");
}
typedef short v4i16_t __attribute__((ext_vector_type(4)));
DI s16x4 ldstr(lds_cptr p) { return __builtin_bit_cast(s16x4, __builtin_amdgcn_ds_read_tr16_b64_v4i16((LAS v4i16_t*)p)); }
DI bf16x8 lds128(lds_cptr p) { return *(const LAS bf16x8*)p; }
DI s16x4 lds64(lds_cptr p) { return *(const LAS s16x4*)p; }
DI unsigned rfl(unsigned x) { return __builtin_amdgcn_readfirstlane(x); }
#define STAMP(k, i) do { } while (0)

DI float rdl(float x, int l) { return __uint_as_float(__builtin_amdgcn_readlane(__float_as_uint(x), l)); }
typedef __amdgpu_buffer_rsrc_t rsrc_t;
typedef unsigned long long u64;
DI rsrc_t ws_rsrc(char* ws) { return __builtin_amdgcn_make_buffer_rsrc(ws, 0, (int)WS_END, 0x00020000); }
DI void st16(rsrc_t rs, unsigned off, u32x4 v) { __builtin_amdgcn_raw_buffer_store_b128(v, rs, off, 0, 16); }
DI void st8(rsrc_t rs, unsigned off, u32x2 v) { __builtin_amdgcn_raw_buffer_store_b64(v, rs, off, 0, 16); }
DI void st4f(char* p, float v) { __hip_atomic_store((float*)p, v, __ATOMIC_RELAXED, __HIP_MEMORY_SCOPE_AGENT); }
DI void flag_set(char* p, u64 tag) { __hip_atomic_store((u64*)p, tag, __ATOMIC_RELAXED, __HIP_MEMORY_SCOPE_AGENT); }
DI u64 flag_get(const char* p) { return __hip_atomic_load((const u64*)p, __ATOMIC_RELAXED, __HIP_MEMORY_SCOPE_AGENT); }
template <int CTRL> DI float dpp_max(float x) { const float y = __uint_as_float(__builtin_amdgcn_update_dpp(0u, __float_as_uint(x), CTRL, 0xf, 0xf, true)); return fmaxf(x, y); }
template <int CTRL> DI float dpp_add(float x) { const float y = __uint_as_float(__builtin_amdgcn_update_dpp(0u, __float_as_uint(x), CTRL, 0xf, 0xf, true)); return x + y; }
DI void gld128(bf16x8& d, const char* sbase, unsigned voff) { asm volatile("s_nop 4\n\tglobal_load_dwordx4 %0, %1, %2" : "=v"(d) : "v"(voff), "s"(sbase) : "memory"); }
DI void gld128_sc1(bf16x8& d, const char* sbase, unsigned voff) { asm volatile("s_nop 4\n\tglobal_load_dwordx4 %0, %1, %2 sc1" : "=v"(d) : "v"(voff), "s"(sbase) : "memory"); }
DI void gld128_rw(bf16x8& d, const char* sbase, unsigned voff) { asm volatile("s_nop 4\n\tglobal_load_dwordx4 %0, %1, %2" : "+v"(d) : "v"(voff), "s"(sbase) : "memory"); }
DI void gld128_sc1_rw(bf16x8& d, const char* sbase, unsigned voff) { asm volatile("s_nop 4\n\tglobal_load_dwordx4 %0, %1, %2 sc1" : "+v"(d) : "v"(voff), "s"(sbase) : "memory"); }
template <int N> DI void wait9(bf16x8 (&a)[4], bf16x8 (&b)[5]) {
    asm volatile("s_waitcnt vmcnt(%9)" : "+v"(a[0]), "+v"(a[1]), "+v"(a[2]), "+v"(a[3]), "+v"(b[0]), "+v"(b[1]), "+v"(b[2]), "+v"(b[3]), "+v"(b[4]) : "i"(N) : "memory");
}
#define WAITB(n) asm volatile("s_waitcnt vmcnt(" #n ")\n\ts_barrier" ::: "memory")
#define BARL() asm volatile("s_waitcnt lgkmcnt(0)\n\ts_barrier" ::: "memory")

constexpr int P1_STG = 32768;
constexpr int RT_OFF = 131072;
constexpr int P2_STG = 28672;
constexpr int CTL_OFF = 163584;
DI constexpr int p2_base(int s) { return CTL_OFF - (s + 1) * P2_STG; }
constexpr int K2_BIG = 16 * KVTILE_B;
constexpr int EP_OFF = 151552;
constexpr int LDS_ALL = 163840;

template <bool N96>
struct GDma {
    static constexpr int NW = N96 ? 3 : 4;
    const char* xbase; const char* wbase; int ldw2, k0; unsigned lds0, xoff0, xoff1, woff[3];
    DI int kact(int kc) const { kc += k0; return kc >= 10 ? kc - 10 : kc; }
    DI static unsigned sbase(int st) { return N96 ? (unsigned)(CTL_OFF - (st + 1) * P2_STG) : (unsigned)(st * P1_STG); }
    DI void init(const char* xb, const char* wb, int ld, char* lds, int lane, int k0_) {
        xbase = xb; wbase = wb; ldw2 = ld; k0 = k0_; lds0 = (unsigned)(uintptr_t)lds;
        const int rr = lane >> 3;
        xoff0 = rr * 1280 + (((lane & 7) ^ ((lane >> 4) & 7)) << 4); xoff1 = rr * 1280 + (((lane & 7) ^ ((4 + (lane >> 4)) & 7)) << 4);
        if (N96) {
#pragma unroll
            for (int p = 0; p < 3; ++p) { const int o = 1024 * p + 16 * lane, row = (o * 21846) >> 22, col = o - row * 192; woff[p] = row * ldw2 + col; }
        } else woff[0] = (lane >> 4) * ldw2 + ((((lane & 15) ^ ((lane >> 4) << 2))) << 4);
    }
    DI void xp(int kc, int st, int j, int par) const { glds16(xbase + (size_t)j * 8 * 1280 + kc * 128, par ? xoff1 : xoff0, lds0 + sbase(st) + j * 1024); }
    DI void wp(int kc, int st, int lw, int jj) const {
        if (N96) glds16(wbase + (size_t)(kc * 64 + 16 * lw) * ldw2, woff[jj], lds0 + sbase(st) + 16384 + (3 * lw + jj) * 1024);
        else glds16(wbase + (size_t)(kc * 64 + 16 * lw + 4 * jj) * ldw2, woff[0], lds0 + sbase(st) + 16384 + (4 * lw + jj) * 1024);
    }
    DI void issue(int kc_, int st, int lw) const {
        const int kc = kact(kc_);
#pragma unroll
        for (int jj = 0; jj < NW; ++jj) wp(kc, st, lw, jj);
#pragma unroll
        for (int jj = 0; jj < 4; ++jj) xp(kc, st, 4 * lw + jj, jj & 1);
    }
};

DI void rt_load(const unsigned short* w2, const unsigned short* wo, int u, unsigned ldsb, int piece, int lane) {
    const int sig = u >> 2, s = (u >> 1) & 1, cb = u & 1;
    const char* src = (sig < 48 ? (const char*)w2 + (size_t)sig * 32 * 1280 : (const char*)wo + (size_t)(sig - 48) * 32 * 1280) + s * 16 * 1280 + cb * 640;
    const int o = piece * 1024 + lane * 16, row = (o * 6554) >> 22;
    glds16(src, row * 1280 + (o - row * 640), ldsb + piece * 1024);
}
DI void rt_store(rsrc_t rs, int u, const char* ldsb, int i, int lane) {
    const int sig = u >> 2, s = (u >> 1) & 1, cb = u & 1;
    const int nl = 2 * i + (lane >> 5), hh = (lane >> 4) & 1, q = (lane & 15) >> 2, p = lane & 3;
    lds_cptr a = (lds_cptr)ldsb + (4 * hh + q) * 640 + (16 * nl + 4 * p) * 2;
    const s16x4 lo = ldstr(a), hi = ldstr(a + 8 * 640);
    const bf16x8 f = __builtin_shufflevector(lo, hi, 0, 1, 2, 3, 4, 5, 6, 7);
    const int nu = cb * 20 + nl, ng = (nu * 205) >> 10;
    st16(rs, (unsigned)WS_BF + (unsigned)((ng * NSIG + sig) * 5 + (nu - 5 * ng)) * 1024 + (s * 32 + (lane & 31)) * 16, __builtin_bit_cast(u32x4, f));
}

template <bool N96, bool VT, int MT, int NTL, class HOOK>
DI void gemm_loop(f32x16 (&acc)[MT][NTL], char* lds, int wid, int lane, HOOK hook) {
    const int h = lane >> 5, r = lane & 31;
    constexpr int NST = N96 ? 3 : 4;
    unsigned xa[4];
    { const int row = (N96 ? wid * 32 : (wid >> 1) * 64) + r; const int sw = (row >> 1) & 7;
#pragma unroll
      for (int ks = 0; ks < 4; ++ks) xa[ks] = row * 128 + (((2 * ks + h) ^ sw) << 4); }
    unsigned wa[2];
    { const int blk = (lane >> 4) & 1, q = (lane & 15) >> 2, p = lane & 3;
      if (N96) wa[0] = wa[1] = 16384 + (8 * h + q) * 192 + blk * 32 + p * 8;
      else {
#pragma unroll
          for (int nt = 0; nt < 2; ++nt) { const int ch = (wid & 1) * 8 + nt * 4 + 2 * blk + (p >> 1); wa[nt] = 16384 + (8 * h + q) * 256 + ((ch ^ (q << 2)) << 4) + 8 * (p & 1); } } }
    bf16x8 xf[2][MT], wf[2][NTL];
    auto rdw = [&](int buf, int nt, lds_cptr sb, int ks) {
        lds_cptr a = N96 ? sb + wa[0] + ks * 16 * 192 + nt * 64 : sb + wa[nt & 1] + ks * 4096;
        const s16x4 lo = ldstr(a), hi = ldstr(a + (N96 ? 4 * 192 : 1024));
        wf[buf][nt] = __builtin_shufflevector(lo, hi, 0, 1, 2, 3, 4, 5, 6, 7);
    };
    auto rdx = [&](int buf, int mt, lds_cptr sb, int ks) { xf[buf][mt] = lds128(sb + xa[ks] + mt * 4096); };
    auto mm = [&](int buf, int mt, int nt) { acc[mt][nt] = VT ? MFMA32(xf[buf][mt], wf[buf][nt], acc[mt][nt]) : MFMA32(wf[buf][nt], xf[buf][mt], acc[mt][nt]); };
    { lds_cptr sb = (lds_cptr)lds + GDma<N96>::sbase(0); rdw(0, 0, sb, 0); rdx(0, 0, sb, 0); rdw(0, 1, sb, 0); if (N96) rdw(0, 2 % NTL, sb, 0); else rdx(0, 1 % MT, sb, 0); }
    int st = 0;
#define G_KSTEP(ks, NEXT) do { const int b = (ks) & 1, n = b ^ 1; lds_cptr np = (ks) < 3 ? sb : sn; const int nk = ((ks) + 1) & 3; \
        SBAR(); \
        if constexpr (N96) { \
            mm(b, 0, 0); SBAR(); if (NEXT) { rdw(n, 0, np, nk); rdx(n, 0, np, nk); } SBAR(); \
            mm(b, 0, 1); SBAR(); if (NEXT) { rdw(n, 1, np, nk); } SBAR(); \
            mm(b, 0, 2 % NTL); SBAR(); if (NEXT) { rdw(n, 2 % NTL, np, nk); } SBAR(); \
        } else { \
            mm(b, 0, 0); SBAR(); if (NEXT) { rdw(n, 0, np, nk); } SBAR(); \
            mm(b, 0, 1); SBAR(); if (NEXT) { rdx(n, 0, np, nk); } SBAR(); \
            mm(b, 1 % MT, 0); SBAR(); if (NEXT) { rdw(n, 1, np, nk); } SBAR(); \
            mm(b, 1 % MT, 1); SBAR(); if (NEXT) { rdx(n, 1 % MT, np, nk); } SBAR(); \
        } } while (0)
#pragma unroll 1
    for (int kc = 0; kc < 9; ++kc) {
        lds_cptr sb = (lds_cptr)lds + GDma<N96>::sbase(st);
        st = st == NST - 1 ? 0 : st + 1;
        lds_cptr sn = (lds_cptr)lds + GDma<N96>::sbase(st);
        G_KSTEP(0, true); G_KSTEP(1, true); G_KSTEP(2, true);
        hook(kc, true);
        BARL();
        hook(kc, false);
        G_KSTEP(3, true);
    }
    { lds_cptr sb = (lds_cptr)lds + GDma<N96>::sbase(st); lds_cptr sn = sb;
      G_KSTEP(0, true); G_KSTEP(1, true); G_KSTEP(2, true); G_KSTEP(3, false); }
}

DI float swap32_add(float x) { auto rr = __builtin_amdgcn_permlane32_swap(__float_as_uint(x), __float_as_uint(x), false, false); return __uint_as_float(rr[0]) + __uint_as_float(rr[1]); }
DI float swap32_max(float x) { auto rr = __builtin_amdgcn_permlane32_swap(__float_as_uint(x), __float_as_uint(x), false, false); return fmaxf(__uint_as_float(rr[0]), __uint_as_float(rr[1])); }
DI s16x4 make_qb(unsigned q0, unsigned q1, float M, int h) {
    const float mh = bf_hi(__float_as_uint(M)), r1 = M - mh, mm = bf_hi(__float_as_uint(r1)), r2 = r1 - mm, ml = bf_hi(__float_as_uint(r2));
    u32x2 w;
    w[0] = h ? ((__float_as_uint(mh) >> 16) | (__float_as_uint(mm) & 0xffff0000u)) ^ 0x80008000u : q0;
    w[1] = h ? (__float_as_uint(ml) >> 16) ^ 0x8000u : q1;
    return __builtin_bit_cast(s16x4, w);
}

DI void attn_fallback(f32x16& o, const char* kv, bf16x8 qa, unsigned q0, unsigned q1, int lane, int kboff, int voff) {
    const int h = lane >> 5;
    const f32x16 zero = {};
    float m = -INFINITY;
    const s16x4 qb0 = make_qb(q0, q1, 0.f, h);
    for (int t = 0; t < NT; ++t) {
        const char* tb = kv + t * KVTILE_B;
        const bf16x8 ka = *(const bf16x8*)(tb + lane * 16); const s16x4 kb = *(const s16x4*)(tb + kboff);
        f32x16 s = MFMA32(ka, qa, zero); s = MFMA8(kb, qb0, s);
#pragma unroll
        for (int i = 0; i < 16; ++i) m = fmaxf(m, s[i]);
    }
    m = swap32_max(m);
    const s16x4 qb = make_qb(q0, q1, m, h);
    o = zero;
    for (int t = 0; t < NT; ++t) {
        const char* tb = kv + t * KVTILE_B;
        const bf16x8 ka = *(const bf16x8*)(tb + lane * 16); const s16x4 kb = *(const s16x4*)(tb + kboff);
        const bf16x8 va = *(const bf16x8*)(tb + voff), vb = *(const bf16x8*)(tb + voff + V_STEP);
        f32x16 s = MFMA32(ka, qa, zero); s = MFMA8(kb, qb, s);
        u32x4 p0, p1;
#pragma unroll
        for (int i = 0; i < 4; ++i) { p0[i] = cvtpk(__builtin_amdgcn_exp2f(s[2 * i]), __builtin_amdgcn_exp2f(s[2 * i + 1])); p1[i] = cvtpk(__builtin_amdgcn_exp2f(s[8 + 2 * i]), __builtin_amdgcn_exp2f(s[9 + 2 * i])); }
        o = MFMA32(va, __builtin_bit_cast(bf16x8, p0), o);
        o = MFMA32(vb, __builtin_bit_cast(bf16x8, p1), o);
    }
}

constexpr int SPIN_MAX = 1 << 20;
__global__ __launch_bounds__(512, 2) void gated_mix_mega(const unsigned short* __restrict__ x, const unsigned short* __restrict__ wqkv, const unsigned short* __restrict__ wo, const unsigned short* __restrict__ w1,
                                                         const unsigned short* __restrict__ w2, const unsigned short* __restrict__ gw, char* __restrict__ ws, unsigned short* __restrict__ out) {
    extern __shared__ __attribute__((aligned(16))) char lds[];
    const int tid = threadIdx.x, lane = tid & 63, wid = rfl(tid >> 6);
    const int bid = blockIdx.x, xcd = bid & 7, slot = bid >> 3;
    const int h = lane >> 5, r = lane & 31;
    const rsrc_t rs = ws_rsrc(ws);
    const unsigned lds0 = (unsigned)(uintptr_t)lds, lane16 = lane * 16;
    volatile LAS unsigned* ctl = (volatile LAS unsigned*)(lds + CTL_OFF);
    const int rt = 4 * (xcd >> 1) + (slot & 3), ci = slot >> 2, ct = 8 * (xcd & 1) + ci;
    const bool tile1 = ct < 15;
    const char* xbase = (const char*)x + (size_t)rt * 128 * 1280;
#define K0EXPR 0
    const int k0 = K0EXPR;
    const int head = xcd * 4 + (slot & 3), qblk = slot >> 2;
    const char* kv = ws + WS_KV + (size_t)head * NT * KVTILE_B;
    const int Tq = qblk * 8 + wid;
    const char* qt = ws + WS_Q + ((size_t)head * NT + Tq) * QTILE_B;
    bf16x8 qa; u32x2 qbr; f32x4 km4; float gate;
    auto q_loads = [&]() {
        asm volatile("s_nop 4\n\tglobal_load_dwordx4 %0, %1, %2" : "=v"(qa) : "v"(lane16), "s"(qt) : "memory");
        asm volatile("s_nop 4\n\tglobal_load_dwordx2 %0, %1, %2" : "=v"(qbr) : "v"((unsigned)(1024 + r * 8)), "s"(qt) : "memory");
        asm volatile("s_nop 4\n\tglobal_load_dwordx4 %0, %1, %2" : "=v"(km4) : "v"((unsigned)((lane < 40 ? lane : 0) * 16)), "s"(ws + WS_KMAX + (size_t)head * 640) : "memory");
        asm volatile("s_nop 4\n\tglobal_load_dword %0, %1, %2" : "=v"(gate) : "v"((unsigned)((Tq * 32 + r) * 4)), "s"(ws + WS_GATE) : "memory");
    };
    auto wait_seen = [&]() { for (int it = 0; it < SPIN_MAX; ++it) { if (ctl[1] != 0u) break; __builtin_amdgcn_s_sleep(1); } };
    auto gates = [&]() {
        const int aidx = (xcd >> 1) * 4 + (slot & 3);
        const u32x4* gr = (const u32x4*)gw + (lane & 7);
#pragma unroll 1
        for (int ps = 0; ps < 2; ++ps) {
            const int tok = aidx * 128 + wid * 16 + ps * 8 + (lane >> 3);
            const u32x4* xr = (const u32x4*)(x + (size_t)tok * DM) + (lane & 7);
            float sm = 0.f;
#pragma unroll
            for (int i = 0; i < 10; ++i) { const u32x4 xv = xr[i * 8], gv = gr[i * 8];
#pragma unroll
                for (int k = 0; k < 4; ++k) { const unsigned a_ = xv[k], b_ = gv[k]; sm = __builtin_amdgcn_fdot2_f32_bf16(__builtin_bit_cast(bf16x2_t, a_), __builtin_bit_cast(bf16x2_t, b_), sm, false); } }
            sm = dpp_add<0xB1>(sm); sm = dpp_add<0x4E>(sm); sm = dpp_add<0x141>(sm);
            const float lg = bf_round(sm);
            const float g = bf_round(__builtin_amdgcn_rcpf(1.f + __builtin_amdgcn_exp2f(lg * -1.4426950408889634f)));
            if ((lane & 7) == 0) st4f(ws + WS_GATE + tok * 4, g);
        }
    };
    GDma<true> d2; d2.init(xbase, (const char*)w1 + ct * 192, FF * 2, lds, lane, k0);
    u64 pc0; asm volatile("s_getpc_b64 %0" : "=s"(pc0));
    STAMP(0, 0);
    if (wid < 4) {
        GDma<false> d1; d1.init(xbase, (const char*)wqkv + ct * 256, 3 * DM * 2, lds, lane, k0);
        if (tile1) {
#pragma unroll
            for (int jj = 0; jj < 4; ++jj) d1.xp(d1.kact(0), 0, 4 * wid + jj, jj & 1);
        }
        if (tid == 0) { ctl[0] = 0u; ctl[1] = 0u; ctl[4] = 0u; ctl[5] = 0u; ctl[6] = 0u; ctl[7] = 0u; }
        {
#pragma unroll
            for (int i = 0; i < 7; ++i) {
                int u = (bid * 4 + wid) * 7 + i; if (u > 6879) u = 6879;
                const char* src = u < 2560 ? (const char*)x + (size_t)u * 1024 : u < 4960 ? (const char*)wqkv + (size_t)(u - 2560) * 1024 : (const char*)w1 + (size_t)(u - 4960) * 1024;
                glds16(src, lane16, lds0 + 152 * 1024);
            }
        }
        if (tile1) asm volatile("s_waitcnt vmcnt(7) lgkmcnt(0)\n\ts_barrier" ::: "memory");
        if (slot * 4 + wid < 42) glds16((const char*)((pc0 + 15ull) & ~15ull) + (slot * 4 + wid) * 1024, lane16, lds0 + 152 * 1024);
        for (int pc = wid; pc < 10; pc += 4) rt_load(w2, wo, bid, lds0 + RT_OFF, pc, lane);
        if (!tile1) for (int pc = wid; pc < 10; pc += 4) rt_load(w2, wo, 256 + (xcd >> 1) * 4 + (slot & 3), lds0 + RT_OFF + 10240, pc, lane);
        if (wid == 0) glds16_sc1(ws + WS_EPOCH, lane < 1 ? 0u : 0u, lds0 + EP_OFF);
        auto tag_ready = [&]() {
            if (tid == 0) { const u64 tg = *(const volatile LAS u64*)(lds + EP_OFF) + 1ull; ctl[2] = (unsigned)tg; ctl[3] = (unsigned)(tg >> 32); }
        };
        if (!tile1) { asm volatile("s_waitcnt vmcnt(0)" ::: "memory"); tag_ready(); asm volatile("s_waitcnt lgkmcnt(0)\n\ts_barrier" ::: "memory"); }
        if (tile1) {
            const int wm = wid >> 1, wn = wid & 1;
            f32x16 acc[2][2] = {};
            const char* w1w = (const char*)w1 + (size_t)(slot * 20) * 3072 + (xcd & 1) * 1536;
            auto nohook = [&](int kc, bool before) {
                if (kc == 1 && before) { asm volatile("s_waitcnt vmcnt(0)" ::: "memory"); tag_ready(); }
                if (!before && kc < 4) {
#pragma unroll
                    for (int e = 0; e < 2; ++e) { const int u = kc * 8 + wid * 2 + e;
                        if (u < 30) { const int o = u * 1024 + (int)lane16, row = o / 1536; glds16(w1w, (unsigned)(row * 3072 + (o - row * 1536)), lds0 + 152 * 1024); } } }
                if (kc == 5 && before) asm volatile("s_waitcnt vmcnt(0)" ::: "memory");
            };
            if (ct >= 10) {
                gemm_loop<false, true, 2, 2>(acc, lds, wid, lane, nohook);
                STAMP(0, 1);
#pragma unroll
                for (int nt = 0; nt < 2; ++nt) {
                    const int phi = (ct - 10) * 128 + wn * 64 + nt * 32 + r;
                    const int hd = (phi * 3277) >> 16, d = phi - hd * 20;
                    const unsigned dst = (unsigned)WS_KV + (unsigned)(hd * NT + rt * 4 + wm * 2) * KVTILE_B + V_OFF + (h * V_ROWS + d) * 16;
#pragma unroll
                    for (int mt = 0; mt < 2; ++mt)
#pragma unroll
                        for (int s = 0; s < 2; ++s) {
                            const f32x16& a = acc[mt][nt];
                            const u32x4 w = {cvtpk(a[8 * s], a[8 * s + 1]), cvtpk(a[8 * s + 2], a[8 * s + 3]), cvtpk(a[8 * s + 4], a[8 * s + 5]), cvtpk(a[8 * s + 6], a[8 * s + 7])};
                            st16(rs, dst + mt * KVTILE_B + s * V_STEP, w);
                        }
                }
                BARL();
            } else {
                gemm_loop<false, false, 2, 2>(acc, lds, wid, lane, nohook);
                STAMP(0, 1);
                const bool isQ = ct < 5;
                const float sc = isQ ? QSCALE : 1.f;
                const unsigned img = (unsigned)(isQ ? WS_Q : WS_KV), tileb = isQ ? QTILE_B : KVTILE_B;
                const int G = (isQ ? ct : ct - 5) * 32 + wn * 16 + h;
                u32x2 P[2][8]; float nrm[8];
#pragma unroll
                for (int p = 0; p < 8; ++p) {
                    const int nt = p >> 2, g = p & 3; float m = 0.f;
#pragma unroll
                    for (int mt = 0; mt < 2; ++mt) {
                        const f32x16& a = acc[mt][nt];
                        P[mt][p] = (u32x2){cvtpk(a[4 * g] * sc, a[4 * g + 1] * sc), cvtpk(a[4 * g + 2] * sc, a[4 * g + 3] * sc)};
                        m = fmaxf(m, __builtin_fmaf(a[4 * g], a[4 * g], a[4 * g + 1] * a[4 * g + 1]) + __builtin_fmaf(a[4 * g + 2], a[4 * g + 2], a[4 * g + 3] * a[4 * g + 3]));
                    }
                    nrm[p] = m;
                }
#pragma unroll
                for (int p = 0; p < 8; ++p) {
                    const int gam = G + 2 * p, hd = (gam * 205) >> 10, dg = gam - 5 * hd;
                    const unsigned base = img + (unsigned)(hd * NT + rt * 4 + wm * 2) * tileb;
#pragma unroll
                    for (int mt = 0; mt < 2; ++mt) {
                        const unsigned bm = base + mt * tileb;
                        if (dg < 2) {
                            if (p < 7) st16(rs, bm + dg * 512 + r * 16, (u32x4){P[mt][p][0], P[mt][p][1], P[mt][(p + 1) & 7][0], P[mt][(p + 1) & 7][1]});
                            else st8(rs, bm + dg * 512 + r * 16, P[mt][p]);
                        } else if (dg == 4) st8(rs, bm + 1024 + r * 8, P[mt][p]);
                        else if (p == 0) st8(rs, bm + (dg - 2) * 512 + r * 16 + 8, P[mt][p]);
                    }
                }
                if (!isQ) {
#pragma unroll
                    for (int i = 0; i < 8; ++i) { float m = nrm[i]; m = dpp_max<0xB1>(m); m = dpp_max<0x4E>(m); m = dpp_max<0x141>(m); m = dpp_max<0x140>(m);
                        auto rr = __builtin_amdgcn_permlane16_swap(__float_as_uint(m), __float_as_uint(m), false, false); nrm[i] = fmaxf(__uint_as_float(rr[0]), __uint_as_float(rr[1])); }
                    float mine = nrm[0];
#pragma unroll
                    for (int i = 1; i < 8; ++i) mine = r == i ? nrm[i] : mine;
                    if (r < 8) st4f(ws + WS_KMAX + (size_t)((G + 2 * r) * 32 + rt * 2 + wm) * 4, mine);
                }
                BARL();
            }
        } else { gates(); BARL(); }
        STAMP(0, 2);
        BARL();
        f32x16 acc2[1][3] = {};
        auto hook2 = [&](int kc, bool before) {
            if (kc == 2) { if (before) asm volatile("s_waitcnt vmcnt(0)" ::: "memory");
                           else if (tid == 0) flag_set(ws + WS_F1 + bid * 8, ((u64)ctl[3] << 32) | ctl[2]); }
        };
        gemm_loop<true, false, 1, 3>(acc2, lds, wid, lane, hook2);
        STAMP(0, 3);
        wait_seen();
        q_loads();
        {   const int tok = rt * 128 + wid * 32 + r, rho = tok >> 4;
#pragma unroll
            for (int nt = 0; nt < 3; ++nt) {
                const unsigned dst = (unsigned)WS_AF + (unsigned)(rho * NSIG + ct * 3 + nt) * 1024 + (h * 16 + (r & 15)) * 16;
                float sv[16];
#pragma unroll
                for (int i = 0; i < 16; ++i) { const float v = acc2[0][nt][i]; sv[i] = v * __builtin_amdgcn_rcpf(1.f + __builtin_amdgcn_exp2f(v * -1.4426950408889634f)); }
#pragma unroll
                for (int s = 0; s < 2; ++s) {
                    const u32x4 w = {cvtpk(sv[8 * s], sv[8 * s + 1]), cvtpk(sv[8 * s + 2], sv[8 * s + 3]), cvtpk(sv[8 * s + 4], sv[8 * s + 5]), cvtpk(sv[8 * s + 6], sv[8 * s + 7])};
                    st16(rs, dst + s * 512, w);
                }
            }
        }
        asm volatile("s_waitcnt vmcnt(6)" : "+v"(qa), "+v"(qbr), "+v"(km4), "+v"(gate) :: "memory");
    } else {
        const int lw = wid - 4;
        auto side_stores = [&]() {
            for (int i = lw; i < 10; i += 4) rt_store(rs, bid, lds + RT_OFF, i, lane);
            if (!tile1) for (int i = lw; i < 10; i += 4) rt_store(rs, 256 + (xcd >> 1) * 4 + (slot & 3), lds + RT_OFF + 10240, i, lane);
            if (lw == 0) { const int i = bid * 64 + lane;
                const unsigned f = (i & 1) ? 0u : ONES2; st16(rs, (unsigned)WS_KV + (unsigned)(i >> 3) * KVTILE_B + V_OFF + ((i >> 2) & 1) * V_STEP + (((i >> 1) & 1) * V_ROWS + 20 + (i & 1)) * 16, (u32x4){f, f, f, f}); }
        };
        if (tile1) {
            GDma<false> d1; d1.init(xbase, (const char*)wqkv + ct * 256, 3 * DM * 2, lds, lane, k0);
#pragma unroll
            for (int jj = 0; jj < 4; ++jj) d1.wp(d1.kact(0), 0, lw, jj);
            d1.issue(1, 1, lw);
            WAITB(8);
            d1.issue(2, 2, lw); d1.issue(3, 3, lw);
            for (int kc = 0; kc < 9; ++kc) {
                if (kc < 7) WAITB(16); else if (kc == 7) WAITB(15); else WAITB(14);
                if (kc + 4 < 10) d1.issue(kc + 4, kc & 3, lw);
                if (kc == 4) side_stores();
                if (kc >= 6) d2.issue(kc - 6, kc - 6, lw);
            }
            asm volatile("s_barrier" ::: "memory");
        } else {
            asm volatile("s_barrier" ::: "memory");
            side_stores(); gates();
            BARL();
            d2.issue(0, 0, lw); d2.issue(1, 1, lw); d2.issue(2, 2, lw);
        }
        WAITB(14);
        { int st = 0;
          for (int kc = 0; kc < 9; ++kc) {
              if (kc < 8) WAITB(7); else WAITB(0);
              if (kc + 3 < 10) d2.issue(kc + 3, st, lw);
              st = st == 2 ? 0 : st + 1;
          } }
        if (lw == 0) {
            const u64 tag = ((u64)ctl[3] << 32) | ctl[2];
            for (int it = 0; it < SPIN_MAX; ++it) {
                bool ok = true;
#pragma unroll
                for (int i = 0; i < 4; ++i) ok &= flag_get(ws + WS_F1 + (lane + 64 * i) * 8) == tag;
                if (__all(ok)) break;
                __builtin_amdgcn_s_sleep(2);
            }
            __builtin_amdgcn_fence(__ATOMIC_ACQUIRE, "agent");
            asm volatile("s_waitcnt vmcnt(0)" ::: "memory");
            if (lane == 0) { ctl[1] = 1u; if (bid == 0) flag_set(ws + WS_EPOCH, tag); }
        } else wait_seen();
        q_loads();
        for (int j = 0; j < 21; ++j) glds16(kv + (size_t)(lw + 4 * j) * 1024, lane16, lds0 + (lw + 4 * j) * 1024);
        asm volatile("s_waitcnt vmcnt(21)" : "+v"(qa), "+v"(qbr), "+v"(km4), "+v"(gate) :: "memory");
    }
    STAMP(1, 0);
    float km = fmaxf(fmaxf(km4[0], km4[1]), fmaxf(km4[2], km4[3]));
    km = dpp_max<0xB1>(km); km = dpp_max<0x4E>(km); km = dpp_max<0x141>(km);
    km = __builtin_amdgcn_sqrtf(km);
    float M;
    { const float c0 = rdl(km, 0), c1 = rdl(km, 8), c2 = rdl(km, 16), c3 = rdl(km, 24), c4 = rdl(km, 32);
      const u32x4 qw = __builtin_bit_cast(u32x4, qa);
      float n0, n1, n2;
      { const float a = bf_lo(qw[0]), b = bf_hi(qw[0]), c = bf_lo(qw[1]), d = bf_hi(qw[1]); n0 = (a * a + b * b) + (c * c + d * d); }
      { const float a = bf_lo(qw[2]), b = bf_hi(qw[2]), c = bf_lo(qw[3]), d = bf_hi(qw[3]); n1 = (a * a + b * b) + (c * c + d * d); }
      { const unsigned q0 = qbr[0], q1 = qbr[1]; const float a = bf_lo(q0), b = bf_hi(q0), c = bf_lo(q1), d = bf_hi(q1); n2 = (a * a + b * b) + (c * c + d * d); }
      float part = __builtin_amdgcn_sqrtf(n0) * (h ? c1 : c0) + __builtin_amdgcn_sqrtf(n1) * (h ? c3 : c2);
      if (!h) part += __builtin_amdgcn_sqrtf(n2) * c4;
      M = swap32_add(part) * 1.01f; }
    const s16x4 qb = make_qb(qbr[0], qbr[1], M, h);
    const int vrow = r <= 20 ? r : (r < 28 ? 21 : r - 12);
    const int voff = V_OFF + (h * V_ROWS + vrow) * 16, kboff = h ? V_OFF + 20 * 16 : 1024 + r * 8;
    lds_cptr kaC = (lds_cptr)lds + lane * 16, kbC = (lds_cptr)lds + kboff, vC = (lds_cptr)lds + voff;
    lds_cptr kaN = kaC + K2_BIG, kbN = kbC + K2_BIG, vN = vC + K2_BIG;
    const f32x16 zero = {};
    f32x16 o = {}, sA, sB;
    bf16x8 ka, va, vb; s16x4 kb;
    u32x4 plo, phi;
    if (wid >= 4) asm volatile("s_waitcnt vmcnt(15)" ::: "memory");
    asm volatile("s_barrier" ::: "memory");
    STAMP(1, 1);
    ka = lds128(kaC); kb = lds64(kbC);
    sA = MFMA32(ka, qa, zero); sA = MFMA8(kb, qb, sA);
    ka = lds128(kaC + KVTILE_B); kb = lds64(kbC + KVTILE_B);
    va = lds128(vC);
    vb = (bf16x8){}; phi = (u32x4){};
    f32x4 e3 = {};
    const int ng = (xcd & 1) * 4 + (slot & 3), bt = (xcd >> 1) * 8 + (slot >> 2);
    const char* ap = ws + WS_AF + (size_t)bt * 4 * NSIG * 1024;
    const char* bp = ws + WS_BF + (size_t)ng * NSIG * 5 * 1024;
#define ROTEXPR 0
    const int rot = ROTEXPR;
    auto hs = [&](int i) { int st = i + rot; if (st >= 6) st -= 6; return wid * 6 + st; };
    bf16x8 a[3][4], b[3][5];
    auto ldA1 = [&](int sg, int mt, bf16x8& d) { gld128_sc1(d, ap + ((size_t)mt * NSIG + sg) * 1024, lane16); };
    auto ldB1 = [&](int sg, int nt, bf16x8& d) { gld128(d, bp + sg * 5120 + nt * 1024, lane16); };
    const u64 tag = ((u64)ctl[3] << 32) | ctl[2];
#define EXP4(X, i) do { X[i] = __builtin_amdgcn_exp2f(X[i]); X[i + 1] = __builtin_amdgcn_exp2f(X[i + 1]); X[i + 2] = __builtin_amdgcn_exp2f(X[i + 2]); X[i + 3] = __builtin_amdgcn_exp2f(X[i + 3]); } while (0)
#define KADDR(A, i) (((i) < 16) ? (A##C + (i) * KVTILE_B) : (A##N + ((i) - 16) * KVTILE_B))
#define STEP(C, N, i, X0, X1) do { \
        bf16x8 ka2, va2, vb2; s16x4 kb2; \
        N = MFMA32(ka, qa, zero); SBAR(); \
        ka2 = lds128(KADDR(ka, (i) + 2)); \
        phi[2] = cvtpk(e3[0], e3[1]); phi[3] = cvtpk(e3[2], e3[3]); PIN(phi); SBAR(); EXP4(C, 0); PIN(C); SBAR(); \
        o = MFMA32(vb, __builtin_bit_cast(bf16x8, phi), o); SBAR(); \
        X0; \
        vb2 = lds128(vC + (i) * KVTILE_B + V_STEP); \
        plo[0] = cvtpk(C[0], C[1]); plo[1] = cvtpk(C[2], C[3]); PIN(plo); SBAR(); EXP4(C, 4); PIN(C); SBAR(); \
        N = MFMA8(kb, qb, N); SBAR(); \
        kb2 = lds64(KADDR(kb, (i) + 2)); \
        plo[2] = cvtpk(C[4], C[5]); plo[3] = cvtpk(C[6], C[7]); PIN(plo); SBAR(); EXP4(C, 8); PIN(C); SBAR(); \
        o = MFMA32(va, __builtin_bit_cast(bf16x8, plo), o); SBAR(); \
        X1; \
        va2 = lds128(KADDR(v, (i) + 1)); \
        phi[0] = cvtpk(C[8], C[9]); phi[1] = cvtpk(C[10], C[11]); PIN(phi); SBAR(); \
        e3[0] = __builtin_amdgcn_exp2f(C[12]); e3[1] = __builtin_amdgcn_exp2f(C[13]); e3[2] = __builtin_amdgcn_exp2f(C[14]); e3[3] = __builtin_amdgcn_exp2f(C[15]); PIN(e3); SBAR(); \
        ka = ka2; kb = kb2; va = va2; vb = vb2; \
    } while (0)
#define PIECE(j) do { if (8 * (j) + 7 < 42 || 8 * (j) + wid < 42) glds16(src + (j) * 8192, lane16, dst + (j) * 8192); } while (0)
    const char* src = kv + (84 + wid) * 1024; unsigned dst = lds0 + wid * 1024 + 2 * K2_BIG;
#pragma unroll 1
    for (int c = 0; c < 3; ++c) {
        STEP(sA, sB, 0, , ); STEP(sB, sA, 1, , ); STEP(sA, sB, 2, , ); STEP(sB, sA, 3, , ); STEP(sA, sB, 4, , );
        if (c == 0) { if (wid >= 4) asm volatile("s_waitcnt vmcnt(10)" ::: "memory"); asm volatile("s_barrier" ::: "memory"); }
        STEP(sB, sA, 5, , ); STEP(sA, sB, 6, , ); STEP(sB, sA, 7, , ); STEP(sA, sB, 8, , ); STEP(sB, sA, 9, , ); STEP(sA, sB, 10, , ); STEP(sB, sA, 11, , ); STEP(sA, sB, 12, , );
        if (c == 2 && wid == 0) {
            for (int it = 0; it < SPIN_MAX; ++it) {
                if (__all(flag_get(ws + WS_F2 + ((bt >> 1) * 16 + (lane & 15)) * 8) == tag)) break;
                __builtin_amdgcn_s_sleep(2);
            }
        }
        WAITB(0);
        if (c == 0 && tid == 0) flag_set(ws + WS_F2 + (rt * 16 + ct) * 8, tag);
        if (c < 2) { STEP(sB, sA, 13, PIECE(0), PIECE(1)); STEP(sA, sB, 14, PIECE(2), PIECE(3)); STEP(sB, sA, 15, PIECE(4), PIECE(5)); }
        else { STEP(sB, sA, 13, , ); STEP(sA, sB, 14, , ); STEP(sB, sA, 15, , ); }
        const int d = (c == 1) ? -2 * K2_BIG : K2_BIG;
        kaC = kaN; kbC = kbN; vC = vN; kaN += d; kbN += d; vN += d;
        src += 42 * 1024; dst -= 2 * K2_BIG;
    }
#define PA(s, m) ldA1(hs(s), m, a[s][m])
#define PB(s, n) ldB1(hs(s), n, b[s][n])
    STEP(sA, sB, 0, PA(0, 0), PA(0, 1)); STEP(sB, sA, 1, PA(0, 2), PA(0, 3)); STEP(sA, sB, 2, PB(0, 0), PB(0, 1));
    asm volatile("s_barrier" ::: "memory");
    STEP(sB, sA, 3, PB(0, 2), PB(0, 3));
    STEP(sA, sB, 4, PB(0, 4), PA(1, 0)); STEP(sB, sA, 5, PA(1, 1), PA(1, 2)); STEP(sA, sB, 6, PA(1, 3), PB(1, 0)); STEP(sB, sA, 7, PB(1, 1), PB(1, 2));
    STEP(sA, sB, 8, PB(1, 3), PB(1, 4)); STEP(sB, sA, 9, PA(2, 0), PA(2, 1)); STEP(sA, sB, 10, PA(2, 2), PA(2, 3)); STEP(sB, sA, 11, PB(2, 0), PB(2, 1));
    STEP(sA, sB, 12, PB(2, 2), PB(2, 3)); STEP(sB, sA, 13, PB(2, 4), ); STEP(sA, sB, 14, , ); STEP(sB, sA, 15, , );
    phi[2] = cvtpk(e3[0], e3[1]); phi[3] = cvtpk(e3[2], e3[3]);
    o = MFMA32(vb, __builtin_bit_cast(bf16x8, phi), o);
    STAMP(1, 2);
    float l; { auto rr = __builtin_amdgcn_permlane32_swap(__float_as_uint(o[8]), __float_as_uint(o[8]), false, false); l = __uint_as_float(rr[1]); }
    if (__builtin_expect(__any(!(l >= 8.6736174e-19f && l <= 1.0e30f)), 0)) {
        asm volatile("s_waitcnt vmcnt(0)" ::: "memory");
        attn_fallback(o, kv, qa, qbr[0], qbr[1], lane, kboff, voff);
        auto rr = __builtin_amdgcn_permlane32_swap(__float_as_uint(o[8]), __float_as_uint(o[8]), false, false); l = __uint_as_float(rr[1]);
    }
    const float f = gate * __builtin_amdgcn_rcpf(l);
    const int tok = Tq * 32 + r;
    char* ab = ws + WS_AF + (size_t)(tok >> 4) * NSIG * 1024 + (tok & 15) * 16;
#pragma unroll
    for (int g = 0; g < 3; ++g) {
        const int gam = head * 5 + 2 * g + h;
        const int sig = 48 + (gam >> 3), c = ((gam >> 2) & 1) * 2 + (gam & 1), half = (gam >> 1) & 1;
        const u32x2 w = {cvtpk(o[4 * g] * f, o[4 * g + 1] * f), cvtpk(o[4 * g + 2] * f, o[4 * g + 3] * f)};
        if (g < 2 || h == 0) __hip_atomic_store((u64*)(ab + sig * 1024 + c * 256 + half * 8), ((u64)w[1] << 32) | w[0], __ATOMIC_RELAXED, __HIP_MEMORY_SCOPE_AGENT);
    }
    STAMP(1, 3);
    {
        f32x4 acc[4][5] = {};
        auto loadA = [&](int sg, bf16x8 (&da)[4]) {
#pragma unroll
            for (int mt = 0; mt < 4; ++mt) gld128_sc1(da[mt], ap + ((size_t)mt * NSIG + sg) * 1024, lane16);
        };
        auto loadB = [&](int sg, bf16x8 (&db)[5]) {
#pragma unroll
            for (int nt = 0; nt < 5; ++nt) gld128(db[nt], bp + sg * 5120 + nt * 1024, lane16);
        };
        auto mma = [&](bf16x8 (&da)[4], bf16x8 (&db)[5]) {
            SBAR();
#pragma unroll
            for (int nt = 0; nt < 5; ++nt)
#pragma unroll
                for (int mt = 0; mt < 4; ++mt) acc[mt][nt] = MFMA16(db[nt], da[mt], acc[mt][nt]);
            SBAR();
        };
        const bool more = wid < 4;
        const unsigned rgn = 43008 + (wid & 3) * 27648;
        auto dma3 = [&](int s0) {
#pragma unroll
            for (int i = 0; i < 3; ++i) {
#pragma unroll
                for (int mt = 0; mt < 4; ++mt) glds16_sc1(ap + ((size_t)mt * NSIG + s0 + i) * 1024, lane16, lds0 + rgn + i * 9216 + mt * 1024);
#pragma unroll
                for (int nt = 0; nt < 5; ++nt) glds16(bp + (s0 + i) * 5120 + nt * 1024, lane16, lds0 + rgn + i * 9216 + 4096 + nt * 1024);
            }
        };
        if (more) { dma3(wid * 6 + 3); asm volatile("s_waitcnt vmcnt(30)" ::: "memory"); } else asm volatile("s_waitcnt vmcnt(3)" ::: "memory");
        asm volatile("" : "+v"(a[0][0]), "+v"(a[0][1]), "+v"(a[0][2]), "+v"(a[0][3]), "+v"(b[0][0]), "+v"(b[0][1]), "+v"(b[0][2]), "+v"(b[0][3]), "+v"(b[0][4]));
        asm volatile("" : "+v"(a[1][0]), "+v"(a[1][1]), "+v"(a[1][2]), "+v"(a[1][3]), "+v"(b[1][0]), "+v"(b[1][1]), "+v"(b[1][2]), "+v"(b[1][3]), "+v"(b[1][4]));
        asm volatile("" : "+v"(a[2][0]), "+v"(a[2][1]), "+v"(a[2][2]), "+v"(a[2][3]), "+v"(b[2][0]), "+v"(b[2][1]), "+v"(b[2][2]), "+v"(b[2][3]), "+v"(b[2][4]));
        mma(a[0], b[0]); loadB(48 + wid, b[0]);
        mma(a[1], b[1]); loadB(56 + wid, b[1]);
        if (more) asm volatile("s_waitcnt vmcnt(37)" ::: "memory"); else asm volatile("s_waitcnt vmcnt(10)" ::: "memory");
        if (lane == 0) {
            const unsigned old = __hip_atomic_fetch_add((unsigned*)(lds + CTL_OFF), 1u, __ATOMIC_RELAXED, __HIP_MEMORY_SCOPE_WORKGROUP);
            if (old == 7u) flag_set(ws + WS_F3 + (qblk * 32 + head) * 8, tag);
        }
        mma(a[2], b[2]); loadB(wid < 4 ? 64 + wid : 67, b[2]);
        float og[4];
#pragma unroll
        for (int mt = 0; mt < 4; ++mt) asm volatile("s_nop 4\n\tglobal_load_dword %0, %1, %2" : "=v"(og[mt]) : "v"((unsigned)((bt * 64 + mt * 16 + (lane & 15)) * 4)), "s"(ws + WS_GATE) : "memory");
        if (more) asm volatile("s_waitcnt vmcnt(19)" ::: "memory");
        else { for (int it = 0; it < SPIN_MAX; ++it) { if (ctl[4 + (wid & 3)] != 0u) break; __builtin_amdgcn_s_sleep(1); } }
#pragma unroll
        for (int i = 0; i < 3; ++i) {
            const lds_cptr rb = (lds_cptr)lds + rgn + i * 9216 + lane16;
            bf16x8 fa[4];
#pragma unroll
            for (int mt = 0; mt < 4; ++mt) fa[mt] = lds128(rb + mt * 1024);
#pragma unroll
            for (int nt = 0; nt < 5; ++nt) { const bf16x8 fb = lds128(rb + 4096 + nt * 1024);
#pragma unroll
                for (int mt = 0; mt < 4; ++mt) acc[mt][nt] = MFMA16(fb, fa[mt], acc[mt][nt]); }
        }
        if (more) {
            asm volatile("s_waitcnt lgkmcnt(0)" ::: "memory");
            dma3((wid + 4) * 6 + 3);
            asm volatile("s_waitcnt vmcnt(0)" ::: "memory");
            if (lane == 0) ctl[4 + wid] = 1u;
        }
        STAMP(2, 0);
        asm volatile("s_waitcnt vmcnt(0)" : "+v"(og[0]), "+v"(og[1]), "+v"(og[2]), "+v"(og[3]) :: "memory");
#pragma unroll
        for (int mt = 0; mt < 4; ++mt) { const float f1 = bf_round(1.f - og[mt]);
#pragma unroll
            for (int nt = 0; nt < 5; ++nt) acc[mt][nt] *= f1; }
        if (wid == 0) {
            for (int it = 0; it < SPIN_MAX; ++it) {
                if (__all(flag_get(ws + WS_F3 + ((bt >> 2) * 32 + r) * 8) == tag)) break;
                __builtin_amdgcn_s_sleep(2);
            }
        }
        asm volatile("s_barrier" ::: "memory");
        STAMP(2, 1);
        loadA(48 + wid, a[0]); loadA(56 + wid, a[1]);
        a[2][0] = (bf16x8){}; a[2][1] = (bf16x8){}; a[2][2] = (bf16x8){}; a[2][3] = (bf16x8){};
        if (wid < 4) {
#pragma unroll
            for (int mt = 0; mt < 4; ++mt) gld128_sc1_rw(a[2][mt], ap + ((size_t)mt * NSIG + 64 + wid) * 1024, lane16);
        }
        wait9<4>(a[0], b[0]); mma(a[0], b[0]);
        wait9<0>(a[1], b[1]); wait9<0>(a[2], b[2]); mma(a[1], b[1]); mma(a[2], b[2]);
        STAMP(2, 2);
#pragma unroll
        for (int mt = 0; mt < 4; ++mt)
#pragma unroll
            for (int nt = 0; nt < 5; ++nt) *(f32x4*)(lds + (wid * 20 + mt * 5 + nt) * 1024 + lane16) = acc[mt][nt];
        BARL();
#pragma unroll
        for (int i = 0; i < 3; ++i) { const int t = wid + 8 * i;
            if (t < 20) {
                f32x4 sum = *(const f32x4*)(lds + t * 1024 + lane16);
#pragma unroll
                for (int w = 1; w < 8; ++w) sum += *(const f32x4*)(lds + (w * 20 + t) * 1024 + lane16);
                const int m4 = t / 5, nt = t - m4 * 5;
                const int tok2 = bt * 64 + m4 * 16 + (lane & 15);
                *(u32x2*)(out + (size_t)tok2 * DM + ng * 80 + nt * 16 + (lane >> 4) * 4) = (u32x2){cvtpk(sum[0], sum[1]), cvtpk(sum[2], sum[3])};
            } }
        STAMP(2, 3);
    }
}

extern "C" void kernel_launch(void* const* d_in, const int* in_sizes, int n_in, void* d_out, int out_size, void* d_ws, size_t ws_size, hipStream_t stream) {
    const unsigned short* x = (const unsigned short*)d_in[0];
    const unsigned short* wqkv = (const unsigned short*)d_in[1];
    const unsigned short* wo = (const unsigned short*)d_in[2];
    const unsigned short* w1 = (const unsigned short*)d_in[4];
    const unsigned short* w2 = (const unsigned short*)d_in[5];
    const unsigned short* gw = (const unsigned short*)d_in[6];
    (void)hipFuncSetAttribute((const void*)gated_mix_mega, hipFuncAttributeMaxDynamicSharedMemorySize, LDS_ALL);
    gated_mix_mega<<<256, 512, LDS_ALL, stream>>>(x, wqkv, wo, w1, w2, gw, (char*)d_ws, (unsigned short*)d_out);
}
```

```cpp
#include <hip/hip_runtime.h>
#include <hip/hip_bf16.h>
#include <stdint.h>

typedef short bf16x8 __attribute__((ext_vector_type(8)));
typedef short s16x4 __attribute__((ext_vector_type(4)));
typedef float f32x16 __attribute__((ext_vector_type(16)));
typedef float f32x4 __attribute__((ext_vector_type(4)));
typedef float f32x2 __attribute__((ext_vector_type(2)));
typedef unsigned u32x4 __attribute__((ext_vector_type(4)));
typedef unsigned u32x2 __attribute__((ext_vector_type(2)));
typedef __bf16 bf16x2_t __attribute__((ext_vector_type(2)));
#define LAS __attribute__((address_space(3)))
typedef LAS const char* lds_cptr;
#define DI __device__ __forceinline__
#define SBAR() __builtin_amdgcn_sched_barrier(0)
#define PIN(x) asm volatile("" : "+v"(x))
#define MFMA32(a, b, c) __builtin_amdgcn_mfma_f32_32x32x16_bf16(a, b, c, 0, 0, 0)
#define MFMA16(a, b, c) __builtin_amdgcn_mfma_f32_16x16x32_bf16(a, b, c, 0, 0, 0)
#define MFMA8(a, b, c) __builtin_amdgcn_mfma_f32_32x32x8bf16_1k(a, b, c, 0, 0, 0)

constexpr int S = 2048, DM = 640, NH = 32, DH = 20, FF = 1536;
constexpr int NT = S / 32;
constexpr int QTILE_B = 1280;
constexpr int KVTILE_B = 2688;
constexpr int V_OFF = 1280, V_STEP = 704, V_ROWS = 22;
constexpr int NSIG = 68;
constexpr size_t WS_Q = 0;
constexpr size_t WS_KV = WS_Q + (size_t)NH * NT * QTILE_B;
constexpr size_t WS_AF = WS_KV + (size_t)NH * NT * KVTILE_B;
constexpr size_t WS_BF = WS_AF + (size_t)128 * NSIG * 1024;
constexpr size_t WS_KMAX = WS_BF + (size_t)8 * NSIG * 5 * 1024;
constexpr size_t WS_GATE = WS_KMAX + (size_t)160 * 32 * 4;
constexpr size_t WS_F1 = WS_GATE + (size_t)S * 4;
constexpr size_t WS_F2 = WS_F1 + 2048;
constexpr size_t WS_F3 = WS_F2 + 2048;
constexpr size_t WS_EPOCH = WS_F3 + 2048;
constexpr size_t WS_END = WS_EPOCH + 256;
constexpr float QSCALE = 0.22360679774997896f * 1.4426950408889634f;
constexpr unsigned ONES2 = 0x3F803F80u;

DI unsigned cvtpk(float lo, float hi) { f32x2 v = {lo, hi}; bf16x2_t b = __builtin_convertvector(v, bf16x2_t); return __builtin_bit_cast(unsigned, b); }
DI float bf_lo(unsigned u) { return __uint_as_float(u << 16); }
DI float bf_hi(unsigned u) { return __uint_as_float(u & 0xffff0000u); }
DI float bf_round(float x) { return bf_lo(cvtpk(x, x)); }
DI void glds16(const void* sbase, unsigned voff, unsigned lds_base) {
    unsigned sv;
    asm volatile("s_mov_b32 %0, m0\n\ts_mov_b32 m0, %3\n\ts_nop 2\n\tglobal_load_lds_dwordx4 %1, %2\n\ts_mov_b32 m0, %0"
                 : "=&s"(sv) : "v"(voff), "s"(sbase), "s"(lds_base) : "memory");
}
DI void glds16_sc1(const void* sbase, unsigned voff, unsigned lds_base) {
    unsigned sv;
    asm volatile("s_mov_b32 %0, m0\n\ts_mov_b32 m0, %3\n\ts_nop 2\n\tglobal_load_lds_dwordx4 %1, %2 sc1\n\ts_mov_b32 m0, %0"
                 : "=&s"(sv) : "v"(voff), "s"(sbase), "s"(lds_base) : "memory");
}
typedef short v4i16_t __attribute__((ext_vector_type(4)));
DI s16x4 ldstr(lds_cptr p) { return __builtin_bit_cast(s16x4, __builtin_amdgcn_ds_read_tr16_b64_v4i16((LAS v4i16_t*)p)); }
DI bf16x8 lds128(lds_cptr p) { return *(const LAS bf16x8*)p; }
DI s16x4 lds64(lds_cptr p) { return *(const LAS s16x4*)p; }
DI unsigned rfl(unsigned x) { return __builtin_amdgcn_readfirstlane(x); }


DI float rdl(float x, int l) { return __uint_as_float(__builtin_amdgcn_readlane(__float_as_uint(x), l)); }
typedef __amdgpu_buffer_rsrc_t rsrc_t;
typedef unsigned long long u64;
DI rsrc_t ws_rsrc(char* ws) { return __builtin_amdgcn_make_buffer_rsrc(ws, 0, (int)WS_END, 0x00020000); }
DI void st16(rsrc_t rs, unsigned off, u32x4 v) { __builtin_amdgcn_raw_buffer_store_b128(v, rs, off, 0, 16); }
DI void st8(rsrc_t rs, unsigned off, u32x2 v) { __builtin_amdgcn_raw_buffer_store_b64(v, rs, off, 0, 16); }
DI void st4f(char* p, float v) { __hip_atomic_store((float*)p, v, __ATOMIC_RELAXED, __HIP_MEMORY_SCOPE_AGENT); }
DI void flag_set(char* p, u64 tag) { __hip_atomic_store((u64*)p, tag, __ATOMIC_RELAXED, __HIP_MEMORY_SCOPE_AGENT); }
DI u64 flag_get(const char* p) { return __hip_atomic_load((const u64*)p, __ATOMIC_RELAXED, __HIP_MEMORY_SCOPE_AGENT); }
template <int CTRL> DI float dpp_max(float x) { const float y = __uint_as_float(__builtin_amdgcn_update_dpp(0u, __float_as_uint(x), CTRL, 0xf, 0xf, true)); return fmaxf(x, y); }
template <int CTRL> DI float dpp_add(float x) { const float y = __uint_as_float(__builtin_amdgcn_update_dpp(0u, __float_as_uint(x), CTRL, 0xf, 0xf, true)); return x + y; }
DI void gld128(bf16x8& d, const char* sbase, unsigned voff) { asm volatile("s_nop 4\n\tglobal_load_dwordx4 %0, %1, %2" : "=v"(d) : "v"(voff), "s"(sbase) : "memory"); }
DI void gld128_sc1(bf16x8& d, const char* sbase, unsigned voff) { asm volatile("s_nop 4\n\tglobal_load_dwordx4 %0, %1, %2 sc1" : "=v"(d) : "v"(voff), "s"(sbase) : "memory"); }
DI void gld128_rw(bf16x8& d, const char* sbase, unsigned voff) { asm volatile("s_nop 4\n\tglobal_load_dwordx4 %0, %1, %2" : "+v"(d) : "v"(voff), "s"(sbase) : "memory"); }
DI void gld128_sc1_rw(bf16x8& d, const char* sbase, unsigned voff) { asm volatile("s_nop 4\n\tglobal_load_dwordx4 %0, %1, %2 sc1" : "+v"(d) : "v"(voff), "s"(sbase) : "memory"); }
template <int N> DI void wait9(bf16x8 (&a)[4], bf16x8 (&b)[5]) {
    asm volatile("s_waitcnt vmcnt(%9)" : "+v"(a[0]), "+v"(a[1]), "+v"(a[2]), "+v"(a[3]), "+v"(b[0]), "+v"(b[1]), "+v"(b[2]), "+v"(b[3]), "+v"(b[4]) : "i"(N) : "memory");
}
#define WAITB(n) asm volatile("s_waitcnt vmcnt(" #n ")\n\ts_barrier" ::: "memory")
#define BARL() asm volatile("s_waitcnt lgkmcnt(0)\n\ts_barrier" ::: "memory")

constexpr int P1_STG = 32768;
constexpr int RT_OFF = 131072;
constexpr int P2_STG = 28672;
constexpr int CTL_OFF = 163584;
DI constexpr int p2_base(int s) { return CTL_OFF - (s + 1) * P2_STG; }
constexpr int K2_BIG = 16 * KVTILE_B;
constexpr int EP_OFF = 151552;
constexpr int LDS_ALL = 163840;

template <bool N96>
struct GDma {
    static constexpr int NW = N96 ? 3 : 4;
    const char* xbase; const char* wbase; int ldw2, k0; unsigned lds0, xoff0, xoff1, woff[3];
    DI int kact(int kc) const { kc += k0; return kc >= 10 ? kc - 10 : kc; }
    DI static unsigned sbase(int st) { return N96 ? (unsigned)(CTL_OFF - (st + 1) * P2_STG) : (unsigned)(st * P1_STG); }
    DI void init(const char* xb, const char* wb, int ld, char* lds, int lane, int k0_) {
        xbase = xb; wbase = wb; ldw2 = ld; k0 = k0_; lds0 = (unsigned)(uintptr_t)lds;
        const int rr = lane >> 3;
        xoff0 = rr * 1280 + (((lane & 7) ^ ((lane >> 4) & 7)) << 4); xoff1 = rr * 1280 + (((lane & 7) ^ ((4 + (lane >> 4)) & 7)) << 4);
        if (N96) {
#pragma unroll
            for (int p = 0; p < 3; ++p) { const int o = 1024 * p + 16 * lane, row = (o * 21846) >> 22, col = o - row * 192; woff[p] = row * ldw2 + col; }
        } else woff[0] = (lane >> 4) * ldw2 + ((((lane & 15) ^ ((lane >> 4) << 2))) << 4);
    }
    DI void xp(int kc, int st, int j, int par) const { glds16(xbase + (size_t)j * 8 * 1280 + kc * 128, par ? xoff1 : xoff0, lds0 + sbase(st) + j * 1024); }
    DI void wp(int kc, int st, int lw, int jj) const {
        if (N96) glds16(wbase + (size_t)(kc * 64 + 16 * lw) * ldw2, woff[jj], lds0 + sbase(st) + 16384 + (3 * lw + jj) * 1024);
        else glds16(wbase + (size_t)(kc * 64 + 16 * lw + 4 * jj) * ldw2, woff[0], lds0 + sbase(st) + 16384 + (4 * lw + jj) * 1024);
    }
    DI void issue(int kc_, int st, int lw) const {
        const int kc = kact(kc_);
#pragma unroll
        for (int jj = 0; jj < NW; ++jj) wp(kc, st, lw, jj);
#pragma unroll
        for (int jj = 0; jj < 4; ++jj) xp(kc, st, 4 * lw + jj, jj & 1);
    }
};

DI void rt_load(const unsigned short* w2, const unsigned short* wo, int u, unsigned ldsb, int piece, int lane) {
    const int sig = u >> 2, s = (u >> 1) & 1, cb = u & 1;
    const char* src = (sig < 48 ? (const char*)w2 + (size_t)sig * 32 * 1280 : (const char*)wo + (size_t)(sig - 48) * 32 * 1280) + s * 16 * 1280 + cb * 640;
    const int o = piece * 1024 + lane * 16, row = (o * 6554) >> 22;
    glds16(src, row * 1280 + (o - row * 640), ldsb + piece * 1024);
}
DI void rt_store(rsrc_t rs, int u, const char* ldsb, int i, int lane) {
    const int sig = u >> 2, s = (u >> 1) & 1, cb = u & 1;
    const int nl = 2 * i + (lane >> 5), hh = (lane >> 4) & 1, q = (lane & 15) >> 2, p = lane & 3;
    lds_cptr a = (lds_cptr)ldsb + (4 * hh + q) * 640 + (16 * nl + 4 * p) * 2;
    const s16x4 lo = ldstr(a), hi = ldstr(a + 8 * 640);
    const bf16x8 f = __builtin_shufflevector(lo, hi, 0, 1, 2, 3, 4, 5, 6, 7);
    const int nu = cb * 20 + nl, ng = (nu * 205) >> 10;
    st16(rs, (unsigned)WS_BF + (unsigned)((ng * NSIG + sig) * 5 + (nu - 5 * ng)) * 1024 + (s * 32 + (lane & 31)) * 16, __builtin_bit_cast(u32x4, f));
}

template <bool N96, bool VT, int MT, int NTL, class HOOK>
DI void gemm_loop(f32x16 (&acc)[MT][NTL], char* lds, int wid, int lane, HOOK hook) {
    const int h = lane >> 5, r = lane & 31;
    constexpr int NST = N96 ? 3 : 4;
    unsigned xa[4];
    { const int row = (N96 ? wid * 32 : (wid >> 1) * 64) + r; const int sw = (row >> 1) & 7;
#pragma unroll
      for (int ks = 0; ks < 4; ++ks) xa[ks] = row * 128 + (((2 * ks + h) ^ sw) << 4); }
    unsigned wa[2];
    { const int blk = (lane >> 4) & 1, q = (lane & 15) >> 2, p = lane & 3;
      if (N96) wa[0] = wa[1] = 16384 + (8 * h + q) * 192 + blk * 32 + p * 8;
      else {
#pragma unroll
          for (int nt = 0; nt < 2; ++nt) { const int ch = (wid & 1) * 8 + nt * 4 + 2 * blk + (p >> 1); wa[nt] = 16384 + (8 * h + q) * 256 + ((ch ^ (q << 2)) << 4) + 8 * (p & 1); } } }
    bf16x8 xf[2][MT], wf[2][NTL];
    auto rdw = [&](int buf, int nt, lds_cptr sb, int ks) {
        lds_cptr a = N96 ? sb + wa[0] + ks * 16 * 192 + nt * 64 : sb + wa[nt & 1] + ks * 4096;
        const s16x4 lo = ldstr(a), hi = ldstr(a + (N96 ? 4 * 192 : 1024));
        wf[buf][nt] = __builtin_shufflevector(lo, hi, 0, 1, 2, 3, 4, 5, 6, 7);
    };
    auto rdx = [&](int buf, int mt, lds_cptr sb, int ks) { xf[buf][mt] = lds128(sb + xa[ks] + mt * 4096); };
    auto mm = [&](int buf, int mt, int nt) { acc[mt][nt] = VT ? MFMA32(xf[buf][mt], wf[buf][nt], acc[mt][nt]) : MFMA32(wf[buf][nt], xf[buf][mt], acc[mt][nt]); };
    { lds_cptr sb = (lds_cptr)lds + GDma<N96>::sbase(0); rdw(0, 0, sb, 0); rdx(0, 0, sb, 0); rdw(0, 1, sb, 0); if (N96) rdw(0, 2 % NTL, sb, 0); else rdx(0, 1 % MT, sb, 0); }
    int st = 0;
#define G_KSTEP(ks, NEXT) do { const int b = (ks) & 1, n = b ^ 1; lds_cptr np = (ks) < 3 ? sb : sn; const int nk = ((ks) + 1) & 3; \
        SBAR(); \
        if constexpr (N96) { \
            mm(b, 0, 0); SBAR(); if (NEXT) { rdw(n, 0, np, nk); rdx(n, 0, np, nk); } SBAR(); \
            mm(b, 0, 1); SBAR(); if (NEXT) { rdw(n, 1, np, nk); } SBAR(); \
            mm(b, 0, 2 % NTL); SBAR(); if (NEXT) { rdw(n, 2 % NTL, np, nk); } SBAR(); \
        } else { \
            mm(b, 0, 0); SBAR(); if (NEXT) { rdw(n, 0, np, nk); } SBAR(); \
            mm(b, 0, 1); SBAR(); if (NEXT) { rdx(n, 0, np, nk); } SBAR(); \
            mm(b, 1 % MT, 0); SBAR(); if (NEXT) { rdw(n, 1, np, nk); } SBAR(); \
            mm(b, 1 % MT, 1); SBAR(); if (NEXT) { rdx(n, 1 % MT, np, nk); } SBAR(); \
        } } while (0)
#pragma unroll 1
    for (int kc = 0; kc < 9; ++kc) {
        lds_cptr sb = (lds_cptr)lds + GDma<N96>::sbase(st);
        st = st == NST - 1 ? 0 : st + 1;
        lds_cptr sn = (lds_cptr)lds + GDma<N96>::sbase(st);
        G_KSTEP(0, true); G_KSTEP(1, true); G_KSTEP(2, true);
        hook(kc, true);
        BARL();
        hook(kc, false);
        G_KSTEP(3, true);
    }
    { lds_cptr sb = (lds_cptr)lds + GDma<N96>::sbase(st); lds_cptr sn = sb;
      G_KSTEP(0, true); G_KSTEP(1, true); G_KSTEP(2, true); G_KSTEP(3, false); }
}

DI float swap32_add(float x) { auto rr = __builtin_amdgcn_permlane32_swap(__float_as_uint(x), __float_as_uint(x), false, false); return __uint_as_float(rr[0]) + __uint_as_float(rr[1]); }
DI float swap32_max(float x) { auto rr = __builtin_amdgcn_permlane32_swap(__float_as_uint(x), __float_as_uint(x), false, false); return fmaxf(__uint_as_float(rr[0]), __uint_as_float(rr[1])); }
DI s16x4 make_qb(unsigned q0, unsigned q1, float M, int h) {
    const float mh = bf_hi(__float_as_uint(M)), r1 = M - mh, mm = bf_hi(__float_as_uint(r1)), r2 = r1 - mm, ml = bf_hi(__float_as_uint(r2));
    u32x2 w;
    w[0] = h ? ((__float_as_uint(mh) >> 16) | (__float_as_uint(mm) & 0xffff0000u)) ^ 0x80008000u : q0;
    w[1] = h ? (__float_as_uint(ml) >> 16) ^ 0x8000u : q1;
    return __builtin_bit_cast(s16x4, w);
}

DI void attn_fallback(f32x16& o, const char* kv, bf16x8 qa, unsigned q0, unsigned q1, int lane, int kboff, int voff) {
    const int h = lane >> 5;
    const f32x16 zero = {};
    float m = -INFINITY;
    const s16x4 qb0 = make_qb(q0, q1, 0.f, h);
    for (int t = 0; t < NT; ++t) {
        const char* tb = kv + t * KVTILE_B;
        const bf16x8 ka = *(const bf16x8*)(tb + lane * 16); const s16x4 kb = *(const s16x4*)(tb + kboff);
        f32x16 s = MFMA32(ka, qa, zero); s = MFMA8(kb, qb0, s);
#pragma unroll
        for (int i = 0; i < 16; ++i) m = fmaxf(m, s[i]);
    }
    m = swap32_max(m);
    const s16x4 qb = make_qb(q0, q1, m, h);
    o = zero;
    for (int t = 0; t < NT; ++t) {
        const char* tb = kv + t * KVTILE_B;
        const bf16x8 ka = *(const bf16x8*)(tb + lane * 16); const s16x4 kb = *(const s16x4*)(tb + kboff);
        const bf16x8 va = *(const bf16x8*)(tb + voff), vb = *(const bf16x8*)(tb + voff + V_STEP);
        f32x16 s = MFMA32(ka, qa, zero); s = MFMA8(kb, qb, s);
        u32x4 p0, p1;
#pragma unroll
        for (int i = 0; i < 4; ++i) { p0[i] = cvtpk(__builtin_amdgcn_exp2f(s[2 * i]), __builtin_amdgcn_exp2f(s[2 * i + 1])); p1[i] = cvtpk(__builtin_amdgcn_exp2f(s[8 + 2 * i]), __builtin_amdgcn_exp2f(s[9 + 2 * i])); }
        o = MFMA32(va, __builtin_bit_cast(bf16x8, p0), o);
        o = MFMA32(vb, __builtin_bit_cast(bf16x8, p1), o);
    }
}


constexpr int SPIN_MAX = 1 << 18;
__global__ __launch_bounds__(512, 2) void gated_mix_mega(const unsigned short* __restrict__ x, const unsigned short* __restrict__ wqkv, const unsigned short* __restrict__ wo, const unsigned short* __restrict__ w1,
                                                         const unsigned short* __restrict__ w2, const unsigned short* __restrict__ gw, char* __restrict__ ws, unsigned short* __restrict__ out) {
    extern __shared__ __attribute__((aligned(16))) char lds[];
    const int tid = threadIdx.x, lane = tid & 63, wid = rfl(tid >> 6);
    const int bid = blockIdx.x, xcd = bid & 7, slot = bid >> 3;
    const int h = lane >> 5, r = lane & 31;
    const rsrc_t rs = ws_rsrc(ws);
    const unsigned lds0 = (unsigned)(uintptr_t)lds, lane16 = lane * 16;
    volatile LAS unsigned* ctl = (volatile LAS unsigned*)(lds + CTL_OFF);
    const int rt = 4 * (xcd >> 1) + (slot & 3), ci = slot >> 2, ct = 8 * (xcd & 1) + ci;
    const bool tile1 = ct < 15;
    const char* xbase = (const char*)x + (size_t)rt * 128 * 1280;
        const int k0 = 0;
    const int head = xcd * 4 + (slot & 3), qblk = slot >> 2;
    const char* kv = ws + WS_KV + (size_t)head * NT * KVTILE_B;
    const int Tq = qblk * 8 + wid;
    const char* qt = ws + WS_Q + ((size_t)head * NT + Tq) * QTILE_B;
    bf16x8 qa; u32x2 qbr; f32x4 km4; float gate;
    auto q_loads = [&]() {
        asm volatile("s_nop 4\n\tglobal_load_dwordx4 %0, %1, %2" : "=v"(qa) : "v"(lane16), "s"(qt) : "memory");
        asm volatile("s_nop 4\n\tglobal_load_dwordx2 %0, %1, %2" : "=v"(qbr) : "v"((unsigned)(1024 + r * 8)), "s"(qt) : "memory");
        asm volatile("s_nop 4\n\tglobal_load_dwordx4 %0, %1, %2" : "=v"(km4) : "v"((unsigned)((lane < 40 ? lane : 0) * 16)), "s"(ws + WS_KMAX + (size_t)head * 640) : "memory");
        asm volatile("s_nop 4\n\tglobal_load_dword %0, %1, %2" : "=v"(gate) : "v"((unsigned)((Tq * 32 + r) * 4)), "s"(ws + WS_GATE) : "memory");
    };
    auto wait_seen = [&]() { for (int it = 0; it < SPIN_MAX; ++it) { if (ctl[1] != 0u) break; __builtin_amdgcn_s_sleep(1); } };
    auto gates = [&]() {
        const int aidx = (xcd >> 1) * 4 + (slot & 3);
        const u32x4* gr = (const u32x4*)gw + (lane & 7);
#pragma unroll 1
        for (int ps = 0; ps < 2; ++ps) {
            const int tok = aidx * 128 + wid * 16 + ps * 8 + (lane >> 3);
            const u32x4* xr = (const u32x4*)(x + (size_t)tok * DM) + (lane & 7);
            float sm = 0.f;
#pragma unroll
            for (int i = 0; i < 10; ++i) { const u32x4 xv = xr[i * 8], gv = gr[i * 8];
#pragma unroll
                for (int k = 0; k < 4; ++k) { const unsigned a_ = xv[k], b_ = gv[k]; sm = __builtin_amdgcn_fdot2_f32_bf16(__builtin_bit_cast(bf16x2_t, a_), __builtin_bit_cast(bf16x2_t, b_), sm, false); } }
            sm = dpp_add<0xB1>(sm); sm = dpp_add<0x4E>(sm); sm = dpp_add<0x141>(sm);
            const float lg = bf_round(sm);
            const float g = bf_round(__builtin_amdgcn_rcpf(1.f + __builtin_amdgcn_exp2f(lg * -1.4426950408889634f)));
            if ((lane & 7) == 0) st4f(ws + WS_GATE + tok * 4, g);
        }
    };
    GDma<true> d2; d2.init(xbase, (const char*)w1 + ct * 192, FF * 2, lds, lane, k0);
    u64 pc0; asm volatile("s_getpc_b64 %0" : "=s"(pc0));
    if (wid < 4) {
        GDma<false> d1; d1.init(xbase, (const char*)wqkv + ct * 256, 3 * DM * 2, lds, lane, k0);
        if (tile1) {
#pragma unroll
            for (int jj = 0; jj < 4; ++jj) d1.xp(d1.kact(0), 0, 4 * wid + jj, jj & 1);
        }
        if (tid == 0) { ctl[0] = 0u; ctl[1] = 0u; }
        {
#pragma unroll
            for (int i = 0; i < 7; ++i) {
                int u = (bid * 4 + wid) * 7 + i; if (u > 6879) u = 6879;
                const char* src = u < 2560 ? (const char*)x + (size_t)u * 1024 : u < 4960 ? (const char*)wqkv + (size_t)(u - 2560) * 1024 : (const char*)w1 + (size_t)(u - 4960) * 1024;
                glds16(src, lane16, lds0 + 152 * 1024);
            }
        }
        if (tile1) asm volatile("s_waitcnt vmcnt(7) lgkmcnt(0)\n\ts_barrier" ::: "memory");
        if (slot * 4 + wid < 42) glds16((const char*)((pc0 + 15ull) & ~15ull) + (slot * 4 + wid) * 1024, lane16, lds0 + 152 * 1024);
        for (int pc = wid; pc < 10; pc += 4) rt_load(w2, wo, bid, lds0 + RT_OFF, pc, lane);
        if (!tile1) for (int pc = wid; pc < 10; pc += 4) rt_load(w2, wo, 256 + (xcd >> 1) * 4 + (slot & 3), lds0 + RT_OFF + 10240, pc, lane);
        if (wid == 0) glds16_sc1(ws + WS_EPOCH, lane < 1 ? 0u : 0u, lds0 + EP_OFF);
        auto tag_ready = [&]() {
            if (tid == 0) { const u64 tg = *(const volatile LAS u64*)(lds + EP_OFF) + 1ull; ctl[2] = (unsigned)tg; ctl[3] = (unsigned)(tg >> 32); }
        };
        if (!tile1) { asm volatile("s_waitcnt vmcnt(0)" ::: "memory"); tag_ready(); asm volatile("s_waitcnt lgkmcnt(0)\n\ts_barrier" ::: "memory"); }
        if (tile1) {
            const int wm = wid >> 1, wn = wid & 1;
            f32x16 acc[2][2] = {};
            const char* w1w = (const char*)w1 + (size_t)(slot * 20) * 3072 + (xcd & 1) * 1536;
            auto nohook = [&](int kc, bool before) {
                if (kc == 1 && before) { asm volatile("s_waitcnt vmcnt(0)" ::: "memory"); tag_ready(); }
                if (!before && kc < 4) {
#pragma unroll
                    for (int e = 0; e < 2; ++e) { const int u = kc * 8 + wid * 2 + e;
                        if (u < 30) { const int o = u * 1024 + (int)lane16, row = o / 1536; glds16(w1w, (unsigned)(row * 3072 + (o - row * 1536)), lds0 + 152 * 1024); } } }
                if (kc == 5 && before) asm volatile("s_waitcnt vmcnt(0)" ::: "memory");
            };
            if (ct >= 10) {
                gemm_loop<false, true, 2, 2>(acc, lds, wid, lane, nohook);
#pragma unroll
                for (int nt = 0; nt < 2; ++nt) {
                    const int phi = (ct - 10) * 128 + wn * 64 + nt * 32 + r;
                    const int hd = (phi * 3277) >> 16, d = phi - hd * 20;
                    const unsigned dst = (unsigned)WS_KV + (unsigned)(hd * NT + rt * 4 + wm * 2) * KVTILE_B + V_OFF + (h * V_ROWS + d) * 16;
#pragma unroll
                    for (int mt = 0; mt < 2; ++mt)
#pragma unroll
                        for (int s = 0; s < 2; ++s) {
                            const f32x16& a = acc[mt][nt];
                            const u32x4 w = {cvtpk(a[8 * s], a[8 * s + 1]), cvtpk(a[8 * s + 2], a[8 * s + 3]), cvtpk(a[8 * s + 4], a[8 * s + 5]), cvtpk(a[8 * s + 6], a[8 * s + 7])};
                            st16(rs, dst + mt * KVTILE_B + s * V_STEP, w);
                        }
                }
                BARL();
            } else {
                gemm_loop<false, false, 2, 2>(acc, lds, wid, lane, nohook);
                const bool isQ = ct < 5;
                const float sc = isQ ? QSCALE : 1.f;
                const unsigned img = (unsigned)(isQ ? WS_Q : WS_KV), tileb = isQ ? QTILE_B : KVTILE_B;
                const int G = (isQ ? ct : ct - 5) * 32 + wn * 16 + h;
                u32x2 P[2][8]; float nrm[8];
#pragma unroll
                for (int p = 0; p < 8; ++p) {
                    const int nt = p >> 2, g = p & 3; float m = 0.f;
#pragma unroll
                    for (int mt = 0; mt < 2; ++mt) {
                        const f32x16& a = acc[mt][nt];
                        P[mt][p] = (u32x2){cvtpk(a[4 * g] * sc, a[4 * g + 1] * sc), cvtpk(a[4 * g + 2] * sc, a[4 * g + 3] * sc)};
                        m = fmaxf(m, __builtin_fmaf(a[4 * g], a[4 * g], a[4 * g + 1] * a[4 * g + 1]) + __builtin_fmaf(a[4 * g + 2], a[4 * g + 2], a[4 * g + 3] * a[4 * g + 3]));
                    }
                    nrm[p] = m;
                }
#pragma unroll
                for (int p = 0; p < 8; ++p) {
                    const int gam = G + 2 * p, hd = (gam * 205) >> 10, dg = gam - 5 * hd;
                    const unsigned base = img + (unsigned)(hd * NT + rt * 4 + wm * 2) * tileb;
#pragma unroll
                    for (int mt = 0; mt < 2; ++mt) {
                        const unsigned bm = base + mt * tileb;
                        if (dg < 2) {
                            if (p < 7) st16(rs, bm + dg * 512 + r * 16, (u32x4){P[mt][p][0], P[mt][p][1], P[mt][(p + 1) & 7][0], P[mt][(p + 1) & 7][1]});
                            else st8(rs, bm + dg * 512 + r * 16, P[mt][p]);
                        } else if (dg == 4) st8(rs, bm + 1024 + r * 8, P[mt][p]);
                        else if (p == 0) st8(rs, bm + (dg - 2) * 512 + r * 16 + 8, P[mt][p]);
                    }
                }
                if (!isQ) {
#pragma unroll
                    for (int i = 0; i < 8; ++i) { float m = nrm[i]; m = dpp_max<0xB1>(m); m = dpp_max<0x4E>(m); m = dpp_max<0x141>(m); m = dpp_max<0x140>(m);
                        auto rr = __builtin_amdgcn_permlane16_swap(__float_as_uint(m), __float_as_uint(m), false, false); nrm[i] = fmaxf(__uint_as_float(rr[0]), __uint_as_float(rr[1])); }
                    float mine = nrm[0];
#pragma unroll
                    for (int i = 1; i < 8; ++i) mine = r == i ? nrm[i] : mine;
                    if (r < 8) st4f(ws + WS_KMAX + (size_t)((G + 2 * r) * 32 + rt * 2 + wm) * 4, mine);
                }
                BARL();
            }
        } else { gates(); BARL(); }
        BARL();
        f32x16 acc2[1][3] = {};
        auto hook2 = [&](int kc, bool before) {
            if (kc == 2) { if (before) asm volatile("s_waitcnt vmcnt(0)" ::: "memory");
                           else if (tid == 0) flag_set(ws + WS_F1 + bid * 8, ((u64)ctl[3] << 32) | ctl[2]); }
        };
        gemm_loop<true, false, 1, 3>(acc2, lds, wid, lane, hook2);
        wait_seen();
        q_loads();
        {   const int tok = rt * 128 + wid * 32 + r, rho = tok >> 4;
#pragma unroll
            for (int nt = 0; nt < 3; ++nt) {
                const unsigned dst = (unsigned)WS_AF + (unsigned)(rho * NSIG + ct * 3 + nt) * 1024 + (h * 16 + (r & 15)) * 16;
                float sv[16];
#pragma unroll
                for (int i = 0; i < 16; ++i) { const float v = acc2[0][nt][i]; sv[i] = v * __builtin_amdgcn_rcpf(1.f + __builtin_amdgcn_exp2f(v * -1.4426950408889634f)); }
#pragma unroll
                for (int s = 0; s < 2; ++s) {
                    const u32x4 w = {cvtpk(sv[8 * s], sv[8 * s + 1]), cvtpk(sv[8 * s + 2], sv[8 * s + 3]), cvtpk(sv[8 * s + 4], sv[8 * s + 5]), cvtpk(sv[8 * s + 6], sv[8 * s + 7])};
                    st16(rs, dst + s * 512, w);
                }
            }
        }
        asm volatile("s_waitcnt vmcnt(6)" : "+v"(qa), "+v"(qbr), "+v"(km4), "+v"(gate) :: "memory");
    } else {
        const int lw = wid - 4;
        auto side_stores = [&]() {
            for (int i = lw; i < 10; i += 4) rt_store(rs, bid, lds + RT_OFF, i, lane);
            if (!tile1) for (int i = lw; i < 10; i += 4) rt_store(rs, 256 + (xcd >> 1) * 4 + (slot & 3), lds + RT_OFF + 10240, i, lane);
            if (lw == 0) { const int i = bid * 64 + lane;
                const unsigned f = (i & 1) ? 0u : ONES2; st16(rs, (unsigned)WS_KV + (unsigned)(i >> 3) * KVTILE_B + V_OFF + ((i >> 2) & 1) * V_STEP + (((i >> 1) & 1) * V_ROWS + 20 + (i & 1)) * 16, (u32x4){f, f, f, f}); }
        };
        if (tile1) {
            GDma<false> d1; d1.init(xbase, (const char*)wqkv + ct * 256, 3 * DM * 2, lds, lane, k0);
#pragma unroll
            for (int jj = 0; jj < 4; ++jj) d1.wp(d1.kact(0), 0, lw, jj);
            d1.issue(1, 1, lw);
            WAITB(8);
            d1.issue(2, 2, lw); d1.issue(3, 3, lw);
            for (int kc = 0; kc < 9; ++kc) {
                if (kc < 7) WAITB(16); else if (kc == 7) WAITB(15); else WAITB(14);
                if (kc + 4 < 10) d1.issue(kc + 4, kc & 3, lw);
                if (kc == 4) side_stores();
                if (kc >= 6) d2.issue(kc - 6, kc - 6, lw);
            }
            asm volatile("s_barrier" ::: "memory");
        } else {
            asm volatile("s_barrier" ::: "memory");
            side_stores(); gates();
            BARL();
            d2.issue(0, 0, lw); d2.issue(1, 1, lw); d2.issue(2, 2, lw);
        }
        WAITB(14);
        { int st = 0;
          for (int kc = 0; kc < 9; ++kc) {
              if (kc < 8) WAITB(7); else WAITB(0);
              if (kc + 3 < 10) d2.issue(kc + 3, st, lw);
              st = st == 2 ? 0 : st + 1;
          } }
        if (lw == 0) {
            const u64 tag = ((u64)ctl[3] << 32) | ctl[2];
            for (int it = 0; it < SPIN_MAX; ++it) {
                bool ok = true;
#pragma unroll
                for (int i = 0; i < 4; ++i) ok &= flag_get(ws + WS_F1 + (lane + 64 * i) * 8) == tag;
                if (__all(ok)) break;
                __builtin_amdgcn_s_sleep(2);
            }
            __builtin_amdgcn_fence(__ATOMIC_ACQUIRE, "agent");
            asm volatile("s_waitcnt vmcnt(0)" ::: "memory");
            if (lane == 0) { ctl[1] = 1u; if (bid == 0) flag_set(ws + WS_EPOCH, tag); }
        } else wait_seen();
        q_loads();
        for (int j = 0; j < 21; ++j) glds16(kv + (size_t)(lw + 4 * j) * 1024, lane16, lds0 + (lw + 4 * j) * 1024);
        asm volatile("s_waitcnt vmcnt(21)" : "+v"(qa), "+v"(qbr), "+v"(km4), "+v"(gate) :: "memory");
    }
    float km = fmaxf(fmaxf(km4[0], km4[1]), fmaxf(km4[2], km4[3]));
    km = dpp_max<0xB1>(km); km = dpp_max<0x4E>(km); km = dpp_max<0x141>(km);
    km = __builtin_amdgcn_sqrtf(km);
    float M;
    { const float c0 = rdl(km, 0), c1 = rdl(km, 8), c2 = rdl(km, 16), c3 = rdl(km, 24), c4 = rdl(km, 32);
      const u32x4 qw = __builtin_bit_cast(u32x4, qa);
      float n0, n1, n2;
      { const float a = bf_lo(qw[0]), b = bf_hi(qw[0]), c = bf_lo(qw[1]), d = bf_hi(qw[1]); n0 = (a * a + b * b) + (c * c + d * d); }
      { const float a = bf_lo(qw[2]), b = bf_hi(qw[2]), c = bf_lo(qw[3]), d = bf_hi(qw[3]); n1 = (a * a + b * b) + (c * c + d * d); }
      { const unsigned q0 = qbr[0], q1 = qbr[1]; const float a = bf_lo(q0), b = bf_hi(q0), c = bf_lo(q1), d = bf_hi(q1); n2 = (a * a + b * b) + (c * c + d * d); }
      float part = __builtin_amdgcn_sqrtf(n0) * (h ? c1 : c0) + __builtin_amdgcn_sqrtf(n1) * (h ? c3 : c2);
      if (!h) part += __builtin_amdgcn_sqrtf(n2) * c4;
      M = swap32_add(part) * 1.01f; }
    const s16x4 qb = make_qb(qbr[0], qbr[1], M, h);
    const int vrow = r <= 20 ? r : (r < 28 ? 21 : r - 12);
    const int voff = V_OFF + (h * V_ROWS + vrow) * 16, kboff = h ? V_OFF + 20 * 16 : 1024 + r * 8;
    lds_cptr kaC = (lds_cptr)lds + lane * 16, kbC = (lds_cptr)lds + kboff, vC = (lds_cptr)lds + voff;
    lds_cptr kaN = kaC + K2_BIG, kbN = kbC + K2_BIG, vN = vC + K2_BIG;
    const f32x16 zero = {};
    f32x16 o = {}, sA, sB;
    bf16x8 ka, va, vb; s16x4 kb;
    u32x4 plo, phi;
    if (wid >= 4) asm volatile("s_waitcnt vmcnt(15)" ::: "memory");
    asm volatile("s_barrier" ::: "memory");
    ka = lds128(kaC); kb = lds64(kbC);
    sA = MFMA32(ka, qa, zero); sA = MFMA8(kb, qb, sA);
    ka = lds128(kaC + KVTILE_B); kb = lds64(kbC + KVTILE_B);
    va = lds128(vC);
    vb = (bf16x8){}; phi = (u32x4){};
    f32x4 e3 = {};
    const int ng = (xcd & 1) * 4 + (slot & 3), bt = (xcd >> 1) * 8 + (slot >> 2);
    const char* ap = ws + WS_AF + (size_t)bt * 4 * NSIG * 1024;
    const char* bp = ws + WS_BF + (size_t)ng * NSIG * 5 * 1024;
        const int rot = ((slot >> 2) + 3 * (slot & 3)) % 6;
    auto hs = [&](int i) { int st = i + rot; if (st >= 6) st -= 6; return wid * 6 + st; };
    bf16x8 a[3][4], b[3][5];
    auto ldA1 = [&](int sg, int mt, bf16x8& d) { gld128_sc1(d, ap + ((size_t)mt * NSIG + sg) * 1024, lane16); };
    auto ldB1 = [&](int sg, int nt, bf16x8& d) { gld128(d, bp + sg * 5120 + nt * 1024, lane16); };
    const u64 tag = ((u64)ctl[3] << 32) | ctl[2];
#define EXP4(X, i) do { X[i] = __builtin_amdgcn_exp2f(X[i]); X[i + 1] = __builtin_amdgcn_exp2f(X[i + 1]); X[i + 2] = __builtin_amdgcn_exp2f(X[i + 2]); X[i + 3] = __builtin_amdgcn_exp2f(X[i + 3]); } while (0)
#define KADDR(A, i) (((i) < 16) ? (A##C + (i) * KVTILE_B) : (A##N + ((i) - 16) * KVTILE_B))
#define STEP(C, N, i, X0, X1) do { \
        bf16x8 ka2, va2, vb2; s16x4 kb2; \
        N = MFMA32(ka, qa, zero); SBAR(); \
        ka2 = lds128(KADDR(ka, (i) + 2)); \
        phi[2] = cvtpk(e3[0], e3[1]); phi[3] = cvtpk(e3[2], e3[3]); PIN(phi); SBAR(); EXP4(C, 0); PIN(C); SBAR(); \
        o = MFMA32(vb, __builtin_bit_cast(bf16x8, phi), o); SBAR(); \
        X0; \
        vb2 = lds128(vC + (i) * KVTILE_B + V_STEP); \
        plo[0] = cvtpk(C[0], C[1]); plo[1] = cvtpk(C[2], C[3]); PIN(plo); SBAR(); EXP4(C, 4); PIN(C); SBAR(); \
        N = MFMA8(kb, qb, N); SBAR(); \
        kb2 = lds64(KADDR(kb, (i) + 2)); \
        plo[2] = cvtpk(C[4], C[5]); plo[3] = cvtpk(C[6], C[7]); PIN(plo); SBAR(); EXP4(C, 8); PIN(C); SBAR(); \
        o = MFMA32(va, __builtin_bit_cast(bf16x8, plo), o); SBAR(); \
        X1; \
        va2 = lds128(KADDR(v, (i) + 1)); \
        phi[0] = cvtpk(C[8], C[9]); phi[1] = cvtpk(C[10], C[11]); PIN(phi); SBAR(); \
        e3[0] = __builtin_amdgcn_exp2f(C[12]); e3[1] = __builtin_amdgcn_exp2f(C[13]); e3[2] = __builtin_amdgcn_exp2f(C[14]); e3[3] = __builtin_amdgcn_exp2f(C[15]); PIN(e3); SBAR(); \
        ka = ka2; kb = kb2; va = va2; vb = vb2; \
    } while (0)
#define PIECE(j) do { if (8 * (j) + 7 < 42 || 8 * (j) + wid < 42) glds16(src + (j) * 8192, lane16, dst + (j) * 8192); } while (0)
    const char* src = kv + (84 + wid) * 1024; unsigned dst = lds0 + wid * 1024 + 2 * K2_BIG;
#pragma unroll 1
    for (int c = 0; c < 3; ++c) {
        STEP(sA, sB, 0, , ); STEP(sB, sA, 1, , ); STEP(sA, sB, 2, , ); STEP(sB, sA, 3, , ); STEP(sA, sB, 4, , );
        if (c == 0) { if (wid >= 4) asm volatile("s_waitcnt vmcnt(10)" ::: "memory"); asm volatile("s_barrier" ::: "memory"); }
        STEP(sB, sA, 5, , ); STEP(sA, sB, 6, , ); STEP(sB, sA, 7, , ); STEP(sA, sB, 8, , ); STEP(sB, sA, 9, , ); STEP(sA, sB, 10, , ); STEP(sB, sA, 11, , ); STEP(sA, sB, 12, , );
        if (c == 2 && wid == 0) {
            for (int it = 0; it < SPIN_MAX; ++it) {
                if (__all(flag_get(ws + WS_F2 + ((bt >> 1) * 16 + (lane & 15)) * 8) == tag)) break;
                __builtin_amdgcn_s_sleep(2);
            }
        }
        WAITB(0);
        if (c == 0 && tid == 0) flag_set(ws + WS_F2 + (rt * 16 + ct) * 8, tag);
        if (c < 2) { STEP(sB, sA, 13, PIECE(0), PIECE(1)); STEP(sA, sB, 14, PIECE(2), PIECE(3)); STEP(sB, sA, 15, PIECE(4), PIECE(5)); }
        else { STEP(sB, sA, 13, , ); STEP(sA, sB, 14, , ); STEP(sB, sA, 15, , ); }
        const int d = (c == 1) ? -2 * K2_BIG : K2_BIG;
        kaC = kaN; kbC = kbN; vC = vN; kaN += d; kbN += d; vN += d;
        src += 42 * 1024; dst -= 2 * K2_BIG;
    }
#define PA(s, m) ldA1(hs(s), m, a[s][m])
#define PB(s, n) ldB1(hs(s), n, b[s][n])
    STEP(sA, sB, 0, PA(0, 0), PA(0, 1)); STEP(sB, sA, 1, PA(0, 2), PA(0, 3)); STEP(sA, sB, 2, PB(0, 0), PB(0, 1)); STEP(sB, sA, 3, PB(0, 2), PB(0, 3));
    STEP(sA, sB, 4, PB(0, 4), PA(1, 0)); STEP(sB, sA, 5, PA(1, 1), PA(1, 2)); STEP(sA, sB, 6, PA(1, 3), PB(1, 0)); STEP(sB, sA, 7, PB(1, 1), PB(1, 2));
    STEP(sA, sB, 8, PB(1, 3), PB(1, 4)); STEP(sB, sA, 9, PA(2, 0), PA(2, 1)); STEP(sA, sB, 10, PA(2, 2), PA(2, 3)); STEP(sB, sA, 11, PB(2, 0), PB(2, 1));
    STEP(sA, sB, 12, PB(2, 2), PB(2, 3)); STEP(sB, sA, 13, PB(2, 4), ); STEP(sA, sB, 14, , ); STEP(sB, sA, 15, , );
    phi[2] = cvtpk(e3[0], e3[1]); phi[3] = cvtpk(e3[2], e3[3]);
    o = MFMA32(vb, __builtin_bit_cast(bf16x8, phi), o);
    float l; { auto rr = __builtin_amdgcn_permlane32_swap(__float_as_uint(o[8]), __float_as_uint(o[8]), false, false); l = __uint_as_float(rr[1]); }
    if (__builtin_expect(__any(!(l >= 8.6736174e-19f && l <= 1.0e30f)), 0)) {
        asm volatile("s_waitcnt vmcnt(0)" ::: "memory");
        attn_fallback(o, kv, qa, qbr[0], qbr[1], lane, kboff, voff);
        auto rr = __builtin_amdgcn_permlane32_swap(__float_as_uint(o[8]), __float_as_uint(o[8]), false, false); l = __uint_as_float(rr[1]);
    }
    const float f = gate * __builtin_amdgcn_rcpf(l);
    const int tok = Tq * 32 + r;
    char* ab = ws + WS_AF + (size_t)(tok >> 4) * NSIG * 1024 + (tok & 15) * 16;
#pragma unroll
    for (int g = 0; g < 3; ++g) {
        const int gam = head * 5 + 2 * g + h;
        const int sig = 48 + (gam >> 3), c = ((gam >> 2) & 1) * 2 + (gam & 1), half = (gam >> 1) & 1;
        const u32x2 w = {cvtpk(o[4 * g] * f, o[4 * g + 1] * f), cvtpk(o[4 * g + 2] * f, o[4 * g + 3] * f)};
        if (g < 2 || h == 0) __hip_atomic_store((u64*)(ab + sig * 1024 + c * 256 + half * 8), ((u64)w[1] << 32) | w[0], __ATOMIC_RELAXED, __HIP_MEMORY_SCOPE_AGENT);
    }
    {
        f32x4 acc[4][5] = {};
        auto loadA = [&](int sg, bf16x8 (&da)[4]) {
#pragma unroll
            for (int mt = 0; mt < 4; ++mt) gld128_sc1(da[mt], ap + ((size_t)mt * NSIG + sg) * 1024, lane16);
        };
        auto loadB = [&](int sg, bf16x8 (&db)[5]) {
#pragma unroll
            for (int nt = 0; nt < 5; ++nt) gld128(db[nt], bp + sg * 5120 + nt * 1024, lane16);
        };
        auto mma = [&](bf16x8 (&da)[4], bf16x8 (&db)[5]) {
            SBAR();
#pragma unroll
            for (int nt = 0; nt < 5; ++nt)
#pragma unroll
                for (int mt = 0; mt < 4; ++mt) acc[mt][nt] = MFMA16(db[nt], da[mt], acc[mt][nt]);
            SBAR();
        };
        wait9<3>(a[0], b[0]);
        asm volatile("" : "+v"(a[1][0]), "+v"(a[1][1]), "+v"(a[1][2]), "+v"(a[1][3]), "+v"(b[1][0]), "+v"(b[1][1]), "+v"(b[1][2]), "+v"(b[1][3]), "+v"(b[1][4]));
        asm volatile("" : "+v"(a[2][0]), "+v"(a[2][1]), "+v"(a[2][2]), "+v"(a[2][3]), "+v"(b[2][0]), "+v"(b[2][1]), "+v"(b[2][2]), "+v"(b[2][3]), "+v"(b[2][4]));
        mma(a[0], b[0]); loadA(hs(3), a[0]); loadB(hs(3), b[0]);
        mma(a[1], b[1]); loadA(hs(4), a[1]); loadB(hs(4), b[1]);
        asm volatile("s_waitcnt vmcnt(18)" ::: "memory");
        if (lane == 0) {
            const unsigned old = __hip_atomic_fetch_add((unsigned*)(lds + CTL_OFF), 1u, __ATOMIC_RELAXED, __HIP_MEMORY_SCOPE_WORKGROUP);
            if (old == 7u) flag_set(ws + WS_F3 + (qblk * 32 + head) * 8, tag);
        }
        mma(a[2], b[2]); loadA(hs(5), a[2]); loadB(hs(5), b[2]);
        float og[4];
#pragma unroll
        for (int mt = 0; mt < 4; ++mt) asm volatile("s_nop 4\n\tglobal_load_dword %0, %1, %2" : "=v"(og[mt]) : "v"((unsigned)((bt * 64 + mt * 16 + (lane & 15)) * 4)), "s"(ws + WS_GATE) : "memory");
        wait9<22>(a[0], b[0]); mma(a[0], b[0]); loadB(48 + wid, b[0]);
        wait9<18>(a[1], b[1]); mma(a[1], b[1]); loadB(56 + wid, b[1]);
        wait9<14>(a[2], b[2]); mma(a[2], b[2]);
        if (wid < 4) {
#pragma unroll
            for (int nt = 0; nt < 5; ++nt) gld128_rw(b[2][nt], bp + (64 + wid) * 5120 + nt * 1024, lane16);
        }
        asm volatile("s_waitcnt vmcnt(0)" : "+v"(og[0]), "+v"(og[1]), "+v"(og[2]), "+v"(og[3]) :: "memory");
#pragma unroll
        for (int mt = 0; mt < 4; ++mt) { const float f1 = bf_round(1.f - og[mt]);
#pragma unroll
            for (int nt = 0; nt < 5; ++nt) acc[mt][nt] *= f1; }
        if (wid == 0) {
            for (int it = 0; it < SPIN_MAX; ++it) {
                if (__all(flag_get(ws + WS_F3 + ((bt >> 2) * 32 + r) * 8) == tag)) break;
                __builtin_amdgcn_s_sleep(2);
            }
        }
        asm volatile("s_barrier" ::: "memory");
        loadA(48 + wid, a[0]); loadA(56 + wid, a[1]);
        a[2][0] = (bf16x8){}; a[2][1] = (bf16x8){}; a[2][2] = (bf16x8){}; a[2][3] = (bf16x8){};
        if (wid < 4) {
#pragma unroll
            for (int mt = 0; mt < 4; ++mt) gld128_sc1_rw(a[2][mt], ap + ((size_t)mt * NSIG + 64 + wid) * 1024, lane16);
        }
        wait9<4>(a[0], b[0]); mma(a[0], b[0]);
        wait9<0>(a[1], b[1]); wait9<0>(a[2], b[2]); mma(a[1], b[1]); mma(a[2], b[2]);
#pragma unroll
        for (int mt = 0; mt < 4; ++mt)
#pragma unroll
            for (int nt = 0; nt < 5; ++nt) *(f32x4*)(lds + (wid * 20 + mt * 5 + nt) * 1024 + lane16) = acc[mt][nt];
        BARL();
#pragma unroll
        for (int i = 0; i < 3; ++i) { const int t = wid + 8 * i;
            if (t < 20) {
                f32x4 sum = *(const f32x4*)(lds + t * 1024 + lane16);
#pragma unroll
                for (int w = 1; w < 8; ++w) sum += *(const f32x4*)(lds + (w * 20 + t) * 1024 + lane16);
                const int m4 = t / 5, nt = t - m4 * 5;
                const int tok2 = bt * 64 + m4 * 16 + (lane & 15);
                *(u32x2*)(out + (size_t)tok2 * DM + ng * 80 + nt * 16 + (lane >> 4) * 4) = (u32x2){cvtpk(sum[0], sum[1]), cvtpk(sum[2], sum[3])};
            } }
    }
}

extern "C" void kernel_launch(void* const* d_in, const int* in_sizes, int n_in, void* d_out, int out_size, void* d_ws, size_t ws_size, hipStream_t stream) {
    const unsigned short* x = (const unsigned short*)d_in[0];
    const unsigned short* wqkv = (const unsigned short*)d_in[1];
    const unsigned short* wo = (const unsigned short*)d_in[2];
    const unsigned short* w1 = (const unsigned short*)d_in[4];
    const unsigned short* w2 = (const unsigned short*)d_in[5];
    const unsigned short* gw = (const unsigned short*)d_in[6];
    (void)hipFuncSetAttribute((const void*)gated_mix_mega, hipFuncAttributeMaxDynamicSharedMemorySize, LDS_ALL);
    gated_mix_mega<<<256, 512, LDS_ALL, stream>>>(x, wqkv, wo, w1, w2, gw, (char*)d_ws, (unsigned short*)d_out);
}
```

```cpp
#include <hip/hip_runtime.h>
#include <hip/hip_bf16.h>
#include <stdint.h>

typedef short bf16x8 __attribute__((ext_vector_type(8)));
typedef short s16x4 __attribute__((ext_vector_type(4)));
typedef float f32x16 __attribute__((ext_vector_type(16)));
typedef float f32x4 __attribute__((ext_vector_type(4)));
typedef float f32x2 __attribute__((ext_vector_type(2)));
typedef unsigned u32x4 __attribute__((ext_vector_type(4)));
typedef unsigned u32x2 __attribute__((ext_vector_type(2)));
typedef __bf16 bf16x2_t __attribute__((ext_vector_type(2)));
#define LAS __attribute__((address_space(3)))
typedef LAS const char* lds_cptr;
#define DI __device__ __forceinline__
#define SBAR() __builtin_amdgcn_sched_barrier(0)
#define PIN(x) asm volatile("" : "+v"(x))
#define MFMA32(a, b, c) __builtin_amdgcn_mfma_f32_32x32x16_bf16(a, b, c, 0, 0, 0)
#define MFMA16(a, b, c) __builtin_amdgcn_mfma_f32_16x16x32_bf16(a, b, c, 0, 0, 0)
#define MFMA8(a, b, c) __builtin_amdgcn_mfma_f32_32x32x8bf16_1k(a, b, c, 0, 0, 0)

constexpr int S = 2048, DM = 640, NH = 32, DH = 20, FF = 1536;
constexpr int NT = S / 32;
constexpr int QTILE_B = 1280;
constexpr int KVTILE_B = 2688;
constexpr int V_OFF = 1280, V_STEP = 704, V_ROWS = 22;
constexpr int NSIG = 68;
constexpr size_t WS_Q = 0;
constexpr size_t WS_KV = WS_Q + (size_t)NH * NT * QTILE_B;
constexpr size_t WS_AF = WS_KV + (size_t)NH * NT * KVTILE_B;
constexpr size_t WS_BF = WS_AF + (size_t)128 * NSIG * 1024;
constexpr size_t WS_KMAX = WS_BF + (size_t)8 * NSIG * 5 * 1024;
constexpr size_t WS_GATE = WS_KMAX + (size_t)160 * 32 * 4;
constexpr size_t WS_F1 = WS_GATE + (size_t)S * 4;
constexpr size_t WS_F2 = WS_F1 + 2048;
constexpr size_t WS_F3 = WS_F2 + 2048;
constexpr size_t WS_EPOCH = WS_F3 + 2048;
constexpr size_t WS_END = WS_EPOCH + 256;
constexpr float QSCALE = 0.22360679774997896f * 1.4426950408889634f;
constexpr unsigned ONES2 = 0x3F803F80u;

DI unsigned cvtpk(float lo, float hi) { f32x2 v = {lo, hi}; bf16x2_t b = __builtin_convertvector(v, bf16x2_t); return __builtin_bit_cast(unsigned, b); }
DI float bf_lo(unsigned u) { return __uint_as_float(u << 16); }
DI float bf_hi(unsigned u) { return __uint_as_float(u & 0xffff0000u); }
DI float bf_round(float x) { return bf_lo(cvtpk(x, x)); }
DI void glds16(const void* sbase, unsigned voff, unsigned lds_base) {
    unsigned sv;
    asm volatile("s_mov_b32 %0, m0\n\ts_mov_b32 m0, %3\n\ts_nop 0\n\tglobal_load_lds_dwordx4 %1, %2\n\ts_mov_b32 m0, %0"
                 : "=&s"(sv) : "v"(voff), "s"(sbase), "s"(lds_base) : "memory");
}
DI void glds16_sc1(const void* sbase, unsigned voff, unsigned lds_base) {
    unsigned sv;
    asm volatile("s_mov_b32 %0, m0\n\ts_mov_b32 m0, %3\n\ts_nop 0\n\tglobal_load_lds_dwordx4 %1, %2 sc1\n\ts_mov_b32 m0, %0"
                 : "=&s"(sv) : "v"(voff), "s"(sbase), "s"(lds_base) : "memory");
}
typedef short v4i16_t __attribute__((ext_vector_type(4)));
DI s16x4 ldstr(lds_cptr p) { return __builtin_bit_cast(s16x4, __builtin_amdgcn_ds_read_tr16_b64_v4i16((LAS v4i16_t*)p)); }
DI bf16x8 lds128(lds_cptr p) { return *(const LAS bf16x8*)p; }
DI s16x4 lds64(lds_cptr p) { return *(const LAS s16x4*)p; }
DI unsigned rfl(unsigned x) { return __builtin_amdgcn_readfirstlane(x); }


DI float rdl(float x, int l) { return __uint_as_float(__builtin_amdgcn_readlane(__float_as_uint(x), l)); }
typedef __amdgpu_buffer_rsrc_t rsrc_t;
typedef unsigned long long u64;
DI rsrc_t ws_rsrc(char* ws) { return __builtin_amdgcn_make_buffer_rsrc(ws, 0, (int)WS_END, 0x00020000); }
DI void st16(rsrc_t rs, unsigned off, u32x4 v) { __builtin_amdgcn_raw_buffer_store_b128(v, rs, off, 0, 16); }
DI void st8(rsrc_t rs, unsigned off, u32x2 v) { __builtin_amdgcn_raw_buffer_store_b64(v, rs, off, 0, 16); }
DI void st4f(char* p, float v) { __hip_atomic_store((float*)p, v, __ATOMIC_RELAXED, __HIP_MEMORY_SCOPE_AGENT); }
DI void flag_set(char* p, u64 tag) { __hip_atomic_store((u64*)p, tag, __ATOMIC_RELAXED, __HIP_MEMORY_SCOPE_AGENT); }
DI u64 flag_get(const char* p) { return __hip_atomic_load((const u64*)p, __ATOMIC_RELAXED, __HIP_MEMORY_SCOPE_AGENT); }
template <int CTRL> DI float dpp_max(float x) { const float y = __uint_as_float(__builtin_amdgcn_update_dpp(0u, __float_as_uint(x), CTRL, 0xf, 0xf, true)); return fmaxf(x, y); }
template <int CTRL> DI float dpp_add(float x) { const float y = __uint_as_float(__builtin_amdgcn_update_dpp(0u, __float_as_uint(x), CTRL, 0xf, 0xf, true)); return x + y; }
DI void gld128(bf16x8& d, const char* sbase, unsigned voff) { asm volatile("global_load_dwordx4 %0, %1, %2" : "=v"(d) : "v"(voff), "s"(sbase) : "memory"); }
DI void gld128_sc1(bf16x8& d, const char* sbase, unsigned voff) { asm volatile("global_load_dwordx4 %0, %1, %2 sc1" : "=v"(d) : "v"(voff), "s"(sbase) : "memory"); }
DI void gld128_rw(bf16x8& d, const char* sbase, unsigned voff) { asm volatile("s_nop 4\n\tglobal_load_dwordx4 %0, %1, %2" : "+v"(d) : "v"(voff), "s"(sbase) : "memory"); }
DI void gld128_sc1_rw(bf16x8& d, const char* sbase, unsigned voff) { asm volatile("s_nop 4\n\tglobal_load_dwordx4 %0, %1, %2 sc1" : "+v"(d) : "v"(voff), "s"(sbase) : "memory"); }
template <int N> DI void wait9(bf16x8 (&a)[4], bf16x8 (&b)[5]) {
    asm volatile("s_waitcnt vmcnt(%9)" : "+v"(a[0]), "+v"(a[1]), "+v"(a[2]), "+v"(a[3]), "+v"(b[0]), "+v"(b[1]), "+v"(b[2]), "+v"(b[3]), "+v"(b[4]) : "i"(N) : "memory");
}
#define WAITB(n) asm volatile("s_waitcnt vmcnt(" #n ")\n\ts_barrier" ::: "memory")
#define BARL() asm volatile("s_waitcnt lgkmcnt(0)\n\ts_barrier" ::: "memory")

constexpr int P1_STG = 32768;
constexpr int RT_OFF = 131072;
constexpr int P2_STG = 28672;
constexpr int CTL_OFF = 163584;
DI constexpr int p2_base(int s) { return CTL_OFF - (s + 1) * P2_STG; }
constexpr int K2_BIG = 16 * KVTILE_B;
constexpr int EP_OFF = 151552;
constexpr int LDS_ALL = 163840;

template <bool N96>
struct GDma {
    static constexpr int NW = N96 ? 3 : 4;
    const char* xbase; const char* wbase; int ldw2, k0; unsigned lds0, xoff0, xoff1, woff[3];
    DI int kact(int kc) const { kc += k0; return kc >= 10 ? kc - 10 : kc; }
    DI static unsigned sbase(int st) { return N96 ? (unsigned)(CTL_OFF - (st + 1) * P2_STG) : (unsigned)(st * P1_STG); }
    DI void init(const char* xb, const char* wb, int ld, char* lds, int lane, int k0_) {
        xbase = xb; wbase = wb; ldw2 = ld; k0 = k0_; lds0 = (unsigned)(uintptr_t)lds;
        const int rr = lane >> 3;
        xoff0 = rr * 1280 + (((lane & 7) ^ ((lane >> 4) & 7)) << 4); xoff1 = rr * 1280 + (((lane & 7) ^ ((4 + (lane >> 4)) & 7)) << 4);
        if (N96) {
#pragma unroll
            for (int p = 0; p < 3; ++p) { const int o = 1024 * p + 16 * lane, row = (o * 21846) >> 22, col = o - row * 192; woff[p] = row * ldw2 + col; }
        } else woff[0] = (lane >> 4) * ldw2 + ((((lane & 15) ^ ((lane >> 4) << 2))) << 4);
    }
    DI void xp(int kc, int st, int j, int par) const { glds16(xbase + (size_t)j * 8 * 1280 + kc * 128, par ? xoff1 : xoff0, lds0 + sbase(st) + j * 1024); }
    DI void wp(int kc, int st, int lw, int jj) const {
        if (N96) glds16(wbase + (size_t)(kc * 64 + 16 * lw) * ldw2, woff[jj], lds0 + sbase(st) + 16384 + (3 * lw + jj) * 1024);
        else glds16(wbase + (size_t)(kc * 64 + 16 * lw + 4 * jj) * ldw2, woff[0], lds0 + sbase(st) + 16384 + (4 * lw + jj) * 1024);
    }
    DI void issue(int kc_, int st, int lw) const {
        const int kc = kact(kc_);
#pragma unroll
        for (int jj = 0; jj < NW; ++jj) wp(kc, st, lw, jj);
#pragma unroll
        for (int jj = 0; jj < 4; ++jj) xp(kc, st, 4 * lw + jj, jj & 1);
    }
};

DI void rt_load(const unsigned short* w2, const unsigned short* wo, int u, unsigned ldsb, int piece, int lane) {
    const int sig = u >> 2, s = (u >> 1) & 1, cb = u & 1;
    const char* src = (sig < 48 ? (const char*)w2 + (size_t)sig * 32 * 1280 : (const char*)wo + (size_t)(sig - 48) * 32 * 1280) + s * 16 * 1280 + cb * 640;
    const int o = piece * 1024 + lane * 16, row = (o * 6554) >> 22;
    glds16(src, row * 1280 + (o - row * 640), ldsb + piece * 1024);
}
DI void rt_store(rsrc_t rs, int u, const char* ldsb, int i, int lane) {
    const int sig = u >> 2, s = (u >> 1) & 1, cb = u & 1;
    const int nl = 2 * i + (lane >> 5), hh = (lane >> 4) & 1, q = (lane & 15) >> 2, p = lane & 3;
    lds_cptr a = (lds_cptr)ldsb + (4 * hh + q) * 640 + (16 * nl + 4 * p) * 2;
    const s16x4 lo = ldstr(a), hi = ldstr(a + 8 * 640);
    const bf16x8 f = __builtin_shufflevector(lo, hi, 0, 1, 2, 3, 4, 5, 6, 7);
    const int nu = cb * 20 + nl, ng = (nu * 205) >> 10;
    st16(rs, (unsigned)WS_BF + (unsigned)((ng * NSIG + sig) * 5 + (nu - 5 * ng)) * 1024 + (s * 32 + (lane & 31)) * 16, __builtin_bit_cast(u32x4, f));
}

template <bool N96, bool VT, int MT, int NTL, class HOOK>
DI void gemm_loop(f32x16 (&acc)[MT][NTL], char* lds, int wid, int lane, HOOK hook) {
    const int h = lane >> 5, r = lane & 31;
    constexpr int NST = N96 ? 3 : 4;
    unsigned xa[4];
    { const int row = (N96 ? wid * 32 : (wid >> 1) * 64) + r; const int sw = (row >> 1) & 7;
#pragma unroll
      for (int ks = 0; ks < 4; ++ks) xa[ks] = row * 128 + (((2 * ks + h) ^ sw) << 4); }
    unsigned wa[2];
    { const int blk = (lane >> 4) & 1, q = (lane & 15) >> 2, p = lane & 3;
      if (N96) wa[0] = wa[1] = 16384 + (8 * h + q) * 192 + blk * 32 + p * 8;
      else {
#pragma unroll
          for (int nt = 0; nt < 2; ++nt) { const int ch = (wid & 1) * 8 + nt * 4 + 2 * blk + (p >> 1); wa[nt] = 16384 + (8 * h + q) * 256 + ((ch ^ (q << 2)) << 4) + 8 * (p & 1); } } }
    bf16x8 xf[2][MT], wf[2][NTL];
    auto rdw = [&](int buf, int nt, lds_cptr sb, int ks) {
        lds_cptr a = N96 ? sb + wa[0] + ks * 16 * 192 + nt * 64 : sb + wa[nt & 1] + ks * 4096;
        const s16x4 lo = ldstr(a), hi = ldstr(a + (N96 ? 4 * 192 : 1024));
        wf[buf][nt] = __builtin_shufflevector(lo, hi, 0, 1, 2, 3, 4, 5, 6, 7);
    };
    auto rdx = [&](int buf, int mt, lds_cptr sb, int ks) { xf[buf][mt] = lds128(sb + xa[ks] + mt * 4096); };
    auto mm = [&](int buf, int mt, int nt) { acc[mt][nt] = VT ? MFMA32(xf[buf][mt], wf[buf][nt], acc[mt][nt]) : MFMA32(wf[buf][nt], xf[buf][mt], acc[mt][nt]); };
    { lds_cptr sb = (lds_cptr)lds + GDma<N96>::sbase(0); rdw(0, 0, sb, 0); rdx(0, 0, sb, 0); rdw(0, 1, sb, 0); if (N96) rdw(0, 2 % NTL, sb, 0); else rdx(0, 1 % MT, sb, 0); }
    int st = 0;
#define G_KSTEP(ks, NEXT) do { const int b = (ks) & 1, n = b ^ 1; lds_cptr np = (ks) < 3 ? sb : sn; const int nk = ((ks) + 1) & 3; \
        SBAR(); \
        if constexpr (N96) { \
            mm(b, 0, 0); SBAR(); if (NEXT) { rdw(n, 0, np, nk); rdx(n, 0, np, nk); } SBAR(); \
            mm(b, 0, 1); SBAR(); if (NEXT) { rdw(n, 1, np, nk); } SBAR(); \
            mm(b, 0, 2 % NTL); SBAR(); if (NEXT) { rdw(n, 2 % NTL, np, nk); } SBAR(); \
        } else { \
            mm(b, 0, 0); SBAR(); if (NEXT) { rdw(n, 0, np, nk); } SBAR(); \
            mm(b, 0, 1); SBAR(); if (NEXT) { rdx(n, 0, np, nk); } SBAR(); \
            mm(b, 1 % MT, 0); SBAR(); if (NEXT) { rdw(n, 1, np, nk); } SBAR(); \
            mm(b, 1 % MT, 1); SBAR(); if (NEXT) { rdx(n, 1 % MT, np, nk); } SBAR(); \
        } } while (0)
#pragma unroll 1
    for (int kc = 0; kc < 9; ++kc) {
        lds_cptr sb = (lds_cptr)lds + GDma<N96>::sbase(st);
        st = st == NST - 1 ? 0 : st + 1;
        lds_cptr sn = (lds_cptr)lds + GDma<N96>::sbase(st);
        G_KSTEP(0, true); G_KSTEP(1, true); G_KSTEP(2, true);
        hook(kc, true);
        BARL();
        hook(kc, false);
        G_KSTEP(3, true);
    }
    { lds_cptr sb = (lds_cptr)lds + GDma<N96>::sbase(st); lds_cptr sn = sb;
      G_KSTEP(0, true); G_KSTEP(1, true); G_KSTEP(2, true); G_KSTEP(3, false); }
}

DI float swap32_add(float x) { auto rr = __builtin_amdgcn_permlane32_swap(__float_as_uint(x), __float_as_uint(x), false, false); return __uint_as_float(rr[0]) + __uint_as_float(rr[1]); }
DI float swap32_max(float x) { auto rr = __builtin_amdgcn_permlane32_swap(__float_as_uint(x), __float_as_uint(x), false, false); return fmaxf(__uint_as_float(rr[0]), __uint_as_float(rr[1])); }
DI s16x4 make_qb(unsigned q0, unsigned q1, float M, int h) {
    const float mh = bf_hi(__float_as_uint(M)), r1 = M - mh, mm = bf_hi(__float_as_uint(r1)), r2 = r1 - mm, ml = bf_hi(__float_as_uint(r2));
    u32x2 w;
    w[0] = h ? ((__float_as_uint(mh) >> 16) | (__float_as_uint(mm) & 0xffff0000u)) ^ 0x80008000u : q0;
    w[1] = h ? (__float_as_uint(ml) >> 16) ^ 0x8000u : q1;
    return __builtin_bit_cast(s16x4, w);
}

DI void attn_fallback(f32x16& o, const char* kv, bf16x8 qa, unsigned q0, unsigned q1, int lane, int kboff, int voff) {
    const int h = lane >> 5;
    const f32x16 zero = {};
    float m = -INFINITY;
    const s16x4 qb0 = make_qb(q0, q1, 0.f, h);
    for (int t = 0; t < NT; ++t) {
        const char* tb = kv + t * KVTILE_B;
        const bf16x8 ka = *(const bf16x8*)(tb + lane * 16); const s16x4 kb = *(const s16x4*)(tb + kboff);
        f32x16 s = MFMA32(ka, qa, zero); s = MFMA8(kb, qb0, s);
#pragma unroll
        for (int i = 0; i < 16; ++i) m = fmaxf(m, s[i]);
    }
    m = swap32_max(m);
    const s16x4 qb = make_qb(q0, q1, m, h);
    o = zero;
    for (int t = 0; t < NT; ++t) {
        const char* tb = kv + t * KVTILE_B;
        const bf16x8 ka = *(const bf16x8*)(tb + lane * 16); const s16x4 kb = *(const s16x4*)(tb + kboff);
        const bf16x8 va = *(const bf16x8*)(tb + voff), vb = *(const bf16x8*)(tb + voff + V_STEP);
        f32x16 s = MFMA32(ka, qa, zero); s = MFMA8(kb, qb, s);
        u32x4 p0, p1;
#pragma unroll
        for (int i = 0; i < 4; ++i) { p0[i] = cvtpk(__builtin_amdgcn_exp2f(s[2 * i]), __builtin_amdgcn_exp2f(s[2 * i + 1])); p1[i] = cvtpk(__builtin_amdgcn_exp2f(s[8 + 2 * i]), __builtin_amdgcn_exp2f(s[9 + 2 * i])); }
        o = MFMA32(va, __builtin_bit_cast(bf16x8, p0), o);
        o = MFMA32(vb, __builtin_bit_cast(bf16x8, p1), o);
    }
}


constexpr int SPIN_MAX = 1 << 18;
__global__ __launch_bounds__(512, 2) void gated_mix_mega(const unsigned short* __restrict__ x, const unsigned short* __restrict__ wqkv, const unsigned short* __restrict__ wo, const unsigned short* __restrict__ w1,
                                                         const unsigned short* __restrict__ w2, const unsigned short* __restrict__ gw, char* __restrict__ ws, unsigned short* __restrict__ out) {
    extern __shared__ __attribute__((aligned(16))) char lds[];
    const int tid = threadIdx.x, lane = tid & 63, wid = rfl(tid >> 6);
    const int bid = blockIdx.x, xcd = bid & 7, slot = bid >> 3;
    const int h = lane >> 5, r = lane & 31;
    const rsrc_t rs = ws_rsrc(ws);
    const unsigned lds0 = (unsigned)(uintptr_t)lds, lane16 = lane * 16;
    volatile LAS unsigned* ctl = (volatile LAS unsigned*)(lds + CTL_OFF);
    const int rt = 4 * (xcd >> 1) + (slot & 3), ci = slot >> 2, ct = 8 * (xcd & 1) + ci;
    const bool tile1 = ct < 15;
    const char* xbase = (const char*)x + (size_t)rt * 128 * 1280;
        const int k0 = 0;
    const int head = xcd * 4 + (slot & 3), qblk = slot >> 2;
    const char* kv = ws + WS_KV + (size_t)head * NT * KVTILE_B;
    const int Tq = qblk * 8 + wid;
    const char* qt = ws + WS_Q + ((size_t)head * NT + Tq) * QTILE_B;
    bf16x8 qa; u32x2 qbr; f32x4 km4; float gate;
    auto q_loads = [&]() {
        asm volatile("s_nop 4\n\tglobal_load_dwordx4 %0, %1, %2" : "=v"(qa) : "v"(lane16), "s"(qt) : "memory");
        asm volatile("s_nop 4\n\tglobal_load_dwordx2 %0, %1, %2" : "=v"(qbr) : "v"((unsigned)(1024 + r * 8)), "s"(qt) : "memory");
        asm volatile("s_nop 4\n\tglobal_load_dwordx4 %0, %1, %2" : "=v"(km4) : "v"((unsigned)((lane < 40 ? lane : 0) * 16)), "s"(ws + WS_KMAX + (size_t)head * 640) : "memory");
        asm volatile("s_nop 4\n\tglobal_load_dword %0, %1, %2" : "=v"(gate) : "v"((unsigned)((Tq * 32 + r) * 4)), "s"(ws + WS_GATE) : "memory");
    };
    auto wait_seen = [&]() { for (int it = 0; it < SPIN_MAX; ++it) { if (ctl[1] != 0u) break; __builtin_amdgcn_s_sleep(1); } };
    auto gates = [&]() {
        const int aidx = (xcd >> 1) * 4 + (slot & 3);
        const u32x4* gr = (const u32x4*)gw + (lane & 7);
#pragma unroll 1
        for (int ps = 0; ps < 2; ++ps) {
            const int tok = aidx * 128 + wid * 16 + ps * 8 + (lane >> 3);
            const u32x4* xr = (const u32x4*)(x + (size_t)tok * DM) + (lane & 7);
            float sm = 0.f;
#pragma unroll
            for (int i = 0; i < 10; ++i) { const u32x4 xv = xr[i * 8], gv = gr[i * 8];
#pragma unroll
                for (int k = 0; k < 4; ++k) { const unsigned a_ = xv[k], b_ = gv[k]; sm = __builtin_amdgcn_fdot2_f32_bf16(__builtin_bit_cast(bf16x2_t, a_), __builtin_bit_cast(bf16x2_t, b_), sm, false); } }
            sm = dpp_add<0xB1>(sm); sm = dpp_add<0x4E>(sm); sm = dpp_add<0x141>(sm);
            const float lg = bf_round(sm);
            const float g = bf_round(__builtin_amdgcn_rcpf(1.f + __builtin_amdgcn_exp2f(lg * -1.4426950408889634f)));
            if ((lane & 7) == 0) st4f(ws + WS_GATE + tok * 4, g);
        }
    };
    GDma<true> d2; d2.init(xbase, (const char*)w1 + ct * 192, FF * 2, lds, lane, k0);
    u64 pc0; asm volatile("s_getpc_b64 %0" : "=s"(pc0));
    if (wid < 4) {
        GDma<false> d1; d1.init(xbase, (const char*)wqkv + ct * 256, 3 * DM * 2, lds, lane, k0);
        if (tile1) {
#pragma unroll
            for (int jj = 0; jj < 4; ++jj) d1.xp(d1.kact(0), 0, 4 * wid + jj, jj & 1);
        }
        if (tid == 0) { ctl[0] = 0u; ctl[1] = 0u; }
        {
#pragma unroll
            for (int i = 0; i < 7; ++i) {
                int u = (bid * 4 + wid) * 7 + i; if (u > 6879) u = 6879;
                const char* src = u < 2560 ? (const char*)x + (size_t)u * 1024 : u < 4960 ? (const char*)wqkv + (size_t)(u - 2560) * 1024 : (const char*)w1 + (size_t)(u - 4960) * 1024;
                glds16(src, lane16, lds0 + 152 * 1024);
            }
        }
        if (tile1) asm volatile("s_waitcnt vmcnt(7) lgkmcnt(0)\n\ts_barrier" ::: "memory");
        if (slot * 4 + wid < 42) glds16((const char*)((pc0 + 15ull) & ~15ull) + (slot * 4 + wid) * 1024, lane16, lds0 + 152 * 1024);
        for (int pc = wid; pc < 10; pc += 4) rt_load(w2, wo, bid, lds0 + RT_OFF, pc, lane);
        if (!tile1) for (int pc = wid; pc < 10; pc += 4) rt_load(w2, wo, 256 + (xcd >> 1) * 4 + (slot & 3), lds0 + RT_OFF + 10240, pc, lane);
        if (wid == 0) glds16_sc1(ws + WS_EPOCH, lane < 1 ? 0u : 0u, lds0 + EP_OFF);
        auto tag_ready = [&]() {
            if (tid == 0) { const u64 tg = *(const volatile LAS u64*)(lds + EP_OFF) + 1ull; ctl[2] = (unsigned)tg; ctl[3] = (unsigned)(tg >> 32); }
        };
        if (!tile1) { asm volatile("s_waitcnt vmcnt(0)" ::: "memory"); tag_ready(); asm volatile("s_waitcnt lgkmcnt(0)\n\ts_barrier" ::: "memory"); }
        if (tile1) {
            const int wm = wid >> 1, wn = wid & 1;
            f32x16 acc[2][2] = {};
            const char* w1w = (const char*)w1 + (size_t)(slot * 20) * 3072 + (xcd & 1) * 1536;
            auto nohook = [&](int kc, bool before) {
                if (kc == 1 && before) { asm volatile("s_waitcnt vmcnt(0)" ::: "memory"); tag_ready(); }
                if (kc == 5 && before) asm volatile("s_waitcnt vmcnt(0)" ::: "memory");
            };
            if (ct >= 10) {
                gemm_loop<false, true, 2, 2>(acc, lds, wid, lane, nohook);
#pragma unroll
                for (int nt = 0; nt < 2; ++nt) {
                    const int phi = (ct - 10) * 128 + wn * 64 + nt * 32 + r;
                    const int hd = (phi * 3277) >> 16, d = phi - hd * 20;
                    const unsigned dst = (unsigned)WS_KV + (unsigned)(hd * NT + rt * 4 + wm * 2) * KVTILE_B + V_OFF + (h * V_ROWS + d) * 16;
#pragma unroll
                    for (int mt = 0; mt < 2; ++mt)
#pragma unroll
                        for (int s = 0; s < 2; ++s) {
                            const f32x16& a = acc[mt][nt];
                            const u32x4 w = {cvtpk(a[8 * s], a[8 * s + 1]), cvtpk(a[8 * s + 2], a[8 * s + 3]), cvtpk(a[8 * s + 4], a[8 * s + 5]), cvtpk(a[8 * s + 6], a[8 * s + 7])};
                            st16(rs, dst + mt * KVTILE_B + s * V_STEP, w);
                        }
                }
                BARL();
            } else {
                gemm_loop<false, false, 2, 2>(acc, lds, wid, lane, nohook);
                const bool isQ = ct < 5;
                const float sc = isQ ? QSCALE : 1.f;
                const unsigned img = (unsigned)(isQ ? WS_Q : WS_KV), tileb = isQ ? QTILE_B : KVTILE_B;
                const int G = (isQ ? ct : ct - 5) * 32 + wn * 16 + h;
                u32x2 P[2][8]; float nrm[8];
#pragma unroll
                for (int p = 0; p < 8; ++p) {
                    const int nt = p >> 2, g = p & 3; float m = 0.f;
#pragma unroll
                    for (int mt = 0; mt < 2; ++mt) {
                        const f32x16& a = acc[mt][nt];
                        P[mt][p] = (u32x2){cvtpk(a[4 * g] * sc, a[4 * g + 1] * sc), cvtpk(a[4 * g + 2] * sc, a[4 * g + 3] * sc)};
                        m = fmaxf(m, __builtin_fmaf(a[4 * g], a[4 * g], a[4 * g + 1] * a[4 * g + 1]) + __builtin_fmaf(a[4 * g + 2], a[4 * g + 2], a[4 * g + 3] * a[4 * g + 3]));
                    }
                    nrm[p] = m;
                }
#pragma unroll
                for (int p = 0; p < 8; ++p) {
                    const int gam = G + 2 * p, hd = (gam * 205) >> 10, dg = gam - 5 * hd;
                    const unsigned base = img + (unsigned)(hd * NT + rt * 4 + wm * 2) * tileb;
#pragma unroll
                    for (int mt = 0; mt < 2; ++mt) {
                        const unsigned bm = base + mt * tileb;
                        if (dg < 2) {
                            if (p < 7) st16(rs, bm + dg * 512 + r * 16, (u32x4){P[mt][p][0], P[mt][p][1], P[mt][(p + 1) & 7][0], P[mt][(p + 1) & 7][1]});
                            else st8(rs, bm + dg * 512 + r * 16, P[mt][p]);
                        } else if (dg == 4) st8(rs, bm + 1024 + r * 8, P[mt][p]);
                        else if (p == 0) st8(rs, bm + (dg - 2) * 512 + r * 16 + 8, P[mt][p]);
                    }
                }
                if (!isQ) {
#pragma unroll
                    for (int i = 0; i < 8; ++i) { float m = nrm[i]; m = dpp_max<0xB1>(m); m = dpp_max<0x4E>(m); m = dpp_max<0x141>(m); m = dpp_max<0x140>(m);
                        auto rr = __builtin_amdgcn_permlane16_swap(__float_as_uint(m), __float_as_uint(m), false, false); nrm[i] = fmaxf(__uint_as_float(rr[0]), __uint_as_float(rr[1])); }
                    float mine = nrm[0];
#pragma unroll
                    for (int i = 1; i < 8; ++i) mine = r == i ? nrm[i] : mine;
                    if (r < 8) st4f(ws + WS_KMAX + (size_t)((G + 2 * r) * 32 + rt * 2 + wm) * 4, mine);
                }
                BARL();
            }
        } else { gates(); BARL(); }
        BARL();
        f32x16 acc2[1][3] = {};
        auto hook2 = [&](int kc, bool before) {
            if (kc == 2) { if (before) asm volatile("s_waitcnt vmcnt(0)" ::: "memory");
                           else if (tid == 0) flag_set(ws + WS_F1 + bid * 8, ((u64)ctl[3] << 32) | ctl[2]); }
        };
        gemm_loop<true, false, 1, 3>(acc2, lds, wid, lane, hook2);
        wait_seen();
        q_loads();
        {   const int tok = rt * 128 + wid * 32 + r, rho = tok >> 4;
#pragma unroll
            for (int nt = 0; nt < 3; ++nt) {
                const unsigned dst = (unsigned)WS_AF + (unsigned)(rho * NSIG + ct * 3 + nt) * 1024 + (h * 16 + (r & 15)) * 16;
                float sv[16];
#pragma unroll
                for (int i = 0; i < 16; ++i) { const float v = acc2[0][nt][i]; sv[i] = v * __builtin_amdgcn_rcpf(1.f + __builtin_amdgcn_exp2f(v * -1.4426950408889634f)); }
#pragma unroll
                for (int s = 0; s < 2; ++s) {
                    const u32x4 w = {cvtpk(sv[8 * s], sv[8 * s + 1]), cvtpk(sv[8 * s + 2], sv[8 * s + 3]), cvtpk(sv[8 * s + 4], sv[8 * s + 5]), cvtpk(sv[8 * s + 6], sv[8 * s + 7])};
                    st16(rs, dst + s * 512, w);
                }
            }
        }
        asm volatile("s_waitcnt vmcnt(6)" : "+v"(qa), "+v"(qbr), "+v"(km4), "+v"(gate) :: "memory");
    } else {
        const int lw = wid - 4;
        auto side_stores = [&]() {
            for (int i = lw; i < 10; i += 4) rt_store(rs, bid, lds + RT_OFF, i, lane);
            if (!tile1) for (int i = lw; i < 10; i += 4) rt_store(rs, 256 + (xcd >> 1) * 4 + (slot & 3), lds + RT_OFF + 10240, i, lane);
            if (lw == 0) { const int i = bid * 64 + lane;
                const unsigned f = (i & 1) ? 0u : ONES2; st16(rs, (unsigned)WS_KV + (unsigned)(i >> 3) * KVTILE_B + V_OFF + ((i >> 2) & 1) * V_STEP + (((i >> 1) & 1) * V_ROWS + 20 + (i & 1)) * 16, (u32x4){f, f, f, f}); }
        };
        if (tile1) {
            GDma<false> d1; d1.init(xbase, (const char*)wqkv + ct * 256, 3 * DM * 2, lds, lane, k0);
#pragma unroll
            for (int jj = 0; jj < 4; ++jj) d1.wp(d1.kact(0), 0, lw, jj);
            d1.issue(1, 1, lw);
            WAITB(8);
            d1.issue(2, 2, lw); d1.issue(3, 3, lw);
            for (int kc = 0; kc < 9; ++kc) {
                if (kc < 7) WAITB(16); else if (kc == 7) WAITB(15); else WAITB(14);
                if (kc + 4 < 10) d1.issue(kc + 4, kc & 3, lw);
                if (kc == 4) side_stores();
                if (kc >= 6) d2.issue(kc - 6, kc - 6, lw);
            }
            asm volatile("s_barrier" ::: "memory");
        } else {
            asm volatile("s_barrier" ::: "memory");
            side_stores(); gates();
            BARL();
            d2.issue(0, 0, lw); d2.issue(1, 1, lw); d2.issue(2, 2, lw);
        }
        WAITB(14);
        { int st = 0;
          for (int kc = 0; kc < 9; ++kc) {
              if (kc < 8) WAITB(7); else WAITB(0);
              if (kc + 3 < 10) d2.issue(kc + 3, st, lw);
              st = st == 2 ? 0 : st + 1;
          } }
        if (lw == 0) {
            const u64 tag = ((u64)ctl[3] << 32) | ctl[2];
            for (int it = 0; it < SPIN_MAX; ++it) {
                bool ok = true;
#pragma unroll
                for (int i = 0; i < 4; ++i) ok &= flag_get(ws + WS_F1 + (lane + 64 * i) * 8) == tag;
                if (__all(ok)) break;
                __builtin_amdgcn_s_sleep(2);
            }
            __builtin_amdgcn_fence(__ATOMIC_ACQUIRE, "agent");
            asm volatile("s_waitcnt vmcnt(0)" ::: "memory");
            if (lane == 0) { ctl[1] = 1u; if (bid == 0) flag_set(ws + WS_EPOCH, tag); }
        } else wait_seen();
        q_loads();
        for (int j = 0; j < 21; ++j) glds16(kv + (size_t)(lw + 4 * j) * 1024, lane16, lds0 + (lw + 4 * j) * 1024);
        asm volatile("s_waitcnt vmcnt(21)" : "+v"(qa), "+v"(qbr), "+v"(km4), "+v"(gate) :: "memory");
    }
    float km = fmaxf(fmaxf(km4[0], km4[1]), fmaxf(km4[2], km4[3]));
    km = dpp_max<0xB1>(km); km = dpp_max<0x4E>(km); km = dpp_max<0x141>(km);
    km = __builtin_amdgcn_sqrtf(km);
    float M;
    { const float c0 = rdl(km, 0), c1 = rdl(km, 8), c2 = rdl(km, 16), c3 = rdl(km, 24), c4 = rdl(km, 32);
      const u32x4 qw = __builtin_bit_cast(u32x4, qa);
      float n0, n1, n2;
      { const float a = bf_lo(qw[0]), b = bf_hi(qw[0]), c = bf_lo(qw[1]), d = bf_hi(qw[1]); n0 = (a * a + b * b) + (c * c + d * d); }
      { const float a = bf_lo(qw[2]), b = bf_hi(qw[2]), c = bf_lo(qw[3]), d = bf_hi(qw[3]); n1 = (a * a + b * b) + (c * c + d * d); }
      { const unsigned q0 = qbr[0], q1 = qbr[1]; const float a = bf_lo(q0), b = bf_hi(q0), c = bf_lo(q1), d = bf_hi(q1); n2 = (a * a + b * b) + (c * c + d * d); }
      float part = __builtin_amdgcn_sqrtf(n0) * (h ? c1 : c0) + __builtin_amdgcn_sqrtf(n1) * (h ? c3 : c2);
      if (!h) part += __builtin_amdgcn_sqrtf(n2) * c4;
      M = swap32_add(part) * 1.01f; }
    const s16x4 qb = make_qb(qbr[0], qbr[1], M, h);
    const int vrow = r <= 20 ? r : (r < 28 ? 21 : r - 12);
    const int voff = V_OFF + (h * V_ROWS + vrow) * 16, kboff = h ? V_OFF + 20 * 16 : 1024 + r * 8;
    lds_cptr kaC = (lds_cptr)lds + lane * 16, kbC = (lds_cptr)lds + kboff, vC = (lds_cptr)lds + voff;
    lds_cptr kaN = kaC + K2_BIG, kbN = kbC + K2_BIG, vN = vC + K2_BIG;
    const f32x16 zero = {};
    f32x16 o = {}, sA, sB;
    bf16x8 ka, va, vb; s16x4 kb;
    u32x4 plo, phi;
    if (wid >= 4) asm volatile("s_waitcnt vmcnt(15)" ::: "memory");
    asm volatile("s_barrier" ::: "memory");
    ka = lds128(kaC); kb = lds64(kbC);
    sA = MFMA32(ka, qa, zero); sA = MFMA8(kb, qb, sA);
    ka = lds128(kaC + KVTILE_B); kb = lds64(kbC + KVTILE_B);
    va = lds128(vC);
    vb = (bf16x8){}; phi = (u32x4){};
    f32x4 e3 = {};
    const int ng = (xcd & 1) * 4 + (slot & 3), bt = (xcd >> 1) * 8 + (slot >> 2);
    const char* ap = ws + WS_AF + (size_t)bt * 4 * NSIG * 1024;
    const char* bp = ws + WS_BF + (size_t)ng * NSIG * 5 * 1024;
        const int rot = ((slot >> 2) + 3 * (slot & 3)) % 6;
    auto hs = [&](int i) { int st = i + rot; if (st >= 6) st -= 6; return wid * 6 + st; };
    bf16x8 a[3][4], b[3][5];
    auto ldA1 = [&](int sg, int mt, bf16x8& d) { gld128_sc1(d, ap + ((size_t)mt * NSIG + sg) * 1024, lane16); };
    auto ldB1 = [&](int sg, int nt, bf16x8& d) { gld128(d, bp + sg * 5120 + nt * 1024, lane16); };
    const u64 tag = ((u64)ctl[3] << 32) | ctl[2];
#define EXP4(X, i) do { X[i] = __builtin_amdgcn_exp2f(X[i]); X[i + 1] = __builtin_amdgcn_exp2f(X[i + 1]); X[i + 2] = __builtin_amdgcn_exp2f(X[i + 2]); X[i + 3] = __builtin_amdgcn_exp2f(X[i + 3]); } while (0)
#define KADDR(A, i) (((i) < 16) ? (A##C + (i) * KVTILE_B) : (A##N + ((i) - 16) * KVTILE_B))
#define STEP(C, N, i, X0, X1) do { \
        bf16x8 ka2, va2, vb2; s16x4 kb2; \
        N = MFMA32(ka, qa, zero); SBAR(); \
        ka2 = lds128(KADDR(ka, (i) + 2)); \
        phi[2] = cvtpk(e3[0], e3[1]); phi[3] = cvtpk(e3[2], e3[3]); PIN(phi); SBAR(); EXP4(C, 0); PIN(C); SBAR(); \
        o = MFMA32(vb, __builtin_bit_cast(bf16x8, phi), o); SBAR(); \
        X0; \
        vb2 = lds128(vC + (i) * KVTILE_B + V_STEP); \
        plo[0] = cvtpk(C[0], C[1]); plo[1] = cvtpk(C[2], C[3]); PIN(plo); SBAR(); EXP4(C, 4); PIN(C); SBAR(); \
        N = MFMA8(kb, qb, N); SBAR(); \
        kb2 = lds64(KADDR(kb, (i) + 2)); \
        plo[2] = cvtpk(C[4], C[5]); plo[3] = cvtpk(C[6], C[7]); PIN(plo); SBAR(); EXP4(C, 8); PIN(C); SBAR(); \
        o = MFMA32(va, __builtin_bit_cast(bf16x8, plo), o); SBAR(); \
        X1; \
        va2 = lds128(KADDR(v, (i) + 1)); \
        phi[0] = cvtpk(C[8], C[9]); phi[1] = cvtpk(C[10], C[11]); PIN(phi); SBAR(); \
        e3[0] = __builtin_amdgcn_exp2f(C[12]); e3[1] = __builtin_amdgcn_exp2f(C[13]); e3[2] = __builtin_amdgcn_exp2f(C[14]); e3[3] = __builtin_amdgcn_exp2f(C[15]); PIN(e3); SBAR(); \
        ka = ka2; kb = kb2; va = va2; vb = vb2; \
    } while (0)
#define PIECE(j) do { if (8 * (j) + 7 < 42 || 8 * (j) + wid < 42) glds16(src + (j) * 8192, lane16, dst + (j) * 8192); } while (0)
    const char* src = kv + (84 + wid) * 1024; unsigned dst = lds0 + wid * 1024 + 2 * K2_BIG;
#pragma unroll 1
    for (int c = 0; c < 3; ++c) {
        STEP(sA, sB, 0, , ); STEP(sB, sA, 1, , ); STEP(sA, sB, 2, , ); STEP(sB, sA, 3, , ); STEP(sA, sB, 4, , );
        if (c == 0) { if (wid >= 4) asm volatile("s_waitcnt vmcnt(10)" ::: "memory"); asm volatile("s_barrier" ::: "memory"); }
        STEP(sB, sA, 5, , ); STEP(sA, sB, 6, , ); STEP(sB, sA, 7, , ); STEP(sA, sB, 8, , ); STEP(sB, sA, 9, , ); STEP(sA, sB, 10, , ); STEP(sB, sA, 11, , ); STEP(sA, sB, 12, , );
        if (c == 2 && wid == 0) {
            for (int it = 0; it < SPIN_MAX; ++it) {
                if (__all(flag_get(ws + WS_F2 + ((bt >> 1) * 16 + (lane & 15)) * 8) == tag)) break;
                __builtin_amdgcn_s_sleep(2);
            }
        }
        WAITB(0);
        if (c == 0 && tid == 0) flag_set(ws + WS_F2 + (rt * 16 + ct) * 8, tag);
        if (c < 2) { STEP(sB, sA, 13, PIECE(0), PIECE(1)); STEP(sA, sB, 14, PIECE(2), PIECE(3)); STEP(sB, sA, 15, PIECE(4), PIECE(5)); }
        else { STEP(sB, sA, 13, , ); STEP(sA, sB, 14, , ); STEP(sB, sA, 15, , ); }
        const int d = (c == 1) ? -2 * K2_BIG : K2_BIG;
        kaC = kaN; kbC = kbN; vC = vN; kaN += d; kbN += d; vN += d;
        src += 42 * 1024; dst -= 2 * K2_BIG;
    }
#define PA(s, m) ldA1(hs(s), m, a[s][m])
#define PB(s, n) ldB1(hs(s), n, b[s][n])
    STEP(sA, sB, 0, PA(0, 0), PA(0, 1)); STEP(sB, sA, 1, PA(0, 2), PA(0, 3)); STEP(sA, sB, 2, PB(0, 0), PB(0, 1)); STEP(sB, sA, 3, PB(0, 2), PB(0, 3));
    STEP(sA, sB, 4, PB(0, 4), PA(1, 0)); STEP(sB, sA, 5, PA(1, 1), PA(1, 2)); STEP(sA, sB, 6, PA(1, 3), PB(1, 0)); STEP(sB, sA, 7, PB(1, 1), PB(1, 2));
    STEP(sA, sB, 8, PB(1, 3), PB(1, 4)); STEP(sB, sA, 9, PA(2, 0), PA(2, 1)); STEP(sA, sB, 10, PA(2, 2), PA(2, 3)); STEP(sB, sA, 11, PB(2, 0), PB(2, 1));
    STEP(sA, sB, 12, PB(2, 2), PB(2, 3)); STEP(sB, sA, 13, PB(2, 4), ); STEP(sA, sB, 14, , ); STEP(sB, sA, 15, , );
    phi[2] = cvtpk(e3[0], e3[1]); phi[3] = cvtpk(e3[2], e3[3]);
    o = MFMA32(vb, __builtin_bit_cast(bf16x8, phi), o);
    float l; { auto rr = __builtin_amdgcn_permlane32_swap(__float_as_uint(o[8]), __float_as_uint(o[8]), false, false); l = __uint_as_float(rr[1]); }
    if (__builtin_expect(__any(!(l >= 8.6736174e-19f && l <= 1.0e30f)), 0)) {
        asm volatile("s_waitcnt vmcnt(0)" ::: "memory");
        attn_fallback(o, kv, qa, qbr[0], qbr[1], lane, kboff, voff);
        auto rr = __builtin_amdgcn_permlane32_swap(__float_as_uint(o[8]), __float_as_uint(o[8]), false, false); l = __uint_as_float(rr[1]);
    }
    const float f = gate * __builtin_amdgcn_rcpf(l);
    const int tok = Tq * 32 + r;
    char* ab = ws + WS_AF + (size_t)(tok >> 4) * NSIG * 1024 + (tok & 15) * 16;
#pragma unroll
    for (int g = 0; g < 3; ++g) {
        const int gam = head * 5 + 2 * g + h;
        const int sig = 48 + (gam >> 3), c = ((gam >> 2) & 1) * 2 + (gam & 1), half = (gam >> 1) & 1;
        const u32x2 w = {cvtpk(o[4 * g] * f, o[4 * g + 1] * f), cvtpk(o[4 * g + 2] * f, o[4 * g + 3] * f)};
        if (g < 2 || h == 0) __hip_atomic_store((u64*)(ab + sig * 1024 + c * 256 + half * 8), ((u64)w[1] << 32) | w[0], __ATOMIC_RELAXED, __HIP_MEMORY_SCOPE_AGENT);
    }
    {
        f32x4 acc[4][5] = {};
        auto loadA = [&](int sg, bf16x8 (&da)[4]) {
#pragma unroll
            for (int mt = 0; mt < 4; ++mt) gld128_sc1(da[mt], ap + ((size_t)mt * NSIG + sg) * 1024, lane16);
        };
        auto loadB = [&](int sg, bf16x8 (&db)[5]) {
#pragma unroll
            for (int nt = 0; nt < 5; ++nt) gld128(db[nt], bp + sg * 5120 + nt * 1024, lane16);
        };
        auto mma = [&](bf16x8 (&da)[4], bf16x8 (&db)[5]) {
            SBAR();
#pragma unroll
            for (int nt = 0; nt < 5; ++nt)
#pragma unroll
                for (int mt = 0; mt < 4; ++mt) acc[mt][nt] = MFMA16(db[nt], da[mt], acc[mt][nt]);
            SBAR();
        };
        wait9<3>(a[0], b[0]);
        asm volatile("" : "+v"(a[1][0]), "+v"(a[1][1]), "+v"(a[1][2]), "+v"(a[1][3]), "+v"(b[1][0]), "+v"(b[1][1]), "+v"(b[1][2]), "+v"(b[1][3]), "+v"(b[1][4]));
        asm volatile("" : "+v"(a[2][0]), "+v"(a[2][1]), "+v"(a[2][2]), "+v"(a[2][3]), "+v"(b[2][0]), "+v"(b[2][1]), "+v"(b[2][2]), "+v"(b[2][3]), "+v"(b[2][4]));
        mma(a[0], b[0]); loadA(hs(3), a[0]); loadB(hs(3), b[0]);
        mma(a[1], b[1]); loadA(hs(4), a[1]); loadB(hs(4), b[1]);
        asm volatile("s_waitcnt vmcnt(18)" ::: "memory");
        if (lane == 0) {
            const unsigned old = __hip_atomic_fetch_add((unsigned*)(lds + CTL_OFF), 1u, __ATOMIC_RELAXED, __HIP_MEMORY_SCOPE_WORKGROUP);
            if (old == 7u) flag_set(ws + WS_F3 + (qblk * 32 + head) * 8, tag);
        }
        mma(a[2], b[2]); loadA(hs(5), a[2]); loadB(hs(5), b[2]);
        float og[4];
#pragma unroll
        for (int mt = 0; mt < 4; ++mt) asm volatile("s_nop 4\n\tglobal_load_dword %0, %1, %2" : "=v"(og[mt]) : "v"((unsigned)((bt * 64 + mt * 16 + (lane & 15)) * 4)), "s"(ws + WS_GATE) : "memory");
        wait9<22>(a[0], b[0]); mma(a[0], b[0]); loadB(48 + wid, b[0]);
        wait9<18>(a[1], b[1]); mma(a[1], b[1]); loadB(56 + wid, b[1]);
        wait9<14>(a[2], b[2]); mma(a[2], b[2]);
        if (wid < 4) {
#pragma unroll
            for (int nt = 0; nt < 5; ++nt) gld128_rw(b[2][nt], bp + (64 + wid) * 5120 + nt * 1024, lane16);
        }
        asm volatile("s_waitcnt vmcnt(0)" : "+v"(og[0]), "+v"(og[1]), "+v"(og[2]), "+v"(og[3]) :: "memory");
#pragma unroll
        for (int mt = 0; mt < 4; ++mt) { const float f1 = bf_round(1.f - og[mt]);
#pragma unroll
            for (int nt = 0; nt < 5; ++nt) acc[mt][nt] *= f1; }
        if (wid == 0) {
            for (int it = 0; it < SPIN_MAX; ++it) {
                if (__all(flag_get(ws + WS_F3 + ((bt >> 2) * 32 + r) * 8) == tag)) break;
                __builtin_amdgcn_s_sleep(2);
            }
        }
        asm volatile("s_barrier" ::: "memory");
        loadA(48 + wid, a[0]); loadA(56 + wid, a[1]);
        a[2][0] = (bf16x8){}; a[2][1] = (bf16x8){}; a[2][2] = (bf16x8){}; a[2][3] = (bf16x8){};
        if (wid < 4) {
#pragma unroll
            for (int mt = 0; mt < 4; ++mt) gld128_sc1_rw(a[2][mt], ap + ((size_t)mt * NSIG + 64 + wid) * 1024, lane16);
        }
        wait9<4>(a[0], b[0]); mma(a[0], b[0]);
        wait9<0>(a[1], b[1]); wait9<0>(a[2], b[2]); mma(a[1], b[1]); mma(a[2], b[2]);
#pragma unroll
        for (int mt = 0; mt < 4; ++mt)
#pragma unroll
            for (int nt = 0; nt < 5; ++nt) *(f32x4*)(lds + (wid * 20 + mt * 5 + nt) * 1024 + lane16) = acc[mt][nt];
        BARL();
#pragma unroll
        for (int i = 0; i < 3; ++i) { const int t = wid + 8 * i;
            if (t < 20) {
                f32x4 sum = *(const f32x4*)(lds + t * 1024 + lane16);
#pragma unroll
                for (int w = 1; w < 8; ++w) sum += *(const f32x4*)(lds + (w * 20 + t) * 1024 + lane16);
                const int m4 = t / 5, nt = t - m4 * 5;
                const int tok2 = bt * 64 + m4 * 16 + (lane & 15);
                *(u32x2*)(out + (size_t)tok2 * DM + ng * 80 + nt * 16 + (lane >> 4) * 4) = (u32x2){cvtpk(sum[0], sum[1]), cvtpk(sum[2], sum[3])};
            } }
    }
}

extern "C" void kernel_launch(void* const* d_in, const int* in_sizes, int n_in, void* d_out, int out_size, void* d_ws, size_t ws_size, hipStream_t stream) {
    const unsigned short* x = (const unsigned short*)d_in[0];
    const unsigned short* wqkv = (const unsigned short*)d_in[1];
    const unsigned short* wo = (const unsigned short*)d_in[2];
    const unsigned short* w1 = (const unsigned short*)d_in[4];
    const unsigned short* w2 = (const unsigned short*)d_in[5];
    const unsigned short* gw = (const unsigned short*)d_in[6];
    (void)hipFuncSetAttribute((const void*)gated_mix_mega, hipFuncAttributeMaxDynamicSharedMemorySize, LDS_ALL);
    gated_mix_mega<<<256, 512, LDS_ALL, stream>>>(x, wqkv, wo, w1, w2, gw, (char*)d_ws, (unsigned short*)d_out);
}
```

```cpp
#include <hip/hip_runtime.h>
#include <hip/hip_bf16.h>
#include <stdint.h>

typedef short bf16x8 __attribute__((ext_vector_type(8)));
typedef short s16x4 __attribute__((ext_vector_type(4)));
typedef float f32x16 __attribute__((ext_vector_type(16)));
typedef float f32x4 __attribute__((ext_vector_type(4)));
typedef float f32x2 __attribute__((ext_vector_type(2)));
typedef unsigned u32x4 __attribute__((ext_vector_type(4)));
typedef unsigned u32x2 __attribute__((ext_vector_type(2)));
typedef __bf16 bf16x2_t __attribute__((ext_vector_type(2)));
#define LAS __attribute__((address_space(3)))
typedef LAS const char* lds_cptr;
#define DI __device__ __forceinline__
#define SBAR() __builtin_amdgcn_sched_barrier(0)
#define PIN(x) asm volatile("" : "+v"(x))
#define MFMA32(a, b, c) __builtin_amdgcn_mfma_f32_32x32x16_bf16(a, b, c, 0, 0, 0)
#define MFMA16(a, b, c) __builtin_amdgcn_mfma_f32_16x16x32_bf16(a, b, c, 0, 0, 0)
#define MFMA8(a, b, c) __builtin_amdgcn_mfma_f32_32x32x8bf16_1k(a, b, c, 0, 0, 0)

constexpr int S = 2048, DM = 640, NH = 32, DH = 20, FF = 1536;
constexpr int NT = S / 32;
constexpr int QTILE_B = 1280;
constexpr int KVTILE_B = 2688;
constexpr int V_OFF = 1280, V_STEP = 704, V_ROWS = 22;
constexpr int NSIG = 68;
constexpr size_t WS_Q = 0;
constexpr size_t WS_KV = WS_Q + (size_t)NH * NT * QTILE_B;
constexpr size_t WS_AF = WS_KV + (size_t)NH * NT * KVTILE_B;
constexpr size_t WS_BF = WS_AF + (size_t)128 * NSIG * 1024;
constexpr size_t WS_KMAX = WS_BF + (size_t)8 * NSIG * 5 * 1024;
constexpr size_t WS_GATE = WS_KMAX + (size_t)160 * 32 * 4;
constexpr size_t WS_F1 = WS_GATE + (size_t)S * 4;
constexpr size_t WS_F2 = WS_F1 + 2048;
constexpr size_t WS_F3 = WS_F2 + 2048;
constexpr size_t WS_EPOCH = WS_F3 + 2048;
constexpr size_t WS_END = WS_EPOCH + 256;
constexpr float QSCALE = 0.22360679774997896f * 1.4426950408889634f;
constexpr unsigned ONES2 = 0x3F803F80u;

DI unsigned cvtpk(float lo, float hi) { f32x2 v = {lo, hi}; bf16x2_t b = __builtin_convertvector(v, bf16x2_t); return __builtin_bit_cast(unsigned, b); }
DI float bf_lo(unsigned u) { return __uint_as_float(u << 16); }
DI float bf_hi(unsigned u) { return __uint_as_float(u & 0xffff0000u); }
DI float bf_round(float x) { return bf_lo(cvtpk(x, x)); }
DI void glds16(const void* sbase, unsigned voff, unsigned lds_base) {
    unsigned sv;
    asm volatile("s_mov_b32 %0, m0\n\ts_mov_b32 m0, %3\n\ts_nop 0\n\tglobal_load_lds_dwordx4 %1, %2\n\ts_mov_b32 m0, %0"
                 : "=&s"(sv) : "v"(voff), "s"(sbase), "s"(lds_base) : "memory");
}
DI void glds16_sc1(const void* sbase, unsigned voff, unsigned lds_base) {
    unsigned sv;
    asm volatile("s_mov_b32 %0, m0\n\ts_mov_b32 m0, %3\n\ts_nop 0\n\tglobal_load_lds_dwordx4 %1, %2 sc1\n\ts_mov_b32 m0, %0"
                 : "=&s"(sv) : "v"(voff), "s"(sbase), "s"(lds_base) : "memory");
}
typedef short v4i16_t __attribute__((ext_vector_type(4)));
DI s16x4 ldstr(lds_cptr p) { return __builtin_bit_cast(s16x4, __builtin_amdgcn_ds_read_tr16_b64_v4i16((LAS v4i16_t*)p)); }
DI bf16x8 lds128(lds_cptr p) { return *(const LAS bf16x8*)p; }
DI s16x4 lds64(lds_cptr p) { return *(const LAS s16x4*)p; }
DI unsigned rfl(unsigned x) { return __builtin_amdgcn_readfirstlane(x); }


DI float rdl(float x, int l) { return __uint_as_float(__builtin_amdgcn_readlane(__float_as_uint(x), l)); }
typedef __amdgpu_buffer_rsrc_t rsrc_t;
typedef unsigned long long u64;
DI rsrc_t ws_rsrc(char* ws) { return __builtin_amdgcn_make_buffer_rsrc(ws, 0, (int)WS_END, 0x00020000); }
DI void st16(rsrc_t rs, unsigned off, u32x4 v) { __builtin_amdgcn_raw_buffer_store_b128(v, rs, off, 0, 16); }
DI void st8(rsrc_t rs, unsigned off, u32x2 v) { __builtin_amdgcn_raw_buffer_store_b64(v, rs, off, 0, 16); }
DI void st4f(char* p, float v) { __hip_atomic_store((float*)p, v, __ATOMIC_RELAXED, __HIP_MEMORY_SCOPE_AGENT); }
DI void flag_set(char* p, u64 tag) { __hip_atomic_store((u64*)p, tag, __ATOMIC_RELAXED, __HIP_MEMORY_SCOPE_AGENT); }
DI u64 flag_get(const char* p) { return __hip_atomic_load((const u64*)p, __ATOMIC_RELAXED, __HIP_MEMORY_SCOPE_AGENT); }
template <int CTRL> DI float dpp_max(float x) { const float y = __uint_as_float(__builtin_amdgcn_update_dpp(0u, __float_as_uint(x), CTRL, 0xf, 0xf, true)); return fmaxf(x, y); }
template <int CTRL> DI float dpp_add(float x) { const float y = __uint_as_float(__builtin_amdgcn_update_dpp(0u, __float_as_uint(x), CTRL, 0xf, 0xf, true)); return x + y; }
DI void gld128(bf16x8& d, const char* sbase, unsigned voff) { asm volatile("global_load_dwordx4 %0, %1, %2" : "=v"(d) : "v"(voff), "s"(sbase) : "memory"); }
DI void gld128_sc1(bf16x8& d, const char* sbase, unsigned voff) { asm volatile("global_load_dwordx4 %0, %1, %2 sc1" : "=v"(d) : "v"(voff), "s"(sbase) : "memory"); }
DI void gld128_rw(bf16x8& d, const char* sbase, unsigned voff) { asm volatile("s_nop 4\n\tglobal_load_dwordx4 %0, %1, %2" : "+v"(d) : "v"(voff), "s"(sbase) : "memory"); }
DI void gld128_sc1_rw(bf16x8& d, const char* sbase, unsigned voff) { asm volatile("s_nop 4\n\tglobal_load_dwordx4 %0, %1, %2 sc1" : "+v"(d) : "v"(voff), "s"(sbase) : "memory"); }
template <int N> DI void wait9(bf16x8 (&a)[4], bf16x8 (&b)[5]) {
    asm volatile("s_waitcnt vmcnt(%9)" : "+v"(a[0]), "+v"(a[1]), "+v"(a[2]), "+v"(a[3]), "+v"(b[0]), "+v"(b[1]), "+v"(b[2]), "+v"(b[3]), "+v"(b[4]) : "i"(N) : "memory");
}
#define WAITB(n) asm volatile("s_waitcnt vmcnt(" #n ")\n\ts_barrier" ::: "memory")
#define BARL() asm volatile("s_waitcnt lgkmcnt(0)\n\ts_barrier" ::: "memory")

constexpr int P1_STG = 32768;
constexpr int RT_OFF = 131072;
constexpr int P2_STG = 28672;
constexpr int CTL_OFF = 163584;
DI constexpr int p2_base(int s) { return CTL_OFF - (s + 1) * P2_STG; }
constexpr int K2_BIG = 16 * KVTILE_B;
constexpr int EP_OFF = 151552;
constexpr int LDS_ALL = 163840;

template <bool N96>
struct GDma {
    static constexpr int NW = N96 ? 3 : 4;
    const char* xbase; const char* wbase; int ldw2, k0; unsigned lds0, xoff0, xoff1, woff[3];
    DI int kact(int kc) const { kc += k0; return kc >= 10 ? kc - 10 : kc; }
    DI static unsigned sbase(int st) { return N96 ? (unsigned)(CTL_OFF - (st + 1) * P2_STG) : (unsigned)(st * P1_STG); }
    DI void init(const char* xb, const char* wb, int ld, char* lds, int lane, int k0_) {
        xbase = xb; wbase = wb; ldw2 = ld; k0 = k0_; lds0 = (unsigned)(uintptr_t)lds;
        const int rr = lane >> 3;
        xoff0 = rr * 1280 + (((lane & 7) ^ ((lane >> 4) & 7)) << 4); xoff1 = rr * 1280 + (((lane & 7) ^ ((4 + (lane >> 4)) & 7)) << 4);
        if (N96) {
#pragma unroll
            for (int p = 0; p < 3; ++p) { const int o = 1024 * p + 16 * lane, row = (o * 21846) >> 22, col = o - row * 192; woff[p] = row * ldw2 + col; }
        } else woff[0] = (lane >> 4) * ldw2 + ((((lane & 15) ^ ((lane >> 4) << 2))) << 4);
    }
    DI void xp(int kc, int st, int j, int par) const { glds16(xbase + (size_t)j * 8 * 1280 + kc * 128, par ? xoff1 : xoff0, lds0 + sbase(st) + j * 1024); }
    DI void wp(int kc, int st, int lw, int jj) const {
        if (N96) glds16(wbase + (size_t)(kc * 64 + 16 * lw) * ldw2, woff[jj], lds0 + sbase(st) + 16384 + (3 * lw + jj) * 1024);
        else glds16(wbase + (size_t)(kc * 64 + 16 * lw + 4 * jj) * ldw2, woff[0], lds0 + sbase(st) + 16384 + (4 * lw + jj) * 1024);
    }
    DI void issue(int kc_, int st, int lw) const {
        const int kc = kact(kc_);
#pragma unroll
        for (int jj = 0; jj < NW; ++jj) wp(kc, st, lw, jj);
#pragma unroll
        for (int jj = 0; jj < 4; ++jj) xp(kc, st, 4 * lw + jj, jj & 1);
    }
};

DI void rt_load(const unsigned short* w2, const unsigned short* wo, int u, unsigned ldsb, int piece, int lane) {
    const int sig = u >> 2, s = (u >> 1) & 1, cb = u & 1;
    const char* src = (sig < 48 ? (const char*)w2 + (size_t)sig * 32 * 1280 : (const char*)wo + (size_t)(sig - 48) * 32 * 1280) + s * 16 * 1280 + cb * 640;
    const int o = piece * 1024 + lane * 16, row = (o * 6554) >> 22;
    glds16(src, row * 1280 + (o - row * 640), ldsb + piece * 1024);
}
DI void rt_store(rsrc_t rs, int u, const char* ldsb, int i, int lane) {
    const int sig = u >> 2, s = (u >> 1) & 1, cb = u & 1;
    const int nl = 2 * i + (lane >> 5), hh = (lane >> 4) & 1, q = (lane & 15) >> 2, p = lane & 3;
    lds_cptr a = (lds_cptr)ldsb + (4 * hh + q) * 640 + (16 * nl + 4 * p) * 2;
    const s16x4 lo = ldstr(a), hi = ldstr(a + 8 * 640);
    const bf16x8 f = __builtin_shufflevector(lo, hi, 0, 1, 2, 3, 4, 5, 6, 7);
    const int nu = cb * 20 + nl, ng = (nu * 205) >> 10;
    st16(rs, (unsigned)WS_BF + (unsigned)((ng * NSIG + sig) * 5 + (nu - 5 * ng)) * 1024 + (s * 32 + (lane & 31)) * 16, __builtin_bit_cast(u32x4, f));
}

template <bool N96, bool VT, int MT, int NTL, class HOOK>
DI void gemm_loop(f32x16 (&acc)[MT][NTL], char* lds, int wid, int lane, HOOK hook) {
    const int h = lane >> 5, r = lane & 31;
    constexpr int NST = N96 ? 3 : 4;
    unsigned xa[4];
    { const int row = (N96 ? wid * 32 : (wid >> 1) * 64) + r; const int sw = (row >> 1) & 7;
#pragma unroll
      for (int ks = 0; ks < 4; ++ks) xa[ks] = row * 128 + (((2 * ks + h) ^ sw) << 4); }
    unsigned wa[2];
    { const int blk = (lane >> 4) & 1, q = (lane & 15) >> 2, p = lane & 3;
      if (N96) wa[0] = wa[1] = 16384 + (8 * h + q) * 192 + blk * 32 + p * 8;
      else {
#pragma unroll
          for (int nt = 0; nt < 2; ++nt) { const int ch = (wid & 1) * 8 + nt * 4 + 2 * blk + (p >> 1); wa[nt] = 16384 + (8 * h + q) * 256 + ((ch ^ (q << 2)) << 4) + 8 * (p & 1); } } }
    bf16x8 xf[2][MT], wf[2][NTL];
    auto rdw = [&](int buf, int nt, lds_cptr sb, int ks) {
        lds_cptr a = N96 ? sb + wa[0] + ks * 16 * 192 + nt * 64 : sb + wa[nt & 1] + ks * 4096;
        const s16x4 lo = ldstr(a), hi = ldstr(a + (N96 ? 4 * 192 : 1024));
        wf[buf][nt] = __builtin_shufflevector(lo, hi, 0, 1, 2, 3, 4, 5, 6, 7);
    };
    auto rdx = [&](int buf, int mt, lds_cptr sb, int ks) { xf[buf][mt] = lds128(sb + xa[ks] + mt * 4096); };
    auto mm = [&](int buf, int mt, int nt) { acc[mt][nt] = VT ? MFMA32(xf[buf][mt], wf[buf][nt], acc[mt][nt]) : MFMA32(wf[buf][nt], xf[buf][mt], acc[mt][nt]); };
    { lds_cptr sb = (lds_cptr)lds + GDma<N96>::sbase(0); rdw(0, 0, sb, 0); rdx(0, 0, sb, 0); rdw(0, 1, sb, 0); if (N96) rdw(0, 2 % NTL, sb, 0); else rdx(0, 1 % MT, sb, 0); }
    int st = 0;
#define G_KSTEP(ks, NEXT) do { const int b = (ks) & 1, n = b ^ 1; lds_cptr np = (ks) < 3 ? sb : sn; const int nk = ((ks) + 1) & 3; \
        SBAR(); \
        if constexpr (N96) { \
            mm(b, 0, 0); SBAR(); if (NEXT) { rdw(n, 0, np, nk); rdx(n, 0, np, nk); } SBAR(); \
            mm(b, 0, 1); SBAR(); if (NEXT) { rdw(n, 1, np, nk); } SBAR(); \
            mm(b, 0, 2 % NTL); SBAR(); if (NEXT) { rdw(n, 2 % NTL, np, nk); } SBAR(); \
        } else { \
            mm(b, 0, 0); SBAR(); if (NEXT) { rdw(n, 0, np, nk); } SBAR(); \
            mm(b, 0, 1); SBAR(); if (NEXT) { rdx(n, 0, np, nk); } SBAR(); \
            mm(b, 1 % MT, 0); SBAR(); if (NEXT) { rdw(n, 1, np, nk); } SBAR(); \
            mm(b, 1 % MT, 1); SBAR(); if (NEXT) { rdx(n, 1 % MT, np, nk); } SBAR(); \
        } } while (0)
#pragma unroll 1
    for (int kc = 0; kc < 9; ++kc) {
        lds_cptr sb = (lds_cptr)lds + GDma<N96>::sbase(st);
        st = st == NST - 1 ? 0 : st + 1;
        lds_cptr sn = (lds_cptr)lds + GDma<N96>::sbase(st);
        G_KSTEP(0, true); G_KSTEP(1, true); G_KSTEP(2, true);
        hook(kc, true);
        BARL();
        hook(kc, false);
        G_KSTEP(3, true);
    }
    { lds_cptr sb = (lds_cptr)lds + GDma<N96>::sbase(st); lds_cptr sn = sb;
      G_KSTEP(0, true); G_KSTEP(1, true); G_KSTEP(2, true); G_KSTEP(3, false); }
}

DI float swap32_add(float x) { auto rr = __builtin_amdgcn_permlane32_swap(__float_as_uint(x), __float_as_uint(x), false, false); return __uint_as_float(rr[0]) + __uint_as_float(rr[1]); }
DI float swap32_max(float x) { auto rr = __builtin_amdgcn_permlane32_swap(__float_as_uint(x), __float_as_uint(x), false, false); return fmaxf(__uint_as_float(rr[0]), __uint_as_float(rr[1])); }
DI s16x4 make_qb(unsigned q0, unsigned q1, float M, int h) {
    const float mh = bf_hi(__float_as_uint(M)), r1 = M - mh, mm = bf_hi(__float_as_uint(r1)), r2 = r1 - mm, ml = bf_hi(__float_as_uint(r2));
    u32x2 w;
    w[0] = h ? ((__float_as_uint(mh) >> 16) | (__float_as_uint(mm) & 0xffff0000u)) ^ 0x80008000u : q0;
    w[1] = h ? (__float_as_uint(ml) >> 16) ^ 0x8000u : q1;
    return __builtin_bit_cast(s16x4, w);
}

DI void attn_fallback(f32x16& o, const char* kv, bf16x8 qa, unsigned q0, unsigned q1, int lane, int kboff, int voff) {
    const int h = lane >> 5;
    const f32x16 zero = {};
    float m = -INFINITY;
    const s16x4 qb0 = make_qb(q0, q1, 0.f, h);
    for (int t = 0; t < NT; ++t) {
        const char* tb = kv + t * KVTILE_B;
        const bf16x8 ka = *(const bf16x8*)(tb + lane * 16); const s16x4 kb = *(const s16x4*)(tb + kboff);
        f32x16 s = MFMA32(ka, qa, zero); s = MFMA8(kb, qb0, s);
#pragma unroll
        for (int i = 0; i < 16; ++i) m = fmaxf(m, s[i]);
    }
    m = swap32_max(m);
    const s16x4 qb = make_qb(q0, q1, m, h);
    o = zero;
    for (int t = 0; t < NT; ++t) {
        const char* tb = kv + t * KVTILE_B;
        const bf16x8 ka = *(const bf16x8*)(tb + lane * 16); const s16x4 kb = *(const s16x4*)(tb + kboff);
        const bf16x8 va = *(const bf16x8*)(tb + voff), vb = *(const bf16x8*)(tb + voff + V_STEP);
        f32x16 s = MFMA32(ka, qa, zero); s = MFMA8(kb, qb, s);
        u32x4 p0, p1;
#pragma unroll
        for (int i = 0; i < 4; ++i) { p0[i] = cvtpk(__builtin_amdgcn_exp2f(s[2 * i]), __builtin_amdgcn_exp2f(s[2 * i + 1])); p1[i] = cvtpk(__builtin_amdgcn_exp2f(s[8 + 2 * i]), __builtin_amdgcn_exp2f(s[9 + 2 * i])); }
        o = MFMA32(va, __builtin_bit_cast(bf16x8, p0), o);
        o = MFMA32(vb, __builtin_bit_cast(bf16x8, p1), o);
    }
}


constexpr int SPIN_MAX = 1 << 18;
__global__ __launch_bounds__(512, 2) void gated_mix_mega(const unsigned short* __restrict__ x, const unsigned short* __restrict__ wqkv, const unsigned short* __restrict__ wo, const unsigned short* __restrict__ w1,
                                                         const unsigned short* __restrict__ w2, const unsigned short* __restrict__ gw, char* __restrict__ ws, unsigned short* __restrict__ out) {
    extern __shared__ __attribute__((aligned(16))) char lds[];
    const int tid = threadIdx.x, lane = tid & 63, wid = rfl(tid >> 6);
    const int bid = blockIdx.x, xcd = bid & 7, slot = bid >> 3;
    const int h = lane >> 5, r = lane & 31;
    const rsrc_t rs = ws_rsrc(ws);
    const unsigned lds0 = (unsigned)(uintptr_t)lds, lane16 = lane * 16;
    volatile LAS unsigned* ctl = (volatile LAS unsigned*)(lds + CTL_OFF);
    const int rt = 4 * (xcd >> 1) + (slot & 3), ci = slot >> 2, ct = 8 * (xcd & 1) + ci;
    const bool tile1 = ct < 15;
    const char* xbase = (const char*)x + (size_t)rt * 128 * 1280;
        const int k0 = 0;
    const int head = xcd * 4 + (slot & 3), qblk = slot >> 2;
    const char* kv = ws + WS_KV + (size_t)head * NT * KVTILE_B;
    const int Tq = qblk * 8 + wid;
    const char* qt = ws + WS_Q + ((size_t)head * NT + Tq) * QTILE_B;
    bf16x8 qa; u32x2 qbr; f32x4 km4; float gate;
    auto q_loads = [&]() {
        asm volatile("s_nop 4\n\tglobal_load_dwordx4 %0, %1, %2" : "=v"(qa) : "v"(lane16), "s"(qt) : "memory");
        asm volatile("s_nop 4\n\tglobal_load_dwordx2 %0, %1, %2" : "=v"(qbr) : "v"((unsigned)(1024 + r * 8)), "s"(qt) : "memory");
        asm volatile("s_nop 4\n\tglobal_load_dwordx4 %0, %1, %2" : "=v"(km4) : "v"((unsigned)((lane < 40 ? lane : 0) * 16)), "s"(ws + WS_KMAX + (size_t)head * 640) : "memory");
        asm volatile("s_nop 4\n\tglobal_load_dword %0, %1, %2" : "=v"(gate) : "v"((unsigned)((Tq * 32 + r) * 4)), "s"(ws + WS_GATE) : "memory");
    };
    auto wait_seen = [&]() { for (int it = 0; it < SPIN_MAX; ++it) { if (ctl[1] != 0u) break; __builtin_amdgcn_s_sleep(1); } };
    auto gates = [&]() {
        const int aidx = (xcd >> 1) * 4 + (slot & 3);
        const u32x4* gr = (const u32x4*)gw + (lane & 7);
#pragma unroll 1
        for (int ps = 0; ps < 2; ++ps) {
            const int tok = aidx * 128 + wid * 16 + ps * 8 + (lane >> 3);
            const u32x4* xr = (const u32x4*)(x + (size_t)tok * DM) + (lane & 7);
            float sm = 0.f;
#pragma unroll
            for (int i = 0; i < 10; ++i) { const u32x4 xv = xr[i * 8], gv = gr[i * 8];
#pragma unroll
                for (int k = 0; k < 4; ++k) { const unsigned a_ = xv[k], b_ = gv[k]; sm = __builtin_amdgcn_fdot2_f32_bf16(__builtin_bit_cast(bf16x2_t, a_), __builtin_bit_cast(bf16x2_t, b_), sm, false); } }
            sm = dpp_add<0xB1>(sm); sm = dpp_add<0x4E>(sm); sm = dpp_add<0x141>(sm);
            const float lg = bf_round(sm);
            const float g = bf_round(__builtin_amdgcn_rcpf(1.f + __builtin_amdgcn_exp2f(lg * -1.4426950408889634f)));
            if ((lane & 7) == 0) st4f(ws + WS_GATE + tok * 4, g);
        }
    };
    GDma<true> d2; d2.init(xbase, (const char*)w1 + ct * 192, FF * 2, lds, lane, k0);
    u64 pc0; asm volatile("s_getpc_b64 %0" : "=s"(pc0));
    if (wid < 4) {
        GDma<false> d1; d1.init(xbase, (const char*)wqkv + ct * 256, 3 * DM * 2, lds, lane, k0);
        if (tile1) {
#pragma unroll
            for (int jj = 0; jj < 4; ++jj) d1.xp(d1.kact(0), 0, 4 * wid + jj, jj & 1);
        }
        if (tid == 0) { ctl[0] = 0u; ctl[1] = 0u; }
        {
#pragma unroll
            for (int i = 0; i < 7; ++i) {
                int u = (bid * 4 + wid) * 7 + i; if (u > 6879) u = 6879;
                const char* src = u < 2560 ? (const char*)x + (size_t)u * 1024 : u < 4960 ? (const char*)wqkv + (size_t)(u - 2560) * 1024 : (const char*)w1 + (size_t)(u - 4960) * 1024;
                glds16(src, lane16, lds0 + 152 * 1024);
            }
        }
        if (tile1) asm volatile("s_waitcnt vmcnt(7) lgkmcnt(0)\n\ts_barrier" ::: "memory");
        if (slot * 4 + wid < 44) glds16((const char*)((pc0 + 15ull) & ~15ull) + (slot * 4 + wid) * 1024, lane16, lds0 + 152 * 1024);
        for (int pc = wid; pc < 10; pc += 4) rt_load(w2, wo, bid, lds0 + RT_OFF, pc, lane);
        if (!tile1) for (int pc = wid; pc < 10; pc += 4) rt_load(w2, wo, 256 + (xcd >> 1) * 4 + (slot & 3), lds0 + RT_OFF + 10240, pc, lane);
        if (wid == 0) glds16_sc1(ws + WS_EPOCH, lane < 1 ? 0u : 0u, lds0 + EP_OFF);
        auto tag_ready = [&]() {
            if (tid == 0) { const u64 tg = *(const volatile LAS u64*)(lds + EP_OFF) + 1ull; ctl[2] = (unsigned)tg; ctl[3] = (unsigned)(tg >> 32); }
        };
        if (!tile1) { asm volatile("s_waitcnt vmcnt(0)" ::: "memory"); tag_ready(); asm volatile("s_waitcnt lgkmcnt(0)\n\ts_barrier" ::: "memory"); }
        if (tile1) {
            const int wm = wid >> 1, wn = wid & 1;
            f32x16 acc[2][2] = {};
                        auto nohook = [&](int kc, bool before) {
                if (kc == 1 && before) { asm volatile("s_waitcnt vmcnt(0)" ::: "memory"); tag_ready(); }
            };
            if (ct >= 10) {
                gemm_loop<false, true, 2, 2>(acc, lds, wid, lane, nohook);
#pragma unroll
                for (int nt = 0; nt < 2; ++nt) {
                    const int phi = (ct - 10) * 128 + wn * 64 + nt * 32 + r;
                    const int hd = (phi * 3277) >> 16, d = phi - hd * 20;
                    const unsigned dst = (unsigned)WS_KV + (unsigned)(hd * NT + rt * 4 + wm * 2) * KVTILE_B + V_OFF + (h * V_ROWS + d) * 16;
#pragma unroll
                    for (int mt = 0; mt < 2; ++mt)
#pragma unroll
                        for (int s = 0; s < 2; ++s) {
                            const f32x16& a = acc[mt][nt];
                            const u32x4 w = {cvtpk(a[8 * s], a[8 * s + 1]), cvtpk(a[8 * s + 2], a[8 * s + 3]), cvtpk(a[8 * s + 4], a[8 * s + 5]), cvtpk(a[8 * s + 6], a[8 * s + 7])};
                            st16(rs, dst + mt * KVTILE_B + s * V_STEP, w);
                        }
                }
                BARL();
            } else {
                gemm_loop<false, false, 2, 2>(acc, lds, wid, lane, nohook);
                const bool isQ = ct < 5;
                const float sc = isQ ? QSCALE : 1.f;
                const unsigned img = (unsigned)(isQ ? WS_Q : WS_KV), tileb = isQ ? QTILE_B : KVTILE_B;
                const int G = (isQ ? ct : ct - 5) * 32 + wn * 16 + h;
                u32x2 P[2][8]; float nrm[8];
#pragma unroll
                for (int p = 0; p < 8; ++p) {
                    const int nt = p >> 2, g = p & 3; float m = 0.f;
#pragma unroll
                    for (int mt = 0; mt < 2; ++mt) {
                        const f32x16& a = acc[mt][nt];
                        P[mt][p] = (u32x2){cvtpk(a[4 * g] * sc, a[4 * g + 1] * sc), cvtpk(a[4 * g + 2] * sc, a[4 * g + 3] * sc)};
                        m = fmaxf(m, __builtin_fmaf(a[4 * g], a[4 * g], a[4 * g + 1] * a[4 * g + 1]) + __builtin_fmaf(a[4 * g + 2], a[4 * g + 2], a[4 * g + 3] * a[4 * g + 3]));
                    }
                    nrm[p] = m;
                }
#pragma unroll
                for (int p = 0; p < 8; ++p) {
                    const int gam = G + 2 * p, hd = (gam * 205) >> 10, dg = gam - 5 * hd;
                    const unsigned base = img + (unsigned)(hd * NT + rt * 4 + wm * 2) * tileb;
#pragma unroll
                    for (int mt = 0; mt < 2; ++mt) {
                        const unsigned bm = base + mt * tileb;
                        if (dg < 2) {
                            if (p < 7) st16(rs, bm + dg * 512 + r * 16, (u32x4){P[mt][p][0], P[mt][p][1], P[mt][(p + 1) & 7][0], P[mt][(p + 1) & 7][1]});
                            else st8(rs, bm + dg * 512 + r * 16, P[mt][p]);
                        } else if (dg == 4) st8(rs, bm + 1024 + r * 8, P[mt][p]);
                        else if (p == 0) st8(rs, bm + (dg - 2) * 512 + r * 16 + 8, P[mt][p]);
                    }
                }
                if (!isQ) {
#pragma unroll
                    for (int i = 0; i < 8; ++i) { float m = nrm[i]; m = dpp_max<0xB1>(m); m = dpp_max<0x4E>(m); m = dpp_max<0x141>(m); m = dpp_max<0x140>(m);
                        auto rr = __builtin_amdgcn_permlane16_swap(__float_as_uint(m), __float_as_uint(m), false, false); nrm[i] = fmaxf(__uint_as_float(rr[0]), __uint_as_float(rr[1])); }
                    float mine = nrm[0];
#pragma unroll
                    for (int i = 1; i < 8; ++i) mine = r == i ? nrm[i] : mine;
                    if (r < 8) st4f(ws + WS_KMAX + (size_t)((G + 2 * r) * 32 + rt * 2 + wm) * 4, mine);
                }
                BARL();
            }
        } else { gates(); BARL(); }
        BARL();
        f32x16 acc2[1][3] = {};
        auto hook2 = [&](int kc, bool before) {
            if (kc == 2) { if (before) asm volatile("s_waitcnt vmcnt(0)" ::: "memory");
                           else if (tid == 0) flag_set(ws + WS_F1 + bid * 8, ((u64)ctl[3] << 32) | ctl[2]); }
        };
        gemm_loop<true, false, 1, 3>(acc2, lds, wid, lane, hook2);
        wait_seen();
        q_loads();
        {   const int tok = rt * 128 + wid * 32 + r, rho = tok >> 4;
#pragma unroll
            for (int nt = 0; nt < 3; ++nt) {
                const unsigned dst = (unsigned)WS_AF + (unsigned)(rho * NSIG + ct * 3 + nt) * 1024 + (h * 16 + (r & 15)) * 16;
                float sv[16];
#pragma unroll
                for (int i = 0; i < 16; ++i) { const float v = acc2[0][nt][i]; sv[i] = v * __builtin_amdgcn_rcpf(1.f + __builtin_amdgcn_exp2f(v * -1.4426950408889634f)); }
#pragma unroll
                for (int s = 0; s < 2; ++s) {
                    const u32x4 w = {cvtpk(sv[8 * s], sv[8 * s + 1]), cvtpk(sv[8 * s + 2], sv[8 * s + 3]), cvtpk(sv[8 * s + 4], sv[8 * s + 5]), cvtpk(sv[8 * s + 6], sv[8 * s + 7])};
                    st16(rs, dst + s * 512, w);
                }
            }
        }
        asm volatile("s_waitcnt vmcnt(6)" : "+v"(qa), "+v"(qbr), "+v"(km4), "+v"(gate) :: "memory");
    } else {
        const int lw = wid - 4;
        auto side_stores = [&]() {
            for (int i = lw; i < 10; i += 4) rt_store(rs, bid, lds + RT_OFF, i, lane);
            if (!tile1) for (int i = lw; i < 10; i += 4) rt_store(rs, 256 + (xcd >> 1) * 4 + (slot & 3), lds + RT_OFF + 10240, i, lane);
            if (lw == 0) { const int i = bid * 64 + lane;
                const unsigned f = (i & 1) ? 0u : ONES2; st16(rs, (unsigned)WS_KV + (unsigned)(i >> 3) * KVTILE_B + V_OFF + ((i >> 2) & 1) * V_STEP + (((i >> 1) & 1) * V_ROWS + 20 + (i & 1)) * 16, (u32x4){f, f, f, f}); }
        };
        if (tile1) {
            GDma<false> d1; d1.init(xbase, (const char*)wqkv + ct * 256, 3 * DM * 2, lds, lane, k0);
#pragma unroll
            for (int jj = 0; jj < 4; ++jj) d1.wp(d1.kact(0), 0, lw, jj);
            d1.issue(1, 1, lw);
            WAITB(8);
            d1.issue(2, 2, lw); d1.issue(3, 3, lw);
            for (int kc = 0; kc < 9; ++kc) {
                if (kc < 7) WAITB(16); else if (kc == 7) WAITB(15); else WAITB(14);
                if (kc + 4 < 10) d1.issue(kc + 4, kc & 3, lw);
                if (kc == 4) side_stores();
                if (kc >= 6) d2.issue(kc - 6, kc - 6, lw);
            }
            asm volatile("s_barrier" ::: "memory");
        } else {
            asm volatile("s_barrier" ::: "memory");
            side_stores(); gates();
            BARL();
            d2.issue(0, 0, lw); d2.issue(1, 1, lw); d2.issue(2, 2, lw);
        }
        WAITB(14);
        { int st = 0;
          for (int kc = 0; kc < 9; ++kc) {
              if (kc < 8) WAITB(7); else WAITB(0);
              if (kc + 3 < 10) d2.issue(kc + 3, st, lw);
              st = st == 2 ? 0 : st + 1;
          } }
        if (lw == 0) {
            const u64 tag = ((u64)ctl[3] << 32) | ctl[2];
            for (int it = 0; it < SPIN_MAX; ++it) {
                bool ok = true;
#pragma unroll
                for (int i = 0; i < 4; ++i) ok &= flag_get(ws + WS_F1 + (lane + 64 * i) * 8) == tag;
                if (__all(ok)) break;
                __builtin_amdgcn_s_sleep(2);
            }
            __builtin_amdgcn_fence(__ATOMIC_ACQUIRE, "agent");
            asm volatile("s_waitcnt vmcnt(0)" ::: "memory");
            if (lane == 0) { ctl[1] = 1u; if (bid == 0) flag_set(ws + WS_EPOCH, tag); }
        } else wait_seen();
        q_loads();
        for (int j = 0; j < 21; ++j) glds16(kv + (size_t)(lw + 4 * j) * 1024, lane16, lds0 + (lw + 4 * j) * 1024);
        asm volatile("s_waitcnt vmcnt(21)" : "+v"(qa), "+v"(qbr), "+v"(km4), "+v"(gate) :: "memory");
    }
    float km = fmaxf(fmaxf(km4[0], km4[1]), fmaxf(km4[2], km4[3]));
    km = dpp_max<0xB1>(km); km = dpp_max<0x4E>(km); km = dpp_max<0x141>(km);
    km = __builtin_amdgcn_sqrtf(km);
    float M;
    { const float c0 = rdl(km, 0), c1 = rdl(km, 8), c2 = rdl(km, 16), c3 = rdl(km, 24), c4 = rdl(km, 32);
      const u32x4 qw = __builtin_bit_cast(u32x4, qa);
      float n0, n1, n2;
      { const float a = bf_lo(qw[0]), b = bf_hi(qw[0]), c = bf_lo(qw[1]), d = bf_hi(qw[1]); n0 = (a * a + b * b) + (c * c + d * d); }
      { const float a = bf_lo(qw[2]), b = bf_hi(qw[2]), c = bf_lo(qw[3]), d = bf_hi(qw[3]); n1 = (a * a + b * b) + (c * c + d * d); }
      { const unsigned q0 = qbr[0], q1 = qbr[1]; const float a = bf_lo(q0), b = bf_hi(q0), c = bf_lo(q1), d = bf_hi(q1); n2 = (a * a + b * b) + (c * c + d * d); }
      float part = __builtin_amdgcn_sqrtf(n0) * (h ? c1 : c0) + __builtin_amdgcn_sqrtf(n1) * (h ? c3 : c2);
      if (!h) part += __builtin_amdgcn_sqrtf(n2) * c4;
      M = swap32_add(part) * 1.01f; }
    const s16x4 qb = make_qb(qbr[0], qbr[1], M, h);
    const int vrow = r <= 20 ? r : (r < 28 ? 21 : r - 12);
    const int voff = V_OFF + (h * V_ROWS + vrow) * 16, kboff = h ? V_OFF + 20 * 16 : 1024 + r * 8;
    lds_cptr kaC = (lds_cptr)lds + lane * 16, kbC = (lds_cptr)lds + kboff, vC = (lds_cptr)lds + voff;
    lds_cptr kaN = kaC + K2_BIG, kbN = kbC + K2_BIG, vN = vC + K2_BIG;
    const f32x16 zero = {};
    f32x16 o = {}, sA, sB;
    bf16x8 ka, va, vb; s16x4 kb;
    u32x4 plo, phi;
    if (wid >= 4) asm volatile("s_waitcnt vmcnt(15)" ::: "memory");
    asm volatile("s_barrier" ::: "memory");
    ka = lds128(kaC); kb = lds64(kbC);
    sA = MFMA32(ka, qa, zero); sA = MFMA8(kb, qb, sA);
    ka = lds128(kaC + KVTILE_B); kb = lds64(kbC + KVTILE_B);
    va = lds128(vC);
    vb = (bf16x8){}; phi = (u32x4){};
    f32x4 e3 = {};
    const int ng = (xcd & 1) * 4 + (slot & 3), bt = (xcd >> 1) * 8 + (slot >> 2);
    const char* ap = ws + WS_AF + (size_t)bt * 4 * NSIG * 1024;
    const char* bp = ws + WS_BF + (size_t)ng * NSIG * 5 * 1024;
        const int rot = ((slot >> 2) + 3 * (slot & 3)) % 6;
    auto hs = [&](int i) { int st = i + rot; if (st >= 6) st -= 6; return wid * 6 + st; };
    bf16x8 a[3][4], b[3][5];
    auto ldA1 = [&](int sg, int mt, bf16x8& d) { gld128_sc1(d, ap + ((size_t)mt * NSIG + sg) * 1024, lane16); };
    auto ldB1 = [&](int sg, int nt, bf16x8& d) { gld128(d, bp + sg * 5120 + nt * 1024, lane16); };
    const u64 tag = ((u64)ctl[3] << 32) | ctl[2];
#define EXP4(X, i) do { X[i] = __builtin_amdgcn_exp2f(X[i]); X[i + 1] = __builtin_amdgcn_exp2f(X[i + 1]); X[i + 2] = __builtin_amdgcn_exp2f(X[i + 2]); X[i + 3] = __builtin_amdgcn_exp2f(X[i + 3]); } while (0)
#define KADDR(A, i) (((i) < 16) ? (A##C + (i) * KVTILE_B) : (A##N + ((i) - 16) * KVTILE_B))
#define STEP(C, N, i, X0, X1) do { \
        bf16x8 ka2, va2, vb2; s16x4 kb2; \
        N = MFMA32(ka, qa, zero); SBAR(); \
        ka2 = lds128(KADDR(ka, (i) + 2)); \
        phi[2] = cvtpk(e3[0], e3[1]); phi[3] = cvtpk(e3[2], e3[3]); PIN(phi); SBAR(); EXP4(C, 0); PIN(C); SBAR(); \
        o = MFMA32(vb, __builtin_bit_cast(bf16x8, phi), o); SBAR(); \
        X0; \
        vb2 = lds128(vC + (i) * KVTILE_B + V_STEP); \
        plo[0] = cvtpk(C[0], C[1]); plo[1] = cvtpk(C[2], C[3]); PIN(plo); SBAR(); EXP4(C, 4); PIN(C); SBAR(); \
        N = MFMA8(kb, qb, N); SBAR(); \
        kb2 = lds64(KADDR(kb, (i) + 2)); \
        plo[2] = cvtpk(C[4], C[5]); plo[3] = cvtpk(C[6], C[7]); PIN(plo); SBAR(); EXP4(C, 8); PIN(C); SBAR(); \
        o = MFMA32(va, __builtin_bit_cast(bf16x8, plo), o); SBAR(); \
        X1; \
        va2 = lds128(KADDR(v, (i) + 1)); \
        phi[0] = cvtpk(C[8], C[9]); phi[1] = cvtpk(C[10], C[11]); PIN(phi); SBAR(); \
        e3[0] = __builtin_amdgcn_exp2f(C[12]); e3[1] = __builtin_amdgcn_exp2f(C[13]); e3[2] = __builtin_amdgcn_exp2f(C[14]); e3[3] = __builtin_amdgcn_exp2f(C[15]); PIN(e3); SBAR(); \
        ka = ka2; kb = kb2; va = va2; vb = vb2; \
    } while (0)
#define PIECE(j) do { if (8 * (j) + 7 < 42 || 8 * (j) + wid < 42) glds16(src + (j) * 8192, lane16, dst + (j) * 8192); } while (0)
    const char* src = kv + (84 + wid) * 1024; unsigned dst = lds0 + wid * 1024 + 2 * K2_BIG;
#pragma unroll 1
    for (int c = 0; c < 3; ++c) {
        STEP(sA, sB, 0, , ); STEP(sB, sA, 1, , ); STEP(sA, sB, 2, , ); STEP(sB, sA, 3, , ); STEP(sA, sB, 4, , );
        if (c == 0) { if (wid >= 4) asm volatile("s_waitcnt vmcnt(10)" ::: "memory"); asm volatile("s_barrier" ::: "memory"); }
        STEP(sB, sA, 5, , ); STEP(sA, sB, 6, , ); STEP(sB, sA, 7, , ); STEP(sA, sB, 8, , ); STEP(sB, sA, 9, , ); STEP(sA, sB, 10, , ); STEP(sB, sA, 11, , ); STEP(sA, sB, 12, , );
        if (c == 2 && wid == 0) {
            for (int it = 0; it < SPIN_MAX; ++it) {
                if (__all(flag_get(ws + WS_F2 + ((bt >> 1) * 16 + (lane & 15)) * 8) == tag)) break;
                __builtin_amdgcn_s_sleep(2);
            }
        }
        WAITB(0);
        if (c == 0 && tid == 0) flag_set(ws + WS_F2 + (rt * 16 + ct) * 8, tag);
        if (c < 2) { STEP(sB, sA, 13, PIECE(0), PIECE(1)); STEP(sA, sB, 14, PIECE(2), PIECE(3)); STEP(sB, sA, 15, PIECE(4), PIECE(5)); }
        else { STEP(sB, sA, 13, , ); STEP(sA, sB, 14, , ); STEP(sB, sA, 15, , ); }
        const int d = (c == 1) ? -2 * K2_BIG : K2_BIG;
        kaC = kaN; kbC = kbN; vC = vN; kaN += d; kbN += d; vN += d;
        src += 42 * 1024; dst -= 2 * K2_BIG;
    }
#define PA(s, m) ldA1(hs(s), m, a[s][m])
#define PB(s, n) ldB1(hs(s), n, b[s][n])
    STEP(sA, sB, 0, PA(0, 0), PA(0, 1)); STEP(sB, sA, 1, PA(0, 2), PA(0, 3)); STEP(sA, sB, 2, PB(0, 0), PB(0, 1)); STEP(sB, sA, 3, PB(0, 2), PB(0, 3));
    STEP(sA, sB, 4, PB(0, 4), PA(1, 0)); STEP(sB, sA, 5, PA(1, 1), PA(1, 2)); STEP(sA, sB, 6, PA(1, 3), PB(1, 0)); STEP(sB, sA, 7, PB(1, 1), PB(1, 2));
    STEP(sA, sB, 8, PB(1, 3), PB(1, 4)); STEP(sB, sA, 9, PA(2, 0), PA(2, 1)); STEP(sA, sB, 10, PA(2, 2), PA(2, 3)); STEP(sB, sA, 11, PB(2, 0), PB(2, 1));
    STEP(sA, sB, 12, PB(2, 2), PB(2, 3)); STEP(sB, sA, 13, PB(2, 4), ); STEP(sA, sB, 14, , ); STEP(sB, sA, 15, , );
    phi[2] = cvtpk(e3[0], e3[1]); phi[3] = cvtpk(e3[2], e3[3]);
    o = MFMA32(vb, __builtin_bit_cast(bf16x8, phi), o);
    float l; { auto rr = __builtin_amdgcn_permlane32_swap(__float_as_uint(o[8]), __float_as_uint(o[8]), false, false); l = __uint_as_float(rr[1]); }
    if (__builtin_expect(__any(!(l >= 8.6736174e-19f && l <= 1.0e30f)), 0)) {
        asm volatile("s_waitcnt vmcnt(0)" ::: "memory");
        attn_fallback(o, kv, qa, qbr[0], qbr[1], lane, kboff, voff);
        auto rr = __builtin_amdgcn_permlane32_swap(__float_as_uint(o[8]), __float_as_uint(o[8]), false, false); l = __uint_as_float(rr[1]);
    }
    const float f = gate * __builtin_amdgcn_rcpf(l);
    const int tok = Tq * 32 + r;
    char* ab = ws + WS_AF + (size_t)(tok >> 4) * NSIG * 1024 + (tok & 15) * 16;
#pragma unroll
    for (int g = 0; g < 3; ++g) {
        const int gam = head * 5 + 2 * g + h;
        const int sig = 48 + (gam >> 3), c = ((gam >> 2) & 1) * 2 + (gam & 1), half = (gam >> 1) & 1;
        const u32x2 w = {cvtpk(o[4 * g] * f, o[4 * g + 1] * f), cvtpk(o[4 * g + 2] * f, o[4 * g + 3] * f)};
        if (g < 2 || h == 0) __hip_atomic_store((u64*)(ab + sig * 1024 + c * 256 + half * 8), ((u64)w[1] << 32) | w[0], __ATOMIC_RELAXED, __HIP_MEMORY_SCOPE_AGENT);
    }
    {
        f32x4 acc[4][5] = {};
        auto loadA = [&](int sg, bf16x8 (&da)[4]) {
#pragma unroll
            for (int mt = 0; mt < 4; ++mt) gld128_sc1(da[mt], ap + ((size_t)mt * NSIG + sg) * 1024, lane16);
        };
        auto loadB = [&](int sg, bf16x8 (&db)[5]) {
#pragma unroll
            for (int nt = 0; nt < 5; ++nt) gld128(db[nt], bp + sg * 5120 + nt * 1024, lane16);
        };
        auto mma = [&](bf16x8 (&da)[4], bf16x8 (&db)[5]) {
            SBAR();
#pragma unroll
            for (int nt = 0; nt < 5; ++nt)
#pragma unroll
                for (int mt = 0; mt < 4; ++mt) acc[mt][nt] = MFMA16(db[nt], da[mt], acc[mt][nt]);
            SBAR();
        };
        wait9<3>(a[0], b[0]);
        asm volatile("" : "+v"(a[1][0]), "+v"(a[1][1]), "+v"(a[1][2]), "+v"(a[1][3]), "+v"(b[1][0]), "+v"(b[1][1]), "+v"(b[1][2]), "+v"(b[1][3]), "+v"(b[1][4]));
        asm volatile("" : "+v"(a[2][0]), "+v"(a[2][1]), "+v"(a[2][2]), "+v"(a[2][3]), "+v"(b[2][0]), "+v"(b[2][1]), "+v"(b[2][2]), "+v"(b[2][3]), "+v"(b[2][4]));
        mma(a[0], b[0]); loadA(hs(3), a[0]); loadB(hs(3), b[0]);
        mma(a[1], b[1]); loadA(hs(4), a[1]); loadB(hs(4), b[1]);
        asm volatile("s_waitcnt vmcnt(18)" ::: "memory");
        if (lane == 0) {
            const unsigned old = __hip_atomic_fetch_add((unsigned*)(lds + CTL_OFF), 1u, __ATOMIC_RELAXED, __HIP_MEMORY_SCOPE_WORKGROUP);
            if (old == 7u) flag_set(ws + WS_F3 + (qblk * 32 + head) * 8, tag);
        }
        mma(a[2], b[2]); loadA(hs(5), a[2]); loadB(hs(5), b[2]);
        float og[4];
#pragma unroll
        for (int mt = 0; mt < 4; ++mt) asm volatile("s_nop 4\n\tglobal_load_dword %0, %1, %2" : "=v"(og[mt]) : "v"((unsigned)((bt * 64 + mt * 16 + (lane & 15)) * 4)), "s"(ws + WS_GATE) : "memory");
        wait9<22>(a[0], b[0]); mma(a[0], b[0]); loadB(48 + wid, b[0]);
        wait9<18>(a[1], b[1]); mma(a[1], b[1]); loadB(56 + wid, b[1]);
        wait9<14>(a[2], b[2]); mma(a[2], b[2]);
        if (wid < 4) {
#pragma unroll
            for (int nt = 0; nt < 5; ++nt) gld128_rw(b[2][nt], bp + (64 + wid) * 5120 + nt * 1024, lane16);
        }
        asm volatile("s_waitcnt vmcnt(0)" : "+v"(og[0]), "+v"(og[1]), "+v"(og[2]), "+v"(og[3]) :: "memory");
#pragma unroll
        for (int mt = 0; mt < 4; ++mt) { const float f1 = bf_round(1.f - og[mt]);
#pragma unroll
            for (int nt = 0; nt < 5; ++nt) acc[mt][nt] *= f1; }
        if (wid == 0) {
            for (int it = 0; it < SPIN_MAX; ++it) {
                if (__all(flag_get(ws + WS_F3 + ((bt >> 2) * 32 + r) * 8) == tag)) break;
                __builtin_amdgcn_s_sleep(2);
            }
        }
        asm volatile("s_barrier" ::: "memory");
        loadA(48 + wid, a[0]); loadA(56 + wid, a[1]);
        a[2][0] = (bf16x8){}; a[2][1] = (bf16x8){}; a[2][2] = (bf16x8){}; a[2][3] = (bf16x8){};
        if (wid < 4) {
#pragma unroll
            for (int mt = 0; mt < 4; ++mt) gld128_sc1_rw(a[2][mt], ap + ((size_t)mt * NSIG + 64 + wid) * 1024, lane16);
        }
        wait9<4>(a[0], b[0]); mma(a[0], b[0]);
        wait9<0>(a[1], b[1]); wait9<0>(a[2], b[2]); mma(a[1], b[1]); mma(a[2], b[2]);
#pragma unroll
        for (int mt = 0; mt < 4; ++mt)
#pragma unroll
            for (int nt = 0; nt < 5; ++nt) *(f32x4*)(lds + (wid * 20 + mt * 5 + nt) * 1024 + lane16) = acc[mt][nt];
        BARL();
#pragma unroll
        for (int i = 0; i < 3; ++i) { const int t = wid + 8 * i;
            if (t < 20) {
                f32x4 sum = *(const f32x4*)(lds + t * 1024 + lane16);
#pragma unroll
                for (int w = 1; w < 8; ++w) sum += *(const f32x4*)(lds + (w * 20 + t) * 1024 + lane16);
                const int m4 = t / 5, nt = t - m4 * 5;
                const int tok2 = bt * 64 + m4 * 16 + (lane & 15);
                *(u32x2*)(out + (size_t)tok2 * DM + ng * 80 + nt * 16 + (lane >> 4) * 4) = (u32x2){cvtpk(sum[0], sum[1]), cvtpk(sum[2], sum[3])};
            } }
    }
}

extern "C" void kernel_launch(void* const* d_in, const int* in_sizes, int n_in, void* d_out, int out_size, void* d_ws, size_t ws_size, hipStream_t stream) {
    const unsigned short* x = (const unsigned short*)d_in[0];
    const unsigned short* wqkv = (const unsigned short*)d_in[1];
    const unsigned short* wo = (const unsigned short*)d_in[2];
    const unsigned short* w1 = (const unsigned short*)d_in[4];
    const unsigned short* w2 = (const unsigned short*)d_in[5];
    const unsigned short* gw = (const unsigned short*)d_in[6];
    (void)hipFuncSetAttribute((const void*)gated_mix_mega, hipFuncAttributeMaxDynamicSharedMemorySize, LDS_ALL);
    gated_mix_mega<<<256, 512, LDS_ALL, stream>>>(x, wqkv, wo, w1, w2, gw, (char*)d_ws, (unsigned short*)d_out);
}
```

```cpp
#include <hip/hip_runtime.h>
#include <hip/hip_bf16.h>
#include <stdint.h>

typedef short bf16x8 __attribute__((ext_vector_type(8)));
typedef short s16x4 __attribute__((ext_vector_type(4)));
typedef float f32x16 __attribute__((ext_vector_type(16)));
typedef float f32x4 __attribute__((ext_vector_type(4)));
typedef float f32x2 __attribute__((ext_vector_type(2)));
typedef unsigned u32x4 __attribute__((ext_vector_type(4)));
typedef unsigned u32x2 __attribute__((ext_vector_type(2)));
typedef __bf16 bf16x2_t __attribute__((ext_vector_type(2)));
#define LAS __attribute__((address_space(3)))
typedef LAS const char* lds_cptr;
#define DI __device__ __forceinline__
#define SBAR() __builtin_amdgcn_sched_barrier(0)
#define PIN(x) asm volatile("" : "+v"(x))
#define MFMA32(a, b, c) __builtin_amdgcn_mfma_f32_32x32x16_bf16(a, b, c, 0, 0, 0)
#define MFMA16(a, b, c) __builtin_amdgcn_mfma_f32_16x16x32_bf16(a, b, c, 0, 0, 0)
#define MFMA8(a, b, c) __builtin_amdgcn_mfma_f32_32x32x8bf16_1k(a, b, c, 0, 0, 0)

constexpr int S = 2048, DM = 640, NH = 32, DH = 20, FF = 1536;
constexpr int NT = S / 32;
constexpr int QTILE_B = 1280;
constexpr int KVTILE_B = 2688;
constexpr int V_OFF = 1280, V_STEP = 704, V_ROWS = 22;
constexpr int NSIG = 68;
constexpr size_t WS_Q = 0;
constexpr size_t WS_KV = WS_Q + (size_t)NH * NT * QTILE_B;
constexpr size_t WS_AF = WS_KV + (size_t)NH * NT * KVTILE_B;
constexpr size_t WS_BF = WS_AF + (size_t)128 * NSIG * 1024;
constexpr size_t WS_KMAX = WS_BF + (size_t)8 * NSIG * 5 * 1024;
constexpr size_t WS_GATE = WS_KMAX + (size_t)160 * 32 * 4;
constexpr size_t WS_F1 = WS_GATE + (size_t)S * 4;
constexpr size_t WS_F2 = WS_F1 + 2048;
constexpr size_t WS_F3 = WS_F2 + 2048;
constexpr size_t WS_EPOCH = WS_F3 + 2048;
constexpr size_t WS_END = WS_EPOCH + 256;
constexpr float QSCALE = 0.22360679774997896f * 1.4426950408889634f;
constexpr unsigned ONES2 = 0x3F803F80u;

DI unsigned cvtpk(float lo, float hi) { f32x2 v = {lo, hi}; bf16x2_t b = __builtin_convertvector(v, bf16x2_t); return __builtin_bit_cast(unsigned, b); }
DI float bf_lo(unsigned u) { return __uint_as_float(u << 16); }
DI float bf_hi(unsigned u) { return __uint_as_float(u & 0xffff0000u); }
DI float bf_round(float x) { return bf_lo(cvtpk(x, x)); }
DI void glds16(const void* sbase, unsigned voff, unsigned lds_base) {
    unsigned sv;
    asm volatile("s_mov_b32 %0, m0\n\ts_mov_b32 m0, %3\n\ts_nop 0\n\tglobal_load_lds_dwordx4 %1, %2\n\ts_mov_b32 m0, %0"
                 : "=&s"(sv) : "v"(voff), "s"(sbase), "s"(lds_base) : "memory");
}
DI void glds16_sc1(const void* sbase, unsigned voff, unsigned lds_base) {
    unsigned sv;
    asm volatile("s_mov_b32 %0, m0\n\ts_mov_b32 m0, %3\n\ts_nop 0\n\tglobal_load_lds_dwordx4 %1, %2 sc1\n\ts_mov_b32 m0, %0"
                 : "=&s"(sv) : "v"(voff), "s"(sbase), "s"(lds_base) : "memory");
}
typedef short v4i16_t __attribute__((ext_vector_type(4)));
DI s16x4 ldstr(lds_cptr p) { return __builtin_bit_cast(s16x4, __builtin_amdgcn_ds_read_tr16_b64_v4i16((LAS v4i16_t*)p)); }
DI bf16x8 lds128(lds_cptr p) { return *(const LAS bf16x8*)p; }
DI s16x4 lds64(lds_cptr p) { return *(const LAS s16x4*)p; }
DI unsigned rfl(unsigned x) { return __builtin_amdgcn_readfirstlane(x); }


DI float rdl(float x, int l) { return __uint_as_float(__builtin_amdgcn_readlane(__float_as_uint(x), l)); }
typedef __amdgpu_buffer_rsrc_t rsrc_t;
typedef unsigned long long u64;
DI rsrc_t ws_rsrc(char* ws) { return __builtin_amdgcn_make_buffer_rsrc(ws, 0, (int)WS_END, 0x00020000); }
DI void st16(rsrc_t rs, unsigned off, u32x4 v) { __builtin_amdgcn_raw_buffer_store_b128(v, rs, off, 0, 16); }
DI void st8(rsrc_t rs, unsigned off, u32x2 v) { __builtin_amdgcn_raw_buffer_store_b64(v, rs, off, 0, 16); }
DI void st4f(char* p, float v) { __hip_atomic_store((float*)p, v, __ATOMIC_RELAXED, __HIP_MEMORY_SCOPE_AGENT); }
DI void flag_set(char* p, u64 tag) { __hip_atomic_store((u64*)p, tag, __ATOMIC_RELAXED, __HIP_MEMORY_SCOPE_AGENT); }
DI u64 flag_get(const char* p) { return __hip_atomic_load((const u64*)p, __ATOMIC_RELAXED, __HIP_MEMORY_SCOPE_AGENT); }
template <int CTRL> DI float dpp_max(float x) { const float y = __uint_as_float(__builtin_amdgcn_update_dpp(0u, __float_as_uint(x), CTRL, 0xf, 0xf, true)); return fmaxf(x, y); }
template <int CTRL> DI float dpp_add(float x) { const float y = __uint_as_float(__builtin_amdgcn_update_dpp(0u, __float_as_uint(x), CTRL, 0xf, 0xf, true)); return x + y; }
DI void gld128(bf16x8& d, const char* sbase, unsigned voff) { asm volatile("global_load_dwordx4 %0, %1, %2" : "=v"(d) : "v"(voff), "s"(sbase) : "memory"); }
DI void gld128_sc1(bf16x8& d, const char* sbase, unsigned voff) { asm volatile("global_load_dwordx4 %0, %1, %2 sc1" : "=v"(d) : "v"(voff), "s"(sbase) : "memory"); }
DI void gld128_rw(bf16x8& d, const char* sbase, unsigned voff) { asm volatile("s_nop 4\n\tglobal_load_dwordx4 %0, %1, %2" : "+v"(d) : "v"(voff), "s"(sbase) : "memory"); }
DI void gld128_sc1_rw(bf16x8& d, const char* sbase, unsigned voff) { asm volatile("s_nop 4\n\tglobal_load_dwordx4 %0, %1, %2 sc1" : "+v"(d) : "v"(voff), "s"(sbase) : "memory"); }
template <int N> DI void wait9(bf16x8 (&a)[4], bf16x8 (&b)[5]) {
    asm volatile("s_waitcnt vmcnt(%9)" : "+v"(a[0]), "+v"(a[1]), "+v"(a[2]), "+v"(a[3]), "+v"(b[0]), "+v"(b[1]), "+v"(b[2]), "+v"(b[3]), "+v"(b[4]) : "i"(N) : "memory");
}
#define WAITB(n) asm volatile("s_waitcnt vmcnt(" #n ")\n\ts_barrier" ::: "memory")
#define BARL() asm volatile("s_waitcnt lgkmcnt(0)\n\ts_barrier" ::: "memory")

constexpr int P1_STG = 32768;
constexpr int RT_OFF = 131072;
constexpr int P2_STG = 28672;
constexpr int CTL_OFF = 163584;
DI constexpr int p2_base(int s) { return CTL_OFF - (s + 1) * P2_STG; }
constexpr int K2_BIG = 16 * KVTILE_B;
constexpr int EP_OFF = 151552;
constexpr int LDS_ALL = 163840;

template <bool N96>
struct GDma {
    static constexpr int NW = N96 ? 3 : 4;
    const char* xbase; const char* wbase; int ldw2, k0; unsigned lds0, xoff0, xoff1, woff[3];
    DI int kact(int kc) const { kc += k0; return kc >= 10 ? kc - 10 : kc; }
    DI static unsigned sbase(int st) { return N96 ? (unsigned)(CTL_OFF - (st + 1) * P2_STG) : (unsigned)(st * P1_STG); }
    DI void init(const char* xb, const char* wb, int ld, char* lds, int lane, int k0_) {
        xbase = xb; wbase = wb; ldw2 = ld; k0 = k0_; lds0 = (unsigned)(uintptr_t)lds;
        const int rr = lane >> 3;
        xoff0 = rr * 1280 + (((lane & 7) ^ ((lane >> 4) & 7)) << 4); xoff1 = rr * 1280 + (((lane & 7) ^ ((4 + (lane >> 4)) & 7)) << 4);
        if (N96) {
#pragma unroll
            for (int p = 0; p < 3; ++p) { const int o = 1024 * p + 16 * lane, row = (o * 21846) >> 22, col = o - row * 192; woff[p] = row * ldw2 + col; }
        } else woff[0] = (lane >> 4) * ldw2 + ((((lane & 15) ^ ((lane >> 4) << 2))) << 4);
    }
    DI void xp(int kc, int st, int j, int par) const { glds16(xbase + (size_t)j * 8 * 1280 + kc * 128, par ? xoff1 : xoff0, lds0 + sbase(st) + j * 1024); }
    DI void wp(int kc, int st, int lw, int jj) const {
        if (N96) glds16(wbase + (size_t)(kc * 64 + 16 * lw) * ldw2, woff[jj], lds0 + sbase(st) + 16384 + (3 * lw + jj) * 1024);
        else glds16(wbase + (size_t)(kc * 64 + 16 * lw + 4 * jj) * ldw2, woff[0], lds0 + sbase(st) + 16384 + (4 * lw + jj) * 1024);
    }
    DI void issue(int kc_, int st, int lw) const {
        const int kc = kact(kc_);
#pragma unroll
        for (int jj = 0; jj < NW; ++jj) wp(kc, st, lw, jj);
#pragma unroll
        for (int jj = 0; jj < 4; ++jj) xp(kc, st, 4 * lw + jj, jj & 1);
    }
};

DI void rt_load(const unsigned short* w2, const unsigned short* wo, int u, unsigned ldsb, int piece, int lane) {
    const int sig = u >> 2, s = (u >> 1) & 1, cb = u & 1;
    const char* src = (sig < 48 ? (const char*)w2 + (size_t)sig * 32 * 1280 : (const char*)wo + (size_t)(sig - 48) * 32 * 1280) + s * 16 * 1280 + cb * 640;
    const int o = piece * 1024 + lane * 16, row = (o * 6554) >> 22;
    glds16(src, row * 1280 + (o - row * 640), ldsb + piece * 1024);
}
DI void rt_store(rsrc_t rs, int u, const char* ldsb, int i, int lane) {
    const int sig = u >> 2, s = (u >> 1) & 1, cb = u & 1;
    const int nl = 2 * i + (lane >> 5), hh = (lane >> 4) & 1, q = (lane & 15) >> 2, p = lane & 3;
    lds_cptr a = (lds_cptr)ldsb + (4 * hh + q) * 640 + (16 * nl + 4 * p) * 2;
    const s16x4 lo = ldstr(a), hi = ldstr(a + 8 * 640);
    const bf16x8 f = __builtin_shufflevector(lo, hi, 0, 1, 2, 3, 4, 5, 6, 7);
    const int nu = cb * 20 + nl, ng = (nu * 205) >> 10;
    st16(rs, (unsigned)WS_BF + (unsigned)((ng * NSIG + sig) * 5 + (nu - 5 * ng)) * 1024 + (s * 32 + (lane & 31)) * 16, __builtin_bit_cast(u32x4, f));
}

template <bool N96, bool VT, int MT, int NTL, class HOOK>
DI void gemm_loop(f32x16 (&acc)[MT][NTL], char* lds, int wid, int lane, HOOK hook) {
    const int h = lane >> 5, r = lane & 31;
    constexpr int NST = N96 ? 3 : 4;
    unsigned xa[4];
    { const int row = (N96 ? wid * 32 : (wid >> 1) * 64) + r; const int sw = (row >> 1) & 7;
#pragma unroll
      for (int ks = 0; ks < 4; ++ks) xa[ks] = row * 128 + (((2 * ks + h) ^ sw) << 4); }
    unsigned wa[2];
    { const int blk = (lane >> 4) & 1, q = (lane & 15) >> 2, p = lane & 3;
      if (N96) wa[0] = wa[1] = 16384 + (8 * h + q) * 192 + blk * 32 + p * 8;
      else {
#pragma unroll
          for (int nt = 0; nt < 2; ++nt) { const int ch = (wid & 1) * 8 + nt * 4 + 2 * blk + (p >> 1); wa[nt] = 16384 + (8 * h + q) * 256 + ((ch ^ (q << 2)) << 4) + 8 * (p & 1); } } }
    bf16x8 xf[2][MT], wf[2][NTL];
    auto rdw = [&](int buf, int nt, lds_cptr sb, int ks) {
        lds_cptr a = N96 ? sb + wa[0] + ks * 16 * 192 + nt * 64 : sb + wa[nt & 1] + ks * 4096;
        const s16x4 lo = ldstr(a), hi = ldstr(a + (N96 ? 4 * 192 : 1024));
        wf[buf][nt] = __builtin_shufflevector(lo, hi, 0, 1, 2, 3, 4, 5, 6, 7);
    };
    auto rdx = [&](int buf, int mt, lds_cptr sb, int ks) { xf[buf][mt] = lds128(sb + xa[ks] + mt * 4096); };
    auto mm = [&](int buf, int mt, int nt) { acc[mt][nt] = VT ? MFMA32(xf[buf][mt], wf[buf][nt], acc[mt][nt]) : MFMA32(wf[buf][nt], xf[buf][mt], acc[mt][nt]); };
    { lds_cptr sb = (lds_cptr)lds + GDma<N96>::sbase(0); rdw(0, 0, sb, 0); rdx(0, 0, sb, 0); rdw(0, 1, sb, 0); if (N96) rdw(0, 2 % NTL, sb, 0); else rdx(0, 1 % MT, sb, 0); }
    int st = 0;
#define G_KSTEP(ks, NEXT) do { const int b = (ks) & 1, n = b ^ 1; lds_cptr np = (ks) < 3 ? sb : sn; const int nk = ((ks) + 1) & 3; \
        SBAR(); \
        if constexpr (N96) { \
            mm(b, 0, 0); SBAR(); if (NEXT) { rdw(n, 0, np, nk); rdx(n, 0, np, nk); } SBAR(); \
            mm(b, 0, 1); SBAR(); if (NEXT) { rdw(n, 1, np, nk); } SBAR(); \
            mm(b, 0, 2 % NTL); SBAR(); if (NEXT) { rdw(n, 2 % NTL, np, nk); } SBAR(); \
        } else { \
            mm(b, 0, 0); SBAR(); if (NEXT) { rdw(n, 0, np, nk); } SBAR(); \
            mm(b, 0, 1); SBAR(); if (NEXT) { rdx(n, 0, np, nk); } SBAR(); \
            mm(b, 1 % MT, 0); SBAR(); if (NEXT) { rdw(n, 1, np, nk); } SBAR(); \
            mm(b, 1 % MT, 1); SBAR(); if (NEXT) { rdx(n, 1 % MT, np, nk); } SBAR(); \
        } } while (0)
#pragma unroll 1
    for (int kc = 0; kc < 9; ++kc) {
        lds_cptr sb = (lds_cptr)lds + GDma<N96>::sbase(st);
        st = st == NST - 1 ? 0 : st + 1;
        lds_cptr sn = (lds_cptr)lds + GDma<N96>::sbase(st);
        G_KSTEP(0, true); G_KSTEP(1, true); G_KSTEP(2, true);
        hook(kc, true);
        BARL();
        hook(kc, false);
        G_KSTEP(3, true);
    }
    { lds_cptr sb = (lds_cptr)lds + GDma<N96>::sbase(st); lds_cptr sn = sb;
      G_KSTEP(0, true); G_KSTEP(1, true); G_KSTEP(2, true); G_KSTEP(3, false); }
}

DI float swap32_add(float x) { auto rr = __builtin_amdgcn_permlane32_swap(__float_as_uint(x), __float_as_uint(x), false, false); return __uint_as_float(rr[0]) + __uint_as_float(rr[1]); }
DI float swap32_max(float x) { auto rr = __builtin_amdgcn_permlane32_swap(__float_as_uint(x), __float_as_uint(x), false, false); return fmaxf(__uint_as_float(rr[0]), __uint_as_float(rr[1])); }
DI s16x4 make_qb(unsigned q0, unsigned q1, float M, int h) {
    const float mh = bf_hi(__float_as_uint(M)), r1 = M - mh, mm = bf_hi(__float_as_uint(r1)), r2 = r1 - mm, ml = bf_hi(__float_as_uint(r2));
    u32x2 w;
    w[0] = h ? ((__float_as_uint(mh) >> 16) | (__float_as_uint(mm) & 0xffff0000u)) ^ 0x80008000u : q0;
    w[1] = h ? (__float_as_uint(ml) >> 16) ^ 0x8000u : q1;
    return __builtin_bit_cast(s16x4, w);
}

DI void attn_fallback(f32x16& o, const char* kv, bf16x8 qa, unsigned q0, unsigned q1, int lane, int kboff, int voff) {
    const int h = lane >> 5;
    const f32x16 zero = {};
    float m = -INFINITY;
    const s16x4 qb0 = make_qb(q0, q1, 0.f, h);
    for (int t = 0; t < NT; ++t) {
        const char* tb = kv + t * KVTILE_B;
        const bf16x8 ka = *(const bf16x8*)(tb + lane * 16); const s16x4 kb = *(const s16x4*)(tb + kboff);
        f32x16 s = MFMA32(ka, qa, zero); s = MFMA8(kb, qb0, s);
#pragma unroll
        for (int i = 0; i < 16; ++i) m = fmaxf(m, s[i]);
    }
    m = swap32_max(m);
    const s16x4 qb = make_qb(q0, q1, m, h);
    o = zero;
    for (int t = 0; t < NT; ++t) {
        const char* tb = kv + t * KVTILE_B;
        const bf16x8 ka = *(const bf16x8*)(tb + lane * 16); const s16x4 kb = *(const s16x4*)(tb + kboff);
        const bf16x8 va = *(const bf16x8*)(tb + voff), vb = *(const bf16x8*)(tb + voff + V_STEP);
        f32x16 s = MFMA32(ka, qa, zero); s = MFMA8(kb, qb, s);
        u32x4 p0, p1;
#pragma unroll
        for (int i = 0; i < 4; ++i) { p0[i] = cvtpk(__builtin_amdgcn_exp2f(s[2 * i]), __builtin_amdgcn_exp2f(s[2 * i + 1])); p1[i] = cvtpk(__builtin_amdgcn_exp2f(s[8 + 2 * i]), __builtin_amdgcn_exp2f(s[9 + 2 * i])); }
        o = MFMA32(va, __builtin_bit_cast(bf16x8, p0), o);
        o = MFMA32(vb, __builtin_bit_cast(bf16x8, p1), o);
    }
}


constexpr int SPIN_MAX = 1 << 18;
__global__ __launch_bounds__(512, 2) void gated_mix_mega(const unsigned short* __restrict__ x, const unsigned short* __restrict__ wqkv, const unsigned short* __restrict__ wo, const unsigned short* __restrict__ w1,
                                                         const unsigned short* __restrict__ w2, const unsigned short* __restrict__ gw, char* __restrict__ ws, unsigned short* __restrict__ out) {
    extern __shared__ __attribute__((aligned(16))) char lds[];
    const int tid = threadIdx.x, lane = tid & 63, wid = rfl(tid >> 6);
    const int bid = blockIdx.x, xcd = bid & 7, slot = bid >> 3;
    {
        u64 pcq; asm volatile("s_getpc_b64 %0" : "=s"(pcq));
        if (slot < 2) glds16((const char*)((pcq + 15ull) & ~15ull) + (slot * 8 + wid) * 1024, lane * 16, (unsigned)(uintptr_t)lds + 153 * 1024);
    }
    const int h = lane >> 5, r = lane & 31;
    const rsrc_t rs = ws_rsrc(ws);
    const unsigned lds0 = (unsigned)(uintptr_t)lds, lane16 = lane * 16;
    volatile LAS unsigned* ctl = (volatile LAS unsigned*)(lds + CTL_OFF);
    const int rt = 4 * (xcd >> 1) + (slot & 3), ci = slot >> 2, ct = 8 * (xcd & 1) + ci;
    const bool tile1 = ct < 15;
    const char* xbase = (const char*)x + (size_t)rt * 128 * 1280;
    const int k0 = 0;
    const int head = xcd * 4 + (slot & 3), qblk = slot >> 2;
    const char* kv = ws + WS_KV + (size_t)head * NT * KVTILE_B;
    const int Tq = qblk * 8 + wid;
    const char* qt = ws + WS_Q + ((size_t)head * NT + Tq) * QTILE_B;
    bf16x8 qa; u32x2 qbr; f32x4 km4; float gate;
    auto q_loads = [&]() {
        asm volatile("s_nop 4\n\tglobal_load_dwordx4 %0, %1, %2" : "=v"(qa) : "v"(lane16), "s"(qt) : "memory");
        asm volatile("s_nop 4\n\tglobal_load_dwordx2 %0, %1, %2" : "=v"(qbr) : "v"((unsigned)(1024 + r * 8)), "s"(qt) : "memory");
        asm volatile("s_nop 4\n\tglobal_load_dwordx4 %0, %1, %2" : "=v"(km4) : "v"((unsigned)((lane < 40 ? lane : 0) * 16)), "s"(ws + WS_KMAX + (size_t)head * 640) : "memory");
        asm volatile("s_nop 4\n\tglobal_load_dword %0, %1, %2" : "=v"(gate) : "v"((unsigned)((Tq * 32 + r) * 4)), "s"(ws + WS_GATE) : "memory");
    };
    auto wait_seen = [&]() { for (int it = 0; it < SPIN_MAX; ++it) { if (ctl[1] != 0u) break; __builtin_amdgcn_s_sleep(1); } };
    auto gates = [&]() {
        const int aidx = (xcd >> 1) * 4 + (slot & 3);
        const u32x4* gr = (const u32x4*)gw + (lane & 7);
#pragma unroll 1
        for (int ps = 0; ps < 2; ++ps) {
            const int tok = aidx * 128 + wid * 16 + ps * 8 + (lane >> 3);
            const u32x4* xr = (const u32x4*)(x + (size_t)tok * DM) + (lane & 7);
            float sm = 0.f;
#pragma unroll
            for (int i = 0; i < 10; ++i) { const u32x4 xv = xr[i * 8], gv = gr[i * 8];
#pragma unroll
                for (int k = 0; k < 4; ++k) { const unsigned a_ = xv[k], b_ = gv[k]; sm = __builtin_amdgcn_fdot2_f32_bf16(__builtin_bit_cast(bf16x2_t, a_), __builtin_bit_cast(bf16x2_t, b_), sm, false); } }
            sm = dpp_add<0xB1>(sm); sm = dpp_add<0x4E>(sm); sm = dpp_add<0x141>(sm);
            const float lg = bf_round(sm);
            const float g = bf_round(__builtin_amdgcn_rcpf(1.f + __builtin_amdgcn_exp2f(lg * -1.4426950408889634f)));
            if ((lane & 7) == 0) st4f(ws + WS_GATE + tok * 4, g);
        }
    };
    GDma<true> d2; d2.init(xbase, (const char*)w1 + ct * 192, FF * 2, lds, lane, k0);
    u64 pc0; asm volatile("s_getpc_b64 %0" : "=s"(pc0));
    if (wid < 4) {
        GDma<false> d1; d1.init(xbase, (const char*)wqkv + ct * 256, 3 * DM * 2, lds, lane, k0);
        if (tile1) {
#pragma unroll
            for (int jj = 0; jj < 4; ++jj) d1.xp(d1.kact(0), 0, 4 * wid + jj, jj & 1);
        }
        if (tid == 0) { ctl[0] = 0u; ctl[1] = 0u; }
        {
#pragma unroll
            for (int i = 0; i < 7; ++i) {
                int u = (bid * 4 + wid) * 7 + i; if (u > 6879) u = 6879;
                const char* src = u < 2560 ? (const char*)x + (size_t)u * 1024 : u < 4960 ? (const char*)wqkv + (size_t)(u - 2560) * 1024 : (const char*)w1 + (size_t)(u - 4960) * 1024;
                glds16(src, lane16, lds0 + 152 * 1024);
            }
        }
        if (tile1) asm volatile("s_waitcnt vmcnt(7) lgkmcnt(0)\n\ts_barrier" ::: "memory");
        if (slot * 4 + wid < 37) glds16((const char*)((pc0 + 15ull) & ~15ull) + (slot * 4 + wid) * 1024, lane16, lds0 + 152 * 1024);
        for (int pc = wid; pc < 10; pc += 4) rt_load(w2, wo, bid, lds0 + RT_OFF, pc, lane);
        if (!tile1) for (int pc = wid; pc < 10; pc += 4) rt_load(w2, wo, 256 + (xcd >> 1) * 4 + (slot & 3), lds0 + RT_OFF + 10240, pc, lane);
        if (wid == 0) glds16_sc1(ws + WS_EPOCH, 0u, lds0 + EP_OFF);
        auto tag_ready = [&]() {
            if (tid == 0) { const u64 tg = *(const volatile LAS u64*)(lds + EP_OFF) + 1ull; ctl[2] = (unsigned)tg; ctl[3] = (unsigned)(tg >> 32); }
        };
        if (!tile1) { asm volatile("s_waitcnt vmcnt(0)" ::: "memory"); tag_ready(); asm volatile("s_waitcnt lgkmcnt(0)\n\ts_barrier" ::: "memory"); }
        if (tile1) {
            const int wm = wid >> 1, wn = wid & 1;
            f32x16 acc[2][2] = {};
            auto nohook = [&](int kc, bool before) {
                if (kc == 1 && before) { asm volatile("s_waitcnt vmcnt(0)" ::: "memory"); tag_ready(); }
            };
            if (ct >= 10) {
                gemm_loop<false, true, 2, 2>(acc, lds, wid, lane, nohook);
#pragma unroll
                for (int nt = 0; nt < 2; ++nt) {
                    const int phi = (ct - 10) * 128 + wn * 64 + nt * 32 + r;
                    const int hd = (phi * 3277) >> 16, d = phi - hd * 20;
                    const unsigned dst = (unsigned)WS_KV + (unsigned)(hd * NT + rt * 4 + wm * 2) * KVTILE_B + V_OFF + (h * V_ROWS + d) * 16;
#pragma unroll
                    for (int mt = 0; mt < 2; ++mt)
#pragma unroll
                        for (int s = 0; s < 2; ++s) {
                            const f32x16& a = acc[mt][nt];
                            const u32x4 w = {cvtpk(a[8 * s], a[8 * s + 1]), cvtpk(a[8 * s + 2], a[8 * s + 3]), cvtpk(a[8 * s + 4], a[8 * s + 5]), cvtpk(a[8 * s + 6], a[8 * s + 7])};
                            st16(rs, dst + mt * KVTILE_B + s * V_STEP, w);
                        }
                }
                BARL();
            } else {
                gemm_loop<false, false, 2, 2>(acc, lds, wid, lane, nohook);
                const bool isQ = ct < 5;
                const float sc = isQ ? QSCALE : 1.f;
                const unsigned img = (unsigned)(isQ ? WS_Q : WS_KV), tileb = isQ ? QTILE_B : KVTILE_B;
                const int G = (isQ ? ct : ct - 5) * 32 + wn * 16 + h;
                u32x2 P[2][8]; float nrm[8];
#pragma unroll
                for (int p = 0; p < 8; ++p) {
                    const int nt = p >> 2, g = p & 3; float m = 0.f;
#pragma unroll
                    for (int mt = 0; mt < 2; ++mt) {
                        const f32x16& a = acc[mt][nt];
                        P[mt][p] = (u32x2){cvtpk(a[4 * g] * sc, a[4 * g + 1] * sc), cvtpk(a[4 * g + 2] * sc, a[4 * g + 3] * sc)};
                        m = fmaxf(m, __builtin_fmaf(a[4 * g], a[4 * g], a[4 * g + 1] * a[4 * g + 1]) + __builtin_fmaf(a[4 * g + 2], a[4 * g + 2], a[4 * g + 3] * a[4 * g + 3]));
                    }
                    nrm[p] = m;
                }
#pragma unroll
                for (int p = 0; p < 8; ++p) {
                    const int gam = G + 2 * p, hd = (gam * 205) >> 10, dg = gam - 5 * hd;
                    const unsigned base = img + (unsigned)(hd * NT + rt * 4 + wm * 2) * tileb;
#pragma unroll
                    for (int mt = 0; mt < 2; ++mt) {
                        const unsigned bm = base + mt * tileb;
                        if (dg < 2) {
                            if (p < 7) st16(rs, bm + dg * 512 + r * 16, (u32x4){P[mt][p][0], P[mt][p][1], P[mt][(p + 1) & 7][0], P[mt][(p + 1) & 7][1]});
                            else st8(rs, bm + dg * 512 + r * 16, P[mt][p]);
                        } else if (dg == 4) st8(rs, bm + 1024 + r * 8, P[mt][p]);
                        else if (p == 0) st8(rs, bm + (dg - 2) * 512 + r * 16 + 8, P[mt][p]);
                    }
                }
                if (!isQ) {
#pragma unroll
                    for (int i = 0; i < 8; ++i) { float m = nrm[i]; m = dpp_max<0xB1>(m); m = dpp_max<0x4E>(m); m = dpp_max<0x141>(m); m = dpp_max<0x140>(m);
                        auto rr = __builtin_amdgcn_permlane16_swap(__float_as_uint(m), __float_as_uint(m), false, false); nrm[i] = fmaxf(__uint_as_float(rr[0]), __uint_as_float(rr[1])); }
                    float mine = nrm[0];
#pragma unroll
                    for (int i = 1; i < 8; ++i) mine = r == i ? nrm[i] : mine;
                    if (r < 8) st4f(ws + WS_KMAX + (size_t)((G + 2 * r) * 32 + rt * 2 + wm) * 4, mine);
                }
                BARL();
            }
        } else { gates(); BARL(); }
        BARL();
        f32x16 acc2[1][3] = {};
        auto hook2 = [&](int kc, bool before) {
            if (kc == 2) { if (before) asm volatile("s_waitcnt vmcnt(0)" ::: "memory");
                           else if (tid == 0) flag_set(ws + WS_F1 + bid * 8, ((u64)ctl[3] << 32) | ctl[2]); }
        };
        gemm_loop<true, false, 1, 3>(acc2, lds, wid, lane, hook2);
        wait_seen();
        q_loads();
        {   const int tok = rt * 128 + wid * 32 + r, rho = tok >> 4;
#pragma unroll
            for (int nt = 0; nt < 3; ++nt) {
                const unsigned dst = (unsigned)WS_AF + (unsigned)(rho * NSIG + ct * 3 + nt) * 1024 + (h * 16 + (r & 15)) * 16;
                float sv[16];
#pragma unroll
                for (int i = 0; i < 16; ++i) { const float v = acc2[0][nt][i]; sv[i] = v * __builtin_amdgcn_rcpf(1.f + __builtin_amdgcn_exp2f(v * -1.4426950408889634f)); }
#pragma unroll
                for (int s = 0; s < 2; ++s) {
                    const u32x4 w = {cvtpk(sv[8 * s], sv[8 * s + 1]), cvtpk(sv[8 * s + 2], sv[8 * s + 3]), cvtpk(sv[8 * s + 4], sv[8 * s + 5]), cvtpk(sv[8 * s + 6], sv[8 * s + 7])};
                    st16(rs, dst + s * 512, w);
                }
            }
        }
        asm volatile("s_waitcnt vmcnt(6)" : "+v"(qa), "+v"(qbr), "+v"(km4), "+v"(gate) :: "memory");
    } else {
        const int lw = wid - 4;
        auto side_stores = [&]() {
            for (int i = lw; i < 10; i += 4) rt_store(rs, bid, lds + RT_OFF, i, lane);
            if (!tile1) for (int i = lw; i < 10; i += 4) rt_store(rs, 256 + (xcd >> 1) * 4 + (slot & 3), lds + RT_OFF + 10240, i, lane);
            if (lw == 0) { const int i = bid * 64 + lane;
                const unsigned f = (i & 1) ? 0u : ONES2; st16(rs, (unsigned)WS_KV + (unsigned)(i >> 3) * KVTILE_B + V_OFF + ((i >> 2) & 1) * V_STEP + (((i >> 1) & 1) * V_ROWS + 20 + (i & 1)) * 16, (u32x4){f, f, f, f}); }
        };
        if (tile1) {
            GDma<false> d1; d1.init(xbase, (const char*)wqkv + ct * 256, 3 * DM * 2, lds, lane, k0);
#pragma unroll
            for (int jj = 0; jj < 4; ++jj) d1.wp(d1.kact(0), 0, lw, jj);
            d1.issue(1, 1, lw);
            WAITB(8);
            d1.issue(2, 2, lw); d1.issue(3, 3, lw);
            for (int kc = 0; kc < 9; ++kc) {
                if (kc < 7) WAITB(16); else if (kc == 7) WAITB(15); else WAITB(14);
                if (kc + 4 < 10) d1.issue(kc + 4, kc & 3, lw);
                if (kc == 4) side_stores();
                if (kc >= 6) d2.issue(kc - 6, kc - 6, lw);
            }
            asm volatile("s_barrier" ::: "memory");
        } else {
            asm volatile("s_barrier" ::: "memory");
            side_stores(); gates();
            BARL();
            d2.issue(0, 0, lw); d2.issue(1, 1, lw); d2.issue(2, 2, lw);
        }
        WAITB(14);
        { int st = 0;
          for (int kc = 0; kc < 9; ++kc) {
              if (kc < 8) WAITB(7); else WAITB(0);
              if (kc + 3 < 10) d2.issue(kc + 3, st, lw);
              st = st == 2 ? 0 : st + 1;
          } }
        if (lw == 0) {
            const u64 tag = ((u64)ctl[3] << 32) | ctl[2];
            for (int it = 0; it < SPIN_MAX; ++it) {
                bool ok = true;
#pragma unroll
                for (int i = 0; i < 4; ++i) ok &= flag_get(ws + WS_F1 + (lane + 64 * i) * 8) == tag;
                if (__all(ok)) break;
                __builtin_amdgcn_s_sleep(2);
            }
            __builtin_amdgcn_fence(__ATOMIC_ACQUIRE, "agent");
            asm volatile("s_waitcnt vmcnt(0)" ::: "memory");
            if (lane == 0) { ctl[1] = 1u; if (bid == 0) flag_set(ws + WS_EPOCH, tag); }
        } else wait_seen();
        q_loads();
        for (int j = 0; j < 21; ++j) glds16(kv + (size_t)(lw + 4 * j) * 1024, lane16, lds0 + (lw + 4 * j) * 1024);
        asm volatile("s_waitcnt vmcnt(21)" : "+v"(qa), "+v"(qbr), "+v"(km4), "+v"(gate) :: "memory");
    }
    float km = fmaxf(fmaxf(km4[0], km4[1]), fmaxf(km4[2], km4[3]));
    km = dpp_max<0xB1>(km); km = dpp_max<0x4E>(km); km = dpp_max<0x141>(km);
    km = __builtin_amdgcn_sqrtf(km);
    float M;
    { const float c0 = rdl(km, 0), c1 = rdl(km, 8), c2 = rdl(km, 16), c3 = rdl(km, 24), c4 = rdl(km, 32);
      const u32x4 qw = __builtin_bit_cast(u32x4, qa);
      float n0, n1, n2;
      { const float a = bf_lo(qw[0]), b = bf_hi(qw[0]), c = bf_lo(qw[1]), d = bf_hi(qw[1]); n0 = (a * a + b * b) + (c * c + d * d); }
      { const float a = bf_lo(qw[2]), b = bf_hi(qw[2]), c = bf_lo(qw[3]), d = bf_hi(qw[3]); n1 = (a * a + b * b) + (c * c + d * d); }
      { const unsigned q0 = qbr[0], q1 = qbr[1]; const float a = bf_lo(q0), b = bf_hi(q0), c = bf_lo(q1), d = bf_hi(q1); n2 = (a * a + b * b) + (c * c + d * d); }
      float part = __builtin_amdgcn_sqrtf(n0) * (h ? c1 : c0) + __builtin_amdgcn_sqrtf(n1) * (h ? c3 : c2);
      if (!h) part += __builtin_amdgcn_sqrtf(n2) * c4;
      M = swap32_add(part) * 1.01f; }
    const s16x4 qb = make_qb(qbr[0], qbr[1], M, h);
    const int vrow = r <= 20 ? r : (r < 28 ? 21 : r - 12);
    const int voff = V_OFF + (h * V_ROWS + vrow) * 16, kboff = h ? V_OFF + 20 * 16 : 1024 + r * 8;
    lds_cptr kaC = (lds_cptr)lds + lane * 16, kbC = (lds_cptr)lds + kboff, vC = (lds_cptr)lds + voff;
    lds_cptr kaN = kaC + K2_BIG, kbN = kbC + K2_BIG, vN = vC + K2_BIG;
    const f32x16 zero = {};
    f32x16 o = {}, sA, sB;
    bf16x8 ka, va, vb; s16x4 kb;
    u32x4 plo, phi;
    if (wid >= 4) asm volatile("s_waitcnt vmcnt(15)" ::: "memory");
    asm volatile("s_barrier" ::: "memory");
    ka = lds128(kaC); kb = lds64(kbC);
    sA = MFMA32(ka, qa, zero); sA = MFMA8(kb, qb, sA);
    ka = lds128(kaC + KVTILE_B); kb = lds64(kbC + KVTILE_B);
    va = lds128(vC);
    vb = (bf16x8){}; phi = (u32x4){};
    f32x4 e3 = {};
    const int ng = (xcd & 1) * 4 + (slot & 3), bt = (xcd >> 1) * 8 + (slot >> 2);
    const char* ap = ws + WS_AF + (size_t)bt * 4 * NSIG * 1024;
    const char* bp = ws + WS_BF + (size_t)ng * NSIG * 5 * 1024;
        const int rot = ((slot >> 2) + 3 * (slot & 3)) % 6;
    auto hs = [&](int i) { int st = i + rot; if (st >= 6) st -= 6; return wid * 6 + st; };
    bf16x8 a[3][4], b[3][5];
    auto ldA1 = [&](int sg, int mt, bf16x8& d) { gld128_sc1(d, ap + ((size_t)mt * NSIG + sg) * 1024, lane16); };
    auto ldB1 = [&](int sg, int nt, bf16x8& d) { gld128(d, bp + sg * 5120 + nt * 1024, lane16); };
    const u64 tag = ((u64)ctl[3] << 32) | ctl[2];
#define EXP4(X, i) do { X[i] = __builtin_amdgcn_exp2f(X[i]); X[i + 1] = __builtin_amdgcn_exp2f(X[i + 1]); X[i + 2] = __builtin_amdgcn_exp2f(X[i + 2]); X[i + 3] = __builtin_amdgcn_exp2f(X[i + 3]); } while (0)
#define KADDR(A, i) (((i) < 16) ? (A##C + (i) * KVTILE_B) : (A##N + ((i) - 16) * KVTILE_B))
#define STEP(C, N, i, X0, X1) do { \
        bf16x8 ka2, va2, vb2; s16x4 kb2; \
        N = MFMA32(ka, qa, zero); SBAR(); \
        ka2 = lds128(KADDR(ka, (i) + 2)); \
        phi[2] = cvtpk(e3[0], e3[1]); phi[3] = cvtpk(e3[2], e3[3]); PIN(phi); SBAR(); EXP4(C, 0); PIN(C); SBAR(); \
        o = MFMA32(vb, __builtin_bit_cast(bf16x8, phi), o); SBAR(); \
        X0; \
        vb2 = lds128(vC + (i) * KVTILE_B + V_STEP); \
        plo[0] = cvtpk(C[0], C[1]); plo[1] = cvtpk(C[2], C[3]); PIN(plo); SBAR(); EXP4(C, 4); PIN(C); SBAR(); \
        N = MFMA8(kb, qb, N); SBAR(); \
        kb2 = lds64(KADDR(kb, (i) + 2)); \
        plo[2] = cvtpk(C[4], C[5]); plo[3] = cvtpk(C[6], C[7]); PIN(plo); SBAR(); EXP4(C, 8); PIN(C); SBAR(); \
        o = MFMA32(va, __builtin_bit_cast(bf16x8, plo), o); SBAR(); \
        X1; \
        va2 = lds128(KADDR(v, (i) + 1)); \
        phi[0] = cvtpk(C[8], C[9]); phi[1] = cvtpk(C[10], C[11]); PIN(phi); SBAR(); \
        e3[0] = __builtin_amdgcn_exp2f(C[12]); e3[1] = __builtin_amdgcn_exp2f(C[13]); e3[2] = __builtin_amdgcn_exp2f(C[14]); e3[3] = __builtin_amdgcn_exp2f(C[15]); PIN(e3); SBAR(); \
        ka = ka2; kb = kb2; va = va2; vb = vb2; \
    } while (0)
#define PIECE(j) do { if (8 * (j) + 7 < 42 || 8 * (j) + wid < 42) glds16(src + (j) * 8192, lane16, dst + (j) * 8192); } while (0)
    const char* src = kv + (84 + wid) * 1024; unsigned dst = lds0 + wid * 1024 + 2 * K2_BIG;
#pragma unroll 1
    for (int c = 0; c < 3; ++c) {
        STEP(sA, sB, 0, , ); STEP(sB, sA, 1, , ); STEP(sA, sB, 2, , ); STEP(sB, sA, 3, , ); STEP(sA, sB, 4, , );
        if (c == 0) { if (wid >= 4) asm volatile("s_waitcnt vmcnt(10)" ::: "memory"); asm volatile("s_barrier" ::: "memory"); }
        STEP(sB, sA, 5, , ); STEP(sA, sB, 6, , ); STEP(sB, sA, 7, , ); STEP(sA, sB, 8, , ); STEP(sB, sA, 9, , ); STEP(sA, sB, 10, , ); STEP(sB, sA, 11, , ); STEP(sA, sB, 12, , );
        if (c == 2 && wid == 0) {
            for (int it = 0; it < SPIN_MAX; ++it) {
                if (__all(flag_get(ws + WS_F2 + ((bt >> 1) * 16 + (lane & 15)) * 8) == tag)) break;
                __builtin_amdgcn_s_sleep(2);
            }
        }
        WAITB(0);
        if (c == 0 && tid == 0) flag_set(ws + WS_F2 + (rt * 16 + ct) * 8, tag);
        if (c < 2) { STEP(sB, sA, 13, PIECE(0), PIECE(1)); STEP(sA, sB, 14, PIECE(2), PIECE(3)); STEP(sB, sA, 15, PIECE(4), PIECE(5)); }
        else { STEP(sB, sA, 13, , ); STEP(sA, sB, 14, , ); STEP(sB, sA, 15, , ); }
        const int d = (c == 1) ? -2 * K2_BIG : K2_BIG;
        kaC = kaN; kbC = kbN; vC = vN; kaN += d; kbN += d; vN += d;
        src += 42 * 1024; dst -= 2 * K2_BIG;
    }
#define PA(s, m) ldA1(hs(s), m, a[s][m])
#define PB(s, n) ldB1(hs(s), n, b[s][n])
    STEP(sA, sB, 0, PA(0, 0), PA(0, 1)); STEP(sB, sA, 1, PA(0, 2), PA(0, 3)); STEP(sA, sB, 2, PB(0, 0), PB(0, 1)); STEP(sB, sA, 3, PB(0, 2), PB(0, 3));
    STEP(sA, sB, 4, PB(0, 4), PA(1, 0)); STEP(sB, sA, 5, PA(1, 1), PA(1, 2)); STEP(sA, sB, 6, PA(1, 3), PB(1, 0)); STEP(sB, sA, 7, PB(1, 1), PB(1, 2));
    STEP(sA, sB, 8, PB(1, 3), PB(1, 4)); STEP(sB, sA, 9, PA(2, 0), PA(2, 1)); STEP(sA, sB, 10, PA(2, 2), PA(2, 3)); STEP(sB, sA, 11, PB(2, 0), PB(2, 1));
    STEP(sA, sB, 12, PB(2, 2), PB(2, 3)); STEP(sB, sA, 13, PB(2, 4), ); STEP(sA, sB, 14, , ); STEP(sB, sA, 15, , );
    phi[2] = cvtpk(e3[0], e3[1]); phi[3] = cvtpk(e3[2], e3[3]);
    o = MFMA32(vb, __builtin_bit_cast(bf16x8, phi), o);
    float l; { auto rr = __builtin_amdgcn_permlane32_swap(__float_as_uint(o[8]), __float_as_uint(o[8]), false, false); l = __uint_as_float(rr[1]); }
    if (__builtin_expect(__any(!(l >= 8.6736174e-19f && l <= 1.0e30f)), 0)) {
        asm volatile("s_waitcnt vmcnt(0)" ::: "memory");
        attn_fallback(o, kv, qa, qbr[0], qbr[1], lane, kboff, voff);
        auto rr = __builtin_amdgcn_permlane32_swap(__float_as_uint(o[8]), __float_as_uint(o[8]), false, false); l = __uint_as_float(rr[1]);
    }
    const float f = gate * __builtin_amdgcn_rcpf(l);
    const int tok = Tq * 32 + r;
    char* ab = ws + WS_AF + (size_t)(tok >> 4) * NSIG * 1024 + (tok & 15) * 16;
#pragma unroll
    for (int g = 0; g < 3; ++g) {
        const int gam = head * 5 + 2 * g + h;
        const int sig = 48 + (gam >> 3), c = ((gam >> 2) & 1) * 2 + (gam & 1), half = (gam >> 1) & 1;
        const u32x2 w = {cvtpk(o[4 * g] * f, o[4 * g + 1] * f), cvtpk(o[4 * g + 2] * f, o[4 * g + 3] * f)};
        if (g < 2 || h == 0) __hip_atomic_store((u64*)(ab + sig * 1024 + c * 256 + half * 8), ((u64)w[1] << 32) | w[0], __ATOMIC_RELAXED, __HIP_MEMORY_SCOPE_AGENT);
    }
    {
        f32x4 acc[4][5] = {};
        auto loadA = [&](int sg, bf16x8 (&da)[4]) {
#pragma unroll
            for (int mt = 0; mt < 4; ++mt) gld128_sc1(da[mt], ap + ((size_t)mt * NSIG + sg) * 1024, lane16);
        };
        auto loadB = [&](int sg, bf16x8 (&db)[5]) {
#pragma unroll
            for (int nt = 0; nt < 5; ++nt) gld128(db[nt], bp + sg * 5120 + nt * 1024, lane16);
        };
        auto mma = [&](bf16x8 (&da)[4], bf16x8 (&db)[5]) {
            SBAR();
#pragma unroll
            for (int nt = 0; nt < 5; ++nt)
#pragma unroll
                for (int mt = 0; mt < 4; ++mt) acc[mt][nt] = MFMA16(db[nt], da[mt], acc[mt][nt]);
            SBAR();
        };
        wait9<3>(a[0], b[0]);
        asm volatile("" : "+v"(a[1][0]), "+v"(a[1][1]), "+v"(a[1][2]), "+v"(a[1][3]), "+v"(b[1][0]), "+v"(b[1][1]), "+v"(b[1][2]), "+v"(b[1][3]), "+v"(b[1][4]));
        asm volatile("" : "+v"(a[2][0]), "+v"(a[2][1]), "+v"(a[2][2]), "+v"(a[2][3]), "+v"(b[2][0]), "+v"(b[2][1]), "+v"(b[2][2]), "+v"(b[2][3]), "+v"(b[2][4]));
        mma(a[0], b[0]); loadA(hs(3), a[0]); loadB(hs(3), b[0]);
        mma(a[1], b[1]); loadA(hs(4), a[1]); loadB(hs(4), b[1]);
        asm volatile("s_waitcnt vmcnt(18)" ::: "memory");
        if (lane == 0) {
            const unsigned old = __hip_atomic_fetch_add((unsigned*)(lds + CTL_OFF), 1u, __ATOMIC_RELAXED, __HIP_MEMORY_SCOPE_WORKGROUP);
            if (old == 7u) flag_set(ws + WS_F3 + (qblk * 32 + head) * 8, tag);
        }
        mma(a[2], b[2]); loadA(hs(5), a[2]); loadB(hs(5), b[2]);
        float og[4];
#pragma unroll
        for (int mt = 0; mt < 4; ++mt) asm volatile("s_nop 4\n\tglobal_load_dword %0, %1, %2" : "=v"(og[mt]) : "v"((unsigned)((bt * 64 + mt * 16 + (lane & 15)) * 4)), "s"(ws + WS_GATE) : "memory");
        wait9<22>(a[0], b[0]); mma(a[0], b[0]); loadB(48 + wid, b[0]);
        wait9<18>(a[1], b[1]); mma(a[1], b[1]); loadB(56 + wid, b[1]);
        wait9<14>(a[2], b[2]); mma(a[2], b[2]);
        if (wid < 4) {
#pragma unroll
            for (int nt = 0; nt < 5; ++nt) gld128_rw(b[2][nt], bp + (64 + wid) * 5120 + nt * 1024, lane16);
        }
        asm volatile("s_waitcnt vmcnt(0)" : "+v"(og[0]), "+v"(og[1]), "+v"(og[2]), "+v"(og[3]) :: "memory");
#pragma unroll
        for (int mt = 0; mt < 4; ++mt) { const float f1 = bf_round(1.f - og[mt]);
#pragma unroll
            for (int nt = 0; nt < 5; ++nt) acc[mt][nt] *= f1; }
        if (wid == 0) {
            for (int it = 0; it < SPIN_MAX; ++it) {
                if (__all(flag_get(ws + WS_F3 + ((bt >> 2) * 32 + r) * 8) == tag)) break;
                __builtin_amdgcn_s_sleep(2);
            }
        }
        asm volatile("s_barrier" ::: "memory");
        loadA(48 + wid, a[0]); loadA(56 + wid, a[1]);
        a[2][0] = (bf16x8){}; a[2][1] = (bf16x8){}; a[2][2] = (bf16x8){}; a[2][3] = (bf16x8){};
        if (wid < 4) {
#pragma unroll
            for (int mt = 0; mt < 4; ++mt) gld128_sc1_rw(a[2][mt], ap + ((size_t)mt * NSIG + 64 + wid) * 1024, lane16);
        }
        wait9<4>(a[0], b[0]); mma(a[0], b[0]);
        wait9<0>(a[1], b[1]); wait9<0>(a[2], b[2]); mma(a[1], b[1]); mma(a[2], b[2]);
#pragma unroll
        for (int mt = 0; mt < 4; ++mt)
#pragma unroll
            for (int nt = 0; nt < 5; ++nt) *(f32x4*)(lds + (wid * 20 + mt * 5 + nt) * 1024 + lane16) = acc[mt][nt];
        BARL();
#pragma unroll
        for (int i = 0; i < 3; ++i) { const int t = wid + 8 * i;
            if (t < 20) {
                f32x4 sum = *(const f32x4*)(lds + t * 1024 + lane16);
#pragma unroll
                for (int w = 1; w < 8; ++w) sum += *(const f32x4*)(lds + (w * 20 + t) * 1024 + lane16);
                const int m4 = t / 5, nt = t - m4 * 5;
                const int tok2 = bt * 64 + m4 * 16 + (lane & 15);
                *(u32x2*)(out + (size_t)tok2 * DM + ng * 80 + nt * 16 + (lane >> 4) * 4) = (u32x2){cvtpk(sum[0], sum[1]), cvtpk(sum[2], sum[3])};
            } }
    }
}

extern "C" void kernel_launch(void* const* d_in, const int* in_sizes, int n_in, void* d_out, int out_size, void* d_ws, size_t ws_size, hipStream_t stream) {
    const unsigned short* x = (const unsigned short*)d_in[0];
    const unsigned short* wqkv = (const unsigned short*)d_in[1];
    const unsigned short* wo = (const unsigned short*)d_in[2];
    const unsigned short* w1 = (const unsigned short*)d_in[4];
    const unsigned short* w2 = (const unsigned short*)d_in[5];
    const unsigned short* gw = (const unsigned short*)d_in[6];
    (void)hipFuncSetAttribute((const void*)gated_mix_mega, hipFuncAttributeMaxDynamicSharedMemorySize, LDS_ALL);
    gated_mix_mega<<<256, 512, LDS_ALL, stream>>>(x, wqkv, wo, w1, w2, gw, (char*)d_ws, (unsigned short*)d_out);
}
```

```cpp
#include <hip/hip_runtime.h>
#include <hip/hip_bf16.h>
#include <stdint.h>

typedef short bf16x8 __attribute__((ext_vector_type(8)));
typedef short s16x4 __attribute__((ext_vector_type(4)));
typedef float f32x16 __attribute__((ext_vector_type(16)));
typedef float f32x4 __attribute__((ext_vector_type(4)));
typedef float f32x2 __attribute__((ext_vector_type(2)));
typedef unsigned u32x4 __attribute__((ext_vector_type(4)));
typedef unsigned u32x2 __attribute__((ext_vector_type(2)));
typedef __bf16 bf16x2_t __attribute__((ext_vector_type(2)));
#define LAS __attribute__((address_space(3)))
typedef LAS const char* lds_cptr;
#define DI __device__ __forceinline__
#define SBAR() __builtin_amdgcn_sched_barrier(0)
#define PIN(x) asm volatile("" : "+v"(x))
#define MFMA32(a, b, c) __builtin_amdgcn_mfma_f32_32x32x16_bf16(a, b, c, 0, 0, 0)
#define MFMA16(a, b, c) __builtin_amdgcn_mfma_f32_16x16x32_bf16(a, b, c, 0, 0, 0)
#define MFMA8(a, b, c) __builtin_amdgcn_mfma_f32_32x32x8bf16_1k(a, b, c, 0, 0, 0)

constexpr int S = 2048, DM = 640, NH = 32, DH = 20, FF = 1536;
constexpr int NT = S / 32;
constexpr int QTILE_B = 1280;
constexpr int KVTILE_B = 2688;
constexpr int V_OFF = 1280, V_STEP = 704, V_ROWS = 22;
constexpr int NSIG = 68;
constexpr size_t WS_Q = 0;
constexpr size_t WS_KV = WS_Q + (size_t)NH * NT * QTILE_B;
constexpr size_t WS_AF = WS_KV + (size_t)NH * NT * KVTILE_B;
constexpr size_t WS_BF = WS_AF + (size_t)128 * NSIG * 1024;
constexpr size_t WS_KMAX = WS_BF + (size_t)8 * NSIG * 5 * 1024;
constexpr size_t WS_GATE = WS_KMAX + (size_t)160 * 32 * 4;
constexpr size_t WS_F1 = WS_GATE + (size_t)S * 4;
constexpr size_t WS_F2 = WS_F1 + 2048;
constexpr size_t WS_F3 = WS_F2 + 2048;
constexpr size_t WS_EPOCH = WS_F3 + 2048;
constexpr size_t WS_END = WS_EPOCH + 256;
constexpr float QSCALE = 0.22360679774997896f * 1.4426950408889634f;
constexpr unsigned ONES2 = 0x3F803F80u;

DI unsigned cvtpk(float lo, float hi) { f32x2 v = {lo, hi}; bf16x2_t b = __builtin_convertvector(v, bf16x2_t); return __builtin_bit_cast(unsigned, b); }
DI float bf_lo(unsigned u) { return __uint_as_float(u << 16); }
DI float bf_hi(unsigned u) { return __uint_as_float(u & 0xffff0000u); }
DI float bf_round(float x) { return bf_lo(cvtpk(x, x)); }
DI void glds16(const void* sbase, unsigned voff, unsigned lds_base) {
    unsigned sv;
    asm volatile("s_mov_b32 %0, m0\n\ts_mov_b32 m0, %3\n\ts_nop 0\n\tglobal_load_lds_dwordx4 %1, %2\n\ts_mov_b32 m0, %0"
                 : "=&s"(sv) : "v"(voff), "s"(sbase), "s"(lds_base) : "memory");
}
DI void glds16_sc1(const void* sbase, unsigned voff, unsigned lds_base) {
    unsigned sv;
    asm volatile("s_mov_b32 %0, m0\n\ts_mov_b32 m0, %3\n\ts_nop 0\n\tglobal_load_lds_dwordx4 %1, %2 sc1\n\ts_mov_b32 m0, %0"
                 : "=&s"(sv) : "v"(voff), "s"(sbase), "s"(lds_base) : "memory");
}
typedef short v4i16_t __attribute__((ext_vector_type(4)));
DI s16x4 ldstr(lds_cptr p) { return __builtin_bit_cast(s16x4, __builtin_amdgcn_ds_read_tr16_b64_v4i16((LAS v4i16_t*)p)); }
DI bf16x8 lds128(lds_cptr p) { return *(const LAS bf16x8*)p; }
DI s16x4 lds64(lds_cptr p) { return *(const LAS s16x4*)p; }
DI unsigned rfl(unsigned x) { return __builtin_amdgcn_readfirstlane(x); }


DI float rdl(float x, int l) { return __uint_as_float(__builtin_amdgcn_readlane(__float_as_uint(x), l)); }
typedef __amdgpu_buffer_rsrc_t rsrc_t;
typedef unsigned long long u64;
DI rsrc_t ws_rsrc(char* ws) { return __builtin_amdgcn_make_buffer_rsrc(ws, 0, (int)WS_END, 0x00020000); }
DI void st16(rsrc_t rs, unsigned off, u32x4 v) { __builtin_amdgcn_raw_buffer_store_b128(v, rs, off, 0, 16); }
DI void st8(rsrc_t rs, unsigned off, u32x2 v) { __builtin_amdgcn_raw_buffer_store_b64(v, rs, off, 0, 16); }
DI void st4f(char* p, float v) { __hip_atomic_store((float*)p, v, __ATOMIC_RELAXED, __HIP_MEMORY_SCOPE_AGENT); }
DI void flag_set(char* p, u64 tag) { __hip_atomic_store((u64*)p, tag, __ATOMIC_RELAXED, __HIP_MEMORY_SCOPE_AGENT); }
DI u64 flag_get(const char* p) { return __hip_atomic_load((const u64*)p, __ATOMIC_RELAXED, __HIP_MEMORY_SCOPE_AGENT); }
template <int CTRL> DI float dpp_max(float x) { const float y = __uint_as_float(__builtin_amdgcn_update_dpp(0u, __float_as_uint(x), CTRL, 0xf, 0xf, true)); return fmaxf(x, y); }
template <int CTRL> DI float dpp_add(float x) { const float y = __uint_as_float(__builtin_amdgcn_update_dpp(0u, __float_as_uint(x), CTRL, 0xf, 0xf, true)); return x + y; }
DI void gld128(bf16x8& d, const char* sbase, unsigned voff) { asm volatile("global_load_dwordx4 %0, %1, %2" : "=v"(d) : "v"(voff), "s"(sbase) : "memory"); }
DI void gld128_sc1(bf16x8& d, const char* sbase, unsigned voff) { asm volatile("global_load_dwordx4 %0, %1, %2 sc1" : "=v"(d) : "v"(voff), "s"(sbase) : "memory"); }
DI void gld128_rw(bf16x8& d, const char* sbase, unsigned voff) { asm volatile("s_nop 4\n\tglobal_load_dwordx4 %0, %1, %2" : "+v"(d) : "v"(voff), "s"(sbase) : "memory"); }
DI void gld128_sc1_rw(bf16x8& d, const char* sbase, unsigned voff) { asm volatile("s_nop 4\n\tglobal_load_dwordx4 %0, %1, %2 sc1" : "+v"(d) : "v"(voff), "s"(sbase) : "memory"); }
template <int N> DI void wait9(bf16x8 (&a)[4], bf16x8 (&b)[5]) {
    asm volatile("s_waitcnt vmcnt(%9)" : "+v"(a[0]), "+v"(a[1]), "+v"(a[2]), "+v"(a[3]), "+v"(b[0]), "+v"(b[1]), "+v"(b[2]), "+v"(b[3]), "+v"(b[4]) : "i"(N) : "memory");
}
#define WAITB(n) asm volatile("s_waitcnt vmcnt(" #n ")\n\ts_barrier" ::: "memory")
#define BARL() asm volatile("s_waitcnt lgkmcnt(0)\n\ts_barrier" ::: "memory")

constexpr int P1_STG = 32768;
constexpr int RT_OFF = 131072;
constexpr int P2_STG = 28672;
constexpr int CTL_OFF = 163584;
DI constexpr int p2_base(int s) { return CTL_OFF - (s + 1) * P2_STG; }
constexpr int K2_BIG = 16 * KVTILE_B;
constexpr int EP_OFF = 151552;
constexpr int LDS_ALL = 163840;

template <bool N96>
struct GDma {
    static constexpr int NW = N96 ? 3 : 4;
    const char* xbase; const char* wbase; int ldw2, k0; unsigned lds0, xoff0, xoff1, woff[3];
    DI int kact(int kc) const { kc += k0; return kc >= 10 ? kc - 10 : kc; }
    DI static unsigned sbase(int st) { return N96 ? (unsigned)(CTL_OFF - (st + 1) * P2_STG) : (unsigned)(st * P1_STG); }
    DI void init(const char* xb, const char* wb, int ld, char* lds, int lane, int k0_) {
        xbase = xb; wbase = wb; ldw2 = ld; k0 = k0_; lds0 = (unsigned)(uintptr_t)lds;
        const int rr = lane >> 3;
        xoff0 = rr * 1280 + (((lane & 7) ^ ((lane >> 4) & 7)) << 4); xoff1 = rr * 1280 + (((lane & 7) ^ ((4 + (lane >> 4)) & 7)) << 4);
        if (N96) {
#pragma unroll
            for (int p = 0; p < 3; ++p) { const int o = 1024 * p + 16 * lane, row = (o * 21846) >> 22, col = o - row * 192; woff[p] = row * ldw2 + col; }
        } else woff[0] = (lane >> 4) * ldw2 + ((((lane & 15) ^ ((lane >> 4) << 2))) << 4);
    }
    DI void xp(int kc, int st, int j, int par) const { glds16(xbase + (size_t)j * 8 * 1280 + kc * 128, par ? xoff1 : xoff0, lds0 + sbase(st) + j * 1024); }
    DI void wp(int kc, int st, int lw, int jj) const {
        if (N96) glds16(wbase + (size_t)(kc * 64 + 16 * lw) * ldw2, woff[jj], lds0 + sbase(st) + 16384 + (3 * lw + jj) * 1024);
        else glds16(wbase + (size_t)(kc * 64 + 16 * lw + 4 * jj) * ldw2, woff[0], lds0 + sbase(st) + 16384 + (4 * lw + jj) * 1024);
    }
    DI void issue(int kc_, int st, int lw) const {
        const int kc = kact(kc_);
#pragma unroll
        for (int jj = 0; jj < NW; ++jj) wp(kc, st, lw, jj);
#pragma unroll
        for (int jj = 0; jj < 4; ++jj) xp(kc, st, 4 * lw + jj, jj & 1);
    }
};

DI void rt_load(const unsigned short* w2, const unsigned short* wo, int u, unsigned ldsb, int piece, int lane) {
    const int sig = u >> 2, s = (u >> 1) & 1, cb = u & 1;
    const char* src = (sig < 48 ? (const char*)w2 + (size_t)sig * 32 * 1280 : (const char*)wo + (size_t)(sig - 48) * 32 * 1280) + s * 16 * 1280 + cb * 640;
    const int o = piece * 1024 + lane * 16, row = (o * 6554) >> 22;
    glds16(src, row * 1280 + (o - row * 640), ldsb + piece * 1024);
}
DI void rt_store(rsrc_t rs, int u, const char* ldsb, int i, int lane) {
    const int sig = u >> 2, s = (u >> 1) & 1, cb = u & 1;
    const int nl = 2 * i + (lane >> 5), hh = (lane >> 4) & 1, q = (lane & 15) >> 2, p = lane & 3;
    lds_cptr a = (lds_cptr)ldsb + (4 * hh + q) * 640 + (16 * nl + 4 * p) * 2;
    const s16x4 lo = ldstr(a), hi = ldstr(a + 8 * 640);
    const bf16x8 f = __builtin_shufflevector(lo, hi, 0, 1, 2, 3, 4, 5, 6, 7);
    const int nu = cb * 20 + nl, ng = (nu * 205) >> 10;
    st16(rs, (unsigned)WS_BF + (unsigned)((ng * NSIG + sig) * 5 + (nu - 5 * ng)) * 1024 + (s * 32 + (lane & 31)) * 16, __builtin_bit_cast(u32x4, f));
}

template <bool N96, bool VT, int MT, int NTL, class HOOK>
DI void gemm_loop(f32x16 (&acc)[MT][NTL], char* lds, int wid, int lane, HOOK hook) {
    const int h = lane >> 5, r = lane & 31;
    constexpr int NST = N96 ? 3 : 4;
    unsigned xa[4];
    { const int row = (N96 ? wid * 32 : (wid >> 1) * 64) + r; const int sw = (row >> 1) & 7;
#pragma unroll
      for (int ks = 0; ks < 4; ++ks) xa[ks] = row * 128 + (((2 * ks + h) ^ sw) << 4); }
    unsigned wa[2];
    { const int blk = (lane >> 4) & 1, q = (lane & 15) >> 2, p = lane & 3;
      if (N96) wa[0] = wa[1] = 16384 + (8 * h + q) * 192 + blk * 32 + p * 8;
      else {
#pragma unroll
          for (int nt = 0; nt < 2; ++nt) { const int ch = (wid & 1) * 8 + nt * 4 + 2 * blk + (p >> 1); wa[nt] = 16384 + (8 * h + q) * 256 + ((ch ^ (q << 2)) << 4) + 8 * (p & 1); } } }
    bf16x8 xf[2][MT], wf[2][NTL];
    auto rdw = [&](int buf, int nt, lds_cptr sb, int ks) {
        lds_cptr a = N96 ? sb + wa[0] + ks * 16 * 192 + nt * 64 : sb + wa[nt & 1] + ks * 4096;
        const s16x4 lo = ldstr(a), hi = ldstr(a + (N96 ? 4 * 192 : 1024));
        wf[buf][nt] = __builtin_shufflevector(lo, hi, 0, 1, 2, 3, 4, 5, 6, 7);
    };
    auto rdx = [&](int buf, int mt, lds_cptr sb, int ks) { xf[buf][mt] = lds128(sb + xa[ks] + mt * 4096); };
    auto mm = [&](int buf, int mt, int nt) { acc[mt][nt] = VT ? MFMA32(xf[buf][mt], wf[buf][nt], acc[mt][nt]) : MFMA32(wf[buf][nt], xf[buf][mt], acc[mt][nt]); };
    { lds_cptr sb = (lds_cptr)lds + GDma<N96>::sbase(0); rdw(0, 0, sb, 0); rdx(0, 0, sb, 0); rdw(0, 1, sb, 0); if (N96) rdw(0, 2 % NTL, sb, 0); else rdx(0, 1 % MT, sb, 0); }
    int st = 0;
#define G_KSTEP(ks, NEXT) do { const int b = (ks) & 1, n = b ^ 1; lds_cptr np = (ks) < 3 ? sb : sn; const int nk = ((ks) + 1) & 3; \
        SBAR(); \
        if constexpr (N96) { \
            mm(b, 0, 0); SBAR(); if (NEXT) { rdw(n, 0, np, nk); rdx(n, 0, np, nk); } SBAR(); \
            mm(b, 0, 1); SBAR(); if (NEXT) { rdw(n, 1, np, nk); } SBAR(); \
            mm(b, 0, 2 % NTL); SBAR(); if (NEXT) { rdw(n, 2 % NTL, np, nk); } SBAR(); \
        } else { \
            mm(b, 0, 0); SBAR(); if (NEXT) { rdw(n, 0, np, nk); } SBAR(); \
            mm(b, 0, 1); SBAR(); if (NEXT) { rdx(n, 0, np, nk); } SBAR(); \
            mm(b, 1 % MT, 0); SBAR(); if (NEXT) { rdw(n, 1, np, nk); } SBAR(); \
            mm(b, 1 % MT, 1); SBAR(); if (NEXT) { rdx(n, 1 % MT, np, nk); } SBAR(); \
        } } while (0)
#pragma unroll 1
    for (int kc = 0; kc < 9; ++kc) {
        lds_cptr sb = (lds_cptr)lds + GDma<N96>::sbase(st);
        st = st == NST - 1 ? 0 : st + 1;
        lds_cptr sn = (lds_cptr)lds + GDma<N96>::sbase(st);
        G_KSTEP(0, true); G_KSTEP(1, true); G_KSTEP(2, true);
        hook(kc, true);
        BARL();
        hook(kc, false);
        G_KSTEP(3, true);
    }
    { lds_cptr sb = (lds_cptr)lds + GDma<N96>::sbase(st); lds_cptr sn = sb;
      G_KSTEP(0, true); G_KSTEP(1, true); G_KSTEP(2, true); G_KSTEP(3, false); }
}

DI float swap32_add(float x) { auto rr = __builtin_amdgcn_permlane32_swap(__float_as_uint(x), __float_as_uint(x), false, false); return __uint_as_float(rr[0]) + __uint_as_float(rr[1]); }
DI float swap32_max(float x) { auto rr = __builtin_amdgcn_permlane32_swap(__float_as_uint(x), __float_as_uint(x), false, false); return fmaxf(__uint_as_float(rr[0]), __uint_as_float(rr[1])); }
DI s16x4 make_qb(unsigned q0, unsigned q1, float M, int h) {
    const float mh = bf_hi(__float_as_uint(M)), r1 = M - mh, mm = bf_hi(__float_as_uint(r1)), r2 = r1 - mm, ml = bf_hi(__float_as_uint(r2));
    u32x2 w;
    w[0] = h ? ((__float_as_uint(mh) >> 16) | (__float_as_uint(mm) & 0xffff0000u)) ^ 0x80008000u : q0;
    w[1] = h ? (__float_as_uint(ml) >> 16) ^ 0x8000u : q1;
    return __builtin_bit_cast(s16x4, w);
}

DI void attn_fallback(f32x16& o, const char* kv, bf16x8 qa, unsigned q0, unsigned q1, int lane, int kboff, int voff) {
    const int h = lane >> 5;
    const f32x16 zero = {};
    float m = -INFINITY;
    const s16x4 qb0 = make_qb(q0, q1, 0.f, h);
    for (int t = 0; t < NT; ++t) {
        const char* tb = kv + t * KVTILE_B;
        const bf16x8 ka = *(const bf16x8*)(tb + lane * 16); const s16x4 kb = *(const s16x4*)(tb + kboff);
        f32x16 s = MFMA32(ka, qa, zero); s = MFMA8(kb, qb0, s);
#pragma unroll
        for (int i = 0; i < 16; ++i) m = fmaxf(m, s[i]);
    }
    m = swap32_max(m);
    const s16x4 qb = make_qb(q0, q1, m, h);
    o = zero;
    for (int t = 0; t < NT; ++t) {
        const char* tb = kv + t * KVTILE_B;
        const bf16x8 ka = *(const bf16x8*)(tb + lane * 16); const s16x4 kb = *(const s16x4*)(tb + kboff);
        const bf16x8 va = *(const bf16x8*)(tb + voff), vb = *(const bf16x8*)(tb + voff + V_STEP);
        f32x16 s = MFMA32(ka, qa, zero); s = MFMA8(kb, qb, s);
        u32x4 p0, p1;
#pragma unroll
        for (int i = 0; i < 4; ++i) { p0[i] = cvtpk(__builtin_amdgcn_exp2f(s[2 * i]), __builtin_amdgcn_exp2f(s[2 * i + 1])); p1[i] = cvtpk(__builtin_amdgcn_exp2f(s[8 + 2 * i]), __builtin_amdgcn_exp2f(s[9 + 2 * i])); }
        o = MFMA32(va, __builtin_bit_cast(bf16x8, p0), o);
        o = MFMA32(vb, __builtin_bit_cast(bf16x8, p1), o);
    }
}


constexpr int SPIN_MAX = 1 << 18;
__global__ __launch_bounds__(512, 2) void gated_mix_mega(const unsigned short* __restrict__ x, const unsigned short* __restrict__ wqkv, const unsigned short* __restrict__ wo, const unsigned short* __restrict__ w1,
                                                         const unsigned short* __restrict__ w2, const unsigned short* __restrict__ gw, char* __restrict__ ws, unsigned short* __restrict__ out) {
    extern __shared__ __attribute__((aligned(16))) char lds[];
    const int tid = threadIdx.x, lane = tid & 63, wid = rfl(tid >> 6);
    const int bid = blockIdx.x, xcd = bid & 7, slot = bid >> 3;
    {
        u64 pcq; asm volatile("s_getpc_b64 %0" : "=s"(pcq));
        if (slot < 2) glds16((const char*)((pcq + 15ull) & ~15ull) + (slot * 8 + wid) * 1024, lane * 16, (unsigned)(uintptr_t)lds + 153 * 1024);
    }
    const int h = lane >> 5, r = lane & 31;
    const rsrc_t rs = ws_rsrc(ws);
    const unsigned lds0 = (unsigned)(uintptr_t)lds, lane16 = lane * 16;
    volatile LAS unsigned* ctl = (volatile LAS unsigned*)(lds + CTL_OFF);
    const int rt = 4 * (xcd >> 1) + (slot & 3), ci = slot >> 2, ct = 8 * (xcd & 1) + ci;
    const bool tile1 = ct < 15;
    const char* xbase = (const char*)x + (size_t)rt * 128 * 1280;
    const int k0 = 0;
    const int head = xcd * 4 + (slot & 3), qblk = slot >> 2;
    const char* kv = ws + WS_KV + (size_t)head * NT * KVTILE_B;
    const int Tq = qblk * 8 + wid;
    const char* qt = ws + WS_Q + ((size_t)head * NT + Tq) * QTILE_B;
    bf16x8 qa; u32x2 qbr; f32x4 km4; float gate;
    auto q_loads = [&]() {
        asm volatile("s_nop 4\n\tglobal_load_dwordx4 %0, %1, %2" : "=v"(qa) : "v"(lane16), "s"(qt) : "memory");
        asm volatile("s_nop 4\n\tglobal_load_dwordx2 %0, %1, %2" : "=v"(qbr) : "v"((unsigned)(1024 + r * 8)), "s"(qt) : "memory");
        asm volatile("s_nop 4\n\tglobal_load_dwordx4 %0, %1, %2" : "=v"(km4) : "v"((unsigned)((lane < 40 ? lane : 0) * 16)), "s"(ws + WS_KMAX + (size_t)head * 640) : "memory");
        asm volatile("s_nop 4\n\tglobal_load_dword %0, %1, %2" : "=v"(gate) : "v"((unsigned)((Tq * 32 + r) * 4)), "s"(ws + WS_GATE) : "memory");
    };
    auto wait_seen = [&]() { for (int it = 0; it < SPIN_MAX; ++it) { if (ctl[1] != 0u) break; __builtin_amdgcn_s_sleep(1); } };
    auto gates = [&]() {
        const int aidx = (xcd >> 1) * 4 + (slot & 3);
        const u32x4* gr = (const u32x4*)gw + (lane & 7);
#pragma unroll 1
        for (int ps = 0; ps < 2; ++ps) {
            const int tok = aidx * 128 + wid * 16 + ps * 8 + (lane >> 3);
            const u32x4* xr = (const u32x4*)(x + (size_t)tok * DM) + (lane & 7);
            float sm = 0.f;
#pragma unroll
            for (int i = 0; i < 10; ++i) { const u32x4 xv = xr[i * 8], gv = gr[i * 8];
#pragma unroll
                for (int k = 0; k < 4; ++k) { const unsigned a_ = xv[k], b_ = gv[k]; sm = __builtin_amdgcn_fdot2_f32_bf16(__builtin_bit_cast(bf16x2_t, a_), __builtin_bit_cast(bf16x2_t, b_), sm, false); } }
            sm = dpp_add<0xB1>(sm); sm = dpp_add<0x4E>(sm); sm = dpp_add<0x141>(sm);
            const float lg = bf_round(sm);
            const float g = bf_round(__builtin_amdgcn_rcpf(1.f + __builtin_amdgcn_exp2f(lg * -1.4426950408889634f)));
            if ((lane & 7) == 0) st4f(ws + WS_GATE + tok * 4, g);
        }
    };
    GDma<true> d2; d2.init(xbase, (const char*)w1 + ct * 192, FF * 2, lds, lane, k0);
    u64 pc0; asm volatile("s_getpc_b64 %0" : "=s"(pc0));
    if (wid < 4) {
        GDma<false> d1; d1.init(xbase, (const char*)wqkv + ct * 256, 3 * DM * 2, lds, lane, k0);
        if (tile1) {
#pragma unroll
            for (int jj = 0; jj < 4; ++jj) d1.xp(d1.kact(0), 0, 4 * wid + jj, jj & 1);
        }
        if (tid == 0) { ctl[0] = 0u; ctl[1] = 0u; }
        {
#pragma unroll
            for (int i = 0; i < 7; ++i) {
                int u = (bid * 4 + wid) * 7 + i; if (u > 6879) u = 6879;
                const char* src = u < 2560 ? (const char*)x + (size_t)u * 1024 : u < 4960 ? (const char*)wqkv + (size_t)(u - 2560) * 1024 : (const char*)w1 + (size_t)(u - 4960) * 1024;
                glds16(src, lane16, lds0 + 152 * 1024);
            }
        }
        if (tile1) asm volatile("s_waitcnt vmcnt(7) lgkmcnt(0)\n\ts_barrier" ::: "memory");
        if (slot * 4 + wid < 37) glds16((const char*)((pc0 + 15ull) & ~15ull) + (slot * 4 + wid) * 1024, lane16, lds0 + 152 * 1024);
        for (int pc = wid; pc < 10; pc += 4) rt_load(w2, wo, bid, lds0 + RT_OFF, pc, lane);
        if (!tile1) for (int pc = wid; pc < 10; pc += 4) rt_load(w2, wo, 256 + (xcd >> 1) * 4 + (slot & 3), lds0 + RT_OFF + 10240, pc, lane);
        if (wid == 0) glds16_sc1(ws + WS_EPOCH, 0u, lds0 + EP_OFF);
        auto tag_ready = [&]() {
            if (tid == 0) { const u64 tg = *(const volatile LAS u64*)(lds + EP_OFF) + 1ull; ctl[2] = (unsigned)tg; ctl[3] = (unsigned)(tg >> 32); }
        };
        if (!tile1) { asm volatile("s_waitcnt vmcnt(0)" ::: "memory"); tag_ready(); asm volatile("s_waitcnt lgkmcnt(0)\n\ts_barrier" ::: "memory"); }
        if (tile1) {
            const int wm = wid >> 1, wn = wid & 1;
            f32x16 acc[2][2] = {};
            auto nohook = [&](int kc, bool before) {
                if (kc == 1 && before) { asm volatile("s_waitcnt vmcnt(0)" ::: "memory"); tag_ready(); }
            };
            if (ct >= 10) {
                gemm_loop<false, true, 2, 2>(acc, lds, wid, lane, nohook);
#pragma unroll
                for (int nt = 0; nt < 2; ++nt) {
                    const int phi = (ct - 10) * 128 + wn * 64 + nt * 32 + r;
                    const int hd = (phi * 3277) >> 16, d = phi - hd * 20;
                    const unsigned dst = (unsigned)WS_KV + (unsigned)(hd * NT + rt * 4 + wm * 2) * KVTILE_B + V_OFF + (h * V_ROWS + d) * 16;
#pragma unroll
                    for (int mt = 0; mt < 2; ++mt)
#pragma unroll
                        for (int s = 0; s < 2; ++s) {
                            const f32x16& a = acc[mt][nt];
                            const u32x4 w = {cvtpk(a[8 * s], a[8 * s + 1]), cvtpk(a[8 * s + 2], a[8 * s + 3]), cvtpk(a[8 * s + 4], a[8 * s + 5]), cvtpk(a[8 * s + 6], a[8 * s + 7])};
                            st16(rs, dst + mt * KVTILE_B + s * V_STEP, w);
                        }
                }
                BARL();
            } else {
                gemm_loop<false, false, 2, 2>(acc, lds, wid, lane, nohook);
                const bool isQ = ct < 5;
                const float sc = isQ ? QSCALE : 1.f;
                const unsigned img = (unsigned)(isQ ? WS_Q : WS_KV), tileb = isQ ? QTILE_B : KVTILE_B;
                const int G = (isQ ? ct : ct - 5) * 32 + wn * 16 + h;
                u32x2 P[2][8]; float nrm[8];
#pragma unroll
                for (int p = 0; p < 8; ++p) {
                    const int nt = p >> 2, g = p & 3; float m = 0.f;
#pragma unroll
                    for (int mt = 0; mt < 2; ++mt) {
                        const f32x16& a = acc[mt][nt];
                        P[mt][p] = (u32x2){cvtpk(a[4 * g] * sc, a[4 * g + 1] * sc), cvtpk(a[4 * g + 2] * sc, a[4 * g + 3] * sc)};
                        m = fmaxf(m, __builtin_fmaf(a[4 * g], a[4 * g], a[4 * g + 1] * a[4 * g + 1]) + __builtin_fmaf(a[4 * g + 2], a[4 * g + 2], a[4 * g + 3] * a[4 * g + 3]));
                    }
                    nrm[p] = m;
                }
#pragma unroll
                for (int p = 0; p < 8; ++p) {
                    const int gam = G + 2 * p, hd = (gam * 205) >> 10, dg = gam - 5 * hd;
                    const unsigned base = img + (unsigned)(hd * NT + rt * 4 + wm * 2) * tileb;
#pragma unroll
                    for (int mt = 0; mt < 2; ++mt) {
                        const unsigned bm = base + mt * tileb;
                        if (dg < 2) {
                            if (p < 7) st16(rs, bm + dg * 512 + r * 16, (u32x4){P[mt][p][0], P[mt][p][1], P[mt][(p + 1) & 7][0], P[mt][(p + 1) & 7][1]});
                            else st8(rs, bm + dg * 512 + r * 16, P[mt][p]);
                        } else if (dg == 4) st8(rs, bm + 1024 + r * 8, P[mt][p]);
                        else if (p == 0) st8(rs, bm + (dg - 2) * 512 + r * 16 + 8, P[mt][p]);
                    }
                }
                if (!isQ) {
#pragma unroll
                    for (int i = 0; i < 8; ++i) { float m = nrm[i]; m = dpp_max<0xB1>(m); m = dpp_max<0x4E>(m); m = dpp_max<0x141>(m); m = dpp_max<0x140>(m);
                        auto rr = __builtin_amdgcn_permlane16_swap(__float_as_uint(m), __float_as_uint(m), false, false); nrm[i] = fmaxf(__uint_as_float(rr[0]), __uint_as_float(rr[1])); }
                    float mine = nrm[0];
#pragma unroll
                    for (int i = 1; i < 8; ++i) mine = r == i ? nrm[i] : mine;
                    if (r < 8) st4f(ws + WS_KMAX + (size_t)((G + 2 * r) * 32 + rt * 2 + wm) * 4, mine);
                }
                BARL();
            }
        } else { gates(); BARL(); }
        BARL();
        f32x16 acc2[1][3] = {};
        auto hook2 = [&](int kc, bool before) {
            if (kc == 2) { if (before) asm volatile("s_waitcnt vmcnt(0)" ::: "memory");
                           else if (tid == 0) flag_set(ws + WS_F1 + bid * 8, ((u64)ctl[3] << 32) | ctl[2]); }
        };
        gemm_loop<true, false, 1, 3>(acc2, lds, wid, lane, hook2);
        wait_seen();
        q_loads();
        {   const int tok = rt * 128 + wid * 32 + r, rho = tok >> 4;
#pragma unroll
            for (int nt = 0; nt < 3; ++nt) {
                const unsigned dst = (unsigned)WS_AF + (unsigned)(rho * NSIG + ct * 3 + nt) * 1024 + (h * 16 + (r & 15)) * 16;
                float sv[16];
#pragma unroll
                for (int i = 0; i < 16; ++i) { const float v = acc2[0][nt][i]; sv[i] = v * __builtin_amdgcn_rcpf(1.f + __builtin_amdgcn_exp2f(v * -1.4426950408889634f)); }
#pragma unroll
                for (int s = 0; s < 2; ++s) {
                    const u32x4 w = {cvtpk(sv[8 * s], sv[8 * s + 1]), cvtpk(sv[8 * s + 2], sv[8 * s + 3]), cvtpk(sv[8 * s + 4], sv[8 * s + 5]), cvtpk(sv[8 * s + 6], sv[8 * s + 7])};
                    st16(rs, dst + s * 512, w);
                }
            }
        }
        asm volatile("s_waitcnt vmcnt(6)" : "+v"(qa), "+v"(qbr), "+v"(km4), "+v"(gate) :: "memory");
    } else {
        const int lw = wid - 4;
        auto side_stores = [&]() {
            for (int i = lw; i < 10; i += 4) rt_store(rs, bid, lds + RT_OFF, i, lane);
            if (!tile1) for (int i = lw; i < 10; i += 4) rt_store(rs, 256 + (xcd >> 1) * 4 + (slot & 3), lds + RT_OFF + 10240, i, lane);
            if (lw == 0) { const int i = bid * 64 + lane;
                const unsigned f = (i & 1) ? 0u : ONES2; st16(rs, (unsigned)WS_KV + (unsigned)(i >> 3) * KVTILE_B + V_OFF + ((i >> 2) & 1) * V_STEP + (((i >> 1) & 1) * V_ROWS + 20 + (i & 1)) * 16, (u32x4){f, f, f, f}); }
        };
        if (tile1) {
            GDma<false> d1; d1.init(xbase, (const char*)wqkv + ct * 256, 3 * DM * 2, lds, lane, k0);
#pragma unroll
            for (int jj = 0; jj < 4; ++jj) d1.wp(d1.kact(0), 0, lw, jj);
            d1.issue(1, 1, lw);
            WAITB(8);
            d1.issue(2, 2, lw);
            for (int kc = 0; kc < 9; ++kc) {
                if (kc < 7) WAITB(8); else if (kc == 7) WAITB(15); else WAITB(14);
                if (kc + 3 < 10) d1.issue(kc + 3, (kc + 3) & 3, lw);
                if (kc == 4) side_stores();
                if (kc >= 6) d2.issue(kc - 6, kc - 6, lw);
            }
            asm volatile("s_barrier" ::: "memory");
        } else {
            asm volatile("s_barrier" ::: "memory");
            side_stores(); gates();
            BARL();
            d2.issue(0, 0, lw); d2.issue(1, 1, lw); d2.issue(2, 2, lw);
        }
        WAITB(14);
        { int st = 0;
          for (int kc = 0; kc < 9; ++kc) {
              if (kc < 8) WAITB(7); else WAITB(0);
              if (kc + 3 < 10) d2.issue(kc + 3, st, lw);
              st = st == 2 ? 0 : st + 1;
          } }
        if (lw == 0) {
            const u64 tag = ((u64)ctl[3] << 32) | ctl[2];
            for (int it = 0; it < SPIN_MAX; ++it) {
                bool ok = true;
#pragma unroll
                for (int i = 0; i < 4; ++i) ok &= flag_get(ws + WS_F1 + (lane + 64 * i) * 8) == tag;
                if (__all(ok)) break;
                __builtin_amdgcn_s_sleep(2);
            }
            __builtin_amdgcn_fence(__ATOMIC_ACQUIRE, "agent");
            asm volatile("s_waitcnt vmcnt(0)" ::: "memory");
            if (lane == 0) { ctl[1] = 1u; if (bid == 0) flag_set(ws + WS_EPOCH, tag); }
        } else wait_seen();
        q_loads();
        for (int j = 0; j < 21; ++j) glds16(kv + (size_t)(lw + 4 * j) * 1024, lane16, lds0 + (lw + 4 * j) * 1024);
        asm volatile("s_waitcnt vmcnt(21)" : "+v"(qa), "+v"(qbr), "+v"(km4), "+v"(gate) :: "memory");
    }
    if (wid >= 4) __builtin_amdgcn_s_setprio(1);
    float km = fmaxf(fmaxf(km4[0], km4[1]), fmaxf(km4[2], km4[3]));
    km = dpp_max<0xB1>(km); km = dpp_max<0x4E>(km); km = dpp_max<0x141>(km);
    km = __builtin_amdgcn_sqrtf(km);
    float M;
    { const float c0 = rdl(km, 0), c1 = rdl(km, 8), c2 = rdl(km, 16), c3 = rdl(km, 24), c4 = rdl(km, 32);
      const u32x4 qw = __builtin_bit_cast(u32x4, qa);
      float n0, n1, n2;
      { const float a = bf_lo(qw[0]), b = bf_hi(qw[0]), c = bf_lo(qw[1]), d = bf_hi(qw[1]); n0 = (a * a + b * b) + (c * c + d * d); }
      { const float a = bf_lo(qw[2]), b = bf_hi(qw[2]), c = bf_lo(qw[3]), d = bf_hi(qw[3]); n1 = (a * a + b * b) + (c * c + d * d); }
      { const unsigned q0 = qbr[0], q1 = qbr[1]; const float a = bf_lo(q0), b = bf_hi(q0), c = bf_lo(q1), d = bf_hi(q1); n2 = (a * a + b * b) + (c * c + d * d); }
      float part = __builtin_amdgcn_sqrtf(n0) * (h ? c1 : c0) + __builtin_amdgcn_sqrtf(n1) * (h ? c3 : c2);
      if (!h) part += __builtin_amdgcn_sqrtf(n2) * c4;
      M = swap32_add(part) * 1.01f; }
    const s16x4 qb = make_qb(qbr[0], qbr[1], M, h);
    const int vrow = r <= 20 ? r : (r < 28 ? 21 : r - 12);
    const int voff = V_OFF + (h * V_ROWS + vrow) * 16, kboff = h ? V_OFF + 20 * 16 : 1024 + r * 8;
    lds_cptr kaC = (lds_cptr)lds + lane * 16, kbC = (lds_cptr)lds + kboff, vC = (lds_cptr)lds + voff;
    lds_cptr kaN = kaC + K2_BIG, kbN = kbC + K2_BIG, vN = vC + K2_BIG;
    const f32x16 zero = {};
    f32x16 o = {}, sA, sB;
    bf16x8 ka, va, vb; s16x4 kb;
    u32x4 plo, phi;
    if (wid >= 4) asm volatile("s_waitcnt vmcnt(15)" ::: "memory");
    asm volatile("s_barrier" ::: "memory");
    ka = lds128(kaC); kb = lds64(kbC);
    sA = MFMA32(ka, qa, zero); sA = MFMA8(kb, qb, sA);
    ka = lds128(kaC + KVTILE_B); kb = lds64(kbC + KVTILE_B);
    va = lds128(vC);
    vb = (bf16x8){}; phi = (u32x4){};
    f32x4 e3 = {};
    const int ng = (xcd & 1) * 4 + (slot & 3), bt = (xcd >> 1) * 8 + (slot >> 2);
    const char* ap = ws + WS_AF + (size_t)bt * 4 * NSIG * 1024;
    const char* bp = ws + WS_BF + (size_t)ng * NSIG * 5 * 1024;
        const int rot = ((slot >> 2) + 3 * (slot & 3)) % 6;
    auto hs = [&](int i) { int st = i + rot; if (st >= 6) st -= 6; return wid * 6 + st; };
    bf16x8 a[3][4], b[3][5];
    auto ldA1 = [&](int sg, int mt, bf16x8& d) { gld128_sc1(d, ap + ((size_t)mt * NSIG + sg) * 1024, lane16); };
    auto ldB1 = [&](int sg, int nt, bf16x8& d) { gld128(d, bp + sg * 5120 + nt * 1024, lane16); };
    const u64 tag = ((u64)ctl[3] << 32) | ctl[2];
#define EXP4(X, i) do { X[i] = __builtin_amdgcn_exp2f(X[i]); X[i + 1] = __builtin_amdgcn_exp2f(X[i + 1]); X[i + 2] = __builtin_amdgcn_exp2f(X[i + 2]); X[i + 3] = __builtin_amdgcn_exp2f(X[i + 3]); } while (0)
#define KADDR(A, i) (((i) < 16) ? (A##C + (i) * KVTILE_B) : (A##N + ((i) - 16) * KVTILE_B))
#define STEP(C, N, i, X0, X1) do { \
        bf16x8 ka2, va2, vb2; s16x4 kb2; \
        N = MFMA32(ka, qa, zero); SBAR(); \
        ka2 = lds128(KADDR(ka, (i) + 2)); \
        phi[2] = cvtpk(e3[0], e3[1]); phi[3] = cvtpk(e3[2], e3[3]); PIN(phi); SBAR(); EXP4(C, 0); PIN(C); SBAR(); \
        o = MFMA32(vb, __builtin_bit_cast(bf16x8, phi), o); SBAR(); \
        X0; \
        vb2 = lds128(vC + (i) * KVTILE_B + V_STEP); \
        plo[0] = cvtpk(C[0], C[1]); plo[1] = cvtpk(C[2], C[3]); PIN(plo); SBAR(); EXP4(C, 4); PIN(C); SBAR(); \
        N = MFMA8(kb, qb, N); SBAR(); \
        kb2 = lds64(KADDR(kb, (i) + 2)); \
        plo[2] = cvtpk(C[4], C[5]); plo[3] = cvtpk(C[6], C[7]); PIN(plo); SBAR(); EXP4(C, 8); PIN(C); SBAR(); \
        o = MFMA32(va, __builtin_bit_cast(bf16x8, plo), o); SBAR(); \
        X1; \
        va2 = lds128(KADDR(v, (i) + 1)); \
        phi[0] = cvtpk(C[8], C[9]); phi[1] = cvtpk(C[10], C[11]); PIN(phi); SBAR(); \
        e3[0] = __builtin_amdgcn_exp2f(C[12]); e3[1] = __builtin_amdgcn_exp2f(C[13]); e3[2] = __builtin_amdgcn_exp2f(C[14]); e3[3] = __builtin_amdgcn_exp2f(C[15]); PIN(e3); SBAR(); \
        ka = ka2; kb = kb2; va = va2; vb = vb2; \
    } while (0)
#define PIECE(j) do { if (8 * (j) + 7 < 42 || 8 * (j) + wid < 42) glds16(src + (j) * 8192, lane16, dst + (j) * 8192); } while (0)
    const char* src = kv + (84 + wid) * 1024; unsigned dst = lds0 + wid * 1024 + 2 * K2_BIG;
#pragma unroll 1
    for (int c = 0; c < 3; ++c) {
        STEP(sA, sB, 0, , ); STEP(sB, sA, 1, , ); STEP(sA, sB, 2, , ); STEP(sB, sA, 3, , ); STEP(sA, sB, 4, , );
        if (c == 0) { if (wid >= 4) asm volatile("s_waitcnt vmcnt(10)" ::: "memory"); asm volatile("s_barrier" ::: "memory"); }
        STEP(sB, sA, 5, , ); STEP(sA, sB, 6, , ); STEP(sB, sA, 7, , ); STEP(sA, sB, 8, , ); STEP(sB, sA, 9, , ); STEP(sA, sB, 10, , ); STEP(sB, sA, 11, , ); STEP(sA, sB, 12, , );
        if (c == 2 && wid == 0) {
            for (int it = 0; it < SPIN_MAX; ++it) {
                if (__all(flag_get(ws + WS_F2 + ((bt >> 1) * 16 + (lane & 15)) * 8) == tag)) break;
                __builtin_amdgcn_s_sleep(2);
            }
        }
        WAITB(0);
        if (c == 0 && tid == 0) flag_set(ws + WS_F2 + (rt * 16 + ct) * 8, tag);
        if (c < 2) { STEP(sB, sA, 13, PIECE(0), PIECE(1)); STEP(sA, sB, 14, PIECE(2), PIECE(3)); STEP(sB, sA, 15, PIECE(4), PIECE(5)); }
        else { STEP(sB, sA, 13, , ); STEP(sA, sB, 14, , ); STEP(sB, sA, 15, , ); }
        const int d = (c == 1) ? -2 * K2_BIG : K2_BIG;
        kaC = kaN; kbC = kbN; vC = vN; kaN += d; kbN += d; vN += d;
        src += 42 * 1024; dst -= 2 * K2_BIG;
    }
#define PA(s, m) ldA1(hs(s), m, a[s][m])
#define PB(s, n) ldB1(hs(s), n, b[s][n])
    STEP(sA, sB, 0, PA(0, 0), PA(0, 1)); STEP(sB, sA, 1, PA(0, 2), PA(0, 3)); STEP(sA, sB, 2, PB(0, 0), PB(0, 1)); STEP(sB, sA, 3, PB(0, 2), PB(0, 3));
    STEP(sA, sB, 4, PB(0, 4), PA(1, 0)); STEP(sB, sA, 5, PA(1, 1), PA(1, 2)); STEP(sA, sB, 6, PA(1, 3), PB(1, 0)); STEP(sB, sA, 7, PB(1, 1), PB(1, 2));
    STEP(sA, sB, 8, PB(1, 3), PB(1, 4)); STEP(sB, sA, 9, PA(2, 0), PA(2, 1)); STEP(sA, sB, 10, PA(2, 2), PA(2, 3)); STEP(sB, sA, 11, PB(2, 0), PB(2, 1));
    STEP(sA, sB, 12, PB(2, 2), PB(2, 3)); STEP(sB, sA, 13, PB(2, 4), ); STEP(sA, sB, 14, , ); STEP(sB, sA, 15, , );
    phi[2] = cvtpk(e3[0], e3[1]); phi[3] = cvtpk(e3[2], e3[3]);
    o = MFMA32(vb, __builtin_bit_cast(bf16x8, phi), o);
    float l; { auto rr = __builtin_amdgcn_permlane32_swap(__float_as_uint(o[8]), __float_as_uint(o[8]), false, false); l = __uint_as_float(rr[1]); }
    if (__builtin_expect(__any(!(l >= 8.6736174e-19f && l <= 1.0e30f)), 0)) {
        asm volatile("s_waitcnt vmcnt(0)" ::: "memory");
        attn_fallback(o, kv, qa, qbr[0], qbr[1], lane, kboff, voff);
        auto rr = __builtin_amdgcn_permlane32_swap(__float_as_uint(o[8]), __float_as_uint(o[8]), false, false); l = __uint_as_float(rr[1]);
    }
    const float f = gate * __builtin_amdgcn_rcpf(l);
    const int tok = Tq * 32 + r;
    char* ab = ws + WS_AF + (size_t)(tok >> 4) * NSIG * 1024 + (tok & 15) * 16;
#pragma unroll
    for (int g = 0; g < 3; ++g) {
        const int gam = head * 5 + 2 * g + h;
        const int sig = 48 + (gam >> 3), c = ((gam >> 2) & 1) * 2 + (gam & 1), half = (gam >> 1) & 1;
        const u32x2 w = {cvtpk(o[4 * g] * f, o[4 * g + 1] * f), cvtpk(o[4 * g + 2] * f, o[4 * g + 3] * f)};
        if (g < 2 || h == 0) __hip_atomic_store((u64*)(ab + sig * 1024 + c * 256 + half * 8), ((u64)w[1] << 32) | w[0], __ATOMIC_RELAXED, __HIP_MEMORY_SCOPE_AGENT);
    }
    __builtin_amdgcn_s_setprio(0);
    {
        f32x4 acc[4][5] = {};
        auto loadA = [&](int sg, bf16x8 (&da)[4]) {
#pragma unroll
            for (int mt = 0; mt < 4; ++mt) gld128_sc1(da[mt], ap + ((size_t)mt * NSIG + sg) * 1024, lane16);
        };
        auto loadB = [&](int sg, bf16x8 (&db)[5]) {
#pragma unroll
            for (int nt = 0; nt < 5; ++nt) gld128(db[nt], bp + sg * 5120 + nt * 1024, lane16);
        };
        auto mma = [&](bf16x8 (&da)[4], bf16x8 (&db)[5]) {
            SBAR();
#pragma unroll
            for (int nt = 0; nt < 5; ++nt)
#pragma unroll
                for (int mt = 0; mt < 4; ++mt) acc[mt][nt] = MFMA16(db[nt], da[mt], acc[mt][nt]);
            SBAR();
        };
        wait9<3>(a[0], b[0]);
        asm volatile("" : "+v"(a[1][0]), "+v"(a[1][1]), "+v"(a[1][2]), "+v"(a[1][3]), "+v"(b[1][0]), "+v"(b[1][1]), "+v"(b[1][2]), "+v"(b[1][3]), "+v"(b[1][4]));
        asm volatile("" : "+v"(a[2][0]), "+v"(a[2][1]), "+v"(a[2][2]), "+v"(a[2][3]), "+v"(b[2][0]), "+v"(b[2][1]), "+v"(b[2][2]), "+v"(b[2][3]), "+v"(b[2][4]));
        mma(a[0], b[0]); loadA(hs(3), a[0]); loadB(hs(3), b[0]);
        mma(a[1], b[1]); loadA(hs(4), a[1]); loadB(hs(4), b[1]);
        asm volatile("s_waitcnt vmcnt(18)" ::: "memory");
        if (lane == 0) {
            const unsigned old = __hip_atomic_fetch_add((unsigned*)(lds + CTL_OFF), 1u, __ATOMIC_RELAXED, __HIP_MEMORY_SCOPE_WORKGROUP);
            if (old == 7u) flag_set(ws + WS_F3 + (qblk * 32 + head) * 8, tag);
        }
        mma(a[2], b[2]); loadA(hs(5), a[2]); loadB(hs(5), b[2]);
        float og[4];
#pragma unroll
        for (int mt = 0; mt < 4; ++mt) asm volatile("s_nop 4\n\tglobal_load_dword %0, %1, %2" : "=v"(og[mt]) : "v"((unsigned)((bt * 64 + mt * 16 + (lane & 15)) * 4)), "s"(ws + WS_GATE) : "memory");
        wait9<22>(a[0], b[0]); mma(a[0], b[0]); loadB(48 + wid, b[0]);
        wait9<18>(a[1], b[1]); mma(a[1], b[1]); loadB(56 + wid, b[1]);
        wait9<14>(a[2], b[2]); mma(a[2], b[2]);
        if (wid < 4) {
#pragma unroll
            for (int nt = 0; nt < 5; ++nt) gld128_rw(b[2][nt], bp + (64 + wid) * 5120 + nt * 1024, lane16);
        }
        asm volatile("s_waitcnt vmcnt(0)" : "+v"(og[0]), "+v"(og[1]), "+v"(og[2]), "+v"(og[3]) :: "memory");
#pragma unroll
        for (int mt = 0; mt < 4; ++mt) { const float f1 = bf_round(1.f - og[mt]);
#pragma unroll
            for (int nt = 0; nt < 5; ++nt) acc[mt][nt] *= f1; }
        if (wid == 0) {
            for (int it = 0; it < SPIN_MAX; ++it) {
                if (__all(flag_get(ws + WS_F3 + ((bt >> 2) * 32 + r) * 8) == tag)) break;
                __builtin_amdgcn_s_sleep(2);
            }
        }
        asm volatile("s_barrier" ::: "memory");
        loadA(48 + wid, a[0]); loadA(56 + wid, a[1]);
        a[2][0] = (bf16x8){}; a[2][1] = (bf16x8){}; a[2][2] = (bf16x8){}; a[2][3] = (bf16x8){};
        if (wid < 4) {
#pragma unroll
            for (int mt = 0; mt < 4; ++mt) gld128_sc1_rw(a[2][mt], ap + ((size_t)mt * NSIG + 64 + wid) * 1024, lane16);
        }
        wait9<4>(a[0], b[0]); mma(a[0], b[0]);
        wait9<0>(a[1], b[1]); wait9<0>(a[2], b[2]); mma(a[1], b[1]); mma(a[2], b[2]);
#pragma unroll
        for (int mt = 0; mt < 4; ++mt)
#pragma unroll
            for (int nt = 0; nt < 5; ++nt) *(f32x4*)(lds + (wid * 20 + mt * 5 + nt) * 1024 + lane16) = acc[mt][nt];
        BARL();
#pragma unroll
        for (int i = 0; i < 3; ++i) { const int t = wid + 8 * i;
            if (t < 20) {
                f32x4 sum = *(const f32x4*)(lds + t * 1024 + lane16);
#pragma unroll
                for (int w = 1; w < 8; ++w) sum += *(const f32x4*)(lds + (w * 20 + t) * 1024 + lane16);
                const int m4 = t / 5, nt = t - m4 * 5;
                const int tok2 = bt * 64 + m4 * 16 + (lane & 15);
                *(u32x2*)(out + (size_t)tok2 * DM + ng * 80 + nt * 16 + (lane >> 4) * 4) = (u32x2){cvtpk(sum[0], sum[1]), cvtpk(sum[2], sum[3])};
            } }
    }
}

extern "C" void kernel_launch(void* const* d_in, const int* in_sizes, int n_in, void* d_out, int out_size, void* d_ws, size_t ws_size, hipStream_t stream) {
    const unsigned short* x = (const unsigned short*)d_in[0];
    const unsigned short* wqkv = (const unsigned short*)d_in[1];
    const unsigned short* wo = (const unsigned short*)d_in[2];
    const unsigned short* w1 = (const unsigned short*)d_in[4];
    const unsigned short* w2 = (const unsigned short*)d_in[5];
    const unsigned short* gw = (const unsigned short*)d_in[6];
    (void)hipFuncSetAttribute((const void*)gated_mix_mega, hipFuncAttributeMaxDynamicSharedMemorySize, LDS_ALL);
    gated_mix_mega<<<256, 512, LDS_ALL, stream>>>(x, wqkv, wo, w1, w2, gw, (char*)d_ws, (unsigned short*)d_out);
}
```

```cpp
#include <hip/hip_runtime.h>
#include <hip/hip_bf16.h>
#include <stdint.h>

typedef short bf16x8 __attribute__((ext_vector_type(8)));
typedef short s16x4 __attribute__((ext_vector_type(4)));
typedef float f32x16 __attribute__((ext_vector_type(16)));
typedef float f32x4 __attribute__((ext_vector_type(4)));
typedef float f32x2 __attribute__((ext_vector_type(2)));
typedef unsigned u32x4 __attribute__((ext_vector_type(4)));
typedef unsigned u32x2 __attribute__((ext_vector_type(2)));
typedef __bf16 bf16x2_t __attribute__((ext_vector_type(2)));
#define LAS __attribute__((address_space(3)))
typedef LAS const char* lds_cptr;
#define DI __device__ __forceinline__
#define SBAR() __builtin_amdgcn_sched_barrier(0)
#define PIN(x) asm volatile("" : "+v"(x))
#define MFMA32(a, b, c) __builtin_amdgcn_mfma_f32_32x32x16_bf16(a, b, c, 0, 0, 0)
#define MFMA16(a, b, c) __builtin_amdgcn_mfma_f32_16x16x32_bf16(a, b, c, 0, 0, 0)
#define MFMA8(a, b, c) __builtin_amdgcn_mfma_f32_32x32x8bf16_1k(a, b, c, 0, 0, 0)

constexpr int S = 2048, DM = 640, NH = 32, DH = 20, FF = 1536;
constexpr int NT = S / 32;
constexpr int QTILE_B = 1280;
constexpr int KVTILE_B = 2688;
constexpr int V_OFF = 1280, V_STEP = 704, V_ROWS = 22;
constexpr int NSIG = 68;
constexpr size_t WS_Q = 0;
constexpr size_t WS_KV = WS_Q + (size_t)NH * NT * QTILE_B;
constexpr size_t WS_AF = WS_KV + (size_t)NH * NT * KVTILE_B;
constexpr size_t WS_BF = WS_AF + (size_t)128 * NSIG * 1024;
constexpr size_t WS_KMAX = WS_BF + (size_t)8 * NSIG * 5 * 1024;
constexpr size_t WS_GATE = WS_KMAX + (size_t)160 * 32 * 4;
constexpr size_t WS_F1 = WS_GATE + (size_t)S * 4;
constexpr size_t WS_F2 = WS_F1 + 2048;
constexpr size_t WS_F3 = WS_F2 + 2048;
constexpr size_t WS_EPOCH = WS_F3 + 2048;
constexpr size_t WS_END = WS_EPOCH + 256;
constexpr float QSCALE = 0.22360679774997896f * 1.4426950408889634f;
constexpr unsigned ONES2 = 0x3F803F80u;

DI unsigned cvtpk(float lo, float hi) { f32x2 v = {lo, hi}; bf16x2_t b = __builtin_convertvector(v, bf16x2_t); return __builtin_bit_cast(unsigned, b); }
DI float bf_lo(unsigned u) { return __uint_as_float(u << 16); }
DI float bf_hi(unsigned u) { return __uint_as_float(u & 0xffff0000u); }
DI float bf_round(float x) { return bf_lo(cvtpk(x, x)); }
DI void glds16(const void* sbase, unsigned voff, unsigned lds_base) {
    unsigned sv;
    asm volatile("s_mov_b32 %0, m0\n\ts_mov_b32 m0, %3\n\ts_nop 0\n\tglobal_load_lds_dwordx4 %1, %2\n\ts_mov_b32 m0, %0"
                 : "=&s"(sv) : "v"(voff), "s"(sbase), "s"(lds_base) : "memory");
}
DI void glds16_sc1(const void* sbase, unsigned voff, unsigned lds_base) {
    unsigned sv;
    asm volatile("s_mov_b32 %0, m0\n\ts_mov_b32 m0, %3\n\ts_nop 0\n\tglobal_load_lds_dwordx4 %1, %2 sc1\n\ts_mov_b32 m0, %0"
                 : "=&s"(sv) : "v"(voff), "s"(sbase), "s"(lds_base) : "memory");
}
typedef short v4i16_t __attribute__((ext_vector_type(4)));
DI s16x4 ldstr(lds_cptr p) { return __builtin_bit_cast(s16x4, __builtin_amdgcn_ds_read_tr16_b64_v4i16((LAS v4i16_t*)p)); }
DI bf16x8 lds128(lds_cptr p) { return *(const LAS bf16x8*)p; }
DI s16x4 lds64(lds_cptr p) { return *(const LAS s16x4*)p; }
DI unsigned rfl(unsigned x) { return __builtin_amdgcn_readfirstlane(x); }


DI float rdl(float x, int l) { return __uint_as_float(__builtin_amdgcn_readlane(__float_as_uint(x), l)); }
typedef __amdgpu_buffer_rsrc_t rsrc_t;
typedef unsigned long long u64;
DI rsrc_t ws_rsrc(char* ws) { return __builtin_amdgcn_make_buffer_rsrc(ws, 0, (int)WS_END, 0x00020000); }
DI void st16(rsrc_t rs, unsigned off, u32x4 v) { __builtin_amdgcn_raw_buffer_store_b128(v, rs, off, 0, 16); }
DI void st8(rsrc_t rs, unsigned off, u32x2 v) { __builtin_amdgcn_raw_buffer_store_b64(v, rs, off, 0, 16); }
DI void st4f(char* p, float v) { __hip_atomic_store((float*)p, v, __ATOMIC_RELAXED, __HIP_MEMORY_SCOPE_AGENT); }
DI void flag_set(char* p, u64 tag) { __hip_atomic_store((u64*)p, tag, __ATOMIC_RELAXED, __HIP_MEMORY_SCOPE_AGENT); }
DI u64 flag_get(const char* p) { return __hip_atomic_load((const u64*)p, __ATOMIC_RELAXED, __HIP_MEMORY_SCOPE_AGENT); }
template <int CTRL> DI float dpp_max(float x) { const float y = __uint_as_float(__builtin_amdgcn_update_dpp(0u, __float_as_uint(x), CTRL, 0xf, 0xf, true)); return fmaxf(x, y); }
template <int CTRL> DI float dpp_add(float x) { const float y = __uint_as_float(__builtin_amdgcn_update_dpp(0u, __float_as_uint(x), CTRL, 0xf, 0xf, true)); return x + y; }
DI void gld128(bf16x8& d, const char* sbase, unsigned voff) { asm volatile("global_load_dwordx4 %0, %1, %2" : "=v"(d) : "v"(voff), "s"(sbase) : "memory"); }
DI void gld128_sc1(bf16x8& d, const char* sbase, unsigned voff) { asm volatile("global_load_dwordx4 %0, %1, %2 sc1" : "=v"(d) : "v"(voff), "s"(sbase) : "memory"); }
DI void gld128_rw(bf16x8& d, const char* sbase, unsigned voff) { asm volatile("s_nop 4\n\tglobal_load_dwordx4 %0, %1, %2" : "+v"(d) : "v"(voff), "s"(sbase) : "memory"); }
DI void gld128_sc1_rw(bf16x8& d, const char* sbase, unsigned voff) { asm volatile("s_nop 4\n\tglobal_load_dwordx4 %0, %1, %2 sc1" : "+v"(d) : "v"(voff), "s"(sbase) : "memory"); }
template <int N> DI void wait9(bf16x8 (&a)[4], bf16x8 (&b)[5]) {
    asm volatile("s_waitcnt vmcnt(%9)" : "+v"(a[0]), "+v"(a[1]), "+v"(a[2]), "+v"(a[3]), "+v"(b[0]), "+v"(b[1]), "+v"(b[2]), "+v"(b[3]), "+v"(b[4]) : "i"(N) : "memory");
}
#define WAITB(n) asm volatile("s_waitcnt vmcnt(" #n ")\n\ts_barrier" ::: "memory")
#define BARL() asm volatile("s_waitcnt lgkmcnt(0)\n\ts_barrier" ::: "memory")

constexpr int P1_STG = 32768;
constexpr int RT_OFF = 131072;
constexpr int P2_STG = 28672;
constexpr int CTL_OFF = 163584;
DI constexpr int p2_base(int s) { return CTL_OFF - (s + 1) * P2_STG; }
constexpr int K2_BIG = 16 * KVTILE_B;
constexpr int EP_OFF = 151552;
constexpr int LDS_ALL = 163840;

template <bool N96>
struct GDma {
    static constexpr int NW = N96 ? 3 : 4;
    const char* xbase; const char* wbase; int ldw2, k0; unsigned lds0, xoff0, xoff1, woff[3];
    DI int kact(int kc) const { kc += k0; return kc >= 10 ? kc - 10 : kc; }
    DI static unsigned sbase(int st) { return N96 ? (unsigned)(CTL_OFF - (st + 1) * P2_STG) : (unsigned)(st * P1_STG); }
    DI void init(const char* xb, const char* wb, int ld, char* lds, int lane, int k0_) {
        xbase = xb; wbase = wb; ldw2 = ld; k0 = k0_; lds0 = (unsigned)(uintptr_t)lds;
        const int rr = lane >> 3;
        xoff0 = rr * 1280 + (((lane & 7) ^ ((lane >> 4) & 7)) << 4); xoff1 = rr * 1280 + (((lane & 7) ^ ((4 + (lane >> 4)) & 7)) << 4);
        if (N96) {
#pragma unroll
            for (int p = 0; p < 3; ++p) { const int o = 1024 * p + 16 * lane, row = (o * 21846) >> 22, col = o - row * 192; woff[p] = row * ldw2 + col; }
        } else woff[0] = (lane >> 4) * ldw2 + ((((lane & 15) ^ ((lane >> 4) << 2))) << 4);
    }
    DI void xp(int kc, int st, int j, int par) const { glds16(xbase + (size_t)j * 8 * 1280 + kc * 128, par ? xoff1 : xoff0, lds0 + sbase(st) + j * 1024); }
    DI void wp(int kc, int st, int lw, int jj) const {
        if (N96) glds16(wbase + (size_t)(kc * 64 + 16 * lw) * ldw2, woff[jj], lds0 + sbase(st) + 16384 + (3 * lw + jj) * 1024);
        else glds16(wbase + (size_t)(kc * 64 + 16 * lw + 4 * jj) * ldw2, woff[0], lds0 + sbase(st) + 16384 + (4 * lw + jj) * 1024);
    }
    DI void issue(int kc_, int st, int lw) const {
        const int kc = kact(kc_);
#pragma unroll
        for (int jj = 0; jj < NW; ++jj) wp(kc, st, lw, jj);
#pragma unroll
        for (int jj = 0; jj < 4; ++jj) xp(kc, st, 4 * lw + jj, jj & 1);
    }
};

DI void rt_load(const unsigned short* w2, const unsigned short* wo, int u, unsigned ldsb, int piece, int lane) {
    const int sig = u >> 2, s = (u >> 1) & 1, cb = u & 1;
    const char* src = (sig < 48 ? (const char*)w2 + (size_t)sig * 32 * 1280 : (const char*)wo + (size_t)(sig - 48) * 32 * 1280) + s * 16 * 1280 + cb * 640;
    const int o = piece * 1024 + lane * 16, row = (o * 6554) >> 22;
    glds16(src, row * 1280 + (o - row * 640), ldsb + piece * 1024);
}
DI void rt_store(rsrc_t rs, int u, const char* ldsb, int i, int lane) {
    const int sig = u >> 2, s = (u >> 1) & 1, cb = u & 1;
    const int nl = 2 * i + (lane >> 5), hh = (lane >> 4) & 1, q = (lane & 15) >> 2, p = lane & 3;
    lds_cptr a = (lds_cptr)ldsb + (4 * hh + q) * 640 + (16 * nl + 4 * p) * 2;
    const s16x4 lo = ldstr(a), hi = ldstr(a + 8 * 640);
    const bf16x8 f = __builtin_shufflevector(lo, hi, 0, 1, 2, 3, 4, 5, 6, 7);
    const int nu = cb * 20 + nl, ng = (nu * 205) >> 10;
    st16(rs, (unsigned)WS_BF + (unsigned)((ng * NSIG + sig) * 5 + (nu - 5 * ng)) * 1024 + (s * 32 + (lane & 31)) * 16, __builtin_bit_cast(u32x4, f));
}

template <bool N96, bool VT, int MT, int NTL, class HOOK>
DI void gemm_loop(f32x16 (&acc)[MT][NTL], char* lds, int wid, int lane, HOOK hook) {
    const int h = lane >> 5, r = lane & 31;
    constexpr int NST = N96 ? 3 : 4;
    unsigned xa[4];
    { const int row = (N96 ? wid * 32 : (wid >> 1) * 64) + r; const int sw = (row >> 1) & 7;
#pragma unroll
      for (int ks = 0; ks < 4; ++ks) xa[ks] = row * 128 + (((2 * ks + h) ^ sw) << 4); }
    unsigned wa[2];
    { const int blk = (lane >> 4) & 1, q = (lane & 15) >> 2, p = lane & 3;
      if (N96) wa[0] = wa[1] = 16384 + (8 * h + q) * 192 + blk * 32 + p * 8;
      else {
#pragma unroll
          for (int nt = 0; nt < 2; ++nt) { const int ch = (wid & 1) * 8 + nt * 4 + 2 * blk + (p >> 1); wa[nt] = 16384 + (8 * h + q) * 256 + ((ch ^ (q << 2)) << 4) + 8 * (p & 1); } } }
    bf16x8 xf[2][MT], wf[2][NTL];
    auto rdw = [&](int buf, int nt, lds_cptr sb, int ks) {
        lds_cptr a = N96 ? sb + wa[0] + ks * 16 * 192 + nt * 64 : sb + wa[nt & 1] + ks * 4096;
        const s16x4 lo = ldstr(a), hi = ldstr(a + (N96 ? 4 * 192 : 1024));
        wf[buf][nt] = __builtin_shufflevector(lo, hi, 0, 1, 2, 3, 4, 5, 6, 7);
    };
    auto rdx = [&](int buf, int mt, lds_cptr sb, int ks) { xf[buf][mt] = lds128(sb + xa[ks] + mt * 4096); };
    auto mm = [&](int buf, int mt, int nt) { acc[mt][nt] = VT ? MFMA32(xf[buf][mt], wf[buf][nt], acc[mt][nt]) : MFMA32(wf[buf][nt], xf[buf][mt], acc[mt][nt]); };
    { lds_cptr sb = (lds_cptr)lds + GDma<N96>::sbase(0); rdw(0, 0, sb, 0); rdx(0, 0, sb, 0); rdw(0, 1, sb, 0); if (N96) rdw(0, 2 % NTL, sb, 0); else rdx(0, 1 % MT, sb, 0); }
    int st = 0;
#define G_KSTEP(ks, NEXT) do { const int b = (ks) & 1, n = b ^ 1; lds_cptr np = (ks) < 3 ? sb : sn; const int nk = ((ks) + 1) & 3; \
        SBAR(); \
        if constexpr (N96) { \
            mm(b, 0, 0); SBAR(); if (NEXT) { rdw(n, 0, np, nk); rdx(n, 0, np, nk); } SBAR(); \
            mm(b, 0, 1); SBAR(); if (NEXT) { rdw(n, 1, np, nk); } SBAR(); \
            mm(b, 0, 2 % NTL); SBAR(); if (NEXT) { rdw(n, 2 % NTL, np, nk); } SBAR(); \
        } else { \
            mm(b, 0, 0); SBAR(); if (NEXT) { rdw(n, 0, np, nk); } SBAR(); \
            mm(b, 0, 1); SBAR(); if (NEXT) { rdx(n, 0, np, nk); } SBAR(); \
            mm(b, 1 % MT, 0); SBAR(); if (NEXT) { rdw(n, 1, np, nk); } SBAR(); \
            mm(b, 1 % MT, 1); SBAR(); if (NEXT) { rdx(n, 1 % MT, np, nk); } SBAR(); \
        } } while (0)
#pragma unroll 1
    for (int kc = 0; kc < 9; ++kc) {
        lds_cptr sb = (lds_cptr)lds + GDma<N96>::sbase(st);
        st = st == NST - 1 ? 0 : st + 1;
        lds_cptr sn = (lds_cptr)lds + GDma<N96>::sbase(st);
        G_KSTEP(0, true); G_KSTEP(1, true); G_KSTEP(2, true);
        hook(kc, true);
        BARL();
        hook(kc, false);
        G_KSTEP(3, true);
    }
    { lds_cptr sb = (lds_cptr)lds + GDma<N96>::sbase(st); lds_cptr sn = sb;
      G_KSTEP(0, true); G_KSTEP(1, true); G_KSTEP(2, true); G_KSTEP(3, false); }
}

DI float swap32_add(float x) { auto rr = __builtin_amdgcn_permlane32_swap(__float_as_uint(x), __float_as_uint(x), false, false); return __uint_as_float(rr[0]) + __uint_as_float(rr[1]); }
DI float swap32_max(float x) { auto rr = __builtin_amdgcn_permlane32_swap(__float_as_uint(x), __float_as_uint(x), false, false); return fmaxf(__uint_as_float(rr[0]), __uint_as_float(rr[1])); }
DI s16x4 make_qb(unsigned q0, unsigned q1, float M, int h) {
    const float mh = bf_hi(__float_as_uint(M)), r1 = M - mh, mm = bf_hi(__float_as_uint(r1)), r2 = r1 - mm, ml = bf_hi(__float_as_uint(r2));
    u32x2 w;
    w[0] = h ? ((__float_as_uint(mh) >> 16) | (__float_as_uint(mm) & 0xffff0000u)) ^ 0x80008000u : q0;
    w[1] = h ? (__float_as_uint(ml) >> 16) ^ 0x8000u : q1;
    return __builtin_bit_cast(s16x4, w);
}

DI void attn_fallback(f32x16& o, const char* kv, bf16x8 qa, unsigned q0, unsigned q1, int lane, int kboff, int voff) {
    const int h = lane >> 5;
    const f32x16 zero = {};
    float m = -INFINITY;
    const s16x4 qb0 = make_qb(q0, q1, 0.f, h);
    for (int t = 0; t < NT; ++t) {
        const char* tb = kv + t * KVTILE_B;
        const bf16x8 ka = *(const bf16x8*)(tb + lane * 16); const s16x4 kb = *(const s16x4*)(tb + kboff);
        f32x16 s = MFMA32(ka, qa, zero); s = MFMA8(kb, qb0, s);
#pragma unroll
        for (int i = 0; i < 16; ++i) m = fmaxf(m, s[i]);
    }
    m = swap32_max(m);
    const s16x4 qb = make_qb(q0, q1, m, h);
    o = zero;
    for (int t = 0; t < NT; ++t) {
        const char* tb = kv + t * KVTILE_B;
        const bf16x8 ka = *(const bf16x8*)(tb + lane * 16); const s16x4 kb = *(const s16x4*)(tb + kboff);
        const bf16x8 va = *(const bf16x8*)(tb + voff), vb = *(const bf16x8*)(tb + voff + V_STEP);
        f32x16 s = MFMA32(ka, qa, zero); s = MFMA8(kb, qb, s);
        u32x4 p0, p1;
#pragma unroll
        for (int i = 0; i < 4; ++i) { p0[i] = cvtpk(__builtin_amdgcn_exp2f(s[2 * i]), __builtin_amdgcn_exp2f(s[2 * i + 1])); p1[i] = cvtpk(__builtin_amdgcn_exp2f(s[8 + 2 * i]), __builtin_amdgcn_exp2f(s[9 + 2 * i])); }
        o = MFMA32(va, __builtin_bit_cast(bf16x8, p0), o);
        o = MFMA32(vb, __builtin_bit_cast(bf16x8, p1), o);
    }
}


constexpr int SPIN_MAX = 1 << 18;
__global__ __launch_bounds__(512, 2) void gated_mix_mega(const unsigned short* __restrict__ x, const unsigned short* __restrict__ wqkv, const unsigned short* __restrict__ wo, const unsigned short* __restrict__ w1,
                                                         const unsigned short* __restrict__ w2, const unsigned short* __restrict__ gw, char* __restrict__ ws, unsigned short* __restrict__ out) {
    extern __shared__ __attribute__((aligned(16))) char lds[];
    const int tid = threadIdx.x, lane = tid & 63, wid = rfl(tid >> 6);
    const int bid = blockIdx.x, xcd = bid & 7, slot = bid >> 3;
    {
        u64 pcq; asm volatile("s_getpc_b64 %0" : "=s"(pcq));
        if (slot < 2) glds16((const char*)((pcq + 15ull) & ~15ull) + (slot * 8 + wid) * 1024, lane * 16, (unsigned)(uintptr_t)lds + 153 * 1024);
    }
    const int h = lane >> 5, r = lane & 31;
    const rsrc_t rs = ws_rsrc(ws);
    const unsigned lds0 = (unsigned)(uintptr_t)lds, lane16 = lane * 16;
    volatile LAS unsigned* ctl = (volatile LAS unsigned*)(lds + CTL_OFF);
    const int rt = 4 * (xcd >> 1) + (slot & 3), ci = slot >> 2, ct = 8 * (xcd & 1) + ci;
    const bool tile1 = ct < 15;
    const char* xbase = (const char*)x + (size_t)rt * 128 * 1280;
    const int k0 = 0;
    const int head = xcd * 4 + (slot & 3), qblk = slot >> 2;
    const char* kv = ws + WS_KV + (size_t)head * NT * KVTILE_B;
    const int Tq = qblk * 8 + wid;
    const char* qt = ws + WS_Q + ((size_t)head * NT + Tq) * QTILE_B;
    bf16x8 qa; u32x2 qbr; f32x4 km4; float gate;
    auto q_loads = [&]() {
        asm volatile("s_nop 4\n\tglobal_load_dwordx4 %0, %1, %2" : "=v"(qa) : "v"(lane16), "s"(qt) : "memory");
        asm volatile("s_nop 4\n\tglobal_load_dwordx2 %0, %1, %2" : "=v"(qbr) : "v"((unsigned)(1024 + r * 8)), "s"(qt) : "memory");
        asm volatile("s_nop 4\n\tglobal_load_dwordx4 %0, %1, %2" : "=v"(km4) : "v"((unsigned)((lane < 40 ? lane : 0) * 16)), "s"(ws + WS_KMAX + (size_t)head * 640) : "memory");
        asm volatile("s_nop 4\n\tglobal_load_dword %0, %1, %2" : "=v"(gate) : "v"((unsigned)((Tq * 32 + r) * 4)), "s"(ws + WS_GATE) : "memory");
    };
    auto wait_seen = [&]() { for (int it = 0; it < SPIN_MAX; ++it) { if (ctl[1] != 0u) break; __builtin_amdgcn_s_sleep(1); } };
    auto gates = [&]() {
        const int aidx = (xcd >> 1) * 4 + (slot & 3);
        const u32x4* gr = (const u32x4*)gw + (lane & 7);
#pragma unroll 1
        for (int ps = 0; ps < 2; ++ps) {
            const int tok = aidx * 128 + wid * 16 + ps * 8 + (lane >> 3);
            const u32x4* xr = (const u32x4*)(x + (size_t)tok * DM) + (lane & 7);
            float sm = 0.f;
#pragma unroll
            for (int i = 0; i < 10; ++i) { const u32x4 xv = xr[i * 8], gv = gr[i * 8];
#pragma unroll
                for (int k = 0; k < 4; ++k) { const unsigned a_ = xv[k], b_ = gv[k]; sm = __builtin_amdgcn_fdot2_f32_bf16(__builtin_bit_cast(bf16x2_t, a_), __builtin_bit_cast(bf16x2_t, b_), sm, false); } }
            sm = dpp_add<0xB1>(sm); sm = dpp_add<0x4E>(sm); sm = dpp_add<0x141>(sm);
            const float lg = bf_round(sm);
            const float g = bf_round(__builtin_amdgcn_rcpf(1.f + __builtin_amdgcn_exp2f(lg * -1.4426950408889634f)));
            if ((lane & 7) == 0) st4f(ws + WS_GATE + tok * 4, g);
        }
    };
    GDma<true> d2; d2.init(xbase, (const char*)w1 + ct * 192, FF * 2, lds, lane, k0);
    u64 pc0; asm volatile("s_getpc_b64 %0" : "=s"(pc0));
    if (wid < 4) {
        GDma<false> d1; d1.init(xbase, (const char*)wqkv + ct * 256, 3 * DM * 2, lds, lane, k0);
        if (tile1) {
#pragma unroll
            for (int jj = 0; jj < 4; ++jj) d1.xp(d1.kact(0), 0, 4 * wid + jj, jj & 1);
        }
        if (tid == 0) { ctl[0] = 0u; ctl[1] = 0u; }
        {
#pragma unroll
            for (int i = 0; i < 7; ++i) {
                int u = (bid * 4 + wid) * 7 + i; if (u > 6879) u = 6879;
                const char* src = u < 2560 ? (const char*)x + (size_t)u * 1024 : u < 4960 ? (const char*)wqkv + (size_t)(u - 2560) * 1024 : (const char*)w1 + (size_t)(u - 4960) * 1024;
                glds16(src, lane16, lds0 + 152 * 1024);
            }
        }
        if (tile1) asm volatile("s_waitcnt vmcnt(7) lgkmcnt(0)\n\ts_barrier" ::: "memory");
        if (slot * 4 + wid < 37) glds16((const char*)((pc0 + 15ull) & ~15ull) + (slot * 4 + wid) * 1024, lane16, lds0 + 152 * 1024);
        for (int pc = wid; pc < 10; pc += 4) rt_load(w2, wo, bid, lds0 + RT_OFF, pc, lane);
        if (!tile1) for (int pc = wid; pc < 10; pc += 4) rt_load(w2, wo, 256 + (xcd >> 1) * 4 + (slot & 3), lds0 + RT_OFF + 10240, pc, lane);
        if (wid == 0) glds16_sc1(ws + WS_EPOCH, 0u, lds0 + EP_OFF);
        auto tag_ready = [&]() {
            if (tid == 0) { const u64 tg = *(const volatile LAS u64*)(lds + EP_OFF) + 1ull; ctl[2] = (unsigned)tg; ctl[3] = (unsigned)(tg >> 32); }
        };
        if (!tile1) { asm volatile("s_waitcnt vmcnt(0)" ::: "memory"); tag_ready(); asm volatile("s_waitcnt lgkmcnt(0)\n\ts_barrier" ::: "memory"); }
        if (tile1) {
            const int wm = wid >> 1, wn = wid & 1;
            f32x16 acc[2][2] = {};
            auto nohook = [&](int kc, bool before) {
                if (kc == 1 && before) { asm volatile("s_waitcnt vmcnt(0)" ::: "memory"); tag_ready(); }
            };
            if (ct >= 10) {
                gemm_loop<false, true, 2, 2>(acc, lds, wid, lane, nohook);
#pragma unroll
                for (int nt = 0; nt < 2; ++nt) {
                    const int phi = (ct - 10) * 128 + wn * 64 + nt * 32 + r;
                    const int hd = (phi * 3277) >> 16, d = phi - hd * 20;
                    const unsigned dst = (unsigned)WS_KV + (unsigned)(hd * NT + rt * 4 + wm * 2) * KVTILE_B + V_OFF + (h * V_ROWS + d) * 16;
#pragma unroll
                    for (int mt = 0; mt < 2; ++mt)
#pragma unroll
                        for (int s = 0; s < 2; ++s) {
                            const f32x16& a = acc[mt][nt];
                            const u32x4 w = {cvtpk(a[8 * s], a[8 * s + 1]), cvtpk(a[8 * s + 2], a[8 * s + 3]), cvtpk(a[8 * s + 4], a[8 * s + 5]), cvtpk(a[8 * s + 6], a[8 * s + 7])};
                            st16(rs, dst + mt * KVTILE_B + s * V_STEP, w);
                        }
                }
                BARL();
            } else {
                gemm_loop<false, false, 2, 2>(acc, lds, wid, lane, nohook);
                const bool isQ = ct < 5;
                const float sc = isQ ? QSCALE : 1.f;
                const unsigned img = (unsigned)(isQ ? WS_Q : WS_KV), tileb = isQ ? QTILE_B : KVTILE_B;
                const int G = (isQ ? ct : ct - 5) * 32 + wn * 16 + h;
                u32x2 P[2][8]; float nrm[8];
#pragma unroll
                for (int p = 0; p < 8; ++p) {
                    const int nt = p >> 2, g = p & 3; float m = 0.f;
#pragma unroll
                    for (int mt = 0; mt < 2; ++mt) {
                        const f32x16& a = acc[mt][nt];
                        P[mt][p] = (u32x2){cvtpk(a[4 * g] * sc, a[4 * g + 1] * sc), cvtpk(a[4 * g + 2] * sc, a[4 * g + 3] * sc)};
                        m = fmaxf(m, __builtin_fmaf(a[4 * g], a[4 * g], a[4 * g + 1] * a[4 * g + 1]) + __builtin_fmaf(a[4 * g + 2], a[4 * g + 2], a[4 * g + 3] * a[4 * g + 3]));
                    }
                    nrm[p] = m;
                }
#pragma unroll
                for (int p = 0; p < 8; ++p) {
                    const int gam = G + 2 * p, hd = (gam * 205) >> 10, dg = gam - 5 * hd;
                    const unsigned base = img + (unsigned)(hd * NT + rt * 4 + wm * 2) * tileb;
#pragma unroll
                    for (int mt = 0; mt < 2; ++mt) {
                        const unsigned bm = base + mt * tileb;
                        if (dg < 2) {
                            if (p < 7) st16(rs, bm + dg * 512 + r * 16, (u32x4){P[mt][p][0], P[mt][p][1], P[mt][(p + 1) & 7][0], P[mt][(p + 1) & 7][1]});
                            else st8(rs, bm + dg * 512 + r * 16, P[mt][p]);
                        } else if (dg == 4) st8(rs, bm + 1024 + r * 8, P[mt][p]);
                        else if (p == 0) st8(rs, bm + (dg - 2) * 512 + r * 16 + 8, P[mt][p]);
                    }
                }
                if (!isQ) {
#pragma unroll
                    for (int i = 0; i < 8; ++i) { float m = nrm[i]; m = dpp_max<0xB1>(m); m = dpp_max<0x4E>(m); m = dpp_max<0x141>(m); m = dpp_max<0x140>(m);
                        auto rr = __builtin_amdgcn_permlane16_swap(__float_as_uint(m), __float_as_uint(m), false, false); nrm[i] = fmaxf(__uint_as_float(rr[0]), __uint_as_float(rr[1])); }
                    float mine = nrm[0];
#pragma unroll
                    for (int i = 1; i < 8; ++i) mine = r == i ? nrm[i] : mine;
                    if (r < 8) st4f(ws + WS_KMAX + (size_t)((G + 2 * r) * 32 + rt * 2 + wm) * 4, mine);
                }
                BARL();
            }
        } else { gates(); BARL(); }
        BARL();
        f32x16 acc2[1][3] = {};
        auto hook2 = [&](int kc, bool before) {
            if (kc == 2) { if (before) asm volatile("s_waitcnt vmcnt(0)" ::: "memory");
                           else if (tid == 0) flag_set(ws + WS_F1 + bid * 8, ((u64)ctl[3] << 32) | ctl[2]); }
        };
        gemm_loop<true, false, 1, 3>(acc2, lds, wid, lane, hook2);
        wait_seen();
        q_loads();
        {   const int tok = rt * 128 + wid * 32 + r, rho = tok >> 4;
#pragma unroll
            for (int nt = 0; nt < 3; ++nt) {
                const unsigned dst = (unsigned)WS_AF + (unsigned)(rho * NSIG + ct * 3 + nt) * 1024 + (h * 16 + (r & 15)) * 16;
                float sv[16];
#pragma unroll
                for (int i = 0; i < 16; ++i) { const float v = acc2[0][nt][i]; sv[i] = v * __builtin_amdgcn_rcpf(1.f + __builtin_amdgcn_exp2f(v * -1.4426950408889634f)); }
#pragma unroll
                for (int s = 0; s < 2; ++s) {
                    const u32x4 w = {cvtpk(sv[8 * s], sv[8 * s + 1]), cvtpk(sv[8 * s + 2], sv[8 * s + 3]), cvtpk(sv[8 * s + 4], sv[8 * s + 5]), cvtpk(sv[8 * s + 6], sv[8 * s + 7])};
                    st16(rs, dst + s * 512, w);
                }
            }
        }
        asm volatile("s_waitcnt vmcnt(6)" : "+v"(qa), "+v"(qbr), "+v"(km4), "+v"(gate) :: "memory");
    } else {
        const int lw = wid - 4;
        auto side_stores = [&]() {
            for (int i = lw; i < 10; i += 4) rt_store(rs, bid, lds + RT_OFF, i, lane);
            if (!tile1) for (int i = lw; i < 10; i += 4) rt_store(rs, 256 + (xcd >> 1) * 4 + (slot & 3), lds + RT_OFF + 10240, i, lane);
            if (lw == 0) { const int i = bid * 64 + lane;
                const unsigned f = (i & 1) ? 0u : ONES2; st16(rs, (unsigned)WS_KV + (unsigned)(i >> 3) * KVTILE_B + V_OFF + ((i >> 2) & 1) * V_STEP + (((i >> 1) & 1) * V_ROWS + 20 + (i & 1)) * 16, (u32x4){f, f, f, f}); }
        };
        if (tile1) {
            GDma<false> d1; d1.init(xbase, (const char*)wqkv + ct * 256, 3 * DM * 2, lds, lane, k0);
#pragma unroll
            for (int jj = 0; jj < 4; ++jj) d1.wp(d1.kact(0), 0, lw, jj);
            d1.issue(1, 1, lw);
            WAITB(8);
            d1.issue(2, 2, lw); d1.issue(3, 3, lw);
            for (int kc = 0; kc < 9; ++kc) {
                if (kc < 7) WAITB(16); else if (kc == 7) WAITB(15); else WAITB(14);
                if (kc + 4 < 10) d1.issue(kc + 4, kc & 3, lw);
                if (kc == 4) side_stores();
                if (kc >= 6) d2.issue(kc - 6, kc - 6, lw);
            }
            asm volatile("s_barrier" ::: "memory");
        } else {
            asm volatile("s_barrier" ::: "memory");
            side_stores(); gates();
            BARL();
            d2.issue(0, 0, lw); d2.issue(1, 1, lw); d2.issue(2, 2, lw);
        }
        WAITB(14);
        { int st = 0;
          for (int kc = 0; kc < 9; ++kc) {
              if (kc < 8) WAITB(7); else WAITB(0);
              if (kc + 3 < 10) d2.issue(kc + 3, st, lw);
              st = st == 2 ? 0 : st + 1;
          } }
        if (lw == 0) {
            const u64 tag = ((u64)ctl[3] << 32) | ctl[2];
            for (int it = 0; it < SPIN_MAX; ++it) {
                bool ok = true;
#pragma unroll
                for (int i = 0; i < 4; ++i) ok &= flag_get(ws + WS_F1 + (lane + 64 * i) * 8) == tag;
                if (__all(ok)) break;
                __builtin_amdgcn_s_sleep(2);
            }
            __builtin_amdgcn_fence(__ATOMIC_ACQUIRE, "agent");
            asm volatile("s_waitcnt vmcnt(0)" ::: "memory");
            if (lane == 0) { ctl[1] = 1u; if (bid == 0) flag_set(ws + WS_EPOCH, tag); }
        } else wait_seen();
        q_loads();
        for (int j = 0; j < 21; ++j) glds16(kv + (size_t)(lw + 4 * j) * 1024, lane16, lds0 + (lw + 4 * j) * 1024);
        asm volatile("s_waitcnt vmcnt(21)" : "+v"(qa), "+v"(qbr), "+v"(km4), "+v"(gate) :: "memory");
    }
    if (wid >= 4) __builtin_amdgcn_s_setprio(1);
    float km = fmaxf(fmaxf(km4[0], km4[1]), fmaxf(km4[2], km4[3]));
    km = dpp_max<0xB1>(km); km = dpp_max<0x4E>(km); km = dpp_max<0x141>(km);
    km = __builtin_amdgcn_sqrtf(km);
    float M;
    { const float c0 = rdl(km, 0), c1 = rdl(km, 8), c2 = rdl(km, 16), c3 = rdl(km, 24), c4 = rdl(km, 32);
      const u32x4 qw = __builtin_bit_cast(u32x4, qa);
      float n0, n1, n2;
      { const float a = bf_lo(qw[0]), b = bf_hi(qw[0]), c = bf_lo(qw[1]), d = bf_hi(qw[1]); n0 = (a * a + b * b) + (c * c + d * d); }
      { const float a = bf_lo(qw[2]), b = bf_hi(qw[2]), c = bf_lo(qw[3]), d = bf_hi(qw[3]); n1 = (a * a + b * b) + (c * c + d * d); }
      { const unsigned q0 = qbr[0], q1 = qbr[1]; const float a = bf_lo(q0), b = bf_hi(q0), c = bf_lo(q1), d = bf_hi(q1); n2 = (a * a + b * b) + (c * c + d * d); }
      float part = __builtin_amdgcn_sqrtf(n0) * (h ? c1 : c0) + __builtin_amdgcn_sqrtf(n1) * (h ? c3 : c2);
      if (!h) part += __builtin_amdgcn_sqrtf(n2) * c4;
      M = swap32_add(part) * 1.01f; }
    const s16x4 qb = make_qb(qbr[0], qbr[1], M, h);
    const int vrow = r <= 20 ? r : (r < 28 ? 21 : r - 12);
    const int voff = V_OFF + (h * V_ROWS + vrow) * 16, kboff = h ? V_OFF + 20 * 16 : 1024 + r * 8;
    lds_cptr kaC = (lds_cptr)lds + lane * 16, kbC = (lds_cptr)lds + kboff, vC = (lds_cptr)lds + voff;
    lds_cptr kaN = kaC + K2_BIG, kbN = kbC + K2_BIG, vN = vC + K2_BIG;
    const f32x16 zero = {};
    f32x16 o = {}, sA, sB;
    bf16x8 ka, va, vb; s16x4 kb;
    u32x4 plo, phi;
    if (wid >= 4) asm volatile("s_waitcnt vmcnt(15)" ::: "memory");
    asm volatile("s_barrier" ::: "memory");
    ka = lds128(kaC); kb = lds64(kbC);
    sA = MFMA32(ka, qa, zero); sA = MFMA8(kb, qb, sA);
    ka = lds128(kaC + KVTILE_B); kb = lds64(kbC + KVTILE_B);
    va = lds128(vC);
    vb = (bf16x8){}; phi = (u32x4){};
    f32x4 e3 = {};
    const int ng = (xcd & 1) * 4 + (slot & 3), bt = (xcd >> 1) * 8 + (slot >> 2);
    const char* ap = ws + WS_AF + (size_t)bt * 4 * NSIG * 1024;
    const char* bp = ws + WS_BF + (size_t)ng * NSIG * 5 * 1024;
        const int rot = ((slot >> 2) + 3 * (slot & 3)) % 6;
    auto hs = [&](int i) { int st = i + rot; if (st >= 6) st -= 6; return wid * 6 + st; };
    bf16x8 a[3][4], b[3][5];
    auto ldA1 = [&](int sg, int mt, bf16x8& d) { gld128_sc1(d, ap + ((size_t)mt * NSIG + sg) * 1024, lane16); };
    auto ldB1 = [&](int sg, int nt, bf16x8& d) { gld128(d, bp + sg * 5120 + nt * 1024, lane16); };
    const u64 tag = ((u64)ctl[3] << 32) | ctl[2];
#define EXP4(X, i) do { X[i] = __builtin_amdgcn_exp2f(X[i]); X[i + 1] = __builtin_amdgcn_exp2f(X[i + 1]); X[i + 2] = __builtin_amdgcn_exp2f(X[i + 2]); X[i + 3] = __builtin_amdgcn_exp2f(X[i + 3]); } while (0)
#define KADDR(A, i) (((i) < 16) ? (A##C + (i) * KVTILE_B) : (A##N + ((i) - 16) * KVTILE_B))
#define STEP(C, N, i, X0, X1) do { \
        bf16x8 ka2, va2, vb2; s16x4 kb2; \
        N = MFMA32(ka, qa, zero); SBAR(); \
        ka2 = lds128(KADDR(ka, (i) + 2)); \
        phi[2] = cvtpk(e3[0], e3[1]); phi[3] = cvtpk(e3[2], e3[3]); PIN(phi); SBAR(); EXP4(C, 0); PIN(C); SBAR(); \
        o = MFMA32(vb, __builtin_bit_cast(bf16x8, phi), o); SBAR(); \
        X0; \
        vb2 = lds128(vC + (i) * KVTILE_B + V_STEP); \
        plo[0] = cvtpk(C[0], C[1]); plo[1] = cvtpk(C[2], C[3]); PIN(plo); SBAR(); EXP4(C, 4); PIN(C); SBAR(); \
        N = MFMA8(kb, qb, N); SBAR(); \
        kb2 = lds64(KADDR(kb, (i) + 2)); \
        plo[2] = cvtpk(C[4], C[5]); plo[3] = cvtpk(C[6], C[7]); PIN(plo); SBAR(); EXP4(C, 8); PIN(C); SBAR(); \
        o = MFMA32(va, __builtin_bit_cast(bf16x8, plo), o); SBAR(); \
        X1; \
        va2 = lds128(KADDR(v, (i) + 1)); \
        phi[0] = cvtpk(C[8], C[9]); phi[1] = cvtpk(C[10], C[11]); PIN(phi); SBAR(); \
        e3[0] = __builtin_amdgcn_exp2f(C[12]); e3[1] = __builtin_amdgcn_exp2f(C[13]); e3[2] = __builtin_amdgcn_exp2f(C[14]); e3[3] = __builtin_amdgcn_exp2f(C[15]); PIN(e3); SBAR(); \
        ka = ka2; kb = kb2; va = va2; vb = vb2; \
    } while (0)
#define PIECE(j) do { if (8 * (j) + 7 < 42 || 8 * (j) + wid < 42) glds16(src + (j) * 8192, lane16, dst + (j) * 8192); } while (0)
    const char* src = kv + (84 + wid) * 1024; unsigned dst = lds0 + wid * 1024 + 2 * K2_BIG;
#pragma unroll 1
    for (int c = 0; c < 3; ++c) {
        STEP(sA, sB, 0, , ); STEP(sB, sA, 1, , ); STEP(sA, sB, 2, , ); STEP(sB, sA, 3, , ); STEP(sA, sB, 4, , );
        if (c == 0) { if (wid >= 4) asm volatile("s_waitcnt vmcnt(10)" ::: "memory"); asm volatile("s_barrier" ::: "memory"); }
        STEP(sB, sA, 5, , ); STEP(sA, sB, 6, , ); STEP(sB, sA, 7, , ); STEP(sA, sB, 8, , ); STEP(sB, sA, 9, , ); STEP(sA, sB, 10, , ); STEP(sB, sA, 11, , ); STEP(sA, sB, 12, , );
        if (c == 2 && wid == 0) {
            for (int it = 0; it < SPIN_MAX; ++it) {
                if (__all(flag_get(ws + WS_F2 + ((bt >> 1) * 16 + (lane & 15)) * 8) == tag)) break;
                __builtin_amdgcn_s_sleep(2);
            }
        }
        WAITB(0);
        if (c == 0 && tid == 0) flag_set(ws + WS_F2 + (rt * 16 + ct) * 8, tag);
        if (c < 2) { STEP(sB, sA, 13, PIECE(0), PIECE(1)); STEP(sA, sB, 14, PIECE(2), PIECE(3)); STEP(sB, sA, 15, PIECE(4), PIECE(5)); }
        else { STEP(sB, sA, 13, , ); STEP(sA, sB, 14, , ); STEP(sB, sA, 15, , ); }
        const int d = (c == 1) ? -2 * K2_BIG : K2_BIG;
        kaC = kaN; kbC = kbN; vC = vN; kaN += d; kbN += d; vN += d;
        src += 42 * 1024; dst -= 2 * K2_BIG;
    }
#define PA(s, m) ldA1(hs(s), m, a[s][m])
#define PB(s, n) ldB1(hs(s), n, b[s][n])
    STEP(sA, sB, 0, PA(0, 0), PA(0, 1)); STEP(sB, sA, 1, PA(0, 2), PA(0, 3)); STEP(sA, sB, 2, PB(0, 0), PB(0, 1)); STEP(sB, sA, 3, PB(0, 2), PB(0, 3));
    STEP(sA, sB, 4, PB(0, 4), PA(1, 0)); STEP(sB, sA, 5, PA(1, 1), PA(1, 2)); STEP(sA, sB, 6, PA(1, 3), PB(1, 0)); STEP(sB, sA, 7, PB(1, 1), PB(1, 2));
    STEP(sA, sB, 8, PB(1, 3), PB(1, 4)); STEP(sB, sA, 9, PA(2, 0), PA(2, 1)); STEP(sA, sB, 10, PA(2, 2), PA(2, 3)); STEP(sB, sA, 11, PB(2, 0), PB(2, 1));
    STEP(sA, sB, 12, PB(2, 2), PB(2, 3)); STEP(sB, sA, 13, PB(2, 4), ); STEP(sA, sB, 14, , ); STEP(sB, sA, 15, , );
    phi[2] = cvtpk(e3[0], e3[1]); phi[3] = cvtpk(e3[2], e3[3]);
    o = MFMA32(vb, __builtin_bit_cast(bf16x8, phi), o);
    float l; { auto rr = __builtin_amdgcn_permlane32_swap(__float_as_uint(o[8]), __float_as_uint(o[8]), false, false); l = __uint_as_float(rr[1]); }
    if (__builtin_expect(__any(!(l >= 8.6736174e-19f && l <= 1.0e30f)), 0)) {
        asm volatile("s_waitcnt vmcnt(0)" ::: "memory");
        attn_fallback(o, kv, qa, qbr[0], qbr[1], lane, kboff, voff);
        auto rr = __builtin_amdgcn_permlane32_swap(__float_as_uint(o[8]), __float_as_uint(o[8]), false, false); l = __uint_as_float(rr[1]);
    }
    const float f = gate * __builtin_amdgcn_rcpf(l);
    const int tok = Tq * 32 + r;
    char* ab = ws + WS_AF + (size_t)(tok >> 4) * NSIG * 1024 + (tok & 15) * 16;
#pragma unroll
    for (int g = 0; g < 3; ++g) {
        const int gam = head * 5 + 2 * g + h;
        const int sig = 48 + (gam >> 3), c = ((gam >> 2) & 1) * 2 + (gam & 1), half = (gam >> 1) & 1;
        const u32x2 w = {cvtpk(o[4 * g] * f, o[4 * g + 1] * f), cvtpk(o[4 * g + 2] * f, o[4 * g + 3] * f)};
        if (g < 2 || h == 0) __hip_atomic_store((u64*)(ab + sig * 1024 + c * 256 + half * 8), ((u64)w[1] << 32) | w[0], __ATOMIC_RELAXED, __HIP_MEMORY_SCOPE_AGENT);
    }
    {
        f32x4 acc[4][5] = {};
        auto loadA = [&](int sg, bf16x8 (&da)[4]) {
#pragma unroll
            for (int mt = 0; mt < 4; ++mt) gld128_sc1(da[mt], ap + ((size_t)mt * NSIG + sg) * 1024, lane16);
        };
        auto loadB = [&](int sg, bf16x8 (&db)[5]) {
#pragma unroll
            for (int nt = 0; nt < 5; ++nt) gld128(db[nt], bp + sg * 5120 + nt * 1024, lane16);
        };
        auto mma = [&](bf16x8 (&da)[4], bf16x8 (&db)[5]) {
            SBAR();
#pragma unroll
            for (int nt = 0; nt < 5; ++nt)
#pragma unroll
                for (int mt = 0; mt < 4; ++mt) acc[mt][nt] = MFMA16(db[nt], da[mt], acc[mt][nt]);
            SBAR();
        };
        wait9<3>(a[0], b[0]);
        asm volatile("" : "+v"(a[1][0]), "+v"(a[1][1]), "+v"(a[1][2]), "+v"(a[1][3]), "+v"(b[1][0]), "+v"(b[1][1]), "+v"(b[1][2]), "+v"(b[1][3]), "+v"(b[1][4]));
        asm volatile("" : "+v"(a[2][0]), "+v"(a[2][1]), "+v"(a[2][2]), "+v"(a[2][3]), "+v"(b[2][0]), "+v"(b[2][1]), "+v"(b[2][2]), "+v"(b[2][3]), "+v"(b[2][4]));
        mma(a[0], b[0]); loadA(hs(3), a[0]); loadB(hs(3), b[0]);
        mma(a[1], b[1]); loadA(hs(4), a[1]); loadB(hs(4), b[1]);
        asm volatile("s_waitcnt vmcnt(18)" ::: "memory");
        if (lane == 0) {
            const unsigned old = __hip_atomic_fetch_add((unsigned*)(lds + CTL_OFF), 1u, __ATOMIC_RELAXED, __HIP_MEMORY_SCOPE_WORKGROUP);
            if (old == 7u) flag_set(ws + WS_F3 + (qblk * 32 + head) * 8, tag);
        }
        mma(a[2], b[2]); loadA(hs(5), a[2]); loadB(hs(5), b[2]);
        float og[4];
#pragma unroll
        for (int mt = 0; mt < 4; ++mt) asm volatile("s_nop 4\n\tglobal_load_dword %0, %1, %2" : "=v"(og[mt]) : "v"((unsigned)((bt * 64 + mt * 16 + (lane & 15)) * 4)), "s"(ws + WS_GATE) : "memory");
        wait9<22>(a[0], b[0]); mma(a[0], b[0]); loadB(48 + wid, b[0]);
        wait9<18>(a[1], b[1]); mma(a[1], b[1]); loadB(56 + wid, b[1]);
        wait9<14>(a[2], b[2]); mma(a[2], b[2]);
        if (wid < 4) {
#pragma unroll
            for (int nt = 0; nt < 5; ++nt) gld128_rw(b[2][nt], bp + (64 + wid) * 5120 + nt * 1024, lane16);
        }
        asm volatile("s_waitcnt vmcnt(0)" : "+v"(og[0]), "+v"(og[1]), "+v"(og[2]), "+v"(og[3]) :: "memory");
#pragma unroll
        for (int mt = 0; mt < 4; ++mt) { const float f1 = bf_round(1.f - og[mt]);
#pragma unroll
            for (int nt = 0; nt < 5; ++nt) acc[mt][nt] *= f1; }
        if (wid == 0) {
            for (int it = 0; it < SPIN_MAX; ++it) {
                if (__all(flag_get(ws + WS_F3 + ((bt >> 2) * 32 + r) * 8) == tag)) break;
                __builtin_amdgcn_s_sleep(2);
            }
        }
        asm volatile("s_barrier" ::: "memory");
        loadA(48 + wid, a[0]); loadA(56 + wid, a[1]);
        a[2][0] = (bf16x8){}; a[2][1] = (bf16x8){}; a[2][2] = (bf16x8){}; a[2][3] = (bf16x8){};
        if (wid < 4) {
#pragma unroll
            for (int mt = 0; mt < 4; ++mt) gld128_sc1_rw(a[2][mt], ap + ((size_t)mt * NSIG + 64 + wid) * 1024, lane16);
        }
        wait9<4>(a[0], b[0]); mma(a[0], b[0]);
        wait9<0>(a[1], b[1]); wait9<0>(a[2], b[2]); mma(a[1], b[1]); mma(a[2], b[2]);
#pragma unroll
        for (int mt = 0; mt < 4; ++mt)
#pragma unroll
            for (int nt = 0; nt < 5; ++nt) *(f32x4*)(lds + (wid * 20 + mt * 5 + nt) * 1024 + lane16) = acc[mt][nt];
        BARL();
#pragma unroll
        for (int i = 0; i < 3; ++i) { const int t = wid + 8 * i;
            if (t < 20) {
                f32x4 sum = *(const f32x4*)(lds + t * 1024 + lane16);
#pragma unroll
                for (int w = 1; w < 8; ++w) sum += *(const f32x4*)(lds + (w * 20 + t) * 1024 + lane16);
                const int m4 = t / 5, nt = t - m4 * 5;
                const int tok2 = bt * 64 + m4 * 16 + (lane & 15);
                *(u32x2*)(out + (size_t)tok2 * DM + ng * 80 + nt * 16 + (lane >> 4) * 4) = (u32x2){cvtpk(sum[0], sum[1]), cvtpk(sum[2], sum[3])};
            } }
    }
}

extern "C" void kernel_launch(void* const* d_in, const int* in_sizes, int n_in, void* d_out, int out_size, void* d_ws, size_t ws_size, hipStream_t stream) {
    const unsigned short* x = (const unsigned short*)d_in[0];
    const unsigned short* wqkv = (const unsigned short*)d_in[1];
    const unsigned short* wo = (const unsigned short*)d_in[2];
    const unsigned short* w1 = (const unsigned short*)d_in[4];
    const unsigned short* w2 = (const unsigned short*)d_in[5];
    const unsigned short* gw = (const unsigned short*)d_in[6];
    (void)hipFuncSetAttribute((const void*)gated_mix_mega, hipFuncAttributeMaxDynamicSharedMemorySize, LDS_ALL);
    gated_mix_mega<<<256, 512, LDS_ALL, stream>>>(x, wqkv, wo, w1, w2, gw, (char*)d_ws, (unsigned short*)d_out);
}
```
